```python
import torch

def _softmax(x, axis=-1):
    return torch.softmax(x, dim=axis)
def _gelu(x):
    return torch.nn.functional.gelu(x, approximate="tanh")
def _silu(x):
    return torch.nn.functional.silu(x)
def _sigmoid(x):
    return torch.sigmoid(x)
def _layernorm(x, axis=-1, eps=1e-5):
    m = x.mean(dim=axis, keepdim=True)
    v = x.var(dim=axis, keepdim=True, unbiased=False)
    return (x - m) / torch.sqrt(v + eps)
def _scatter_add(tgt, idx, src, axis=0):
    return tgt.clone().index_add_(axis, idx, src)
def _segment_sum(src, seg, n):
    out = torch.zeros((n,) + src.shape[1:], dtype=src.dtype)
    return out.index_add_(0, seg, src)
def _pad(x, pw):
    flat = tuple(v for pair in reversed(pw) for v in pair)
    return torch.nn.functional.pad(x, flat)

kernel_name = 'standalone__gqa_softmax__near_ridge'

def setup_inputs():

    _w = (lambda a: (lambda t: t.to(torch.bfloat16) if t.is_floating_point() else t)(torch.as_tensor(a)))
    B, Hq, Hkv, Sq, Sk, D = 8, 32, 32, 736, 736, 128
    return {
        "q": _w((lambda *s: torch.randn(*s, dtype=torch.bfloat16) * 0.8)(B, Hq, Sq, D)),
        "k": _w((lambda *s: torch.randn(*s, dtype=torch.bfloat16) * 0.8)(B, Hkv, Sk, D)),
        "v": _w((lambda *s: torch.randn(*s, dtype=torch.bfloat16) * 0.05)(B, Hkv, Sk, D)),
    }

def reference(q, k, v):
    B, Hq, Sq, D = q.shape
    Hkv = k.shape[1]
    rep = Hq // Hkv
    k = (lambda x, n, axis: torch.repeat_interleave(x, n, dim=axis))(k, rep, axis=1)
    v = (lambda x, n, axis: torch.repeat_interleave(x, n, dim=axis))(v, rep, axis=1)
    scale = D ** -0.5
    scores = torch.einsum("bhqd,bhkd->bhqk", q, k) * scale
    p = _softmax(scores, axis=-1)
    return torch.einsum("bhqk,bhkd->bhqd", p, v)
```

```cpp
#include <hip/hip_runtime.h>
#include <hip/hip_bf16.h>

typedef float    f32x16 __attribute__((ext_vector_type(16)));
typedef float    f32x2  __attribute__((ext_vector_type(2)));
typedef __bf16   bf16x8 __attribute__((ext_vector_type(8)));
typedef __bf16   bf16x2 __attribute__((ext_vector_type(2)));
typedef unsigned u32x4  __attribute__((ext_vector_type(4)));
typedef unsigned u32x2  __attribute__((ext_vector_type(2)));

constexpr int S = 736, D = 128, NB = 23;

__device__ __forceinline__ unsigned cvtpk(float lo, float hi) {
  f32x2 v = {lo, hi}; bf16x2 b = __builtin_convertvector(v, bf16x2); return __builtin_bit_cast(unsigned, b);
}

__device__ __forceinline__ void slow_block(const unsigned short* __restrict__ Qh, const unsigned short* __restrict__ Kh,
                                           const unsigned short* __restrict__ Vh, unsigned short* __restrict__ Oh, int qb, int lane) {
  const int r = lane & 31, h = lane >> 5;
  const float C = 0.12751743082f;
  bf16x8 qf[8];
#pragma unroll
  for (int s = 0; s < 8; ++s) qf[s] = *(const bf16x8*)(Qh + (qb * 32 + r) * D + 16 * s + 8 * h);
  f32x16 o[4];
#pragma unroll
  for (int db = 0; db < 4; ++db)
#pragma unroll
    for (int i = 0; i < 16; ++i) o[db][i] = 0.f;
  float m = -INFINITY, l = 0.f;
  for (int t = 0; t < NB; ++t) {
    f32x16 sc;
#pragma unroll
    for (int i = 0; i < 16; ++i) sc[i] = 0.f;
#pragma unroll
    for (int s = 0; s < 8; ++s) {
      bf16x8 kf = *(const bf16x8*)(Kh + (t * 32 + r) * D + 16 * s + 8 * h);
      sc = __builtin_amdgcn_mfma_f32_32x32x16_bf16(kf, qf[s], sc, 0, 0, 0);
    }
    float mx = sc[0];
#pragma unroll
    for (int i = 1; i < 16; ++i) mx = fmaxf(mx, sc[i]);
    mx = fmaxf(mx, __shfl_xor(mx, 32));
    const float mn = fmaxf(m, mx);
    const float alpha = __builtin_amdgcn_exp2f((m - mn) * C);
    m = mn;
    float ps = 0.f;
#pragma unroll
    for (int i = 0; i < 16; ++i) { sc[i] = __builtin_amdgcn_exp2f((sc[i] - mn) * C); ps += sc[i]; }
    l = l * alpha + ps;
#pragma unroll
    for (int db = 0; db < 4; ++db)
#pragma unroll
      for (int i = 0; i < 16; ++i) o[db][i] *= alpha;
#pragma unroll
    for (int s = 0; s < 2; ++s) {
      u32x4 pw;
#pragma unroll
      for (int p = 0; p < 4; ++p) pw[p] = cvtpk(sc[8 * s + 2 * p], sc[8 * s + 2 * p + 1]);
      bf16x8 pf = __builtin_bit_cast(bf16x8, pw);
#pragma unroll
      for (int db = 0; db < 4; ++db) {
        u32x4 vw;
#pragma unroll
        for (int p = 0; p < 4; ++p) {
          const int j0 = 2 * p, j1 = 2 * p + 1;
          const int k0 = t * 32 + 16 * s + 8 * (j0 >> 2) + 4 * h + (j0 & 3);
          const int k1 = t * 32 + 16 * s + 8 * (j1 >> 2) + 4 * h + (j1 & 3);
          vw[p] = (unsigned)Vh[k0 * D + 32 * db + r] | ((unsigned)Vh[k1 * D + 32 * db + r] << 16);
        }
        o[db] = __builtin_amdgcn_mfma_f32_32x32x16_bf16(__builtin_bit_cast(bf16x8, vw), pf, o[db], 0, 0, 0);
      }
    }
  }
  l += __shfl_xor(l, 32);
  const float il = 1.0f / l;
#pragma unroll
  for (int db = 0; db < 4; ++db)
#pragma unroll
    for (int g = 0; g < 4; ++g) {
      u32x2 w = {cvtpk(o[db][4 * g] * il, o[db][4 * g + 1] * il), cvtpk(o[db][4 * g + 2] * il, o[db][4 * g + 3] * il)};
      *(u32x2*)(Oh + (qb * 32 + r) * D + 32 * db + 8 * g + 4 * h) = w;
    }
}

__global__ __launch_bounds__(64) void attn_slow(const unsigned short* __restrict__ Q, const unsigned short* __restrict__ K,
                                                const unsigned short* __restrict__ V, unsigned short* __restrict__ O) {
  const int head = blockIdx.x / NB, qb = blockIdx.x % NB;
  const size_t off = (size_t)head * S * D;
  slow_block(Q + off, K + off, V + off, O + off, qb, threadIdx.x);
}

extern "C" void kernel_launch(void* const* d_in, const int* in_sizes, int n_in, void* d_out, int out_size, void* d_ws, size_t ws_size,
                              hipStream_t stream) {
  attn_slow<<<256 * NB, 64, 0, stream>>>((const unsigned short*)d_in[0], (const unsigned short*)d_in[1], (const unsigned short*)d_in[2],
                                         (unsigned short*)d_out);
}
```

```cpp
#include <hip/hip_runtime.h>
#include <hip/hip_bf16.h>

typedef float    f32x16 __attribute__((ext_vector_type(16)));
typedef float    f32x2  __attribute__((ext_vector_type(2)));
typedef __bf16   bf16x8 __attribute__((ext_vector_type(8)));
typedef __bf16   bf16x2 __attribute__((ext_vector_type(2)));
typedef unsigned u32x4  __attribute__((ext_vector_type(4)));
typedef unsigned u32x2  __attribute__((ext_vector_type(2)));

constexpr int S = 736, D = 128, NB = 23;

__device__ __forceinline__ unsigned cvtpk(float lo, float hi) {
  f32x2 v = {lo, hi}; bf16x2 b = __builtin_convertvector(v, bf16x2); return __builtin_bit_cast(unsigned, b);
}

__device__ __forceinline__ void slow_block(const unsigned short* __restrict__ Qh, const unsigned short* __restrict__ Kh,
                                           const unsigned short* __restrict__ Vh, unsigned short* __restrict__ Oh, int qb, int lane) {
  const int r = lane & 31, h = lane >> 5;
  const float C = 0.12751743082f;
  bf16x8 qf[8];
#pragma unroll
  for (int s = 0; s < 8; ++s) qf[s] = *(const bf16x8*)(Qh + (qb * 32 + r) * D + 16 * s + 8 * h);
  f32x16 o[4];
#pragma unroll
  for (int db = 0; db < 4; ++db)
#pragma unroll
    for (int i = 0; i < 16; ++i) o[db][i] = 0.f;
  float m = -INFINITY, l = 0.f;
  for (int t = 0; t < NB; ++t) {
    f32x16 sc;
#pragma unroll
    for (int i = 0; i < 16; ++i) sc[i] = 0.f;
#pragma unroll
    for (int s = 0; s < 8; ++s) {
      bf16x8 kf = *(const bf16x8*)(Kh + (t * 32 + r) * D + 16 * s + 8 * h);
      sc = __builtin_amdgcn_mfma_f32_32x32x16_bf16(kf, qf[s], sc, 0, 0, 0);
    }
    float mx = sc[0];
#pragma unroll
    for (int i = 1; i < 16; ++i) mx = fmaxf(mx, sc[i]);
    mx = fmaxf(mx, __shfl_xor(mx, 32));
    const float mn = fmaxf(m, mx);
    const float alpha = __builtin_amdgcn_exp2f((m - mn) * C);
    m = mn;
    float ps = 0.f;
#pragma unroll
    for (int i = 0; i < 16; ++i) { sc[i] = __builtin_amdgcn_exp2f((sc[i] - mn) * C); ps += sc[i]; }
    l = l * alpha + ps;
#pragma unroll
    for (int db = 0; db < 4; ++db)
#pragma unroll
      for (int i = 0; i < 16; ++i) o[db][i] *= alpha;
#pragma unroll
    for (int s = 0; s < 2; ++s) {
      u32x4 pw;
#pragma unroll
      for (int p = 0; p < 4; ++p) pw[p] = cvtpk(sc[8 * s + 2 * p], sc[8 * s + 2 * p + 1]);
      bf16x8 pf = __builtin_bit_cast(bf16x8, pw);
#pragma unroll
      for (int db = 0; db < 4; ++db) {
        u32x4 vw;
#pragma unroll
        for (int p = 0; p < 4; ++p) {
          const int j0 = 2 * p, j1 = 2 * p + 1;
          const int k0 = t * 32 + 16 * s + 8 * (j0 >> 2) + 4 * h + (j0 & 3);
          const int k1 = t * 32 + 16 * s + 8 * (j1 >> 2) + 4 * h + (j1 & 3);
          vw[p] = (unsigned)Vh[k0 * D + 32 * db + r] | ((unsigned)Vh[k1 * D + 32 * db + r] << 16);
        }
        o[db] = __builtin_amdgcn_mfma_f32_32x32x16_bf16(__builtin_bit_cast(bf16x8, vw), pf, o[db], 0, 0, 0);
      }
    }
  }
  l += __shfl_xor(l, 32);
  const float il = 1.0f / l;
#pragma unroll
  for (int db = 0; db < 4; ++db)
#pragma unroll
    for (int g = 0; g < 4; ++g) {
      u32x2 w = {cvtpk(o[db][4 * g] * il, o[db][4 * g + 1] * il), cvtpk(o[db][4 * g + 2] * il, o[db][4 * g + 3] * il)};
      *(u32x2*)(Oh + (qb * 32 + r) * D + 32 * db + 8 * g + 4 * h) = w;
    }
}

__global__ __launch_bounds__(64) void attn_slow(const unsigned short* __restrict__ Q, const unsigned short* __restrict__ K,
                                                const unsigned short* __restrict__ V, unsigned short* __restrict__ O) {
  const int head = blockIdx.x / NB, qb = blockIdx.x % NB;
  const size_t off = (size_t)head * S * D;
  slow_block(Q + off, K + off, V + off, O + off, qb, threadIdx.x);
}


__global__ __launch_bounds__(256, 1) void burn(const unsigned short* __restrict__ Q, const unsigned short* __restrict__ K, float* __restrict__ W, int n) {
  const int lane = threadIdx.x & 63, r = lane & 31, h = lane >> 5;
  const size_t off = (size_t)blockIdx.x * S * D + (threadIdx.x >> 6) * 32 * D;
  bf16x8 qf[8], kf[8];
#pragma unroll
  for (int s = 0; s < 8; ++s) { qf[s] = *(const bf16x8*)(Q + off + r * D + 16 * s + 8 * h); kf[s] = *(const bf16x8*)(K + off + r * D + 16 * s + 8 * h); }
  f32x16 a[4];
#pragma unroll
  for (int j = 0; j < 4; ++j)
#pragma unroll
    for (int i = 0; i < 16; ++i) a[j][i] = 0.f;
  for (int it = 0; it < n; ++it) {
#pragma unroll
    for (int s = 0; s < 16; ++s) a[s & 3] = __builtin_amdgcn_mfma_f32_32x32x16_bf16(kf[s & 7], qf[(s + (s >> 3)) & 7], a[s & 3], 0, 0, 0);
  }
  float x = 0.f;
#pragma unroll
  for (int j = 0; j < 4; ++j)
#pragma unroll
    for (int i = 0; i < 16; ++i) x += a[j][i];
  W[blockIdx.x * 256 + threadIdx.x] = x;
}
extern "C" void kernel_launch(void* const* d_in, const int* in_sizes, int n_in, void* d_out, int out_size, void* d_ws, size_t ws_size,
                              hipStream_t stream) {
  burn<<<256, 256, 0, stream>>>((const unsigned short*)d_in[0], (const unsigned short*)d_in[1], (float*)d_ws, 0);
  attn_slow<<<256 * NB, 64, 0, stream>>>((const unsigned short*)d_in[0], (const unsigned short*)d_in[1], (const unsigned short*)d_in[2],
                                         (unsigned short*)d_out);
}
```

```cpp
#include <hip/hip_runtime.h>
#include <hip/hip_bf16.h>

typedef float    f32x16 __attribute__((ext_vector_type(16)));
typedef float    f32x2  __attribute__((ext_vector_type(2)));
typedef __bf16   bf16x8 __attribute__((ext_vector_type(8)));
typedef __bf16   bf16x2 __attribute__((ext_vector_type(2)));
typedef unsigned u32x4  __attribute__((ext_vector_type(4)));
typedef unsigned u32x2  __attribute__((ext_vector_type(2)));

constexpr int S = 736, D = 128, NB = 23;

__device__ __forceinline__ unsigned cvtpk(float lo, float hi) {
  f32x2 v = {lo, hi}; bf16x2 b = __builtin_convertvector(v, bf16x2); return __builtin_bit_cast(unsigned, b);
}

__device__ __forceinline__ void slow_block(const unsigned short* __restrict__ Qh, const unsigned short* __restrict__ Kh,
                                           const unsigned short* __restrict__ Vh, unsigned short* __restrict__ Oh, int qb, int lane) {
  const int r = lane & 31, h = lane >> 5;
  const float C = 0.12751743082f;
  bf16x8 qf[8];
#pragma unroll
  for (int s = 0; s < 8; ++s) qf[s] = *(const bf16x8*)(Qh + (qb * 32 + r) * D + 16 * s + 8 * h);
  f32x16 o[4];
#pragma unroll
  for (int db = 0; db < 4; ++db)
#pragma unroll
    for (int i = 0; i < 16; ++i) o[db][i] = 0.f;
  float m = -INFINITY, l = 0.f;
  for (int t = 0; t < NB; ++t) {
    f32x16 sc;
#pragma unroll
    for (int i = 0; i < 16; ++i) sc[i] = 0.f;
#pragma unroll
    for (int s = 0; s < 8; ++s) {
      bf16x8 kf = *(const bf16x8*)(Kh + (t * 32 + r) * D + 16 * s + 8 * h);
      sc = __builtin_amdgcn_mfma_f32_32x32x16_bf16(kf, qf[s], sc, 0, 0, 0);
    }
    float mx = sc[0];
#pragma unroll
    for (int i = 1; i < 16; ++i) mx = fmaxf(mx, sc[i]);
    mx = fmaxf(mx, __shfl_xor(mx, 32));
    const float mn = fmaxf(m, mx);
    const float alpha = __builtin_amdgcn_exp2f((m - mn) * C);
    m = mn;
    float ps = 0.f;
#pragma unroll
    for (int i = 0; i < 16; ++i) { sc[i] = __builtin_amdgcn_exp2f((sc[i] - mn) * C); ps += sc[i]; }
    l = l * alpha + ps;
#pragma unroll
    for (int db = 0; db < 4; ++db)
#pragma unroll
      for (int i = 0; i < 16; ++i) o[db][i] *= alpha;
#pragma unroll
    for (int s = 0; s < 2; ++s) {
      u32x4 pw;
#pragma unroll
      for (int p = 0; p < 4; ++p) pw[p] = cvtpk(sc[8 * s + 2 * p], sc[8 * s + 2 * p + 1]);
      bf16x8 pf = __builtin_bit_cast(bf16x8, pw);
#pragma unroll
      for (int db = 0; db < 4; ++db) {
        u32x4 vw;
#pragma unroll
        for (int p = 0; p < 4; ++p) {
          const int j0 = 2 * p, j1 = 2 * p + 1;
          const int k0 = t * 32 + 16 * s + 8 * (j0 >> 2) + 4 * h + (j0 & 3);
          const int k1 = t * 32 + 16 * s + 8 * (j1 >> 2) + 4 * h + (j1 & 3);
          vw[p] = (unsigned)Vh[k0 * D + 32 * db + r] | ((unsigned)Vh[k1 * D + 32 * db + r] << 16);
        }
        o[db] = __builtin_amdgcn_mfma_f32_32x32x16_bf16(__builtin_bit_cast(bf16x8, vw), pf, o[db], 0, 0, 0);
      }
    }
  }
  l += __shfl_xor(l, 32);
  const float il = 1.0f / l;
#pragma unroll
  for (int db = 0; db < 4; ++db)
#pragma unroll
    for (int g = 0; g < 4; ++g) {
      u32x2 w = {cvtpk(o[db][4 * g] * il, o[db][4 * g + 1] * il), cvtpk(o[db][4 * g + 2] * il, o[db][4 * g + 3] * il)};
      *(u32x2*)(Oh + (qb * 32 + r) * D + 32 * db + 8 * g + 4 * h) = w;
    }
}

__global__ __launch_bounds__(64) void attn_slow(const unsigned short* __restrict__ Q, const unsigned short* __restrict__ K,
                                                const unsigned short* __restrict__ V, unsigned short* __restrict__ O) {
  const int head = blockIdx.x / NB, qb = blockIdx.x % NB;
  const size_t off = (size_t)head * S * D;
  slow_block(Q + off, K + off, V + off, O + off, qb, threadIdx.x);
}


__global__ __launch_bounds__(256, 1) void probe(unsigned* __restrict__ W) {
  unsigned x = threadIdx.x;
  asm volatile("v_add_u32 %0, 0x12345, %0\n" : "+v"(x) :: "s40", "scc");
  W[blockIdx.x * 256 + threadIdx.x] = x;
}
extern "C" void kernel_launch(void* const* d_in, const int* in_sizes, int n_in, void* d_out, int out_size, void* d_ws, size_t ws_size,
                              hipStream_t stream) {
  probe<<<256, 256, 0, stream>>>((unsigned*)d_ws);
  attn_slow<<<256 * NB, 64, 0, stream>>>((const unsigned short*)d_in[0], (const unsigned short*)d_in[1], (const unsigned short*)d_in[2],
                                         (unsigned short*)d_out);
}
```

```cpp
#include <hip/hip_runtime.h>
#include <hip/hip_bf16.h>

typedef float    f32x4  __attribute__((ext_vector_type(4)));
typedef float    f32x2  __attribute__((ext_vector_type(2)));
typedef __bf16   bf16x8 __attribute__((ext_vector_type(8)));
typedef __bf16   bf16x2 __attribute__((ext_vector_type(2)));
typedef unsigned u32x4  __attribute__((ext_vector_type(4)));
typedef unsigned u32x2  __attribute__((ext_vector_type(2)));

constexpr int S = 736, D = 128, NT = 23;
constexpr int LDS_BYTES = 160992;

__device__ __forceinline__ unsigned cvtpk(float lo, float hi) {
  f32x2 v = {lo, hi};
  bf16x2 b = __builtin_convertvector(v, bf16x2);
  return __builtin_bit_cast(unsigned, b);
}

__device__ __forceinline__ void exact_block(const unsigned short* Qh, const unsigned short* Kh, const unsigned short* Vh,
                                            unsigned short* Oh, int row0, int lane) {
  const int c = lane & 15, g = lane >> 4;
  const float C = 0.12751743082f;
  bf16x8 qf[4];
#pragma unroll
  for (int s = 0; s < 4; ++s) qf[s] = *(const bf16x8*)(Qh + (row0 + c) * D + 32 * s + 8 * g);
  f32x4 o[8];
#pragma unroll
  for (int db = 0; db < 8; ++db) o[db] = f32x4{0.f, 0.f, 0.f, 0.f};
  float m = -INFINITY, l = 0.f;
#pragma unroll 1
  for (int t = 0; t < NT; ++t) {
    f32x4 sc[2];
#pragma unroll
    for (int kb = 0; kb < 2; ++kb) {
      sc[kb] = f32x4{0.f, 0.f, 0.f, 0.f};
#pragma unroll
      for (int s = 0; s < 4; ++s) {
        const bf16x8 kf = *(const bf16x8*)(Kh + (t * 32 + 16 * kb + c) * D + 32 * s + 8 * g);
        sc[kb] = __builtin_amdgcn_mfma_f32_16x16x32_bf16(kf, qf[s], sc[kb], 0, 0, 0);
      }
    }
    float mx = fmaxf(fmaxf(fmaxf(sc[0][0], sc[0][1]), fmaxf(sc[0][2], sc[0][3])), fmaxf(fmaxf(sc[1][0], sc[1][1]), fmaxf(sc[1][2], sc[1][3])));
    mx = fmaxf(mx, __shfl_xor(mx, 32));
    mx = fmaxf(mx, __shfl_xor(mx, 16));
    const float mn = fmaxf(m, mx);
    const float ms = mn == -INFINITY ? 0.f : mn;
    const float alpha = __builtin_amdgcn_exp2f((m - ms) * C);
    m = mn;
    float ps = 0.f;
#pragma unroll
    for (int kb = 0; kb < 2; ++kb)
#pragma unroll
      for (int i = 0; i < 4; ++i) { sc[kb][i] = __builtin_amdgcn_exp2f((sc[kb][i] - ms) * C); ps += sc[kb][i]; }
    l = l * alpha + ps;
    const u32x4 pw = {cvtpk(sc[0][0], sc[0][1]), cvtpk(sc[0][2], sc[0][3]), cvtpk(sc[1][0], sc[1][1]), cvtpk(sc[1][2], sc[1][3])};
#pragma unroll
    for (int db = 0; db < 8; ++db) {
      u32x4 vw;
#pragma unroll
      for (int p = 0; p < 4; ++p) {
        const int k0 = t * 32 + 16 * (p >> 1) + 4 * g + 2 * (p & 1);
        vw[p] = (unsigned)Vh[k0 * D + 16 * db + c] | ((unsigned)Vh[(k0 + 1) * D + 16 * db + c] << 16);
      }
      o[db] *= alpha;
      o[db] = __builtin_amdgcn_mfma_f32_16x16x32_bf16(__builtin_bit_cast(bf16x8, vw), __builtin_bit_cast(bf16x8, pw), o[db], 0, 0, 0);
    }
  }
  l += __shfl_xor(l, 32);
  l += __shfl_xor(l, 16);
  const float il = 1.0f / l;
#pragma unroll
  for (int db = 0; db < 8; ++db) {
    const u32x2 w = {cvtpk(o[db][0] * il, o[db][1] * il), cvtpk(o[db][2] * il, o[db][3] * il)};
    *(u32x2*)(Oh + (row0 + c) * D + 16 * db + 4 * g) = w;
  }
}

__global__ __launch_bounds__(256, 1) __attribute__((aligned(4096))) void attn_fwd(const unsigned short* Q, const unsigned short* K, const unsigned short* V,
                                                   unsigned short* O) {
  const int wave = __builtin_amdgcn_readfirstlane(threadIdx.x >> 6);
  const int bid = blockIdx.x;
  unsigned flag;
  asm volatile(
    ".p2align 3\n"
    "s_load_dwordx8 s[24:31], %1, 0x0\n"
    "s_mov_b32 s71, %3\n"
    "v_mbcnt_lo_u32_b32 v4, -1, 0\n"
    "v_mbcnt_hi_u32_b32 v4, -1, v4\n"
    "s_getpc_b64 s[82:83]\n"
    "LPC:\n"
    "s_bfe_u32 s84, %2, 0x30003\n"
    "s_lshl2_add_u32 s84, s84, s71\n"
    "s_nop 0\n"
    "s_mul_i32 s85, s84, 2560\n"
    "v_mad_u32_u24 v162, v4, 40, s85\n"
    "v_min_u32 v162, LEND-LPC+2552, v162\n"
    "global_load_dword v163, v162, s[82:83]\n"
    "s_mov_b32 s50, 48234496\n"
    "s_mov_b32 s51, 0x20000\n"
    "s_mov_b32 s54, 48234528\n"
    "s_mov_b32 s55, 0x20000\n"
    "s_mov_b32 s38, 48234496\n"
    "s_mov_b32 s39, 0x20000\n"
    "s_mov_b32 s42, 48236544\n"
    "s_mov_b32 s43, 0x20000\n"
    "s_mov_b32 s46, 48236552\n"
    "s_mov_b32 s47, 0x20000\n"
    "s_mov_b32 s58, 48234496\n"
    "s_mov_b32 s59, 0x20000\n"
    "s_and_b32 s60, %2, 7\n"
    "s_lshr_b32 s70, %2, 4\n"
    "s_lshl3_add_u32 s60, s70, s60\n"
    "s_mul_i32 s60, s60, 188416\n"
    "s_bfe_u32 s77, %2, 0x10003\n"
    "s_mul_i32 s77, s77, 94208\n"
    "s_add_u32 s77, s77, s60\n"
    "s_mul_i32 s70, s71, 24576\n"
    "s_add_u32 s66, s77, s70\n"
    "s_mul_i32 s68, s71, 24768\n"
    "s_add_u32 s68, s68, 66048\n"
    "v_bfe_u32 v0, v4, 2, 2\n"
    "v_lshlrev_b32 v0, 8, v0\n"
    "v_lshrrev_b32 v1, 4, v4\n"
    "v_lshl_add_u32 v0, v1, 6, v0\n"
    "v_and_b32 v2, 3, v4\n"
    "v_lshl_add_u32 v253, v2, 4, v0\n"
    "s_getreg_b32 s86, hwreg(HW_REG_XCC_ID)\n"
    "s_and_b32 s86, s86, 1\n"
    "s_cmp_lg_u32 s86, 0\n"
    "s_cbranch_scc1 LSTAGD\n"
    "s_sleep 44\n"
    "LSTAGD:\n"
    "s_lshl_b32 s70, s71, 11\n"
    "s_add_u32 s70, s70, s60\n"
    "s_mul_i32 s73, s71, 4128\n"
    "s_add_u32 s61, s60, 24117248\n"
    "s_add_u32 s60, s60, 40960\n"
    "s_mov_b32 s62, 5\n"
    "s_mov_b32 s64, s77\n"
    "s_add_u32 s77, s77, 24117248\n"
    "s_mov_b32 s78, 66048\n"
    "s_mov_b32 s65, 0\n"
    "s_mov_b32 s79, 2147483648\n"
    "s_mov_b32 s76, 0xffff0000\n"
    "s_mov_b32 s72, 0x53800000\n"
    "s_mov_b32 s80, 0xc1080000\n"
    "v_bfe_u32 v3, v4, 2, 2\n"
    "v_xor_b32 v0, v3, v2\n"
    "v_lshlrev_b32 v249, 4, v0\n"
    "v_lshl_add_u32 v249, v3, 6, v249\n"
    "v_lshl_add_u32 v249, v1, 10, v249\n"
    "v_lshlrev_b32 v164, 4, v3\n"
    "v_lshl_add_u32 v164, v2, 6, v164\n"
    "v_lshl_add_u32 v164, v1, 8, v164\n"
    "v_add_u32 v164, 66048, v164\n"
    "v_lshlrev_b32 v3, 6, v3\n"
    "v_lshl_add_u32 v3, v2, 8, v3\n"
    "v_add_u32 v3, 2048, v3\n"
    "v_lshrrev_b32 v0, 5, v4\n"
    "v_mul_u32_u24 v0, 4128, v0\n"
    "v_add_u32 v3, v3, v0\n"
    "v_bfe_u32 v0, v4, 4, 1\n"
    "v_mul_u32_u24 v0, 1032, v0\n"
    "v_add_u32 v3, v3, v0\n"
    "v_xor_b32 v0, 0, v2\n"
    "s_waitcnt lgkmcnt(0)\n"
    "s_mov_b32 s48, s24\n"
    "s_and_b32 s49, s25, 0xffff\n"
    "s_sub_u32 s52, s24, 32\n"
    "s_subb_u32 s53, s25, 0\n"
    "s_and_b32 s53, s53, 0xffff\n"
    "s_mov_b32 m0, s68\n"
    "s_add_u32 s69, s66, 0\n"
    "buffer_load_dwordx4 v253, s[48:51], s69 offen offset:0 nt lds\n"
    "buffer_load_dwordx4 v253, s[48:51], s69 offen offset:1024 nt lds\n"
    "buffer_load_dwordx4 v253, s[52:55], s69 offen offset:2080 nt lds\n"
    "buffer_load_dwordx4 v253, s[52:55], s69 offen offset:3104 nt lds\n"
    "s_mov_b32 s36, s26\n"
    "s_and_b32 s37, s27, 0xffff\n"
    "s_sub_u32 s40, s28, 2048\n"
    "s_subb_u32 s41, s29, 0\n"
    "s_and_b32 s41, s41, 0xffff\n"
    "s_sub_u32 s44, s28, 2056\n"
    "s_subb_u32 s45, s29, 0\n"
    "s_and_b32 s45, s45, 0xffff\n"
    "s_mov_b32 m0, s73\n"
    "s_add_u32 s69, s70, 0\n"
    "s_nop 0\n"
    "buffer_load_dwordx4 v253, s[36:39], s69 offen offset:0 lds\n"
    "buffer_load_dwordx4 v253, s[36:39], s69 offen offset:1024 lds\n"
    "buffer_load_dwordx4 v253, s[40:43], s69 offen offset:2048 lds\n"
    "buffer_load_dwordx4 v253, s[44:47], s69 offen offset:3080 lds\n"
    "s_add_u32 m0, s73, 16512\n"
    "s_add_u32 s69, s70, 8192\n"
    "buffer_load_dwordx4 v253, s[36:39], s69 offen offset:0 lds\n"
    "buffer_load_dwordx4 v253, s[36:39], s69 offen offset:1024 lds\n"
    "buffer_load_dwordx4 v253, s[40:43], s69 offen offset:2048 lds\n"
    "buffer_load_dwordx4 v253, s[44:47], s69 offen offset:3080 lds\n"
    "s_add_u32 m0, s68, 4128\n"
    "s_add_u32 s69, s66, 4096\n"
    "buffer_load_dwordx4 v253, s[48:51], s69 offen offset:0 nt lds\n"
    "buffer_load_dwordx4 v253, s[48:51], s69 offen offset:1024 nt lds\n"
    "buffer_load_dwordx4 v253, s[52:55], s69 offen offset:2080 nt lds\n"
    "buffer_load_dwordx4 v253, s[52:55], s69 offen offset:3104 nt lds\n"
    "v_lshl_add_u32 v247, v0, 4, v3\n"
    "v_xor_b32 v0, 1, v2\n"
    "v_lshl_add_u32 v251, v0, 4, v3\n"
    "v_xor_b32 v0, 2, v2\n"
    "v_lshl_add_u32 v252, v0, 4, v3\n"
    "v_xor_b32 v0, 3, v2\n"
    "v_lshl_add_u32 v254, v0, 4, v3\n"
    "v_bfe_u32 v0, v4, 2, 1\n"
    "v_lshlrev_b32 v0, 10, v0\n"
    "v_lshl_add_u32 v0, v2, 6, v0\n"
    "v_lshl_add_u32 v0, v1, 4, v0\n"
    "v_bfe_u32 v2, v4, 3, 1\n"
    "v_mul_u32_u24 v246, 4128, v2\n"
    "v_add_u32 v246, v246, v0\n"
    "v_mul_u32_u24 v250, 2080, v2\n"
    "v_add3_u32 v250, v250, v0, s68\n"
    "v_and_b32 v0, 1, v4\n"
    "v_lshlrev_b32 v0, 4, v0\n"
    "v_and_b32 v1, 2, v4\n"
    "v_cmp_eq_u32 vcc, 0, v1\n"
    "v_mov_b32 v1, 15875\n"
    "v_lshlrev_b32 v1, v0, v1\n"
    "v_cndmask_b32 v2, 0, v1, vcc\n"
    "v_accvgpr_write_b32 a248, v2\n"
    "v_xor_b32 v2, v2, v1\n"
    "v_accvgpr_write_b32 a249, v2\n"
    "v_mov_b32 v1, 47576\n"
    "v_lshlrev_b32 v1, v0, v1\n"
    "v_cndmask_b32 v2, 0, v1, vcc\n"
    "v_accvgpr_write_b32 a250, v2\n"
    "v_xor_b32 v2, v2, v1\n"
    "v_accvgpr_write_b32 a251, v2\n"
    "v_mov_b32 v1, 16256\n"
    "v_lshlrev_b32 v1, v0, v1\n"
    "v_cndmask_b32 v2, 0, v1, vcc\n"
    "v_accvgpr_write_b32 a254, v2\n"
    "v_xor_b32 v2, v2, v1\n"
    "v_accvgpr_write_b32 a255, v2\n"
    "v_mov_b32 v1, 1065369472\n"
    "v_accvgpr_write_b32 a252, v1\n"
    "v_accvgpr_write_b32 a253, 0\n"
    "s_bfe_u32 s84, %2, 0x50003\n"
    "s_mul_i32 s84, s84, 2560\n"
    "v_mad_u32_u24 v160, v4, 40, s84\n"
    "v_min_u32 v160, LEND-LPC+2552, v160\n"
    "s_mov_b32 s56, s30\n"
    "s_and_b32 s57, s31, 0xffff\n"
    "s_bitcmp1_b32 %2, 3\n"
    "s_cselect_b64 s[88:89], s[28:29], s[26:27]\n"
    "s_and_b32 s89, s89, 0xffff\n"
    "s_mov_b32 s90, 48234496\n"
    "s_mov_b32 s91, 0x20000\n"
    "v_lshlrev_b32 v165, 7, v4\n"
    "s_waitcnt vmcnt(12)\n"
    "s_nop 0\n"
    "ds_read_b128 a[0:3], v250 offset:0\n"
    "ds_read_b128 a[4:7], v250 offset:256\n"
    "ds_read_b128 a[8:11], v250 offset:512\n"
    "ds_read_b128 a[12:15], v250 offset:768\n"
    "s_add_u32 m0, s68, 8256\n"
    "s_add_u32 s69, s66, 8192\n"
    "buffer_load_dwordx4 v253, s[48:51], s69 offen offset:0 nt lds\n"
    "buffer_load_dwordx4 v253, s[48:51], s69 offen offset:1024 nt lds\n"
    "buffer_load_dwordx4 v253, s[52:55], s69 offen offset:2080 nt lds\n"
    "buffer_load_dwordx4 v253, s[52:55], s69 offen offset:3104 nt lds\n"
    "s_waitcnt lgkmcnt(3)\n"
    "v_mfma_f32_4x4x4_16b_bf16 v[8:11], a[248:249], a[0:1], 0\n"
    "s_nop 1\n"
    "v_mfma_f32_4x4x4_16b_bf16 v[8:11], a[250:251], a[0:1], v[8:11]\n"
    "v_mfma_f32_4x4x4_16b_bf16 v[12:15], a[248:249], a[2:3], 0\n"
    "s_nop 1\n"
    "v_mfma_f32_4x4x4_16b_bf16 v[12:15], a[250:251], a[2:3], v[12:15]\n"
    "v_nop_e64\n"
    "v_cvt_pk_bf16_f32 v8, v8, v9\n"
    "s_waitcnt lgkmcnt(2)\n"
    "v_mfma_f32_4x4x4_16b_bf16 v[4:7], a[248:249], a[4:5], 0\n"
    "v_cvt_pk_bf16_f32 v9, v10, v11\n"
    "v_accvgpr_write_b32 a0, v8\n"
    "v_mfma_f32_4x4x4_16b_bf16 v[4:7], a[250:251], a[4:5], v[4:7]\n"
    "v_cvt_pk_bf16_f32 v12, v12, v13\n"
    "v_accvgpr_write_b32 a1, v9\n"
    "v_mfma_f32_4x4x4_16b_bf16 v[8:11], a[248:249], a[6:7], 0\n"
    "v_cvt_pk_bf16_f32 v13, v14, v15\n"
    "v_accvgpr_write_b32 a2, v12\n"
    "v_mfma_f32_4x4x4_16b_bf16 v[8:11], a[250:251], a[6:7], v[8:11]\n"
    "v_cvt_pk_bf16_f32 v4, v4, v5\n"
    "v_accvgpr_write_b32 a3, v13\n"
    "s_waitcnt lgkmcnt(0)\n"
    "s_nop 0\n"
    "v_mfma_f32_4x4x4_16b_bf16 v[12:15], a[248:249], a[8:9], 0\n"
    "v_cvt_pk_bf16_f32 v5, v6, v7\n"
    "v_accvgpr_write_b32 a4, v4\n"
    "v_mfma_f32_4x4x4_16b_bf16 v[12:15], a[250:251], a[8:9], v[12:15]\n"
    "v_cvt_pk_bf16_f32 v8, v8, v9\n"
    "v_accvgpr_write_b32 a5, v5\n"
    "v_mfma_f32_4x4x4_16b_bf16 v[4:7], a[248:249], a[10:11], 0\n"
    "v_cvt_pk_bf16_f32 v9, v10, v11\n"
    "v_accvgpr_write_b32 a6, v8\n"
    "v_mfma_f32_4x4x4_16b_bf16 v[4:7], a[250:251], a[10:11], v[4:7]\n"
    "v_cvt_pk_bf16_f32 v12, v12, v13\n"
    "v_accvgpr_write_b32 a7, v9\n"
    "v_mfma_f32_4x4x4_16b_bf16 v[8:11], a[248:249], a[12:13], 0\n"
    "v_cvt_pk_bf16_f32 v13, v14, v15\n"
    "v_accvgpr_write_b32 a8, v12\n"
    "v_mfma_f32_4x4x4_16b_bf16 v[8:11], a[250:251], a[12:13], v[8:11]\n"
    "v_cvt_pk_bf16_f32 v4, v4, v5\n"
    "v_accvgpr_write_b32 a9, v13\n"
    "v_mfma_f32_4x4x4_16b_bf16 v[12:15], a[248:249], a[14:15], 0\n"
    "v_cvt_pk_bf16_f32 v5, v6, v7\n"
    "v_accvgpr_write_b32 a10, v4\n"
    "v_mfma_f32_4x4x4_16b_bf16 v[12:15], a[250:251], a[14:15], v[12:15]\n"
    "v_cvt_pk_bf16_f32 v8, v8, v9\n"
    "v_accvgpr_write_b32 a11, v5\n"
    "v_cvt_pk_bf16_f32 v9, v10, v11\n"
    "v_accvgpr_write_b32 a12, v8\n"
    "v_accvgpr_write_b32 a13, v9\n"
    "v_cvt_pk_bf16_f32 v12, v12, v13\n"
    "v_cvt_pk_bf16_f32 v13, v14, v15\n"
    "v_accvgpr_write_b32 a14, v12\n"
    "v_accvgpr_write_b32 a15, v13\n"
    "s_waitcnt vmcnt(12)\n"
    "s_barrier\n"
    "ds_read_b128 a[96:99], v246 offset:0\n"
    "ds_read_b128 a[100:103], v246 offset:256\n"
    "ds_read_b128 a[104:107], v246 offset:512\n"
    "ds_read_b128 a[108:111], v246 offset:768\n"
    "ds_read_b128 a[124:127], v246 offset:9024\n"
    "ds_read_b128 a[120:123], v246 offset:8768\n"
    "ds_read_b128 a[116:119], v246 offset:8512\n"
    "ds_read_b128 a[112:115], v246 offset:8256\n"
    "s_waitcnt lgkmcnt(7)\n"
    "v_mfma_f32_16x16x32_bf16 v[28:31], a[96:99], a[0:3], 0\n"
    "s_waitcnt lgkmcnt(6)\n"
    "ds_read_b64_tr_b16 a[160:161], v247 offset:0\n"
    "ds_read_b64_tr_b16 a[162:163], v247 offset:8256\n"
    "v_mfma_f32_16x16x32_bf16 v[28:31], a[100:103], a[4:7], v[28:31]\n"
    "s_waitcnt lgkmcnt(7)\n"
    "ds_read_b64_tr_b16 a[164:165], v247 offset:8\n"
    "ds_read_b64_tr_b16 a[166:167], v247 offset:8264\n"
    "v_mfma_f32_16x16x32_bf16 v[28:31], a[104:107], a[8:11], v[28:31]\n"
    "s_waitcnt lgkmcnt(8)\n"
    "ds_read_b64_tr_b16 a[168:169], v251 offset:0\n"
    "ds_read_b64_tr_b16 a[170:171], v251 offset:8256\n"
    "v_mfma_f32_16x16x32_bf16 v[28:31], a[108:111], a[12:15], v[28:31]\n"
    "ds_read_b64_tr_b16 a[172:173], v251 offset:8\n"
    "ds_read_b64_tr_b16 a[174:175], v251 offset:8264\n"
    "ds_read_b64_tr_b16 a[176:177], v252 offset:0\n"
    "ds_read_b64_tr_b16 a[178:179], v252 offset:8256\n"
    "ds_read_b64_tr_b16 a[180:181], v252 offset:8\n"
    "ds_read_b64_tr_b16 a[182:183], v252 offset:8264\n"
    "ds_read_b64_tr_b16 a[184:185], v254 offset:0\n"
    "ds_read_b64_tr_b16 a[186:187], v254 offset:8256\n"
    "v_max3_f32 v248, v28, v29, v30\n"
    "v_max_f32 v248, v248, v31\n"
    "v_mul_f32 v248, 0xbe800000, v248\n"
    "v_cvt_pk_bf16_f32 v248, v248, s80\n"
    "s_nop 1\n"
    "v_mfma_f32_16x16x16_bf16 v[216:219], a[252:253], v[248:249], 0\n"
    "ds_read_b64_tr_b16 a[188:189], v254 offset:8\n"
    "ds_read_b64_tr_b16 a[190:191], v254 offset:8264\n"
    "s_nop 5\n"
    "v_add_f32 v192, v28, v216\n"
    "v_add_f32 v193, v29, v217\n"
    "v_add_f32 v194, v30, v218\n"
    "v_add_f32 v195, v31, v219\n"
    "s_waitcnt lgkmcnt(7)\n"
    "v_mfma_f32_16x16x32_bf16 v[208:211], a[124:127], a[12:15], v[216:219]\n"
    "v_mfma_f32_16x16x32_bf16 v[208:211], a[120:123], a[8:11], v[208:211]\n"
    "v_mfma_f32_16x16x32_bf16 v[208:211], a[116:119], a[4:7], v[208:211]\n"
    "v_mfma_f32_16x16x32_bf16 v[208:211], a[112:115], a[0:3], v[208:211]\n"
    "v_exp_f32 v192, v192\n"
    "v_exp_f32 v193, v193\n"
    "v_mov_b32 v240, v192\n"
    "v_add_f32 v240, v240, v193\n"
    "v_exp_f32 v194, v194\n"
    "v_exp_f32 v195, v195\n"
    "v_add_f32 v240, v240, v194\n"
    "v_add_f32 v240, v240, v195\n"
    "v_exp_f32_e64 v208, v208\n"
    "s_waitcnt vmcnt(8) lgkmcnt(0)\n"
    "s_barrier\n"
    "ds_read_b128 a[128:131], v246 offset:16512\n"
    "ds_read_b128 a[132:135], v246 offset:16768\n"
    "ds_read_b128 a[136:139], v246 offset:17024\n"
    "ds_read_b128 a[140:143], v246 offset:17280\n"
    "ds_read_b128 a[156:159], v246 offset:25536\n"
    "ds_read_b128 a[152:155], v246 offset:25280\n"
    "ds_read_b128 a[148:151], v246 offset:25024\n"
    "ds_read_b128 a[144:147], v246 offset:24768\n"
    "ds_read_b64_tr_b16 a[192:193], v247 offset:16512\n"
    "ds_read_b64_tr_b16 a[194:195], v247 offset:24768\n"
    "ds_read_b64_tr_b16 a[196:197], v247 offset:16520\n"
    "ds_read_b64_tr_b16 a[198:199], v247 offset:24776\n"
    "s_waitcnt lgkmcnt(11)\n"
    "v_mfma_f32_16x16x32_bf16 v[196:199], a[128:131], a[0:3], v[216:219]\n"
    "s_waitcnt lgkmcnt(10)\n"
    "ds_read_b64_tr_b16 a[200:201], v251 offset:16512\n"
    "v_add_f32 v240, v240, v208\n"
    "v_cvt_pk_bf16_f32 v192, v192, v193\n"
    "v_mfma_f32_16x16x32_bf16 v[196:199], a[132:135], a[4:7], v[196:199]\n"
    "s_waitcnt lgkmcnt(10)\n"
    "ds_read_b64_tr_b16 a[202:203], v251 offset:24768\n"
    "v_exp_f32 v209, v209\n"
    "v_mfma_f32_16x16x32_bf16 v[196:199], a[136:139], a[8:11], v[196:199]\n"
    "s_waitcnt lgkmcnt(7)\n"
    "ds_read_b64_tr_b16 a[204:205], v251 offset:16520\n"
    "v_add_f32 v240, v240, v209\n"
    "v_cvt_pk_bf16_f32 v193, v194, v195\n"
    "v_mfma_f32_16x16x32_bf16 v[196:199], a[140:143], a[12:15], v[196:199]\n"
    "ds_read_b64_tr_b16 a[206:207], v251 offset:24776\n"
    "v_exp_f32 v210, v210\n"
    "v_mfma_f32_16x16x32_bf16 v[212:215], a[156:159], a[12:15], v[216:219]\n"
    "ds_read_b64_tr_b16 a[208:209], v252 offset:16512\n"
    "v_add_f32 v240, v240, v210\n"
    "v_cvt_pk_bf16_f32 v194, v208, v209\n"
    "v_mfma_f32_16x16x32_bf16 v[212:215], a[152:155], a[8:11], v[212:215]\n"
    "ds_read_b64_tr_b16 a[210:211], v252 offset:24768\n"
    "v_exp_f32 v211, v211\n"
    "v_mfma_f32_16x16x32_bf16 v[212:215], a[148:151], a[4:7], v[212:215]\n"
    "s_waitcnt lgkmcnt(4)\n"
    "ds_read_b64_tr_b16 a[212:213], v252 offset:16520\n"
    "v_add_f32 v240, v240, v211\n"
    "v_cvt_pk_bf16_f32 v195, v210, v211\n"
    "v_mfma_f32_16x16x32_bf16 v[212:215], a[144:147], a[0:3], v[212:215]\n"
    "ds_read_b64_tr_b16 a[214:215], v252 offset:24776\n"
    "v_exp_f32_e64 v196, v196\n"
    "ds_read_b64_tr_b16 a[216:217], v254 offset:16512\n"
    "ds_read_b64_tr_b16 a[218:219], v254 offset:24768\n"
    "ds_read_b64_tr_b16 a[220:221], v254 offset:16520\n"
    "ds_read_b64_tr_b16 a[222:223], v254 offset:24776\n"
    "v_add_f32 v240, v240, v196\n"
    "v_exp_f32 v197, v197\n"
    "v_exp_f32 v198, v198\n"
    "v_add_f32 v240, v240, v197\n"
    "v_add_f32 v240, v240, v198\n"
    "v_exp_f32 v199, v199\n"
    "v_exp_f32 v212, v212\n"
    "v_add_f32_e64 v240, v240, v199\n"
    "s_waitcnt vmcnt(4)\n"
    "ds_read_b128 a[16:19], v250 offset:4128\n"
    "ds_read_b128 a[20:23], v250 offset:4384\n"
    "ds_read_b128 a[24:27], v250 offset:4640\n"
    "ds_read_b128 a[28:31], v250 offset:4896\n"
    "s_add_u32 m0, s68, 12384\n"
    "s_add_u32 s69, s66, 12288\n"
    "buffer_load_dwordx4 v253, s[48:51], s69 offen offset:0 nt lds\n"
    "buffer_load_dwordx4 v253, s[48:51], s69 offen offset:1024 nt lds\n"
    "buffer_load_dwordx4 v253, s[52:55], s69 offen offset:2080 nt lds\n"
    "buffer_load_dwordx4 v253, s[52:55], s69 offen offset:3104 nt lds\n"
    "s_waitcnt lgkmcnt(3)\n"
    "v_mfma_f32_4x4x4_16b_bf16 v[40:43], a[248:249], a[16:17], 0\n"
    "s_nop 1\n"
    "v_mfma_f32_4x4x4_16b_bf16 v[40:43], a[250:251], a[16:17], v[40:43]\n"
    "v_mfma_f32_4x4x4_16b_bf16 v[44:47], a[248:249], a[18:19], 0\n"
    "s_nop 1\n"
    "v_mfma_f32_4x4x4_16b_bf16 v[44:47], a[250:251], a[18:19], v[44:47]\n"
    "v_nop_e64\n"
    "v_cvt_pk_bf16_f32 v40, v40, v41\n"
    "s_waitcnt lgkmcnt(2)\n"
    "v_mfma_f32_4x4x4_16b_bf16 v[36:39], a[248:249], a[20:21], 0\n"
    "v_cvt_pk_bf16_f32 v41, v42, v43\n"
    "v_accvgpr_write_b32 a16, v40\n"
    "v_mfma_f32_4x4x4_16b_bf16 v[36:39], a[250:251], a[20:21], v[36:39]\n"
    "v_cvt_pk_bf16_f32 v44, v44, v45\n"
    "v_accvgpr_write_b32 a17, v41\n"
    "v_mfma_f32_4x4x4_16b_bf16 v[40:43], a[248:249], a[22:23], 0\n"
    "v_cvt_pk_bf16_f32 v45, v46, v47\n"
    "v_accvgpr_write_b32 a18, v44\n"
    "v_mfma_f32_4x4x4_16b_bf16 v[40:43], a[250:251], a[22:23], v[40:43]\n"
    "v_cvt_pk_bf16_f32 v36, v36, v37\n"
    "v_accvgpr_write_b32 a19, v45\n"
    "s_waitcnt lgkmcnt(0)\n"
    "s_nop 0\n"
    "v_mfma_f32_4x4x4_16b_bf16 v[44:47], a[248:249], a[24:25], 0\n"
    "v_cvt_pk_bf16_f32 v37, v38, v39\n"
    "v_accvgpr_write_b32 a20, v36\n"
    "v_mfma_f32_4x4x4_16b_bf16 v[44:47], a[250:251], a[24:25], v[44:47]\n"
    "v_cvt_pk_bf16_f32 v40, v40, v41\n"
    "v_accvgpr_write_b32 a21, v37\n"
    "v_mfma_f32_4x4x4_16b_bf16 v[36:39], a[248:249], a[26:27], 0\n"
    "v_cvt_pk_bf16_f32 v41, v42, v43\n"
    "v_accvgpr_write_b32 a22, v40\n"
    "v_mfma_f32_4x4x4_16b_bf16 v[36:39], a[250:251], a[26:27], v[36:39]\n"
    "v_cvt_pk_bf16_f32 v44, v44, v45\n"
    "v_accvgpr_write_b32 a23, v41\n"
    "v_mfma_f32_4x4x4_16b_bf16 v[40:43], a[248:249], a[28:29], 0\n"
    "v_cvt_pk_bf16_f32 v45, v46, v47\n"
    "v_accvgpr_write_b32 a24, v44\n"
    "v_mfma_f32_4x4x4_16b_bf16 v[40:43], a[250:251], a[28:29], v[40:43]\n"
    "v_cvt_pk_bf16_f32 v36, v36, v37\n"
    "v_accvgpr_write_b32 a25, v45\n"
    "v_mfma_f32_4x4x4_16b_bf16 v[44:47], a[248:249], a[30:31], 0\n"
    "v_cvt_pk_bf16_f32 v37, v38, v39\n"
    "v_accvgpr_write_b32 a26, v36\n"
    "v_mfma_f32_4x4x4_16b_bf16 v[44:47], a[250:251], a[30:31], v[44:47]\n"
    "v_cvt_pk_bf16_f32 v40, v40, v41\n"
    "v_accvgpr_write_b32 a27, v37\n"
    "v_mfma_f32_16x16x32_bf16 v[60:63], a[96:99], a[16:19], 0\n"
    "v_cvt_pk_bf16_f32 v41, v42, v43\n"
    "v_accvgpr_write_b32 a28, v40\n"
    "v_mfma_f32_16x16x32_bf16 v[60:63], a[100:103], a[20:23], v[60:63]\n"
    "v_cvt_pk_bf16_f32 v44, v44, v45\n"
    "v_accvgpr_write_b32 a29, v41\n"
    "v_mfma_f32_16x16x32_bf16 v[60:63], a[104:107], a[24:27], v[60:63]\n"
    "v_cvt_pk_bf16_f32 v45, v46, v47\n"
    "v_accvgpr_write_b32 a30, v44\n"
    "v_accvgpr_write_b32 a31, v45\n"
    "v_add_f32 v240, v240, v212\n"
    "v_cvt_pk_bf16_f32 v196, v196, v197\n"
    "v_mfma_f32_16x16x32_bf16 v[60:63], a[108:111], a[28:31], v[60:63]\n"
    "v_exp_f32 v213, v213\n"
    "v_cvt_pk_bf16_f32 v197, v198, v199\n"
    "v_add_f32 v240, v240, v213\n"
    "v_exp_f32 v214, v214\n"
    "v_cvt_pk_bf16_f32 v198, v212, v213\n"
    "v_add_f32 v240, v240, v214\n"
    "v_exp_f32 v215, v215\n"
    "s_nop 0\n"
    "v_max3_f32 v248, v60, v61, v62\n"
    "v_max_f32 v248, v248, v63\n"
    "v_mul_f32 v248, 0xbe800000, v248\n"
    "v_cvt_pk_bf16_f32 v248, v248, s80\n"
    "v_add_f32 v240, v240, v215\n"
    "v_cvt_pk_bf16_f32 v199, v214, v215\n"
    "v_mfma_f32_16x16x16_bf16 v[220:223], a[252:253], v[248:249], 0\n"
    "s_nop 7\n"
    "v_add_f32 v200, v60, v220\n"
    "v_add_f32 v201, v61, v221\n"
    "v_add_f32 v202, v62, v222\n"
    "v_add_f32 v203, v63, v223\n"
    "v_mfma_f32_16x16x32_bf16 v[208:211], a[124:127], a[28:31], v[220:223]\n"
    "v_mfma_f32_16x16x32_bf16 v[208:211], a[120:123], a[24:27], v[208:211]\n"
    "v_mfma_f32_16x16x32_bf16 v[208:211], a[116:119], a[20:23], v[208:211]\n"
    "v_mfma_f32_16x16x32_bf16 v[208:211], a[112:115], a[16:19], v[208:211]\n"
    "v_exp_f32 v200, v200\n"
    "v_mfma_f32_16x16x32_bf16 v[0:3], v[192:195], a[160:163], 0\n"
    "v_mov_b32 v241, v200\n"
    "v_mfma_f32_16x16x32_bf16 v[0:3], v[196:199], a[192:195], v[0:3]\n"
    "v_exp_f32 v201, v201\n"
    "v_mfma_f32_16x16x32_bf16 v[4:7], v[196:199], a[196:199], 0\n"
    "v_add_f32 v241, v241, v201\n"
    "v_mfma_f32_16x16x32_bf16 v[4:7], v[192:195], a[164:167], v[4:7]\n"
    "v_exp_f32 v202, v202\n"
    "v_mfma_f32_16x16x32_bf16 v[8:11], v[192:195], a[168:171], 0\n"
    "v_add_f32 v241, v241, v202\n"
    "v_mfma_f32_16x16x32_bf16 v[8:11], v[196:199], a[200:203], v[8:11]\n"
    "v_exp_f32 v203, v203\n"
    "v_mfma_f32_16x16x32_bf16 v[12:15], v[196:199], a[204:207], 0\n"
    "v_add_f32 v241, v241, v203\n"
    "v_mfma_f32_16x16x32_bf16 v[12:15], v[192:195], a[172:175], v[12:15]\n"
    "v_exp_f32 v208, v208\n"
    "v_mfma_f32_16x16x32_bf16 v[204:207], a[128:131], a[16:19], v[220:223]\n"
    "v_add_f32 v241, v241, v208\n"
    "v_cvt_pk_bf16_f32 v200, v200, v201\n"
    "v_mfma_f32_16x16x32_bf16 v[204:207], a[132:135], a[20:23], v[204:207]\n"
    "v_exp_f32 v209, v209\n"
    "v_mfma_f32_16x16x32_bf16 v[204:207], a[136:139], a[24:27], v[204:207]\n"
    "v_add_f32 v241, v241, v209\n"
    "v_cvt_pk_bf16_f32 v201, v202, v203\n"
    "v_mfma_f32_16x16x32_bf16 v[204:207], a[140:143], a[28:31], v[204:207]\n"
    "v_exp_f32 v210, v210\n"
    "v_mfma_f32_16x16x32_bf16 v[212:215], a[156:159], a[28:31], v[220:223]\n"
    "v_add_f32 v241, v241, v210\n"
    "v_cvt_pk_bf16_f32 v202, v208, v209\n"
    "v_mfma_f32_16x16x32_bf16 v[212:215], a[152:155], a[24:27], v[212:215]\n"
    "v_exp_f32 v211, v211\n"
    "v_mfma_f32_16x16x32_bf16 v[212:215], a[148:151], a[20:23], v[212:215]\n"
    "v_add_f32 v241, v241, v211\n"
    "v_cvt_pk_bf16_f32 v203, v210, v211\n"
    "v_mfma_f32_16x16x32_bf16 v[212:215], a[144:147], a[16:19], v[212:215]\n"
    "v_exp_f32 v204, v204\n"
    "v_mfma_f32_16x16x32_bf16 v[16:19], v[192:195], a[176:179], 0\n"
    "v_add_f32 v241, v241, v204\n"
    "v_mfma_f32_16x16x32_bf16 v[16:19], v[196:199], a[208:211], v[16:19]\n"
    "v_exp_f32 v205, v205\n"
    "v_mfma_f32_16x16x32_bf16 v[20:23], v[196:199], a[212:215], 0\n"
    "v_add_f32 v241, v241, v205\n"
    "v_mfma_f32_16x16x32_bf16 v[20:23], v[192:195], a[180:183], v[20:23]\n"
    "v_exp_f32 v206, v206\n"
    "v_mfma_f32_16x16x32_bf16 v[24:27], v[192:195], a[184:187], 0\n"
    "v_add_f32 v241, v241, v206\n"
    "v_mfma_f32_16x16x32_bf16 v[24:27], v[196:199], a[216:219], v[24:27]\n"
    "v_exp_f32 v207, v207\n"
    "v_mfma_f32_16x16x32_bf16 v[28:31], v[196:199], a[220:223], 0\n"
    "v_add_f32 v241, v241, v207\n"
    "v_mfma_f32_16x16x32_bf16 v[28:31], v[192:195], a[188:191], v[28:31]\n"
    "v_exp_f32 v212, v212\n"
    "s_waitcnt vmcnt(4)\n"
    "ds_read_b128 a[32:35], v250 offset:8256\n"
    "ds_read_b128 a[36:39], v250 offset:8512\n"
    "ds_read_b128 a[40:43], v250 offset:8768\n"
    "ds_read_b128 a[44:47], v250 offset:9024\n"
    "s_add_u32 m0, s68, 16512\n"
    "s_add_u32 s69, s66, 16384\n"
    "buffer_load_dwordx4 v253, s[48:51], s69 offen offset:0 nt lds\n"
    "buffer_load_dwordx4 v253, s[48:51], s69 offen offset:1024 nt lds\n"
    "buffer_load_dwordx4 v253, s[52:55], s69 offen offset:2080 nt lds\n"
    "buffer_load_dwordx4 v253, s[52:55], s69 offen offset:3104 nt lds\n"
    "s_waitcnt lgkmcnt(3)\n"
    "v_mfma_f32_4x4x4_16b_bf16 v[72:75], a[248:249], a[32:33], 0\n"
    "s_nop 1\n"
    "v_mfma_f32_4x4x4_16b_bf16 v[72:75], a[250:251], a[32:33], v[72:75]\n"
    "v_mfma_f32_4x4x4_16b_bf16 v[76:79], a[248:249], a[34:35], 0\n"
    "s_nop 1\n"
    "v_mfma_f32_4x4x4_16b_bf16 v[76:79], a[250:251], a[34:35], v[76:79]\n"
    "v_nop_e64\n"
    "v_cvt_pk_bf16_f32 v72, v72, v73\n"
    "s_waitcnt lgkmcnt(2)\n"
    "v_mfma_f32_4x4x4_16b_bf16 v[68:71], a[248:249], a[36:37], 0\n"
    "v_cvt_pk_bf16_f32 v73, v74, v75\n"
    "v_accvgpr_write_b32 a32, v72\n"
    "v_mfma_f32_4x4x4_16b_bf16 v[68:71], a[250:251], a[36:37], v[68:71]\n"
    "v_cvt_pk_bf16_f32 v76, v76, v77\n"
    "v_accvgpr_write_b32 a33, v73\n"
    "v_mfma_f32_4x4x4_16b_bf16 v[72:75], a[248:249], a[38:39], 0\n"
    "v_cvt_pk_bf16_f32 v77, v78, v79\n"
    "v_accvgpr_write_b32 a34, v76\n"
    "v_mfma_f32_4x4x4_16b_bf16 v[72:75], a[250:251], a[38:39], v[72:75]\n"
    "v_cvt_pk_bf16_f32 v68, v68, v69\n"
    "v_accvgpr_write_b32 a35, v77\n"
    "s_waitcnt lgkmcnt(0)\n"
    "s_nop 0\n"
    "v_mfma_f32_4x4x4_16b_bf16 v[76:79], a[248:249], a[40:41], 0\n"
    "v_cvt_pk_bf16_f32 v69, v70, v71\n"
    "v_accvgpr_write_b32 a36, v68\n"
    "v_mfma_f32_4x4x4_16b_bf16 v[76:79], a[250:251], a[40:41], v[76:79]\n"
    "v_cvt_pk_bf16_f32 v72, v72, v73\n"
    "v_accvgpr_write_b32 a37, v69\n"
    "v_mfma_f32_4x4x4_16b_bf16 v[68:71], a[248:249], a[42:43], 0\n"
    "v_cvt_pk_bf16_f32 v73, v74, v75\n"
    "v_accvgpr_write_b32 a38, v72\n"
    "v_mfma_f32_4x4x4_16b_bf16 v[68:71], a[250:251], a[42:43], v[68:71]\n"
    "v_cvt_pk_bf16_f32 v76, v76, v77\n"
    "v_accvgpr_write_b32 a39, v73\n"
    "v_mfma_f32_4x4x4_16b_bf16 v[72:75], a[248:249], a[44:45], 0\n"
    "v_cvt_pk_bf16_f32 v77, v78, v79\n"
    "v_accvgpr_write_b32 a40, v76\n"
    "v_mfma_f32_4x4x4_16b_bf16 v[72:75], a[250:251], a[44:45], v[72:75]\n"
    "v_cvt_pk_bf16_f32 v68, v68, v69\n"
    "v_accvgpr_write_b32 a41, v77\n"
    "v_mfma_f32_4x4x4_16b_bf16 v[76:79], a[248:249], a[46:47], 0\n"
    "v_cvt_pk_bf16_f32 v69, v70, v71\n"
    "v_accvgpr_write_b32 a42, v68\n"
    "v_mfma_f32_4x4x4_16b_bf16 v[76:79], a[250:251], a[46:47], v[76:79]\n"
    "v_cvt_pk_bf16_f32 v72, v72, v73\n"
    "v_accvgpr_write_b32 a43, v69\n"
    "v_mfma_f32_16x16x32_bf16 v[92:95], a[96:99], a[32:35], 0\n"
    "v_cvt_pk_bf16_f32 v73, v74, v75\n"
    "v_accvgpr_write_b32 a44, v72\n"
    "v_mfma_f32_16x16x32_bf16 v[92:95], a[100:103], a[36:39], v[92:95]\n"
    "v_cvt_pk_bf16_f32 v76, v76, v77\n"
    "v_accvgpr_write_b32 a45, v73\n"
    "v_mfma_f32_16x16x32_bf16 v[92:95], a[104:107], a[40:43], v[92:95]\n"
    "v_cvt_pk_bf16_f32 v77, v78, v79\n"
    "v_accvgpr_write_b32 a46, v76\n"
    "v_accvgpr_write_b32 a47, v77\n"
    "v_add_f32 v241, v241, v212\n"
    "v_cvt_pk_bf16_f32 v204, v204, v205\n"
    "v_mfma_f32_16x16x32_bf16 v[92:95], a[108:111], a[44:47], v[92:95]\n"
    "v_exp_f32 v213, v213\n"
    "v_cvt_pk_bf16_f32 v205, v206, v207\n"
    "v_add_f32 v241, v241, v213\n"
    "v_exp_f32 v214, v214\n"
    "v_cvt_pk_bf16_f32 v206, v212, v213\n"
    "v_add_f32 v241, v241, v214\n"
    "v_exp_f32 v215, v215\n"
    "s_nop 0\n"
    "v_max3_f32 v248, v92, v93, v94\n"
    "v_max_f32 v248, v248, v95\n"
    "v_mul_f32 v248, 0xbe800000, v248\n"
    "v_cvt_pk_bf16_f32 v248, v248, s80\n"
    "v_add_f32 v241, v241, v215\n"
    "v_cvt_pk_bf16_f32 v207, v214, v215\n"
    "v_mfma_f32_16x16x16_bf16 v[224:227], a[252:253], v[248:249], 0\n"
    "s_nop 7\n"
    "v_add_f32 v192, v92, v224\n"
    "v_add_f32 v193, v93, v225\n"
    "v_add_f32 v194, v94, v226\n"
    "v_add_f32 v195, v95, v227\n"
    "v_mfma_f32_16x16x32_bf16 v[208:211], a[124:127], a[44:47], v[224:227]\n"
    "v_mfma_f32_16x16x32_bf16 v[208:211], a[120:123], a[40:43], v[208:211]\n"
    "v_mfma_f32_16x16x32_bf16 v[208:211], a[116:119], a[36:39], v[208:211]\n"
    "v_mfma_f32_16x16x32_bf16 v[208:211], a[112:115], a[32:35], v[208:211]\n"
    "v_exp_f32 v192, v192\n"
    "v_mfma_f32_16x16x32_bf16 v[32:35], v[200:203], a[160:163], 0\n"
    "v_mov_b32 v242, v192\n"
    "v_mfma_f32_16x16x32_bf16 v[32:35], v[204:207], a[192:195], v[32:35]\n"
    "v_exp_f32 v193, v193\n"
    "v_mfma_f32_16x16x32_bf16 v[36:39], v[204:207], a[196:199], 0\n"
    "v_add_f32 v242, v242, v193\n"
    "v_mfma_f32_16x16x32_bf16 v[36:39], v[200:203], a[164:167], v[36:39]\n"
    "v_exp_f32 v194, v194\n"
    "v_mfma_f32_16x16x32_bf16 v[40:43], v[200:203], a[168:171], 0\n"
    "v_add_f32 v242, v242, v194\n"
    "v_mfma_f32_16x16x32_bf16 v[40:43], v[204:207], a[200:203], v[40:43]\n"
    "v_exp_f32 v195, v195\n"
    "v_mfma_f32_16x16x32_bf16 v[44:47], v[204:207], a[204:207], 0\n"
    "v_add_f32 v242, v242, v195\n"
    "v_mfma_f32_16x16x32_bf16 v[44:47], v[200:203], a[172:175], v[44:47]\n"
    "v_exp_f32 v208, v208\n"
    "v_mfma_f32_16x16x32_bf16 v[196:199], a[128:131], a[32:35], v[224:227]\n"
    "v_add_f32 v242, v242, v208\n"
    "v_cvt_pk_bf16_f32 v192, v192, v193\n"
    "v_mfma_f32_16x16x32_bf16 v[196:199], a[132:135], a[36:39], v[196:199]\n"
    "v_exp_f32 v209, v209\n"
    "v_mfma_f32_16x16x32_bf16 v[196:199], a[136:139], a[40:43], v[196:199]\n"
    "v_add_f32 v242, v242, v209\n"
    "v_cvt_pk_bf16_f32 v193, v194, v195\n"
    "v_mfma_f32_16x16x32_bf16 v[196:199], a[140:143], a[44:47], v[196:199]\n"
    "v_exp_f32 v210, v210\n"
    "v_mfma_f32_16x16x32_bf16 v[212:215], a[156:159], a[44:47], v[224:227]\n"
    "v_add_f32 v242, v242, v210\n"
    "v_cvt_pk_bf16_f32 v194, v208, v209\n"
    "v_mfma_f32_16x16x32_bf16 v[212:215], a[152:155], a[40:43], v[212:215]\n"
    "v_exp_f32 v211, v211\n"
    "v_mfma_f32_16x16x32_bf16 v[212:215], a[148:151], a[36:39], v[212:215]\n"
    "v_add_f32 v242, v242, v211\n"
    "v_cvt_pk_bf16_f32 v195, v210, v211\n"
    "v_mfma_f32_16x16x32_bf16 v[212:215], a[144:147], a[32:35], v[212:215]\n"
    "v_exp_f32 v196, v196\n"
    "v_mfma_f32_16x16x32_bf16 v[48:51], v[200:203], a[176:179], 0\n"
    "v_add_f32 v242, v242, v196\n"
    "v_mfma_f32_16x16x32_bf16 v[48:51], v[204:207], a[208:211], v[48:51]\n"
    "v_exp_f32 v197, v197\n"
    "v_mfma_f32_16x16x32_bf16 v[52:55], v[204:207], a[212:215], 0\n"
    "v_add_f32 v242, v242, v197\n"
    "v_mfma_f32_16x16x32_bf16 v[52:55], v[200:203], a[180:183], v[52:55]\n"
    "v_exp_f32 v198, v198\n"
    "v_mfma_f32_16x16x32_bf16 v[56:59], v[200:203], a[184:187], 0\n"
    "v_add_f32 v242, v242, v198\n"
    "v_mfma_f32_16x16x32_bf16 v[56:59], v[204:207], a[216:219], v[56:59]\n"
    "v_exp_f32 v199, v199\n"
    "v_mfma_f32_16x16x32_bf16 v[60:63], v[204:207], a[220:223], 0\n"
    "v_add_f32 v242, v242, v199\n"
    "v_mfma_f32_16x16x32_bf16 v[60:63], v[200:203], a[188:191], v[60:63]\n"
    "v_exp_f32 v212, v212\n"
    "s_waitcnt vmcnt(4)\n"
    "ds_read_b128 a[48:51], v250 offset:12384\n"
    "ds_read_b128 a[52:55], v250 offset:12640\n"
    "ds_read_b128 a[56:59], v250 offset:12896\n"
    "ds_read_b128 a[60:63], v250 offset:13152\n"
    "s_cmp_eq_u32 s71, 3\n"
    "s_cbranch_scc1 LPROG_B\n"
    "s_add_u32 m0, s68, 20640\n"
    "s_add_u32 s69, s66, 20480\n"
    "buffer_load_dwordx4 v253, s[48:51], s69 offen offset:0 nt lds\n"
    "buffer_load_dwordx4 v253, s[48:51], s69 offen offset:1024 nt lds\n"
    "buffer_load_dwordx4 v253, s[52:55], s69 offen offset:2080 nt lds\n"
    "buffer_load_dwordx4 v253, s[52:55], s69 offen offset:3104 nt lds\n"
    "s_waitcnt lgkmcnt(3)\n"
    "v_mfma_f32_4x4x4_16b_bf16 v[104:107], a[248:249], a[48:49], 0\n"
    "s_nop 1\n"
    "v_mfma_f32_4x4x4_16b_bf16 v[104:107], a[250:251], a[48:49], v[104:107]\n"
    "v_mfma_f32_4x4x4_16b_bf16 v[108:111], a[248:249], a[50:51], 0\n"
    "s_nop 1\n"
    "v_mfma_f32_4x4x4_16b_bf16 v[108:111], a[250:251], a[50:51], v[108:111]\n"
    "v_nop_e64\n"
    "v_cvt_pk_bf16_f32 v104, v104, v105\n"
    "s_waitcnt lgkmcnt(2)\n"
    "v_mfma_f32_4x4x4_16b_bf16 v[100:103], a[248:249], a[52:53], 0\n"
    "v_cvt_pk_bf16_f32 v105, v106, v107\n"
    "v_accvgpr_write_b32 a48, v104\n"
    "v_mfma_f32_4x4x4_16b_bf16 v[100:103], a[250:251], a[52:53], v[100:103]\n"
    "v_cvt_pk_bf16_f32 v108, v108, v109\n"
    "v_accvgpr_write_b32 a49, v105\n"
    "v_mfma_f32_4x4x4_16b_bf16 v[104:107], a[248:249], a[54:55], 0\n"
    "v_cvt_pk_bf16_f32 v109, v110, v111\n"
    "v_accvgpr_write_b32 a50, v108\n"
    "v_mfma_f32_4x4x4_16b_bf16 v[104:107], a[250:251], a[54:55], v[104:107]\n"
    "v_cvt_pk_bf16_f32 v100, v100, v101\n"
    "v_accvgpr_write_b32 a51, v109\n"
    "s_waitcnt lgkmcnt(0)\n"
    "s_nop 0\n"
    "v_mfma_f32_4x4x4_16b_bf16 v[108:111], a[248:249], a[56:57], 0\n"
    "v_cvt_pk_bf16_f32 v101, v102, v103\n"
    "v_accvgpr_write_b32 a52, v100\n"
    "v_mfma_f32_4x4x4_16b_bf16 v[108:111], a[250:251], a[56:57], v[108:111]\n"
    "v_cvt_pk_bf16_f32 v104, v104, v105\n"
    "v_accvgpr_write_b32 a53, v101\n"
    "v_mfma_f32_4x4x4_16b_bf16 v[100:103], a[248:249], a[58:59], 0\n"
    "v_cvt_pk_bf16_f32 v105, v106, v107\n"
    "v_accvgpr_write_b32 a54, v104\n"
    "v_mfma_f32_4x4x4_16b_bf16 v[100:103], a[250:251], a[58:59], v[100:103]\n"
    "v_cvt_pk_bf16_f32 v108, v108, v109\n"
    "v_accvgpr_write_b32 a55, v105\n"
    "v_mfma_f32_4x4x4_16b_bf16 v[104:107], a[248:249], a[60:61], 0\n"
    "v_cvt_pk_bf16_f32 v109, v110, v111\n"
    "v_accvgpr_write_b32 a56, v108\n"
    "v_mfma_f32_4x4x4_16b_bf16 v[104:107], a[250:251], a[60:61], v[104:107]\n"
    "v_cvt_pk_bf16_f32 v100, v100, v101\n"
    "v_accvgpr_write_b32 a57, v109\n"
    "v_mfma_f32_4x4x4_16b_bf16 v[108:111], a[248:249], a[62:63], 0\n"
    "v_cvt_pk_bf16_f32 v101, v102, v103\n"
    "v_accvgpr_write_b32 a58, v100\n"
    "v_mfma_f32_4x4x4_16b_bf16 v[108:111], a[250:251], a[62:63], v[108:111]\n"
    "v_cvt_pk_bf16_f32 v104, v104, v105\n"
    "v_accvgpr_write_b32 a59, v101\n"
    "v_mfma_f32_16x16x32_bf16 v[124:127], a[96:99], a[48:51], 0\n"
    "v_cvt_pk_bf16_f32 v105, v106, v107\n"
    "v_accvgpr_write_b32 a60, v104\n"
    "v_mfma_f32_16x16x32_bf16 v[124:127], a[100:103], a[52:55], v[124:127]\n"
    "v_cvt_pk_bf16_f32 v108, v108, v109\n"
    "v_accvgpr_write_b32 a61, v105\n"
    "v_mfma_f32_16x16x32_bf16 v[124:127], a[104:107], a[56:59], v[124:127]\n"
    "v_cvt_pk_bf16_f32 v109, v110, v111\n"
    "v_accvgpr_write_b32 a62, v108\n"
    "v_accvgpr_write_b32 a63, v109\n"
    "v_add_f32 v242, v242, v212\n"
    "v_cvt_pk_bf16_f32 v196, v196, v197\n"
    "v_mfma_f32_16x16x32_bf16 v[124:127], a[108:111], a[60:63], v[124:127]\n"
    "v_exp_f32 v213, v213\n"
    "v_cvt_pk_bf16_f32 v197, v198, v199\n"
    "v_add_f32 v242, v242, v213\n"
    "v_exp_f32 v214, v214\n"
    "v_cvt_pk_bf16_f32 v198, v212, v213\n"
    "v_add_f32 v242, v242, v214\n"
    "v_exp_f32 v215, v215\n"
    "s_nop 0\n"
    "v_max3_f32 v248, v124, v125, v126\n"
    "v_max_f32 v248, v248, v127\n"
    "v_mul_f32 v248, 0xbe800000, v248\n"
    "v_cvt_pk_bf16_f32 v248, v248, s80\n"
    "v_add_f32 v242, v242, v215\n"
    "v_cvt_pk_bf16_f32 v199, v214, v215\n"
    "v_mfma_f32_16x16x16_bf16 v[228:231], a[252:253], v[248:249], 0\n"
    "s_nop 7\n"
    "v_add_f32 v200, v124, v228\n"
    "v_add_f32 v201, v125, v229\n"
    "v_add_f32 v202, v126, v230\n"
    "v_add_f32 v203, v127, v231\n"
    "v_mfma_f32_16x16x32_bf16 v[208:211], a[124:127], a[60:63], v[228:231]\n"
    "v_mfma_f32_16x16x32_bf16 v[208:211], a[120:123], a[56:59], v[208:211]\n"
    "v_mfma_f32_16x16x32_bf16 v[208:211], a[116:119], a[52:55], v[208:211]\n"
    "v_mfma_f32_16x16x32_bf16 v[208:211], a[112:115], a[48:51], v[208:211]\n"
    "v_exp_f32 v200, v200\n"
    "v_mfma_f32_16x16x32_bf16 v[64:67], v[192:195], a[160:163], 0\n"
    "v_mov_b32 v243, v200\n"
    "v_mfma_f32_16x16x32_bf16 v[64:67], v[196:199], a[192:195], v[64:67]\n"
    "v_exp_f32 v201, v201\n"
    "v_mfma_f32_16x16x32_bf16 v[68:71], v[196:199], a[196:199], 0\n"
    "v_add_f32 v243, v243, v201\n"
    "v_mfma_f32_16x16x32_bf16 v[68:71], v[192:195], a[164:167], v[68:71]\n"
    "v_exp_f32 v202, v202\n"
    "v_mfma_f32_16x16x32_bf16 v[72:75], v[192:195], a[168:171], 0\n"
    "v_add_f32 v243, v243, v202\n"
    "v_mfma_f32_16x16x32_bf16 v[72:75], v[196:199], a[200:203], v[72:75]\n"
    "v_exp_f32 v203, v203\n"
    "v_mfma_f32_16x16x32_bf16 v[76:79], v[196:199], a[204:207], 0\n"
    "v_add_f32 v243, v243, v203\n"
    "v_mfma_f32_16x16x32_bf16 v[76:79], v[192:195], a[172:175], v[76:79]\n"
    "v_exp_f32 v208, v208\n"
    "v_mfma_f32_16x16x32_bf16 v[204:207], a[128:131], a[48:51], v[228:231]\n"
    "v_add_f32 v243, v243, v208\n"
    "v_cvt_pk_bf16_f32 v200, v200, v201\n"
    "v_mfma_f32_16x16x32_bf16 v[204:207], a[132:135], a[52:55], v[204:207]\n"
    "v_exp_f32 v209, v209\n"
    "v_mfma_f32_16x16x32_bf16 v[204:207], a[136:139], a[56:59], v[204:207]\n"
    "v_add_f32 v243, v243, v209\n"
    "v_cvt_pk_bf16_f32 v201, v202, v203\n"
    "v_mfma_f32_16x16x32_bf16 v[204:207], a[140:143], a[60:63], v[204:207]\n"
    "v_exp_f32 v210, v210\n"
    "v_mfma_f32_16x16x32_bf16 v[212:215], a[156:159], a[60:63], v[228:231]\n"
    "v_add_f32 v243, v243, v210\n"
    "v_cvt_pk_bf16_f32 v202, v208, v209\n"
    "v_mfma_f32_16x16x32_bf16 v[212:215], a[152:155], a[56:59], v[212:215]\n"
    "v_exp_f32 v211, v211\n"
    "v_mfma_f32_16x16x32_bf16 v[212:215], a[148:151], a[52:55], v[212:215]\n"
    "v_add_f32 v243, v243, v211\n"
    "v_cvt_pk_bf16_f32 v203, v210, v211\n"
    "v_mfma_f32_16x16x32_bf16 v[212:215], a[144:147], a[48:51], v[212:215]\n"
    "v_exp_f32 v204, v204\n"
    "v_mfma_f32_16x16x32_bf16 v[80:83], v[192:195], a[176:179], 0\n"
    "v_add_f32 v243, v243, v204\n"
    "v_mfma_f32_16x16x32_bf16 v[80:83], v[196:199], a[208:211], v[80:83]\n"
    "v_exp_f32 v205, v205\n"
    "v_mfma_f32_16x16x32_bf16 v[84:87], v[196:199], a[212:215], 0\n"
    "v_add_f32 v243, v243, v205\n"
    "v_mfma_f32_16x16x32_bf16 v[84:87], v[192:195], a[180:183], v[84:87]\n"
    "v_exp_f32 v206, v206\n"
    "v_mfma_f32_16x16x32_bf16 v[88:91], v[192:195], a[184:187], 0\n"
    "v_add_f32 v243, v243, v206\n"
    "v_mfma_f32_16x16x32_bf16 v[88:91], v[196:199], a[216:219], v[88:91]\n"
    "v_exp_f32 v207, v207\n"
    "v_mfma_f32_16x16x32_bf16 v[92:95], v[196:199], a[220:223], 0\n"
    "v_add_f32 v243, v243, v207\n"
    "v_mfma_f32_16x16x32_bf16 v[92:95], v[192:195], a[188:191], v[92:95]\n"
    "v_exp_f32 v212, v212\n"
    "s_waitcnt vmcnt(4)\n"
    "ds_read_b128 a[64:67], v250 offset:16512\n"
    "ds_read_b128 a[68:71], v250 offset:16768\n"
    "ds_read_b128 a[72:75], v250 offset:17024\n"
    "ds_read_b128 a[76:79], v250 offset:17280\n"
    "s_add_u32 m0, s73, 33024\n"
    "s_add_u32 s69, s70, 16384\n"
    "buffer_load_dwordx4 v253, s[36:39], s69 offen offset:0 lds\n"
    "buffer_load_dwordx4 v253, s[36:39], s69 offen offset:1024 lds\n"
    "buffer_load_dwordx4 v253, s[40:43], s69 offen offset:2048 lds\n"
    "buffer_load_dwordx4 v253, s[44:47], s69 offen offset:3080 lds\n"
    "s_waitcnt lgkmcnt(3)\n"
    "v_mfma_f32_4x4x4_16b_bf16 v[136:139], a[248:249], a[64:65], 0\n"
    "s_nop 1\n"
    "v_mfma_f32_4x4x4_16b_bf16 v[136:139], a[250:251], a[64:65], v[136:139]\n"
    "v_mfma_f32_4x4x4_16b_bf16 v[140:143], a[248:249], a[66:67], 0\n"
    "s_nop 1\n"
    "v_mfma_f32_4x4x4_16b_bf16 v[140:143], a[250:251], a[66:67], v[140:143]\n"
    "v_nop_e64\n"
    "v_cvt_pk_bf16_f32 v136, v136, v137\n"
    "s_waitcnt lgkmcnt(2)\n"
    "v_mfma_f32_4x4x4_16b_bf16 v[132:135], a[248:249], a[68:69], 0\n"
    "v_cvt_pk_bf16_f32 v137, v138, v139\n"
    "v_accvgpr_write_b32 a64, v136\n"
    "v_mfma_f32_4x4x4_16b_bf16 v[132:135], a[250:251], a[68:69], v[132:135]\n"
    "v_cvt_pk_bf16_f32 v140, v140, v141\n"
    "v_accvgpr_write_b32 a65, v137\n"
    "v_mfma_f32_4x4x4_16b_bf16 v[136:139], a[248:249], a[70:71], 0\n"
    "v_cvt_pk_bf16_f32 v141, v142, v143\n"
    "v_accvgpr_write_b32 a66, v140\n"
    "v_mfma_f32_4x4x4_16b_bf16 v[136:139], a[250:251], a[70:71], v[136:139]\n"
    "v_cvt_pk_bf16_f32 v132, v132, v133\n"
    "v_accvgpr_write_b32 a67, v141\n"
    "s_waitcnt lgkmcnt(0)\n"
    "s_nop 0\n"
    "v_mfma_f32_4x4x4_16b_bf16 v[140:143], a[248:249], a[72:73], 0\n"
    "v_cvt_pk_bf16_f32 v133, v134, v135\n"
    "v_accvgpr_write_b32 a68, v132\n"
    "v_mfma_f32_4x4x4_16b_bf16 v[140:143], a[250:251], a[72:73], v[140:143]\n"
    "v_cvt_pk_bf16_f32 v136, v136, v137\n"
    "v_accvgpr_write_b32 a69, v133\n"
    "v_mfma_f32_4x4x4_16b_bf16 v[132:135], a[248:249], a[74:75], 0\n"
    "v_cvt_pk_bf16_f32 v137, v138, v139\n"
    "v_accvgpr_write_b32 a70, v136\n"
    "v_mfma_f32_4x4x4_16b_bf16 v[132:135], a[250:251], a[74:75], v[132:135]\n"
    "v_cvt_pk_bf16_f32 v140, v140, v141\n"
    "v_accvgpr_write_b32 a71, v137\n"
    "v_mfma_f32_4x4x4_16b_bf16 v[136:139], a[248:249], a[76:77], 0\n"
    "v_cvt_pk_bf16_f32 v141, v142, v143\n"
    "v_accvgpr_write_b32 a72, v140\n"
    "v_mfma_f32_4x4x4_16b_bf16 v[136:139], a[250:251], a[76:77], v[136:139]\n"
    "v_cvt_pk_bf16_f32 v132, v132, v133\n"
    "v_accvgpr_write_b32 a73, v141\n"
    "v_mfma_f32_4x4x4_16b_bf16 v[140:143], a[248:249], a[78:79], 0\n"
    "v_cvt_pk_bf16_f32 v133, v134, v135\n"
    "v_accvgpr_write_b32 a74, v132\n"
    "v_mfma_f32_4x4x4_16b_bf16 v[140:143], a[250:251], a[78:79], v[140:143]\n"
    "v_cvt_pk_bf16_f32 v136, v136, v137\n"
    "v_accvgpr_write_b32 a75, v133\n"
    "v_mfma_f32_16x16x32_bf16 v[156:159], a[96:99], a[64:67], 0\n"
    "v_cvt_pk_bf16_f32 v137, v138, v139\n"
    "v_accvgpr_write_b32 a76, v136\n"
    "v_mfma_f32_16x16x32_bf16 v[156:159], a[100:103], a[68:71], v[156:159]\n"
    "v_cvt_pk_bf16_f32 v140, v140, v141\n"
    "v_accvgpr_write_b32 a77, v137\n"
    "v_mfma_f32_16x16x32_bf16 v[156:159], a[104:107], a[72:75], v[156:159]\n"
    "v_cvt_pk_bf16_f32 v141, v142, v143\n"
    "v_accvgpr_write_b32 a78, v140\n"
    "v_accvgpr_write_b32 a79, v141\n"
    "v_add_f32 v243, v243, v212\n"
    "v_cvt_pk_bf16_f32 v204, v204, v205\n"
    "v_mfma_f32_16x16x32_bf16 v[156:159], a[108:111], a[76:79], v[156:159]\n"
    "v_exp_f32 v213, v213\n"
    "v_cvt_pk_bf16_f32 v205, v206, v207\n"
    "v_add_f32 v243, v243, v213\n"
    "v_exp_f32 v214, v214\n"
    "v_cvt_pk_bf16_f32 v206, v212, v213\n"
    "v_add_f32 v243, v243, v214\n"
    "v_exp_f32 v215, v215\n"
    "s_nop 0\n"
    "v_max3_f32 v248, v156, v157, v158\n"
    "v_max_f32 v248, v248, v159\n"
    "v_mul_f32 v248, 0xbe800000, v248\n"
    "v_cvt_pk_bf16_f32 v248, v248, s80\n"
    "v_add_f32 v243, v243, v215\n"
    "v_cvt_pk_bf16_f32 v207, v214, v215\n"
    "v_mfma_f32_16x16x16_bf16 v[232:235], a[252:253], v[248:249], 0\n"
    "s_nop 7\n"
    "v_add_f32 v192, v156, v232\n"
    "v_add_f32 v193, v157, v233\n"
    "v_add_f32 v194, v158, v234\n"
    "v_add_f32 v195, v159, v235\n"
    "v_mfma_f32_16x16x32_bf16 v[208:211], a[124:127], a[76:79], v[232:235]\n"
    "v_mfma_f32_16x16x32_bf16 v[208:211], a[120:123], a[72:75], v[208:211]\n"
    "v_mfma_f32_16x16x32_bf16 v[208:211], a[116:119], a[68:71], v[208:211]\n"
    "v_mfma_f32_16x16x32_bf16 v[208:211], a[112:115], a[64:67], v[208:211]\n"
    "v_exp_f32 v192, v192\n"
    "v_mfma_f32_16x16x32_bf16 v[96:99], v[200:203], a[160:163], 0\n"
    "v_mov_b32 v244, v192\n"
    "v_mfma_f32_16x16x32_bf16 v[96:99], v[204:207], a[192:195], v[96:99]\n"
    "v_exp_f32 v193, v193\n"
    "v_mfma_f32_16x16x32_bf16 v[100:103], v[204:207], a[196:199], 0\n"
    "v_add_f32 v244, v244, v193\n"
    "v_mfma_f32_16x16x32_bf16 v[100:103], v[200:203], a[164:167], v[100:103]\n"
    "v_exp_f32 v194, v194\n"
    "v_mfma_f32_16x16x32_bf16 v[104:107], v[200:203], a[168:171], 0\n"
    "v_add_f32 v244, v244, v194\n"
    "v_mfma_f32_16x16x32_bf16 v[104:107], v[204:207], a[200:203], v[104:107]\n"
    "v_exp_f32 v195, v195\n"
    "v_mfma_f32_16x16x32_bf16 v[108:111], v[204:207], a[204:207], 0\n"
    "v_add_f32 v244, v244, v195\n"
    "v_mfma_f32_16x16x32_bf16 v[108:111], v[200:203], a[172:175], v[108:111]\n"
    "v_exp_f32 v208, v208\n"
    "v_mfma_f32_16x16x32_bf16 v[196:199], a[128:131], a[64:67], v[232:235]\n"
    "v_add_f32 v244, v244, v208\n"
    "v_cvt_pk_bf16_f32 v192, v192, v193\n"
    "v_mfma_f32_16x16x32_bf16 v[196:199], a[132:135], a[68:71], v[196:199]\n"
    "v_exp_f32 v209, v209\n"
    "v_mfma_f32_16x16x32_bf16 v[196:199], a[136:139], a[72:75], v[196:199]\n"
    "v_add_f32 v244, v244, v209\n"
    "v_cvt_pk_bf16_f32 v193, v194, v195\n"
    "v_mfma_f32_16x16x32_bf16 v[196:199], a[140:143], a[76:79], v[196:199]\n"
    "v_exp_f32 v210, v210\n"
    "v_mfma_f32_16x16x32_bf16 v[212:215], a[156:159], a[76:79], v[232:235]\n"
    "v_add_f32 v244, v244, v210\n"
    "v_cvt_pk_bf16_f32 v194, v208, v209\n"
    "v_mfma_f32_16x16x32_bf16 v[212:215], a[152:155], a[72:75], v[212:215]\n"
    "v_exp_f32 v211, v211\n"
    "v_mfma_f32_16x16x32_bf16 v[212:215], a[148:151], a[68:71], v[212:215]\n"
    "v_add_f32 v244, v244, v211\n"
    "v_cvt_pk_bf16_f32 v195, v210, v211\n"
    "v_mfma_f32_16x16x32_bf16 v[212:215], a[144:147], a[64:67], v[212:215]\n"
    "v_exp_f32 v196, v196\n"
    "v_mfma_f32_16x16x32_bf16 v[112:115], v[200:203], a[176:179], 0\n"
    "v_add_f32 v244, v244, v196\n"
    "v_mfma_f32_16x16x32_bf16 v[112:115], v[204:207], a[208:211], v[112:115]\n"
    "v_exp_f32 v197, v197\n"
    "v_mfma_f32_16x16x32_bf16 v[116:119], v[204:207], a[212:215], 0\n"
    "v_add_f32 v244, v244, v197\n"
    "v_mfma_f32_16x16x32_bf16 v[116:119], v[200:203], a[180:183], v[116:119]\n"
    "v_exp_f32 v198, v198\n"
    "v_mfma_f32_16x16x32_bf16 v[120:123], v[200:203], a[184:187], 0\n"
    "v_add_f32 v244, v244, v198\n"
    "v_mfma_f32_16x16x32_bf16 v[120:123], v[204:207], a[216:219], v[120:123]\n"
    "v_exp_f32 v199, v199\n"
    "v_mfma_f32_16x16x32_bf16 v[124:127], v[204:207], a[220:223], 0\n"
    "v_add_f32 v244, v244, v199\n"
    "v_mfma_f32_16x16x32_bf16 v[124:127], v[200:203], a[188:191], v[124:127]\n"
    "v_exp_f32 v212, v212\n"
    "s_waitcnt vmcnt(4)\n"
    "ds_read_b128 a[80:83], v250 offset:20640\n"
    "ds_read_b128 a[84:87], v250 offset:20896\n"
    "ds_read_b128 a[88:91], v250 offset:21152\n"
    "ds_read_b128 a[92:95], v250 offset:21408\n"
    "s_add_u32 m0, s73, 49536\n"
    "s_add_u32 s69, s70, 24576\n"
    "buffer_load_dwordx4 v253, s[36:39], s69 offen offset:0 lds\n"
    "buffer_load_dwordx4 v253, s[36:39], s69 offen offset:1024 lds\n"
    "buffer_load_dwordx4 v253, s[40:43], s69 offen offset:2048 lds\n"
    "buffer_load_dwordx4 v253, s[44:47], s69 offen offset:3080 lds\n"
    "s_waitcnt lgkmcnt(3)\n"
    "v_mfma_f32_4x4x4_16b_bf16 v[168:171], a[248:249], a[80:81], 0\n"
    "s_nop 1\n"
    "v_mfma_f32_4x4x4_16b_bf16 v[168:171], a[250:251], a[80:81], v[168:171]\n"
    "v_mfma_f32_4x4x4_16b_bf16 v[172:175], a[248:249], a[82:83], 0\n"
    "s_nop 1\n"
    "v_mfma_f32_4x4x4_16b_bf16 v[172:175], a[250:251], a[82:83], v[172:175]\n"
    "v_nop_e64\n"
    "v_cvt_pk_bf16_f32 v168, v168, v169\n"
    "s_waitcnt lgkmcnt(2)\n"
    "v_mfma_f32_4x4x4_16b_bf16 v[164:167], a[248:249], a[84:85], 0\n"
    "v_cvt_pk_bf16_f32 v169, v170, v171\n"
    "v_accvgpr_write_b32 a80, v168\n"
    "v_mfma_f32_4x4x4_16b_bf16 v[164:167], a[250:251], a[84:85], v[164:167]\n"
    "v_cvt_pk_bf16_f32 v172, v172, v173\n"
    "v_accvgpr_write_b32 a81, v169\n"
    "v_mfma_f32_4x4x4_16b_bf16 v[168:171], a[248:249], a[86:87], 0\n"
    "v_cvt_pk_bf16_f32 v173, v174, v175\n"
    "v_accvgpr_write_b32 a82, v172\n"
    "v_mfma_f32_4x4x4_16b_bf16 v[168:171], a[250:251], a[86:87], v[168:171]\n"
    "v_cvt_pk_bf16_f32 v164, v164, v165\n"
    "v_accvgpr_write_b32 a83, v173\n"
    "s_waitcnt lgkmcnt(0)\n"
    "s_nop 0\n"
    "v_mfma_f32_4x4x4_16b_bf16 v[172:175], a[248:249], a[88:89], 0\n"
    "v_cvt_pk_bf16_f32 v165, v166, v167\n"
    "v_accvgpr_write_b32 a84, v164\n"
    "v_mfma_f32_4x4x4_16b_bf16 v[172:175], a[250:251], a[88:89], v[172:175]\n"
    "v_cvt_pk_bf16_f32 v168, v168, v169\n"
    "v_accvgpr_write_b32 a85, v165\n"
    "v_mfma_f32_4x4x4_16b_bf16 v[164:167], a[248:249], a[90:91], 0\n"
    "v_cvt_pk_bf16_f32 v169, v170, v171\n"
    "v_accvgpr_write_b32 a86, v168\n"
    "v_mfma_f32_4x4x4_16b_bf16 v[164:167], a[250:251], a[90:91], v[164:167]\n"
    "v_cvt_pk_bf16_f32 v172, v172, v173\n"
    "v_accvgpr_write_b32 a87, v169\n"
    "v_mfma_f32_4x4x4_16b_bf16 v[168:171], a[248:249], a[92:93], 0\n"
    "v_cvt_pk_bf16_f32 v173, v174, v175\n"
    "v_accvgpr_write_b32 a88, v172\n"
    "v_mfma_f32_4x4x4_16b_bf16 v[168:171], a[250:251], a[92:93], v[168:171]\n"
    "v_cvt_pk_bf16_f32 v164, v164, v165\n"
    "v_accvgpr_write_b32 a89, v173\n"
    "v_mfma_f32_4x4x4_16b_bf16 v[172:175], a[248:249], a[94:95], 0\n"
    "v_cvt_pk_bf16_f32 v165, v166, v167\n"
    "v_accvgpr_write_b32 a90, v164\n"
    "v_mfma_f32_4x4x4_16b_bf16 v[172:175], a[250:251], a[94:95], v[172:175]\n"
    "v_cvt_pk_bf16_f32 v168, v168, v169\n"
    "v_accvgpr_write_b32 a91, v165\n"
    "v_mfma_f32_16x16x32_bf16 v[188:191], a[96:99], a[80:83], 0\n"
    "v_cvt_pk_bf16_f32 v169, v170, v171\n"
    "v_accvgpr_write_b32 a92, v168\n"
    "v_mfma_f32_16x16x32_bf16 v[188:191], a[100:103], a[84:87], v[188:191]\n"
    "v_cvt_pk_bf16_f32 v172, v172, v173\n"
    "v_accvgpr_write_b32 a93, v169\n"
    "v_mfma_f32_16x16x32_bf16 v[188:191], a[104:107], a[88:91], v[188:191]\n"
    "v_cvt_pk_bf16_f32 v173, v174, v175\n"
    "v_accvgpr_write_b32 a94, v172\n"
    "v_accvgpr_write_b32 a95, v173\n"
    "v_add_f32 v244, v244, v212\n"
    "v_cvt_pk_bf16_f32 v196, v196, v197\n"
    "v_mfma_f32_16x16x32_bf16 v[188:191], a[108:111], a[92:95], v[188:191]\n"
    "v_exp_f32 v213, v213\n"
    "v_cvt_pk_bf16_f32 v197, v198, v199\n"
    "v_add_f32 v244, v244, v213\n"
    "v_exp_f32 v214, v214\n"
    "v_cvt_pk_bf16_f32 v198, v212, v213\n"
    "v_add_f32 v244, v244, v214\n"
    "v_exp_f32 v215, v215\n"
    "s_nop 0\n"
    "v_max3_f32 v248, v188, v189, v190\n"
    "v_max_f32 v248, v248, v191\n"
    "v_mul_f32 v248, 0xbe800000, v248\n"
    "v_cvt_pk_bf16_f32 v248, v248, s80\n"
    "s_nop 1\n"
    "v_mfma_f32_16x16x16_bf16 v[236:239], a[252:253], v[248:249], 0\n"
    "s_nop 7\n"
    "v_add_f32 v200, v188, v236\n"
    "v_add_f32 v201, v189, v237\n"
    "v_add_f32 v202, v190, v238\n"
    "v_add_f32 v203, v191, v239\n"
    "v_mfma_f32_16x16x32_bf16 v[208:211], a[124:127], a[92:95], v[236:239]\n"
    "v_mfma_f32_16x16x32_bf16 v[208:211], a[120:123], a[88:91], v[208:211]\n"
    "v_mfma_f32_16x16x32_bf16 v[208:211], a[116:119], a[84:87], v[208:211]\n"
    "s_waitcnt vmcnt(4) lgkmcnt(0)\n"
    "v_add_f32 v244, v244, v215\n"
    "v_cvt_pk_bf16_f32 v199, v214, v215\n"
    "v_mfma_f32_16x16x32_bf16 v[208:211], a[112:115], a[80:83], v[208:211]\n"
    "s_barrier\n"
    "v_exp_f32 v200, v200\n"
    "v_mfma_f32_16x16x32_bf16 v[128:131], v[192:195], a[160:163], 0\n"
    "ds_read_b128 a[96:99], v246 offset:33024\n"
    "v_mov_b32 v245, v200\n"
    "v_mfma_f32_16x16x32_bf16 v[128:131], v[196:199], a[192:195], v[128:131]\n"
    "v_exp_f32 v201, v201\n"
    "v_mfma_f32_16x16x32_bf16 v[132:135], v[196:199], a[196:199], 0\n"
    "ds_read_b128 a[100:103], v246 offset:33280\n"
    "v_add_f32 v245, v245, v201\n"
    "v_mfma_f32_16x16x32_bf16 v[132:135], v[192:195], a[164:167], v[132:135]\n"
    "v_exp_f32 v202, v202\n"
    "v_mfma_f32_16x16x32_bf16 v[136:139], v[192:195], a[168:171], 0\n"
    "ds_read_b128 a[104:107], v246 offset:33536\n"
    "v_add_f32 v245, v245, v202\n"
    "v_mfma_f32_16x16x32_bf16 v[136:139], v[196:199], a[200:203], v[136:139]\n"
    "v_exp_f32 v203, v203\n"
    "v_mfma_f32_16x16x32_bf16 v[140:143], v[196:199], a[204:207], 0\n"
    "ds_read_b128 a[108:111], v246 offset:33792\n"
    "v_add_f32 v245, v245, v203\n"
    "v_mfma_f32_16x16x32_bf16 v[140:143], v[192:195], a[172:175], v[140:143]\n"
    "v_exp_f32 v208, v208\n"
    "v_mfma_f32_16x16x32_bf16 v[204:207], a[128:131], a[80:83], v[236:239]\n"
    "s_mov_b32 m0, s73\n"
    "v_add_f32 v245, v245, v208\n"
    "v_cvt_pk_bf16_f32 v200, v200, v201\n"
    "v_mfma_f32_16x16x32_bf16 v[204:207], a[132:135], a[84:87], v[204:207]\n"
    "s_add_u32 s69, s70, 32768\n"
    "v_exp_f32 v209, v209\n"
    "v_mfma_f32_16x16x32_bf16 v[204:207], a[136:139], a[88:91], v[204:207]\n"
    "v_add_f32 v245, v245, v209\n"
    "v_cvt_pk_bf16_f32 v201, v202, v203\n"
    "v_mfma_f32_16x16x32_bf16 v[204:207], a[140:143], a[92:95], v[204:207]\n"
    "buffer_load_dwordx4 v253, s[36:39], s69 offen offset:0 lds\n"
    "v_exp_f32 v210, v210\n"
    "v_mfma_f32_16x16x32_bf16 v[212:215], a[156:159], a[92:95], v[236:239]\n"
    "v_add_f32 v245, v245, v210\n"
    "v_cvt_pk_bf16_f32 v202, v208, v209\n"
    "v_mfma_f32_16x16x32_bf16 v[212:215], a[152:155], a[88:91], v[212:215]\n"
    "v_exp_f32 v211, v211\n"
    "v_mfma_f32_16x16x32_bf16 v[212:215], a[148:151], a[84:87], v[212:215]\n"
    "v_add_f32 v245, v245, v211\n"
    "v_cvt_pk_bf16_f32 v203, v210, v211\n"
    "v_mfma_f32_16x16x32_bf16 v[212:215], a[144:147], a[80:83], v[212:215]\n"
    "buffer_load_dwordx4 v253, s[36:39], s69 offen offset:1024 lds\n"
    "v_exp_f32 v204, v204\n"
    "v_mfma_f32_16x16x32_bf16 v[144:147], v[192:195], a[176:179], 0\n"
    "ds_read_b128 a[124:127], v246 offset:42048\n"
    "v_add_f32 v245, v245, v204\n"
    "v_mfma_f32_16x16x32_bf16 v[144:147], v[196:199], a[208:211], v[144:147]\n"
    "v_exp_f32 v205, v205\n"
    "v_mfma_f32_16x16x32_bf16 v[148:151], v[196:199], a[212:215], 0\n"
    "ds_read_b128 a[120:123], v246 offset:41792\n"
    "v_add_f32 v245, v245, v205\n"
    "v_mfma_f32_16x16x32_bf16 v[148:151], v[192:195], a[180:183], v[148:151]\n"
    "buffer_load_dwordx4 v253, s[40:43], s69 offen offset:2048 lds\n"
    "v_exp_f32 v206, v206\n"
    "v_mfma_f32_16x16x32_bf16 v[152:155], v[192:195], a[184:187], 0\n"
    "ds_read_b128 a[116:119], v246 offset:41536\n"
    "v_add_f32 v245, v245, v206\n"
    "v_mfma_f32_16x16x32_bf16 v[152:155], v[196:199], a[216:219], v[152:155]\n"
    "v_exp_f32 v207, v207\n"
    "v_mfma_f32_16x16x32_bf16 v[156:159], v[196:199], a[220:223], 0\n"
    "ds_read_b128 a[112:115], v246 offset:41280\n"
    "v_add_f32 v245, v245, v207\n"
    "v_mfma_f32_16x16x32_bf16 v[156:159], v[192:195], a[188:191], v[156:159]\n"
    "s_waitcnt lgkmcnt(3)\n"
    "buffer_load_dwordx4 v253, s[44:47], s69 offen offset:3080 lds\n"
    "v_exp_f32 v212, v212\n"
    "v_mfma_f32_16x16x32_bf16 v[192:195], a[96:99], a[0:3], v[216:219]\n"
    "v_add_f32 v245, v245, v212\n"
    "v_cvt_pk_bf16_f32 v204, v204, v205\n"
    "v_mfma_f32_16x16x32_bf16 v[192:195], a[100:103], a[4:7], v[192:195]\n"
    "v_exp_f32 v213, v213\n"
    "v_mfma_f32_16x16x32_bf16 v[192:195], a[104:107], a[8:11], v[192:195]\n"
    "v_add_f32 v245, v245, v213\n"
    "v_cvt_pk_bf16_f32 v205, v206, v207\n"
    "v_mfma_f32_16x16x32_bf16 v[192:195], a[108:111], a[12:15], v[192:195]\n"
    "v_exp_f32 v214, v214\n"
    "v_mfma_f32_16x16x32_bf16 v[208:211], a[124:127], a[12:15], v[216:219]\n"
    "s_waitcnt lgkmcnt(0)\n"
    "v_add_f32 v245, v245, v214\n"
    "v_cvt_pk_bf16_f32 v206, v212, v213\n"
    "v_mfma_f32_16x16x32_bf16 v[208:211], a[120:123], a[8:11], v[208:211]\n"
    "v_exp_f32 v215, v215\n"
    "v_mfma_f32_16x16x32_bf16 v[208:211], a[116:119], a[4:7], v[208:211]\n"
    "v_add_f32 v245, v245, v215\n"
    "v_cvt_pk_bf16_f32 v207, v214, v215\n"
    "v_mfma_f32_16x16x32_bf16 v[208:211], a[112:115], a[0:3], v[208:211]\n"
    "v_exp_f32 v192, v192\n"
    "v_mfma_f32_16x16x32_bf16 v[160:163], v[200:203], a[160:163], 0\n"
    "ds_read_b64_tr_b16 a[224:225], v251 offset:33032\n"
    "v_add_f32 v240, v240, v192\n"
    "v_mfma_f32_16x16x32_bf16 v[160:163], v[204:207], a[192:195], v[160:163]\n"
    "v_exp_f32 v193, v193\n"
    "v_mfma_f32_16x16x32_bf16 v[164:167], v[204:207], a[196:199], 0\n"
    "ds_read_b64_tr_b16 a[226:227], v251 offset:41288\n"
    "v_add_f32 v240, v240, v193\n"
    "v_mfma_f32_16x16x32_bf16 v[164:167], v[200:203], a[164:167], v[164:167]\n"
    "v_exp_f32 v194, v194\n"
    "v_mfma_f32_16x16x32_bf16 v[168:171], v[200:203], a[168:171], 0\n"
    "ds_read_b64_tr_b16 a[228:229], v254 offset:33024\n"
    "v_add_f32 v240, v240, v194\n"
    "v_mfma_f32_16x16x32_bf16 v[168:171], v[204:207], a[200:203], v[168:171]\n"
    "ds_read_b64_tr_b16 a[170:171], v251 offset:41280\n"
    "v_exp_f32 v195, v195\n"
    "v_mfma_f32_16x16x32_bf16 v[172:175], v[204:207], a[204:207], 0\n"
    "ds_read_b64_tr_b16 a[230:231], v254 offset:41280\n"
    "v_add_f32 v240, v240, v195\n"
    "v_mfma_f32_16x16x32_bf16 v[172:175], v[200:203], a[172:175], v[172:175]\n"
    "v_exp_f32 v208, v208\n"
    "v_mfma_f32_16x16x32_bf16 v[196:199], a[96:99], a[16:19], v[220:223]\n"
    "v_add_f32 v240, v240, v208\n"
    "v_cvt_pk_bf16_f32 v192, v192, v193\n"
    "v_mfma_f32_16x16x32_bf16 v[196:199], a[100:103], a[20:23], v[196:199]\n"
    "v_exp_f32 v209, v209\n"
    "v_mfma_f32_16x16x32_bf16 v[196:199], a[104:107], a[24:27], v[196:199]\n"
    "ds_read_b64_tr_b16 a[164:165], v247 offset:33032\n"
    "v_add_f32 v240, v240, v209\n"
    "v_cvt_pk_bf16_f32 v193, v194, v195\n"
    "v_mfma_f32_16x16x32_bf16 v[196:199], a[108:111], a[28:31], v[196:199]\n"
    "ds_read_b64_tr_b16 a[166:167], v247 offset:41288\n"
    "v_exp_f32 v210, v210\n"
    "v_mfma_f32_16x16x32_bf16 v[212:215], a[124:127], a[28:31], v[220:223]\n"
    "ds_read_b64_tr_b16 a[168:169], v251 offset:33024\n"
    "v_add_f32 v240, v240, v210\n"
    "v_cvt_pk_bf16_f32 v194, v208, v209\n"
    "v_mfma_f32_16x16x32_bf16 v[212:215], a[120:123], a[24:27], v[212:215]\n"
    "v_exp_f32 v211, v211\n"
    "v_mfma_f32_16x16x32_bf16 v[212:215], a[116:119], a[20:23], v[212:215]\n"
    "v_add_f32 v240, v240, v211\n"
    "v_cvt_pk_bf16_f32 v195, v210, v211\n"
    "v_mfma_f32_16x16x32_bf16 v[212:215], a[112:115], a[16:19], v[212:215]\n"
    "v_exp_f32 v196, v196\n"
    "v_mfma_f32_16x16x32_bf16 v[176:179], v[200:203], a[176:179], 0\n"
    "ds_read_b64_tr_b16 a[232:233], v254 offset:33032\n"
    "v_add_f32 v241, v241, v196\n"
    "v_mfma_f32_16x16x32_bf16 v[176:179], v[204:207], a[208:211], v[176:179]\n"
    "ds_read_b64_tr_b16 a[176:177], v252 offset:33024\n"
    "v_exp_f32 v197, v197\n"
    "v_mfma_f32_16x16x32_bf16 v[180:183], v[204:207], a[212:215], 0\n"
    "ds_read_b64_tr_b16 a[234:235], v254 offset:41288\n"
    "v_add_f32 v241, v241, v197\n"
    "v_mfma_f32_16x16x32_bf16 v[180:183], v[200:203], a[180:183], v[180:183]\n"
    "ds_read_b64_tr_b16 a[180:181], v252 offset:33032\n"
    "v_exp_f32 v198, v198\n"
    "v_mfma_f32_16x16x32_bf16 v[184:187], v[200:203], a[184:187], 0\n"
    "ds_read_b64_tr_b16 a[160:161], v247 offset:33024\n"
    "v_add_f32 v241, v241, v198\n"
    "v_mfma_f32_16x16x32_bf16 v[184:187], v[204:207], a[216:219], v[184:187]\n"
    "ds_read_b64_tr_b16 a[178:179], v252 offset:41280\n"
    "v_exp_f32 v199, v199\n"
    "v_mfma_f32_16x16x32_bf16 v[188:191], v[204:207], a[220:223], 0\n"
    "ds_read_b64_tr_b16 a[162:163], v247 offset:41280\n"
    "ds_read_b64_tr_b16 a[182:183], v252 offset:41288\n"
    "v_add_f32 v241, v241, v199\n"
    "v_mfma_f32_16x16x32_bf16 v[188:191], v[200:203], a[188:191], v[188:191]\n"
    "v_exp_f32 v212, v212\n"
    "v_mfma_f32_16x16x32_bf16 v[200:203], a[96:99], a[32:35], v[224:227]\n"
    "v_add_f32 v241, v241, v212\n"
    "v_cvt_pk_bf16_f32 v196, v196, v197\n"
    "v_mfma_f32_16x16x32_bf16 v[200:203], a[100:103], a[36:39], v[200:203]\n"
    "v_exp_f32 v213, v213\n"
    "v_mfma_f32_16x16x32_bf16 v[200:203], a[104:107], a[40:43], v[200:203]\n"
    "v_add_f32 v241, v241, v213\n"
    "v_cvt_pk_bf16_f32 v197, v198, v199\n"
    "v_mfma_f32_16x16x32_bf16 v[200:203], a[108:111], a[44:47], v[200:203]\n"
    "LPG2:\n"
    "s_cbranch_execz LPG3\n"
    "v_exp_f32 v214, v214\n"
    "v_mfma_f32_16x16x32_bf16 v[208:211], a[124:127], a[44:47], v[224:227]\n"
    "v_add_f32 v241, v241, v214\n"
    "v_cvt_pk_bf16_f32 v198, v212, v213\n"
    "v_mfma_f32_16x16x32_bf16 v[208:211], a[120:123], a[40:43], v[208:211]\n"
    "v_exp_f32 v215, v215\n"
    "v_mfma_f32_16x16x32_bf16 v[208:211], a[116:119], a[36:39], v[208:211]\n"
    "s_waitcnt vmcnt(4) lgkmcnt(0)\n"
    "v_add_f32 v241, v241, v215\n"
    "v_cvt_pk_bf16_f32 v199, v214, v215\n"
    "v_mfma_f32_16x16x32_bf16 v[208:211], a[112:115], a[32:35], v[208:211]\n"
    "s_barrier\n"
    "v_exp_f32 v200, v200\n"
    "v_mfma_f32_16x16x32_bf16 v[0:3], v[192:195], a[160:163], v[0:3]\n"
    "ds_read_b128 a[128:131], v246 offset:49536\n"
    "v_add_f32 v242, v242, v200\n"
    "v_mfma_f32_16x16x32_bf16 v[4:7], v[192:195], a[164:167], v[4:7]\n"
    "v_exp_f32 v201, v201\n"
    "v_mfma_f32_16x16x32_bf16 v[8:11], v[192:195], a[168:171], v[8:11]\n"
    "ds_read_b128 a[132:135], v246 offset:49792\n"
    "v_add_f32 v242, v242, v201\n"
    "v_mfma_f32_16x16x32_bf16 v[12:15], v[192:195], a[224:227], v[12:15]\n"
    "v_exp_f32 v202, v202\n"
    "v_mfma_f32_16x16x32_bf16 v[16:19], v[192:195], a[176:179], v[16:19]\n"
    "ds_read_b128 a[136:139], v246 offset:50048\n"
    "v_add_f32 v242, v242, v202\n"
    "v_mfma_f32_16x16x32_bf16 v[20:23], v[192:195], a[180:183], v[20:23]\n"
    "v_exp_f32 v203, v203\n"
    "v_mfma_f32_16x16x32_bf16 v[24:27], v[192:195], a[228:231], v[24:27]\n"
    "ds_read_b128 a[140:143], v246 offset:50304\n"
    "v_add_f32 v242, v242, v203\n"
    "v_mfma_f32_16x16x32_bf16 v[28:31], v[192:195], a[232:235], v[28:31]\n"
    "v_exp_f32 v208, v208\n"
    "v_mfma_f32_16x16x32_bf16 v[204:207], a[96:99], a[48:51], v[228:231]\n"
    "v_add_f32 v242, v242, v208\n"
    "v_cvt_pk_bf16_f32 v200, v200, v201\n"
    "v_mfma_f32_16x16x32_bf16 v[204:207], a[100:103], a[52:55], v[204:207]\n"
    "v_exp_f32 v209, v209\n"
    "v_mfma_f32_16x16x32_bf16 v[204:207], a[104:107], a[56:59], v[204:207]\n"
    "ds_read_b64_tr_b16 a[200:201], v251 offset:49536\n"
    "v_add_f32 v242, v242, v209\n"
    "v_cvt_pk_bf16_f32 v201, v202, v203\n"
    "v_mfma_f32_16x16x32_bf16 v[204:207], a[108:111], a[60:63], v[204:207]\n"
    "ds_read_b64_tr_b16 a[214:215], v252 offset:57800\n"
    "v_exp_f32 v210, v210\n"
    "v_mfma_f32_16x16x32_bf16 v[212:215], a[124:127], a[60:63], v[228:231]\n"
    "ds_read_b64_tr_b16 a[244:245], v254 offset:49544\n"
    "v_add_f32 v242, v242, v210\n"
    "v_cvt_pk_bf16_f32 v202, v208, v209\n"
    "v_mfma_f32_16x16x32_bf16 v[212:215], a[120:123], a[56:59], v[212:215]\n"
    "v_exp_f32 v211, v211\n"
    "v_mfma_f32_16x16x32_bf16 v[212:215], a[116:119], a[52:55], v[212:215]\n"
    "v_add_f32 v242, v242, v211\n"
    "v_cvt_pk_bf16_f32 v203, v210, v211\n"
    "v_mfma_f32_16x16x32_bf16 v[212:215], a[112:115], a[48:51], v[212:215]\n"
    "v_exp_f32 v204, v204\n"
    "v_mfma_f32_16x16x32_bf16 v[32:35], v[196:199], a[160:163], v[32:35]\n"
    "ds_read_b128 a[156:159], v246 offset:58560\n"
    "v_add_f32 v243, v243, v204\n"
    "v_mfma_f32_16x16x32_bf16 v[36:39], v[196:199], a[164:167], v[36:39]\n"
    "v_exp_f32 v205, v205\n"
    "v_mfma_f32_16x16x32_bf16 v[40:43], v[196:199], a[168:171], v[40:43]\n"
    "ds_read_b128 a[152:155], v246 offset:58304\n"
    "v_add_f32 v243, v243, v205\n"
    "v_mfma_f32_16x16x32_bf16 v[44:47], v[196:199], a[224:227], v[44:47]\n"
    "v_exp_f32 v206, v206\n"
    "v_mfma_f32_16x16x32_bf16 v[48:51], v[196:199], a[176:179], v[48:51]\n"
    "ds_read_b128 a[148:151], v246 offset:58048\n"
    "v_add_f32 v243, v243, v206\n"
    "v_mfma_f32_16x16x32_bf16 v[52:55], v[196:199], a[180:183], v[52:55]\n"
    "v_exp_f32 v207, v207\n"
    "v_mfma_f32_16x16x32_bf16 v[56:59], v[196:199], a[228:231], v[56:59]\n"
    "ds_read_b128 a[144:147], v246 offset:57792\n"
    "v_add_f32 v243, v243, v207\n"
    "v_mfma_f32_16x16x32_bf16 v[60:63], v[196:199], a[232:235], v[60:63]\n"
    "s_waitcnt lgkmcnt(3)\n"
    "v_exp_f32 v212, v212\n"
    "v_mfma_f32_16x16x32_bf16 v[192:195], a[128:131], a[48:51], v[228:231]\n"
    "v_add_f32 v243, v243, v212\n"
    "v_cvt_pk_bf16_f32 v204, v204, v205\n"
    "v_mfma_f32_16x16x32_bf16 v[192:195], a[132:135], a[52:55], v[192:195]\n"
    "v_exp_f32 v213, v213\n"
    "v_mfma_f32_16x16x32_bf16 v[192:195], a[136:139], a[56:59], v[192:195]\n"
    "ds_read_b64_tr_b16 a[202:203], v251 offset:57792\n"
    "v_add_f32 v243, v243, v213\n"
    "v_cvt_pk_bf16_f32 v205, v206, v207\n"
    "v_mfma_f32_16x16x32_bf16 v[192:195], a[140:143], a[60:63], v[192:195]\n"
    "ds_read_b64_tr_b16 a[240:241], v254 offset:49536\n"
    "v_exp_f32 v214, v214\n"
    "v_mfma_f32_16x16x32_bf16 v[208:211], a[156:159], a[60:63], v[228:231]\n"
    "s_waitcnt lgkmcnt(2)\n"
    "ds_read_b64_tr_b16 a[246:247], v254 offset:57800\n"
    "v_add_f32 v243, v243, v214\n"
    "v_cvt_pk_bf16_f32 v206, v212, v213\n"
    "v_mfma_f32_16x16x32_bf16 v[208:211], a[152:155], a[56:59], v[208:211]\n"
    "v_exp_f32 v215, v215\n"
    "v_mfma_f32_16x16x32_bf16 v[208:211], a[148:151], a[52:55], v[208:211]\n"
    "v_add_f32 v243, v243, v215\n"
    "v_cvt_pk_bf16_f32 v207, v214, v215\n"
    "v_mfma_f32_16x16x32_bf16 v[208:211], a[144:147], a[48:51], v[208:211]\n"
    "v_exp_f32 v192, v192\n"
    "v_mfma_f32_16x16x32_bf16 v[64:67], v[200:203], a[160:163], v[64:67]\n"
    "ds_read_b64_tr_b16 a[192:193], v247 offset:49536\n"
    "v_add_f32 v243, v243, v192\n"
    "v_mfma_f32_16x16x32_bf16 v[68:71], v[200:203], a[164:167], v[68:71]\n"
    "v_exp_f32 v193, v193\n"
    "v_mfma_f32_16x16x32_bf16 v[72:75], v[200:203], a[168:171], v[72:75]\n"
    "ds_read_b64_tr_b16 a[194:195], v247 offset:57792\n"
    "v_add_f32 v243, v243, v193\n"
    "v_mfma_f32_16x16x32_bf16 v[76:79], v[200:203], a[224:227], v[76:79]\n"
    "v_exp_f32 v194, v194\n"
    "v_mfma_f32_16x16x32_bf16 v[80:83], v[200:203], a[176:179], v[80:83]\n"
    "ds_read_b64_tr_b16 a[196:197], v247 offset:49544\n"
    "v_add_f32 v243, v243, v194\n"
    "v_mfma_f32_16x16x32_bf16 v[84:87], v[200:203], a[180:183], v[84:87]\n"
    "v_exp_f32 v195, v195\n"
    "v_mfma_f32_16x16x32_bf16 v[88:91], v[200:203], a[228:231], v[88:91]\n"
    "ds_read_b64_tr_b16 a[198:199], v247 offset:57800\n"
    "v_add_f32 v243, v243, v195\n"
    "v_mfma_f32_16x16x32_bf16 v[92:95], v[200:203], a[232:235], v[92:95]\n"
    "v_exp_f32 v208, v208\n"
    "v_mfma_f32_16x16x32_bf16 v[196:199], a[96:99], a[64:67], v[232:235]\n"
    "v_add_f32 v243, v243, v208\n"
    "v_cvt_pk_bf16_f32 v192, v192, v193\n"
    "v_mfma_f32_16x16x32_bf16 v[196:199], a[100:103], a[68:71], v[196:199]\n"
    "v_exp_f32 v209, v209\n"
    "v_mfma_f32_16x16x32_bf16 v[196:199], a[104:107], a[72:75], v[196:199]\n"
    "ds_read_b64_tr_b16 a[238:239], v251 offset:57800\n"
    "v_add_f32 v243, v243, v209\n"
    "v_cvt_pk_bf16_f32 v193, v194, v195\n"
    "v_mfma_f32_16x16x32_bf16 v[196:199], a[108:111], a[76:79], v[196:199]\n"
    "ds_read_b64_tr_b16 a[242:243], v254 offset:57792\n"
    "v_exp_f32 v210, v210\n"
    "v_mfma_f32_16x16x32_bf16 v[212:215], a[124:127], a[76:79], v[232:235]\n"
    "v_add_f32 v243, v243, v210\n"
    "v_cvt_pk_bf16_f32 v194, v208, v209\n"
    "v_mfma_f32_16x16x32_bf16 v[212:215], a[120:123], a[72:75], v[212:215]\n"
    "v_exp_f32 v211, v211\n"
    "v_mfma_f32_16x16x32_bf16 v[212:215], a[116:119], a[68:71], v[212:215]\n"
    "v_add_f32 v243, v243, v211\n"
    "v_cvt_pk_bf16_f32 v195, v210, v211\n"
    "v_mfma_f32_16x16x32_bf16 v[212:215], a[112:115], a[64:67], v[212:215]\n"
    "v_exp_f32 v196, v196\n"
    "v_mfma_f32_16x16x32_bf16 v[96:99], v[204:207], a[160:163], v[96:99]\n"
    "s_waitcnt lgkmcnt(2)\n"
    "ds_read_b64_tr_b16 a[236:237], v251 offset:49544\n"
    "v_add_f32 v244, v244, v196\n"
    "v_mfma_f32_16x16x32_bf16 v[96:99], v[192:195], a[192:195], v[96:99]\n"
    "v_exp_f32 v197, v197\n"
    "v_mfma_f32_16x16x32_bf16 v[100:103], v[192:195], a[196:199], v[100:103]\n"
    "ds_read_b64_tr_b16 a[208:209], v252 offset:49536\n"
    "v_add_f32 v244, v244, v197\n"
    "v_mfma_f32_16x16x32_bf16 v[100:103], v[204:207], a[164:167], v[100:103]\n"
    "v_exp_f32 v198, v198\n"
    "v_mfma_f32_16x16x32_bf16 v[104:107], v[204:207], a[168:171], v[104:107]\n"
    "ds_read_b64_tr_b16 a[210:211], v252 offset:57792\n"
    "v_add_f32 v244, v244, v198\n"
    "v_mfma_f32_16x16x32_bf16 v[104:107], v[192:195], a[200:203], v[104:107]\n"
    "s_waitcnt lgkmcnt(2)\n"
    "v_exp_f32 v199, v199\n"
    "v_mfma_f32_16x16x32_bf16 v[108:111], v[192:195], a[236:239], v[108:111]\n"
    "ds_read_b64_tr_b16 a[212:213], v252 offset:49544\n"
    "v_add_f32 v244, v244, v199\n"
    "v_mfma_f32_16x16x32_bf16 v[108:111], v[204:207], a[224:227], v[108:111]\n"
    "v_exp_f32 v212, v212\n"
    "v_mfma_f32_16x16x32_bf16 v[200:203], a[128:131], a[64:67], v[232:235]\n"
    "v_add_f32 v244, v244, v212\n"
    "v_cvt_pk_bf16_f32 v196, v196, v197\n"
    "v_mfma_f32_16x16x32_bf16 v[200:203], a[132:135], a[68:71], v[200:203]\n"
    "v_exp_f32 v213, v213\n"
    "v_mfma_f32_16x16x32_bf16 v[200:203], a[136:139], a[72:75], v[200:203]\n"
    "v_add_f32 v244, v244, v213\n"
    "v_cvt_pk_bf16_f32 v197, v198, v199\n"
    "v_mfma_f32_16x16x32_bf16 v[200:203], a[140:143], a[76:79], v[200:203]\n"
    "v_exp_f32 v214, v214\n"
    "v_mfma_f32_16x16x32_bf16 v[208:211], a[156:159], a[76:79], v[232:235]\n"
    "v_add_f32 v244, v244, v214\n"
    "v_cvt_pk_bf16_f32 v198, v212, v213\n"
    "v_mfma_f32_16x16x32_bf16 v[208:211], a[152:155], a[72:75], v[208:211]\n"
    "v_exp_f32 v215, v215\n"
    "v_mfma_f32_16x16x32_bf16 v[208:211], a[148:151], a[68:71], v[208:211]\n"
    "s_waitcnt vmcnt(0) lgkmcnt(0)\n"
    "v_add_f32 v244, v244, v215\n"
    "v_cvt_pk_bf16_f32 v199, v214, v215\n"
    "v_mfma_f32_16x16x32_bf16 v[208:211], a[144:147], a[64:67], v[208:211]\n"
    "s_barrier\n"
    "v_exp_f32 v200, v200\n"
    "v_mfma_f32_16x16x32_bf16 v[112:115], v[204:207], a[176:179], v[112:115]\n"
    "ds_read_b64_tr_b16 a[172:173], v251 offset:8\n"
    "v_add_f32 v244, v244, v200\n"
    "v_mfma_f32_16x16x32_bf16 v[112:115], v[192:195], a[208:211], v[112:115]\n"
    "v_exp_f32 v201, v201\n"
    "v_mfma_f32_16x16x32_bf16 v[116:119], v[192:195], a[212:215], v[116:119]\n"
    "ds_read_b64_tr_b16 a[174:175], v251 offset:8264\n"
    "v_add_f32 v244, v244, v201\n"
    "v_mfma_f32_16x16x32_bf16 v[116:119], v[204:207], a[180:183], v[116:119]\n"
    "v_exp_f32 v202, v202\n"
    "v_mfma_f32_16x16x32_bf16 v[120:123], v[204:207], a[228:231], v[120:123]\n"
    "ds_read_b64_tr_b16 a[184:185], v254 offset:0\n"
    "v_add_f32 v244, v244, v202\n"
    "v_mfma_f32_16x16x32_bf16 v[120:123], v[192:195], a[240:243], v[120:123]\n"
    "v_exp_f32 v203, v203\n"
    "v_mfma_f32_16x16x32_bf16 v[124:127], v[192:195], a[244:247], v[124:127]\n"
    "ds_read_b64_tr_b16 a[186:187], v254 offset:8256\n"
    "v_add_f32 v244, v244, v203\n"
    "v_mfma_f32_16x16x32_bf16 v[124:127], v[204:207], a[232:235], v[124:127]\n"
    "v_exp_f32 v208, v208\n"
    "v_mfma_f32_16x16x32_bf16 v[204:207], a[96:99], a[80:83], v[236:239]\n"
    "v_add_f32 v244, v244, v208\n"
    "v_cvt_pk_bf16_f32 v200, v200, v201\n"
    "v_mfma_f32_16x16x32_bf16 v[204:207], a[100:103], a[84:87], v[204:207]\n"
    "s_mov_b32 s63, 36\n"
    "LOOP_A:\n"
    "v_exp_f32 v209, v209\n"
    "v_mfma_f32_16x16x32_bf16 v[204:207], a[104:107], a[88:91], v[204:207]\n"
    "v_add_f32 v244, v244, v209\n"
    "v_cvt_pk_bf16_f32 v201, v202, v203\n"
    "v_mfma_f32_16x16x32_bf16 v[204:207], a[108:111], a[92:95], v[204:207]\n"
    "v_exp_f32 v210, v210\n"
    "v_mfma_f32_16x16x32_bf16 v[212:215], a[124:127], a[92:95], v[236:239]\n"
    "v_add_f32 v244, v244, v210\n"
    "v_cvt_pk_bf16_f32 v202, v208, v209\n"
    "v_mfma_f32_16x16x32_bf16 v[212:215], a[120:123], a[88:91], v[212:215]\n"
    "v_exp_f32 v211, v211\n"
    "v_mfma_f32_16x16x32_bf16 v[212:215], a[116:119], a[84:87], v[212:215]\n"
    "v_add_f32 v244, v244, v211\n"
    "v_cvt_pk_bf16_f32 v203, v210, v211\n"
    "v_mfma_f32_16x16x32_bf16 v[212:215], a[112:115], a[80:83], v[212:215]\n"
    "v_exp_f32 v204, v204\n"
    "v_mfma_f32_16x16x32_bf16 v[128:131], v[196:199], a[160:163], v[128:131]\n"
    "ds_read_b128 a[96:99], v246 offset:0\n"
    "v_add_f32 v245, v245, v204\n"
    "v_mfma_f32_16x16x32_bf16 v[128:131], v[200:203], a[192:195], v[128:131]\n"
    "v_exp_f32 v205, v205\n"
    "v_mfma_f32_16x16x32_bf16 v[132:135], v[200:203], a[196:199], v[132:135]\n"
    "ds_read_b128 a[100:103], v246 offset:256\n"
    "v_add_f32 v245, v245, v205\n"
    "v_mfma_f32_16x16x32_bf16 v[132:135], v[196:199], a[164:167], v[132:135]\n"
    "v_exp_f32 v206, v206\n"
    "v_mfma_f32_16x16x32_bf16 v[136:139], v[196:199], a[168:171], v[136:139]\n"
    "ds_read_b128 a[104:107], v246 offset:512\n"
    "v_add_f32 v245, v245, v206\n"
    "v_mfma_f32_16x16x32_bf16 v[136:139], v[200:203], a[200:203], v[136:139]\n"
    "v_exp_f32 v207, v207\n"
    "v_mfma_f32_16x16x32_bf16 v[140:143], v[200:203], a[236:239], v[140:143]\n"
    "ds_read_b128 a[108:111], v246 offset:768\n"
    "v_add_f32 v245, v245, v207\n"
    "v_mfma_f32_16x16x32_bf16 v[140:143], v[196:199], a[224:227], v[140:143]\n"
    "v_exp_f32 v212, v212\n"
    "v_mfma_f32_16x16x32_bf16 v[192:195], a[128:131], a[80:83], v[236:239]\n"
    "v_add_f32 v245, v245, v212\n"
    "v_cvt_pk_bf16_f32 v204, v204, v205\n"
    "v_mfma_f32_16x16x32_bf16 v[192:195], a[132:135], a[84:87], v[192:195]\n"
    "v_exp_f32 v213, v213\n"
    "v_mfma_f32_16x16x32_bf16 v[192:195], a[136:139], a[88:91], v[192:195]\n"
    "v_add_f32 v245, v245, v213\n"
    "v_cvt_pk_bf16_f32 v205, v206, v207\n"
    "v_mfma_f32_16x16x32_bf16 v[192:195], a[140:143], a[92:95], v[192:195]\n"
    "v_exp_f32 v214, v214\n"
    "v_mfma_f32_16x16x32_bf16 v[208:211], a[156:159], a[92:95], v[236:239]\n"
    "v_add_f32 v245, v245, v214\n"
    "v_cvt_pk_bf16_f32 v206, v212, v213\n"
    "v_mfma_f32_16x16x32_bf16 v[208:211], a[152:155], a[88:91], v[208:211]\n"
    "v_exp_f32 v215, v215\n"
    "v_mfma_f32_16x16x32_bf16 v[208:211], a[148:151], a[84:87], v[208:211]\n"
    "v_add_f32 v245, v245, v215\n"
    "v_cvt_pk_bf16_f32 v207, v214, v215\n"
    "v_mfma_f32_16x16x32_bf16 v[208:211], a[144:147], a[80:83], v[208:211]\n"
    "v_exp_f32 v192, v192\n"
    "v_mfma_f32_16x16x32_bf16 v[144:147], v[196:199], a[176:179], v[144:147]\n"
    "ds_read_b128 a[124:127], v246 offset:9024\n"
    "v_add_f32 v245, v245, v192\n"
    "v_mfma_f32_16x16x32_bf16 v[144:147], v[200:203], a[208:211], v[144:147]\n"
    "v_exp_f32 v193, v193\n"
    "v_mfma_f32_16x16x32_bf16 v[148:151], v[200:203], a[212:215], v[148:151]\n"
    "ds_read_b128 a[120:123], v246 offset:8768\n"
    "v_add_f32 v245, v245, v193\n"
    "v_mfma_f32_16x16x32_bf16 v[148:151], v[196:199], a[180:183], v[148:151]\n"
    "v_exp_f32 v194, v194\n"
    "v_mfma_f32_16x16x32_bf16 v[152:155], v[196:199], a[228:231], v[152:155]\n"
    "ds_read_b128 a[116:119], v246 offset:8512\n"
    "v_add_f32 v245, v245, v194\n"
    "v_mfma_f32_16x16x32_bf16 v[152:155], v[200:203], a[240:243], v[152:155]\n"
    "v_exp_f32 v195, v195\n"
    "v_mfma_f32_16x16x32_bf16 v[156:159], v[200:203], a[244:247], v[156:159]\n"
    "ds_read_b128 a[112:115], v246 offset:8256\n"
    "v_add_f32 v245, v245, v195\n"
    "v_mfma_f32_16x16x32_bf16 v[156:159], v[196:199], a[232:235], v[156:159]\n"
    "v_exp_f32 v208, v208\n"
    "v_mfma_f32_16x16x32_bf16 v[196:199], a[128:131], a[0:3], v[216:219]\n"
    "v_add_f32 v245, v245, v208\n"
    "v_cvt_pk_bf16_f32 v192, v192, v193\n"
    "v_mfma_f32_16x16x32_bf16 v[196:199], a[132:135], a[4:7], v[196:199]\n"
    "v_exp_f32 v209, v209\n"
    "v_mfma_f32_16x16x32_bf16 v[196:199], a[136:139], a[8:11], v[196:199]\n"
    "v_add_f32 v245, v245, v209\n"
    "v_cvt_pk_bf16_f32 v193, v194, v195\n"
    "v_mfma_f32_16x16x32_bf16 v[196:199], a[140:143], a[12:15], v[196:199]\n"
    "v_exp_f32 v210, v210\n"
    "v_mfma_f32_16x16x32_bf16 v[212:215], a[156:159], a[12:15], v[216:219]\n"
    "v_add_f32 v245, v245, v210\n"
    "v_cvt_pk_bf16_f32 v194, v208, v209\n"
    "v_mfma_f32_16x16x32_bf16 v[212:215], a[152:155], a[8:11], v[212:215]\n"
    "v_exp_f32 v211, v211\n"
    "v_mfma_f32_16x16x32_bf16 v[212:215], a[148:151], a[4:7], v[212:215]\n"
    "v_add_f32 v245, v245, v211\n"
    "v_cvt_pk_bf16_f32 v195, v210, v211\n"
    "v_mfma_f32_16x16x32_bf16 v[212:215], a[144:147], a[0:3], v[212:215]\n"
    "v_exp_f32 v196, v196\n"
    "v_mfma_f32_16x16x32_bf16 v[160:163], v[204:207], a[160:163], v[160:163]\n"
    "ds_read_b64_tr_b16 a[160:161], v247 offset:0\n"
    "v_add_f32 v240, v240, v196\n"
    "v_mfma_f32_16x16x32_bf16 v[160:163], v[192:195], a[192:195], v[160:163]\n"
    "v_exp_f32 v197, v197\n"
    "v_mfma_f32_16x16x32_bf16 v[164:167], v[192:195], a[196:199], v[164:167]\n"
    "ds_read_b64_tr_b16 a[162:163], v247 offset:8256\n"
    "v_add_f32 v240, v240, v197\n"
    "v_mfma_f32_16x16x32_bf16 v[164:167], v[204:207], a[164:167], v[164:167]\n"
    "v_exp_f32 v198, v198\n"
    "v_mfma_f32_16x16x32_bf16 v[168:171], v[204:207], a[168:171], v[168:171]\n"
    "ds_read_b64_tr_b16 a[164:165], v247 offset:8\n"
    "v_add_f32 v240, v240, v198\n"
    "v_mfma_f32_16x16x32_bf16 v[168:171], v[192:195], a[200:203], v[168:171]\n"
    "v_exp_f32 v199, v199\n"
    "v_mfma_f32_16x16x32_bf16 v[172:175], v[192:195], a[236:239], v[172:175]\n"
    "ds_read_b64_tr_b16 a[166:167], v247 offset:8264\n"
    "v_add_f32 v240, v240, v199\n"
    "v_mfma_f32_16x16x32_bf16 v[172:175], v[204:207], a[224:227], v[172:175]\n"
    "s_waitcnt lgkmcnt(3)\n"
    "v_exp_f32 v212, v212\n"
    "v_mfma_f32_16x16x32_bf16 v[200:203], a[96:99], a[0:3], v[216:219]\n"
    "v_add_f32 v240, v240, v212\n"
    "v_cvt_pk_bf16_f32 v196, v196, v197\n"
    "v_mfma_f32_16x16x32_bf16 v[200:203], a[100:103], a[4:7], v[200:203]\n"
    "v_exp_f32 v213, v213\n"
    "v_mfma_f32_16x16x32_bf16 v[200:203], a[104:107], a[8:11], v[200:203]\n"
    "v_add_f32 v240, v240, v213\n"
    "v_cvt_pk_bf16_f32 v197, v198, v199\n"
    "v_mfma_f32_16x16x32_bf16 v[200:203], a[108:111], a[12:15], v[200:203]\n"
    "v_exp_f32 v214, v214\n"
    "v_mfma_f32_16x16x32_bf16 v[208:211], a[124:127], a[12:15], v[216:219]\n"
    "v_add_f32 v240, v240, v214\n"
    "v_cvt_pk_bf16_f32 v198, v212, v213\n"
    "v_mfma_f32_16x16x32_bf16 v[208:211], a[120:123], a[8:11], v[208:211]\n"
    "v_exp_f32 v215, v215\n"
    "v_mfma_f32_16x16x32_bf16 v[208:211], a[116:119], a[4:7], v[208:211]\n"
    "v_add_f32 v240, v240, v215\n"
    "v_cvt_pk_bf16_f32 v199, v214, v215\n"
    "v_mfma_f32_16x16x32_bf16 v[208:211], a[112:115], a[0:3], v[208:211]\n"
    "v_exp_f32 v200, v200\n"
    "v_mfma_f32_16x16x32_bf16 v[176:179], v[204:207], a[176:179], v[176:179]\n"
    "ds_read_b64_tr_b16 a[168:169], v251 offset:0\n"
    "v_add_f32 v240, v240, v200\n"
    "v_mfma_f32_16x16x32_bf16 v[176:179], v[192:195], a[208:211], v[176:179]\n"
    "v_exp_f32 v201, v201\n"
    "v_mfma_f32_16x16x32_bf16 v[180:183], v[192:195], a[212:215], v[180:183]\n"
    "ds_read_b64_tr_b16 a[170:171], v251 offset:8256\n"
    "v_add_f32 v240, v240, v201\n"
    "v_mfma_f32_16x16x32_bf16 v[180:183], v[204:207], a[180:183], v[180:183]\n"
    "v_exp_f32 v202, v202\n"
    "v_mfma_f32_16x16x32_bf16 v[184:187], v[204:207], a[228:231], v[184:187]\n"
    "ds_read_b64_tr_b16 a[188:189], v254 offset:8\n"
    "v_add_f32 v240, v240, v202\n"
    "v_mfma_f32_16x16x32_bf16 v[184:187], v[192:195], a[240:243], v[184:187]\n"
    "v_exp_f32 v203, v203\n"
    "v_mfma_f32_16x16x32_bf16 v[188:191], v[192:195], a[244:247], v[188:191]\n"
    "ds_read_b64_tr_b16 a[190:191], v254 offset:8264\n"
    "v_add_f32 v240, v240, v203\n"
    "v_mfma_f32_16x16x32_bf16 v[188:191], v[204:207], a[232:235], v[188:191]\n"
    "v_exp_f32 v208, v208\n"
    "v_mfma_f32_16x16x32_bf16 v[204:207], a[128:131], a[16:19], v[220:223]\n"
    "v_add_f32 v240, v240, v208\n"
    "v_cvt_pk_bf16_f32 v200, v200, v201\n"
    "v_mfma_f32_16x16x32_bf16 v[204:207], a[132:135], a[20:23], v[204:207]\n"
    "v_exp_f32 v209, v209\n"
    "v_mfma_f32_16x16x32_bf16 v[204:207], a[136:139], a[24:27], v[204:207]\n"
    "v_add_f32 v240, v240, v209\n"
    "v_cvt_pk_bf16_f32 v201, v202, v203\n"
    "v_mfma_f32_16x16x32_bf16 v[204:207], a[140:143], a[28:31], v[204:207]\n"
    "v_exp_f32 v210, v210\n"
    "v_mfma_f32_16x16x32_bf16 v[212:215], a[156:159], a[28:31], v[220:223]\n"
    "v_add_f32 v240, v240, v210\n"
    "v_cvt_pk_bf16_f32 v202, v208, v209\n"
    "v_mfma_f32_16x16x32_bf16 v[212:215], a[152:155], a[24:27], v[212:215]\n"
    "v_exp_f32 v211, v211\n"
    "v_mfma_f32_16x16x32_bf16 v[212:215], a[148:151], a[20:23], v[212:215]\n"
    "v_add_f32 v240, v240, v211\n"
    "v_cvt_pk_bf16_f32 v203, v210, v211\n"
    "v_mfma_f32_16x16x32_bf16 v[212:215], a[144:147], a[16:19], v[212:215]\n"
    "v_exp_f32 v204, v204\n"
    "v_mfma_f32_16x16x32_bf16 v[0:3], v[196:199], a[192:195], v[0:3]\n"
    "s_waitcnt lgkmcnt(0)\n"
    "ds_read_b64_tr_b16 a[176:177], v252 offset:0\n"
    "v_add_f32 v241, v241, v204\n"
    "v_mfma_f32_16x16x32_bf16 v[0:3], v[200:203], a[160:163], v[0:3]\n"
    "v_exp_f32 v205, v205\n"
    "v_mfma_f32_16x16x32_bf16 v[4:7], v[200:203], a[164:167], v[4:7]\n"
    "ds_read_b64_tr_b16 a[178:179], v252 offset:8256\n"
    "v_add_f32 v241, v241, v205\n"
    "v_mfma_f32_16x16x32_bf16 v[4:7], v[196:199], a[196:199], v[4:7]\n"
    "v_exp_f32 v206, v206\n"
    "v_mfma_f32_16x16x32_bf16 v[8:11], v[196:199], a[200:203], v[8:11]\n"
    "ds_read_b64_tr_b16 a[180:181], v252 offset:8\n"
    "v_add_f32 v241, v241, v206\n"
    "v_mfma_f32_16x16x32_bf16 v[8:11], v[200:203], a[168:171], v[8:11]\n"
    "v_exp_f32 v207, v207\n"
    "v_mfma_f32_16x16x32_bf16 v[12:15], v[200:203], a[172:175], v[12:15]\n"
    "ds_read_b64_tr_b16 a[182:183], v252 offset:8264\n"
    "v_add_f32 v241, v241, v207\n"
    "v_mfma_f32_16x16x32_bf16 v[12:15], v[196:199], a[236:239], v[12:15]\n"
    "v_exp_f32 v212, v212\n"
    "v_mfma_f32_16x16x32_bf16 v[192:195], a[96:99], a[16:19], v[220:223]\n"
    "v_add_f32 v241, v241, v212\n"
    "v_cvt_pk_bf16_f32 v204, v204, v205\n"
    "v_mfma_f32_16x16x32_bf16 v[192:195], a[100:103], a[20:23], v[192:195]\n"
    "v_exp_f32 v213, v213\n"
    "v_mfma_f32_16x16x32_bf16 v[192:195], a[104:107], a[24:27], v[192:195]\n"
    "v_add_f32 v241, v241, v213\n"
    "v_cvt_pk_bf16_f32 v205, v206, v207\n"
    "v_mfma_f32_16x16x32_bf16 v[192:195], a[108:111], a[28:31], v[192:195]\n"
    "v_exp_f32 v214, v214\n"
    "v_mfma_f32_16x16x32_bf16 v[208:211], a[124:127], a[28:31], v[220:223]\n"
    "v_add_f32 v241, v241, v214\n"
    "v_cvt_pk_bf16_f32 v206, v212, v213\n"
    "v_mfma_f32_16x16x32_bf16 v[208:211], a[120:123], a[24:27], v[208:211]\n"
    "v_exp_f32 v215, v215\n"
    "v_mfma_f32_16x16x32_bf16 v[208:211], a[116:119], a[20:23], v[208:211]\n"
    "s_waitcnt vmcnt(63) lgkmcnt(0)\n"
    "v_add_f32 v241, v241, v215\n"
    "v_cvt_pk_bf16_f32 v207, v214, v215\n"
    "v_mfma_f32_16x16x32_bf16 v[208:211], a[112:115], a[16:19], v[208:211]\n"
    "s_barrier\n"
    "v_exp_f32 v192, v192\n"
    "v_mfma_f32_16x16x32_bf16 v[16:19], v[196:199], a[208:211], v[16:19]\n"
    "ds_read_b64_tr_b16 a[204:205], v251 offset:16520\n"
    "v_add_f32 v241, v241, v192\n"
    "v_mfma_f32_16x16x32_bf16 v[16:19], v[200:203], a[176:179], v[16:19]\n"
    "v_exp_f32 v193, v193\n"
    "v_mfma_f32_16x16x32_bf16 v[20:23], v[200:203], a[180:183], v[20:23]\n"
    "ds_read_b64_tr_b16 a[206:207], v251 offset:24776\n"
    "v_add_f32 v241, v241, v193\n"
    "v_mfma_f32_16x16x32_bf16 v[20:23], v[196:199], a[212:215], v[20:23]\n"
    "v_exp_f32 v194, v194\n"
    "v_mfma_f32_16x16x32_bf16 v[24:27], v[196:199], a[240:243], v[24:27]\n"
    "ds_read_b64_tr_b16 a[216:217], v254 offset:16512\n"
    "v_add_f32 v241, v241, v194\n"
    "v_mfma_f32_16x16x32_bf16 v[24:27], v[200:203], a[184:187], v[24:27]\n"
    "v_exp_f32 v195, v195\n"
    "v_mfma_f32_16x16x32_bf16 v[28:31], v[200:203], a[188:191], v[28:31]\n"
    "ds_read_b64_tr_b16 a[218:219], v254 offset:24768\n"
    "v_add_f32 v241, v241, v195\n"
    "v_mfma_f32_16x16x32_bf16 v[28:31], v[196:199], a[244:247], v[28:31]\n"
    "v_exp_f32 v208, v208\n"
    "v_mfma_f32_16x16x32_bf16 v[196:199], a[128:131], a[32:35], v[224:227]\n"
    "v_add_f32 v241, v241, v208\n"
    "v_cvt_pk_bf16_f32 v192, v192, v193\n"
    "v_mfma_f32_16x16x32_bf16 v[196:199], a[132:135], a[36:39], v[196:199]\n"
    "v_exp_f32 v209, v209\n"
    "v_mfma_f32_16x16x32_bf16 v[196:199], a[136:139], a[40:43], v[196:199]\n"
    "v_add_f32 v241, v241, v209\n"
    "v_cvt_pk_bf16_f32 v193, v194, v195\n"
    "v_mfma_f32_16x16x32_bf16 v[196:199], a[140:143], a[44:47], v[196:199]\n"
    "v_exp_f32 v210, v210\n"
    "v_mfma_f32_16x16x32_bf16 v[212:215], a[156:159], a[44:47], v[224:227]\n"
    "v_add_f32 v241, v241, v210\n"
    "v_cvt_pk_bf16_f32 v194, v208, v209\n"
    "v_mfma_f32_16x16x32_bf16 v[212:215], a[152:155], a[40:43], v[212:215]\n"
    "v_exp_f32 v211, v211\n"
    "v_mfma_f32_16x16x32_bf16 v[212:215], a[148:151], a[36:39], v[212:215]\n"
    "v_add_f32 v241, v241, v211\n"
    "v_cvt_pk_bf16_f32 v195, v210, v211\n"
    "v_mfma_f32_16x16x32_bf16 v[212:215], a[144:147], a[32:35], v[212:215]\n"
    "v_exp_f32 v196, v196\n"
    "v_mfma_f32_16x16x32_bf16 v[32:35], v[204:207], a[192:195], v[32:35]\n"
    "ds_read_b128 a[128:131], v246 offset:16512\n"
    "v_add_f32 v242, v242, v196\n"
    "v_mfma_f32_16x16x32_bf16 v[32:35], v[192:195], a[160:163], v[32:35]\n"
    "v_exp_f32 v197, v197\n"
    "v_mfma_f32_16x16x32_bf16 v[36:39], v[192:195], a[164:167], v[36:39]\n"
    "ds_read_b128 a[132:135], v246 offset:16768\n"
    "v_add_f32 v242, v242, v197\n"
    "v_mfma_f32_16x16x32_bf16 v[36:39], v[204:207], a[196:199], v[36:39]\n"
    "v_exp_f32 v198, v198\n"
    "v_mfma_f32_16x16x32_bf16 v[40:43], v[204:207], a[200:203], v[40:43]\n"
    "ds_read_b128 a[136:139], v246 offset:17024\n"
    "v_add_f32 v242, v242, v198\n"
    "v_mfma_f32_16x16x32_bf16 v[40:43], v[192:195], a[168:171], v[40:43]\n"
    "v_exp_f32 v199, v199\n"
    "v_mfma_f32_16x16x32_bf16 v[44:47], v[192:195], a[172:175], v[44:47]\n"
    "ds_read_b128 a[140:143], v246 offset:17280\n"
    "v_add_f32 v242, v242, v199\n"
    "v_mfma_f32_16x16x32_bf16 v[44:47], v[204:207], a[236:239], v[44:47]\n"
    "v_exp_f32 v212, v212\n"
    "v_mfma_f32_16x16x32_bf16 v[200:203], a[96:99], a[32:35], v[224:227]\n"
    "v_add_f32 v242, v242, v212\n"
    "v_cvt_pk_bf16_f32 v196, v196, v197\n"
    "v_mfma_f32_16x16x32_bf16 v[200:203], a[100:103], a[36:39], v[200:203]\n"
    "v_exp_f32 v213, v213\n"
    "v_mfma_f32_16x16x32_bf16 v[200:203], a[104:107], a[40:43], v[200:203]\n"
    "v_add_f32 v242, v242, v213\n"
    "v_cvt_pk_bf16_f32 v197, v198, v199\n"
    "v_mfma_f32_16x16x32_bf16 v[200:203], a[108:111], a[44:47], v[200:203]\n"
    "v_exp_f32 v214, v214\n"
    "v_mfma_f32_16x16x32_bf16 v[208:211], a[124:127], a[44:47], v[224:227]\n"
    "v_add_f32 v242, v242, v214\n"
    "v_cvt_pk_bf16_f32 v198, v212, v213\n"
    "v_mfma_f32_16x16x32_bf16 v[208:211], a[120:123], a[40:43], v[208:211]\n"
    "v_exp_f32 v215, v215\n"
    "v_mfma_f32_16x16x32_bf16 v[208:211], a[116:119], a[36:39], v[208:211]\n"
    "v_add_f32 v242, v242, v215\n"
    "v_cvt_pk_bf16_f32 v199, v214, v215\n"
    "v_mfma_f32_16x16x32_bf16 v[208:211], a[112:115], a[32:35], v[208:211]\n"
    "v_exp_f32 v200, v200\n"
    "v_mfma_f32_16x16x32_bf16 v[48:51], v[204:207], a[208:211], v[48:51]\n"
    "ds_read_b128 a[156:159], v246 offset:25536\n"
    "v_add_f32 v242, v242, v200\n"
    "v_mfma_f32_16x16x32_bf16 v[48:51], v[192:195], a[176:179], v[48:51]\n"
    "v_exp_f32 v201, v201\n"
    "v_mfma_f32_16x16x32_bf16 v[52:55], v[192:195], a[180:183], v[52:55]\n"
    "ds_read_b128 a[152:155], v246 offset:25280\n"
    "v_add_f32 v242, v242, v201\n"
    "v_mfma_f32_16x16x32_bf16 v[52:55], v[204:207], a[212:215], v[52:55]\n"
    "v_exp_f32 v202, v202\n"
    "v_mfma_f32_16x16x32_bf16 v[56:59], v[204:207], a[240:243], v[56:59]\n"
    "ds_read_b128 a[148:151], v246 offset:25024\n"
    "v_add_f32 v242, v242, v202\n"
    "v_mfma_f32_16x16x32_bf16 v[56:59], v[192:195], a[184:187], v[56:59]\n"
    "v_exp_f32 v203, v203\n"
    "v_mfma_f32_16x16x32_bf16 v[60:63], v[192:195], a[188:191], v[60:63]\n"
    "ds_read_b128 a[144:147], v246 offset:24768\n"
    "v_add_f32 v242, v242, v203\n"
    "v_mfma_f32_16x16x32_bf16 v[60:63], v[204:207], a[244:247], v[60:63]\n"
    "v_exp_f32 v208, v208\n"
    "v_mfma_f32_16x16x32_bf16 v[204:207], a[96:99], a[48:51], v[228:231]\n"
    "v_add_f32 v242, v242, v208\n"
    "v_cvt_pk_bf16_f32 v200, v200, v201\n"
    "v_mfma_f32_16x16x32_bf16 v[204:207], a[100:103], a[52:55], v[204:207]\n"
    "v_exp_f32 v209, v209\n"
    "v_mfma_f32_16x16x32_bf16 v[204:207], a[104:107], a[56:59], v[204:207]\n"
    "v_add_f32 v242, v242, v209\n"
    "v_cvt_pk_bf16_f32 v201, v202, v203\n"
    "v_mfma_f32_16x16x32_bf16 v[204:207], a[108:111], a[60:63], v[204:207]\n"
    "v_exp_f32 v210, v210\n"
    "v_mfma_f32_16x16x32_bf16 v[212:215], a[124:127], a[60:63], v[228:231]\n"
    "v_add_f32 v242, v242, v210\n"
    "v_cvt_pk_bf16_f32 v202, v208, v209\n"
    "v_mfma_f32_16x16x32_bf16 v[212:215], a[120:123], a[56:59], v[212:215]\n"
    "v_exp_f32 v211, v211\n"
    "v_mfma_f32_16x16x32_bf16 v[212:215], a[116:119], a[52:55], v[212:215]\n"
    "v_add_f32 v242, v242, v211\n"
    "v_cvt_pk_bf16_f32 v203, v210, v211\n"
    "v_mfma_f32_16x16x32_bf16 v[212:215], a[112:115], a[48:51], v[212:215]\n"
    "v_exp_f32 v204, v204\n"
    "v_mfma_f32_16x16x32_bf16 v[64:67], v[196:199], a[192:195], v[64:67]\n"
    "ds_read_b64_tr_b16 a[192:193], v247 offset:16512\n"
    "v_add_f32 v243, v243, v204\n"
    "v_mfma_f32_16x16x32_bf16 v[64:67], v[200:203], a[160:163], v[64:67]\n"
    "v_exp_f32 v205, v205\n"
    "v_mfma_f32_16x16x32_bf16 v[68:71], v[200:203], a[164:167], v[68:71]\n"
    "ds_read_b64_tr_b16 a[194:195], v247 offset:24768\n"
    "v_add_f32 v243, v243, v205\n"
    "v_mfma_f32_16x16x32_bf16 v[68:71], v[196:199], a[196:199], v[68:71]\n"
    "v_exp_f32 v206, v206\n"
    "v_mfma_f32_16x16x32_bf16 v[72:75], v[196:199], a[200:203], v[72:75]\n"
    "ds_read_b64_tr_b16 a[196:197], v247 offset:16520\n"
    "v_add_f32 v243, v243, v206\n"
    "v_mfma_f32_16x16x32_bf16 v[72:75], v[200:203], a[168:171], v[72:75]\n"
    "v_exp_f32 v207, v207\n"
    "v_mfma_f32_16x16x32_bf16 v[76:79], v[200:203], a[172:175], v[76:79]\n"
    "ds_read_b64_tr_b16 a[198:199], v247 offset:24776\n"
    "v_add_f32 v243, v243, v207\n"
    "v_mfma_f32_16x16x32_bf16 v[76:79], v[196:199], a[236:239], v[76:79]\n"
    "s_waitcnt lgkmcnt(3)\n"
    "v_exp_f32 v212, v212\n"
    "v_mfma_f32_16x16x32_bf16 v[192:195], a[128:131], a[48:51], v[228:231]\n"
    "v_add_f32 v243, v243, v212\n"
    "v_cvt_pk_bf16_f32 v204, v204, v205\n"
    "v_mfma_f32_16x16x32_bf16 v[192:195], a[132:135], a[52:55], v[192:195]\n"
    "v_exp_f32 v213, v213\n"
    "v_mfma_f32_16x16x32_bf16 v[192:195], a[136:139], a[56:59], v[192:195]\n"
    "v_add_f32 v243, v243, v213\n"
    "v_cvt_pk_bf16_f32 v205, v206, v207\n"
    "v_mfma_f32_16x16x32_bf16 v[192:195], a[140:143], a[60:63], v[192:195]\n"
    "v_exp_f32 v214, v214\n"
    "v_mfma_f32_16x16x32_bf16 v[208:211], a[156:159], a[60:63], v[228:231]\n"
    "v_add_f32 v243, v243, v214\n"
    "v_cvt_pk_bf16_f32 v206, v212, v213\n"
    "v_mfma_f32_16x16x32_bf16 v[208:211], a[152:155], a[56:59], v[208:211]\n"
    "v_exp_f32 v215, v215\n"
    "v_mfma_f32_16x16x32_bf16 v[208:211], a[148:151], a[52:55], v[208:211]\n"
    "v_add_f32 v243, v243, v215\n"
    "v_cvt_pk_bf16_f32 v207, v214, v215\n"
    "v_mfma_f32_16x16x32_bf16 v[208:211], a[144:147], a[48:51], v[208:211]\n"
    "v_exp_f32 v192, v192\n"
    "v_mfma_f32_16x16x32_bf16 v[80:83], v[196:199], a[208:211], v[80:83]\n"
    "ds_read_b64_tr_b16 a[200:201], v251 offset:16512\n"
    "v_add_f32 v243, v243, v192\n"
    "v_mfma_f32_16x16x32_bf16 v[80:83], v[200:203], a[176:179], v[80:83]\n"
    "v_exp_f32 v193, v193\n"
    "v_mfma_f32_16x16x32_bf16 v[84:87], v[200:203], a[180:183], v[84:87]\n"
    "ds_read_b64_tr_b16 a[202:203], v251 offset:24768\n"
    "v_add_f32 v243, v243, v193\n"
    "v_mfma_f32_16x16x32_bf16 v[84:87], v[196:199], a[212:215], v[84:87]\n"
    "v_exp_f32 v194, v194\n"
    "v_mfma_f32_16x16x32_bf16 v[88:91], v[196:199], a[240:243], v[88:91]\n"
    "ds_read_b64_tr_b16 a[220:221], v254 offset:16520\n"
    "v_add_f32 v243, v243, v194\n"
    "v_mfma_f32_16x16x32_bf16 v[88:91], v[200:203], a[184:187], v[88:91]\n"
    "v_exp_f32 v195, v195\n"
    "v_mfma_f32_16x16x32_bf16 v[92:95], v[200:203], a[188:191], v[92:95]\n"
    "ds_read_b64_tr_b16 a[222:223], v254 offset:24776\n"
    "v_add_f32 v243, v243, v195\n"
    "v_mfma_f32_16x16x32_bf16 v[92:95], v[196:199], a[244:247], v[92:95]\n"
    "v_exp_f32 v208, v208\n"
    "v_mfma_f32_16x16x32_bf16 v[196:199], a[96:99], a[64:67], v[232:235]\n"
    "v_add_f32 v243, v243, v208\n"
    "v_cvt_pk_bf16_f32 v192, v192, v193\n"
    "v_mfma_f32_16x16x32_bf16 v[196:199], a[100:103], a[68:71], v[196:199]\n"
    "LE2_A:\n"
    "v_exp_f32 v209, v209\n"
    "v_mfma_f32_16x16x32_bf16 v[196:199], a[104:107], a[72:75], v[196:199]\n"
    "v_add_f32 v243, v243, v209\n"
    "v_cvt_pk_bf16_f32 v193, v194, v195\n"
    "v_mfma_f32_16x16x32_bf16 v[196:199], a[108:111], a[76:79], v[196:199]\n"
    "v_exp_f32 v210, v210\n"
    "v_mfma_f32_16x16x32_bf16 v[212:215], a[124:127], a[76:79], v[232:235]\n"
    "v_add_f32 v243, v243, v210\n"
    "v_cvt_pk_bf16_f32 v194, v208, v209\n"
    "v_mfma_f32_16x16x32_bf16 v[212:215], a[120:123], a[72:75], v[212:215]\n"
    "v_exp_f32 v211, v211\n"
    "v_mfma_f32_16x16x32_bf16 v[212:215], a[116:119], a[68:71], v[212:215]\n"
    "v_add_f32 v243, v243, v211\n"
    "v_cvt_pk_bf16_f32 v195, v210, v211\n"
    "v_mfma_f32_16x16x32_bf16 v[212:215], a[112:115], a[64:67], v[212:215]\n"
    "v_exp_f32 v196, v196\n"
    "v_mfma_f32_16x16x32_bf16 v[96:99], v[204:207], a[160:163], v[96:99]\n"
    "s_waitcnt lgkmcnt(0)\n"
    "ds_read_b64_tr_b16 a[208:209], v252 offset:16512\n"
    "v_add_f32 v244, v244, v196\n"
    "v_mfma_f32_16x16x32_bf16 v[96:99], v[192:195], a[192:195], v[96:99]\n"
    "v_exp_f32 v197, v197\n"
    "v_mfma_f32_16x16x32_bf16 v[100:103], v[192:195], a[196:199], v[100:103]\n"
    "ds_read_b64_tr_b16 a[210:211], v252 offset:24768\n"
    "v_add_f32 v244, v244, v197\n"
    "v_mfma_f32_16x16x32_bf16 v[100:103], v[204:207], a[164:167], v[100:103]\n"
    "LPG3:\n"
    "s_cbranch_execz LPG4\n"
    "v_exp_f32 v198, v198\n"
    "v_mfma_f32_16x16x32_bf16 v[104:107], v[204:207], a[168:171], v[104:107]\n"
    "ds_read_b64_tr_b16 a[212:213], v252 offset:16520\n"
    "v_add_f32 v244, v244, v198\n"
    "v_mfma_f32_16x16x32_bf16 v[104:107], v[192:195], a[200:203], v[104:107]\n"
    "v_exp_f32 v199, v199\n"
    "v_mfma_f32_16x16x32_bf16 v[108:111], v[192:195], a[204:207], v[108:111]\n"
    "ds_read_b64_tr_b16 a[214:215], v252 offset:24776\n"
    "v_add_f32 v244, v244, v199\n"
    "v_mfma_f32_16x16x32_bf16 v[108:111], v[204:207], a[172:175], v[108:111]\n"
    "v_exp_f32 v212, v212\n"
    "v_mfma_f32_16x16x32_bf16 v[200:203], a[128:131], a[64:67], v[232:235]\n"
    "v_add_f32 v244, v244, v212\n"
    "v_cvt_pk_bf16_f32 v196, v196, v197\n"
    "v_mfma_f32_16x16x32_bf16 v[200:203], a[132:135], a[68:71], v[200:203]\n"
    "v_exp_f32 v213, v213\n"
    "v_mfma_f32_16x16x32_bf16 v[200:203], a[136:139], a[72:75], v[200:203]\n"
    "v_add_f32 v244, v244, v213\n"
    "v_cvt_pk_bf16_f32 v197, v198, v199\n"
    "v_mfma_f32_16x16x32_bf16 v[200:203], a[140:143], a[76:79], v[200:203]\n"
    "v_exp_f32 v214, v214\n"
    "v_mfma_f32_16x16x32_bf16 v[208:211], a[156:159], a[76:79], v[232:235]\n"
    "v_add_f32 v244, v244, v214\n"
    "v_cvt_pk_bf16_f32 v198, v212, v213\n"
    "v_mfma_f32_16x16x32_bf16 v[208:211], a[152:155], a[72:75], v[208:211]\n"
    "v_exp_f32 v215, v215\n"
    "v_mfma_f32_16x16x32_bf16 v[208:211], a[148:151], a[68:71], v[208:211]\n"
    "s_waitcnt vmcnt(63) lgkmcnt(0)\n"
    "v_add_f32 v244, v244, v215\n"
    "v_cvt_pk_bf16_f32 v199, v214, v215\n"
    "v_mfma_f32_16x16x32_bf16 v[208:211], a[144:147], a[64:67], v[208:211]\n"
    "s_barrier\n"
    "v_exp_f32 v200, v200\n"
    "v_mfma_f32_16x16x32_bf16 v[112:115], v[204:207], a[176:179], v[112:115]\n"
    "ds_read_b64_tr_b16 a[224:225], v251 offset:33032\n"
    "v_add_f32 v244, v244, v200\n"
    "v_mfma_f32_16x16x32_bf16 v[112:115], v[192:195], a[208:211], v[112:115]\n"
    "v_exp_f32 v201, v201\n"
    "v_mfma_f32_16x16x32_bf16 v[116:119], v[192:195], a[212:215], v[116:119]\n"
    "ds_read_b64_tr_b16 a[226:227], v251 offset:41288\n"
    "v_add_f32 v244, v244, v201\n"
    "v_mfma_f32_16x16x32_bf16 v[116:119], v[204:207], a[180:183], v[116:119]\n"
    "v_exp_f32 v202, v202\n"
    "v_mfma_f32_16x16x32_bf16 v[120:123], v[204:207], a[184:187], v[120:123]\n"
    "ds_read_b64_tr_b16 a[228:229], v254 offset:33024\n"
    "v_add_f32 v244, v244, v202\n"
    "v_mfma_f32_16x16x32_bf16 v[120:123], v[192:195], a[216:219], v[120:123]\n"
    "v_exp_f32 v203, v203\n"
    "v_mfma_f32_16x16x32_bf16 v[124:127], v[192:195], a[220:223], v[124:127]\n"
    "ds_read_b64_tr_b16 a[230:231], v254 offset:41280\n"
    "v_add_f32 v244, v244, v203\n"
    "v_mfma_f32_16x16x32_bf16 v[124:127], v[204:207], a[188:191], v[124:127]\n"
    "v_exp_f32 v208, v208\n"
    "v_mfma_f32_16x16x32_bf16 v[204:207], a[96:99], a[80:83], v[236:239]\n"
    "v_add_f32 v244, v244, v208\n"
    "v_cvt_pk_bf16_f32 v200, v200, v201\n"
    "v_mfma_f32_16x16x32_bf16 v[204:207], a[100:103], a[84:87], v[204:207]\n"
    "v_exp_f32 v209, v209\n"
    "v_mfma_f32_16x16x32_bf16 v[204:207], a[104:107], a[88:91], v[204:207]\n"
    "v_add_f32 v244, v244, v209\n"
    "v_cvt_pk_bf16_f32 v201, v202, v203\n"
    "v_mfma_f32_16x16x32_bf16 v[204:207], a[108:111], a[92:95], v[204:207]\n"
    "v_exp_f32 v210, v210\n"
    "v_mfma_f32_16x16x32_bf16 v[212:215], a[124:127], a[92:95], v[236:239]\n"
    "v_add_f32 v244, v244, v210\n"
    "v_cvt_pk_bf16_f32 v202, v208, v209\n"
    "v_mfma_f32_16x16x32_bf16 v[212:215], a[120:123], a[88:91], v[212:215]\n"
    "v_exp_f32 v211, v211\n"
    "v_mfma_f32_16x16x32_bf16 v[212:215], a[116:119], a[84:87], v[212:215]\n"
    "v_add_f32 v244, v244, v211\n"
    "v_cvt_pk_bf16_f32 v203, v210, v211\n"
    "v_mfma_f32_16x16x32_bf16 v[212:215], a[112:115], a[80:83], v[212:215]\n"
    "v_exp_f32 v204, v204\n"
    "v_mfma_f32_16x16x32_bf16 v[128:131], v[196:199], a[160:163], v[128:131]\n"
    "ds_read_b128 a[96:99], v246 offset:33024\n"
    "v_add_f32 v245, v245, v204\n"
    "v_mfma_f32_16x16x32_bf16 v[128:131], v[200:203], a[192:195], v[128:131]\n"
    "v_exp_f32 v205, v205\n"
    "v_mfma_f32_16x16x32_bf16 v[132:135], v[200:203], a[196:199], v[132:135]\n"
    "ds_read_b128 a[100:103], v246 offset:33280\n"
    "v_add_f32 v245, v245, v205\n"
    "v_mfma_f32_16x16x32_bf16 v[132:135], v[196:199], a[164:167], v[132:135]\n"
    "v_exp_f32 v206, v206\n"
    "v_mfma_f32_16x16x32_bf16 v[136:139], v[196:199], a[168:171], v[136:139]\n"
    "ds_read_b128 a[104:107], v246 offset:33536\n"
    "v_add_f32 v245, v245, v206\n"
    "v_mfma_f32_16x16x32_bf16 v[136:139], v[200:203], a[200:203], v[136:139]\n"
    "v_exp_f32 v207, v207\n"
    "v_mfma_f32_16x16x32_bf16 v[140:143], v[200:203], a[204:207], v[140:143]\n"
    "ds_read_b128 a[108:111], v246 offset:33792\n"
    "v_add_f32 v245, v245, v207\n"
    "v_mfma_f32_16x16x32_bf16 v[140:143], v[196:199], a[172:175], v[140:143]\n"
    "v_exp_f32 v212, v212\n"
    "v_mfma_f32_16x16x32_bf16 v[192:195], a[128:131], a[80:83], v[236:239]\n"
    "v_add_f32 v245, v245, v212\n"
    "v_cvt_pk_bf16_f32 v204, v204, v205\n"
    "v_mfma_f32_16x16x32_bf16 v[192:195], a[132:135], a[84:87], v[192:195]\n"
    "v_exp_f32 v213, v213\n"
    "v_mfma_f32_16x16x32_bf16 v[192:195], a[136:139], a[88:91], v[192:195]\n"
    "v_add_f32 v245, v245, v213\n"
    "v_cvt_pk_bf16_f32 v205, v206, v207\n"
    "v_mfma_f32_16x16x32_bf16 v[192:195], a[140:143], a[92:95], v[192:195]\n"
    "v_exp_f32 v214, v214\n"
    "v_mfma_f32_16x16x32_bf16 v[208:211], a[156:159], a[92:95], v[236:239]\n"
    "v_add_f32 v245, v245, v214\n"
    "v_cvt_pk_bf16_f32 v206, v212, v213\n"
    "v_mfma_f32_16x16x32_bf16 v[208:211], a[152:155], a[88:91], v[208:211]\n"
    "v_exp_f32 v215, v215\n"
    "v_mfma_f32_16x16x32_bf16 v[208:211], a[148:151], a[84:87], v[208:211]\n"
    "v_add_f32 v245, v245, v215\n"
    "v_cvt_pk_bf16_f32 v207, v214, v215\n"
    "v_mfma_f32_16x16x32_bf16 v[208:211], a[144:147], a[80:83], v[208:211]\n"
    "v_exp_f32 v192, v192\n"
    "v_mfma_f32_16x16x32_bf16 v[144:147], v[196:199], a[176:179], v[144:147]\n"
    "ds_read_b128 a[124:127], v246 offset:42048\n"
    "v_add_f32 v245, v245, v192\n"
    "v_mfma_f32_16x16x32_bf16 v[144:147], v[200:203], a[208:211], v[144:147]\n"
    "v_exp_f32 v193, v193\n"
    "v_mfma_f32_16x16x32_bf16 v[148:151], v[200:203], a[212:215], v[148:151]\n"
    "ds_read_b128 a[120:123], v246 offset:41792\n"
    "v_add_f32 v245, v245, v193\n"
    "v_mfma_f32_16x16x32_bf16 v[148:151], v[196:199], a[180:183], v[148:151]\n"
    "v_exp_f32 v194, v194\n"
    "v_mfma_f32_16x16x32_bf16 v[152:155], v[196:199], a[184:187], v[152:155]\n"
    "ds_read_b128 a[116:119], v246 offset:41536\n"
    "v_add_f32 v245, v245, v194\n"
    "v_mfma_f32_16x16x32_bf16 v[152:155], v[200:203], a[216:219], v[152:155]\n"
    "v_exp_f32 v195, v195\n"
    "v_mfma_f32_16x16x32_bf16 v[156:159], v[200:203], a[220:223], v[156:159]\n"
    "ds_read_b128 a[112:115], v246 offset:41280\n"
    "v_add_f32 v245, v245, v195\n"
    "v_mfma_f32_16x16x32_bf16 v[156:159], v[196:199], a[188:191], v[156:159]\n"
    "v_exp_f32 v208, v208\n"
    "v_mfma_f32_16x16x32_bf16 v[196:199], a[128:131], a[0:3], v[216:219]\n"
    "v_add_f32 v245, v245, v208\n"
    "v_cvt_pk_bf16_f32 v192, v192, v193\n"
    "v_mfma_f32_16x16x32_bf16 v[196:199], a[132:135], a[4:7], v[196:199]\n"
    "v_exp_f32 v209, v209\n"
    "v_mfma_f32_16x16x32_bf16 v[196:199], a[136:139], a[8:11], v[196:199]\n"
    "v_add_f32 v245, v245, v209\n"
    "v_cvt_pk_bf16_f32 v193, v194, v195\n"
    "v_mfma_f32_16x16x32_bf16 v[196:199], a[140:143], a[12:15], v[196:199]\n"
    "v_exp_f32 v210, v210\n"
    "v_mfma_f32_16x16x32_bf16 v[212:215], a[156:159], a[12:15], v[216:219]\n"
    "v_add_f32 v245, v245, v210\n"
    "v_cvt_pk_bf16_f32 v194, v208, v209\n"
    "v_mfma_f32_16x16x32_bf16 v[212:215], a[152:155], a[8:11], v[212:215]\n"
    "v_exp_f32 v211, v211\n"
    "v_mfma_f32_16x16x32_bf16 v[212:215], a[148:151], a[4:7], v[212:215]\n"
    "v_add_f32 v245, v245, v211\n"
    "v_cvt_pk_bf16_f32 v195, v210, v211\n"
    "v_mfma_f32_16x16x32_bf16 v[212:215], a[144:147], a[0:3], v[212:215]\n"
    "v_exp_f32 v196, v196\n"
    "v_mfma_f32_16x16x32_bf16 v[160:163], v[204:207], a[160:163], v[160:163]\n"
    "ds_read_b64_tr_b16 a[160:161], v247 offset:33024\n"
    "v_add_f32 v240, v240, v196\n"
    "v_mfma_f32_16x16x32_bf16 v[160:163], v[192:195], a[192:195], v[160:163]\n"
    "v_exp_f32 v197, v197\n"
    "v_mfma_f32_16x16x32_bf16 v[164:167], v[192:195], a[196:199], v[164:167]\n"
    "ds_read_b64_tr_b16 a[162:163], v247 offset:41280\n"
    "v_add_f32 v240, v240, v197\n"
    "v_mfma_f32_16x16x32_bf16 v[164:167], v[204:207], a[164:167], v[164:167]\n"
    "v_exp_f32 v198, v198\n"
    "v_mfma_f32_16x16x32_bf16 v[168:171], v[204:207], a[168:171], v[168:171]\n"
    "ds_read_b64_tr_b16 a[164:165], v247 offset:33032\n"
    "v_add_f32 v240, v240, v198\n"
    "v_mfma_f32_16x16x32_bf16 v[168:171], v[192:195], a[200:203], v[168:171]\n"
    "v_exp_f32 v199, v199\n"
    "v_mfma_f32_16x16x32_bf16 v[172:175], v[192:195], a[204:207], v[172:175]\n"
    "ds_read_b64_tr_b16 a[166:167], v247 offset:41288\n"
    "v_add_f32 v240, v240, v199\n"
    "v_mfma_f32_16x16x32_bf16 v[172:175], v[204:207], a[172:175], v[172:175]\n"
    "s_waitcnt lgkmcnt(3)\n"
    "v_exp_f32 v212, v212\n"
    "v_mfma_f32_16x16x32_bf16 v[200:203], a[96:99], a[0:3], v[216:219]\n"
    "v_add_f32 v240, v240, v212\n"
    "v_cvt_pk_bf16_f32 v196, v196, v197\n"
    "v_mfma_f32_16x16x32_bf16 v[200:203], a[100:103], a[4:7], v[200:203]\n"
    "v_exp_f32 v213, v213\n"
    "v_mfma_f32_16x16x32_bf16 v[200:203], a[104:107], a[8:11], v[200:203]\n"
    "v_add_f32 v240, v240, v213\n"
    "v_cvt_pk_bf16_f32 v197, v198, v199\n"
    "v_mfma_f32_16x16x32_bf16 v[200:203], a[108:111], a[12:15], v[200:203]\n"
    "v_exp_f32 v214, v214\n"
    "v_mfma_f32_16x16x32_bf16 v[208:211], a[124:127], a[12:15], v[216:219]\n"
    "v_add_f32 v240, v240, v214\n"
    "v_cvt_pk_bf16_f32 v198, v212, v213\n"
    "v_mfma_f32_16x16x32_bf16 v[208:211], a[120:123], a[8:11], v[208:211]\n"
    "v_exp_f32 v215, v215\n"
    "v_mfma_f32_16x16x32_bf16 v[208:211], a[116:119], a[4:7], v[208:211]\n"
    "v_add_f32 v240, v240, v215\n"
    "v_cvt_pk_bf16_f32 v199, v214, v215\n"
    "v_mfma_f32_16x16x32_bf16 v[208:211], a[112:115], a[0:3], v[208:211]\n"
    "v_exp_f32 v200, v200\n"
    "v_mfma_f32_16x16x32_bf16 v[176:179], v[204:207], a[176:179], v[176:179]\n"
    "ds_read_b64_tr_b16 a[168:169], v251 offset:33024\n"
    "v_add_f32 v240, v240, v200\n"
    "v_mfma_f32_16x16x32_bf16 v[176:179], v[192:195], a[208:211], v[176:179]\n"
    "v_exp_f32 v201, v201\n"
    "v_mfma_f32_16x16x32_bf16 v[180:183], v[192:195], a[212:215], v[180:183]\n"
    "ds_read_b64_tr_b16 a[170:171], v251 offset:41280\n"
    "v_add_f32 v240, v240, v201\n"
    "v_mfma_f32_16x16x32_bf16 v[180:183], v[204:207], a[180:183], v[180:183]\n"
    "v_exp_f32 v202, v202\n"
    "v_mfma_f32_16x16x32_bf16 v[184:187], v[204:207], a[184:187], v[184:187]\n"
    "ds_read_b64_tr_b16 a[232:233], v254 offset:33032\n"
    "v_add_f32 v240, v240, v202\n"
    "v_mfma_f32_16x16x32_bf16 v[184:187], v[192:195], a[216:219], v[184:187]\n"
    "v_exp_f32 v203, v203\n"
    "v_mfma_f32_16x16x32_bf16 v[188:191], v[192:195], a[220:223], v[188:191]\n"
    "ds_read_b64_tr_b16 a[234:235], v254 offset:41288\n"
    "v_add_f32 v240, v240, v203\n"
    "v_mfma_f32_16x16x32_bf16 v[188:191], v[204:207], a[188:191], v[188:191]\n"
    "v_exp_f32 v208, v208\n"
    "v_mfma_f32_16x16x32_bf16 v[204:207], a[128:131], a[16:19], v[220:223]\n"
    "v_add_f32 v240, v240, v208\n"
    "v_cvt_pk_bf16_f32 v200, v200, v201\n"
    "v_mfma_f32_16x16x32_bf16 v[204:207], a[132:135], a[20:23], v[204:207]\n"
    "v_exp_f32 v209, v209\n"
    "v_mfma_f32_16x16x32_bf16 v[204:207], a[136:139], a[24:27], v[204:207]\n"
    "v_add_f32 v240, v240, v209\n"
    "v_cvt_pk_bf16_f32 v201, v202, v203\n"
    "v_mfma_f32_16x16x32_bf16 v[204:207], a[140:143], a[28:31], v[204:207]\n"
    "v_exp_f32 v210, v210\n"
    "v_mfma_f32_16x16x32_bf16 v[212:215], a[156:159], a[28:31], v[220:223]\n"
    "v_add_f32 v240, v240, v210\n"
    "v_cvt_pk_bf16_f32 v202, v208, v209\n"
    "v_mfma_f32_16x16x32_bf16 v[212:215], a[152:155], a[24:27], v[212:215]\n"
    "v_exp_f32 v211, v211\n"
    "v_mfma_f32_16x16x32_bf16 v[212:215], a[148:151], a[20:23], v[212:215]\n"
    "v_add_f32 v240, v240, v211\n"
    "v_cvt_pk_bf16_f32 v203, v210, v211\n"
    "v_mfma_f32_16x16x32_bf16 v[212:215], a[144:147], a[16:19], v[212:215]\n"
    "v_exp_f32 v204, v204\n"
    "v_mfma_f32_16x16x32_bf16 v[0:3], v[196:199], a[192:195], v[0:3]\n"
    "s_waitcnt lgkmcnt(0)\n"
    "ds_read_b64_tr_b16 a[176:177], v252 offset:33024\n"
    "v_add_f32 v241, v241, v204\n"
    "v_mfma_f32_16x16x32_bf16 v[0:3], v[200:203], a[160:163], v[0:3]\n"
    "v_exp_f32 v205, v205\n"
    "v_mfma_f32_16x16x32_bf16 v[4:7], v[200:203], a[164:167], v[4:7]\n"
    "ds_read_b64_tr_b16 a[178:179], v252 offset:41280\n"
    "v_add_f32 v241, v241, v205\n"
    "v_mfma_f32_16x16x32_bf16 v[4:7], v[196:199], a[196:199], v[4:7]\n"
    "v_exp_f32 v206, v206\n"
    "v_mfma_f32_16x16x32_bf16 v[8:11], v[196:199], a[200:203], v[8:11]\n"
    "ds_read_b64_tr_b16 a[180:181], v252 offset:33032\n"
    "v_add_f32 v241, v241, v206\n"
    "v_mfma_f32_16x16x32_bf16 v[8:11], v[200:203], a[168:171], v[8:11]\n"
    "v_exp_f32 v207, v207\n"
    "v_mfma_f32_16x16x32_bf16 v[12:15], v[200:203], a[224:227], v[12:15]\n"
    "ds_read_b64_tr_b16 a[182:183], v252 offset:41288\n"
    "v_add_f32 v241, v241, v207\n"
    "v_mfma_f32_16x16x32_bf16 v[12:15], v[196:199], a[204:207], v[12:15]\n"
    "v_exp_f32 v212, v212\n"
    "v_mfma_f32_16x16x32_bf16 v[192:195], a[96:99], a[16:19], v[220:223]\n"
    "v_add_f32 v241, v241, v212\n"
    "v_cvt_pk_bf16_f32 v204, v204, v205\n"
    "v_mfma_f32_16x16x32_bf16 v[192:195], a[100:103], a[20:23], v[192:195]\n"
    "v_exp_f32 v213, v213\n"
    "v_mfma_f32_16x16x32_bf16 v[192:195], a[104:107], a[24:27], v[192:195]\n"
    "v_add_f32 v241, v241, v213\n"
    "v_cvt_pk_bf16_f32 v205, v206, v207\n"
    "v_mfma_f32_16x16x32_bf16 v[192:195], a[108:111], a[28:31], v[192:195]\n"
    "v_exp_f32 v214, v214\n"
    "v_mfma_f32_16x16x32_bf16 v[208:211], a[124:127], a[28:31], v[220:223]\n"
    "v_add_f32 v241, v241, v214\n"
    "v_cvt_pk_bf16_f32 v206, v212, v213\n"
    "v_mfma_f32_16x16x32_bf16 v[208:211], a[120:123], a[24:27], v[208:211]\n"
    "v_exp_f32 v215, v215\n"
    "v_mfma_f32_16x16x32_bf16 v[208:211], a[116:119], a[20:23], v[208:211]\n"
    "s_waitcnt vmcnt(63) lgkmcnt(0)\n"
    "v_add_f32 v241, v241, v215\n"
    "v_cvt_pk_bf16_f32 v207, v214, v215\n"
    "v_mfma_f32_16x16x32_bf16 v[208:211], a[112:115], a[16:19], v[208:211]\n"
    "s_barrier\n"
    "v_exp_f32 v192, v192\n"
    "v_mfma_f32_16x16x32_bf16 v[16:19], v[196:199], a[208:211], v[16:19]\n"
    "ds_read_b64_tr_b16 a[236:237], v251 offset:49544\n"
    "v_add_f32 v241, v241, v192\n"
    "v_mfma_f32_16x16x32_bf16 v[16:19], v[200:203], a[176:179], v[16:19]\n"
    "v_exp_f32 v193, v193\n"
    "v_mfma_f32_16x16x32_bf16 v[20:23], v[200:203], a[180:183], v[20:23]\n"
    "ds_read_b64_tr_b16 a[238:239], v251 offset:57800\n"
    "v_add_f32 v241, v241, v193\n"
    "v_mfma_f32_16x16x32_bf16 v[20:23], v[196:199], a[212:215], v[20:23]\n"
    "v_exp_f32 v194, v194\n"
    "v_mfma_f32_16x16x32_bf16 v[24:27], v[196:199], a[216:219], v[24:27]\n"
    "ds_read_b64_tr_b16 a[240:241], v254 offset:49536\n"
    "v_add_f32 v241, v241, v194\n"
    "v_mfma_f32_16x16x32_bf16 v[24:27], v[200:203], a[228:231], v[24:27]\n"
    "v_exp_f32 v195, v195\n"
    "v_mfma_f32_16x16x32_bf16 v[28:31], v[200:203], a[232:235], v[28:31]\n"
    "ds_read_b64_tr_b16 a[242:243], v254 offset:57792\n"
    "v_add_f32 v241, v241, v195\n"
    "v_mfma_f32_16x16x32_bf16 v[28:31], v[196:199], a[220:223], v[28:31]\n"
    "v_exp_f32 v208, v208\n"
    "v_mfma_f32_16x16x32_bf16 v[196:199], a[128:131], a[32:35], v[224:227]\n"
    "v_add_f32 v241, v241, v208\n"
    "v_cvt_pk_bf16_f32 v192, v192, v193\n"
    "v_mfma_f32_16x16x32_bf16 v[196:199], a[132:135], a[36:39], v[196:199]\n"
    "v_exp_f32 v209, v209\n"
    "v_mfma_f32_16x16x32_bf16 v[196:199], a[136:139], a[40:43], v[196:199]\n"
    "v_add_f32 v241, v241, v209\n"
    "v_cvt_pk_bf16_f32 v193, v194, v195\n"
    "v_mfma_f32_16x16x32_bf16 v[196:199], a[140:143], a[44:47], v[196:199]\n"
    "v_exp_f32 v210, v210\n"
    "v_mfma_f32_16x16x32_bf16 v[212:215], a[156:159], a[44:47], v[224:227]\n"
    "v_add_f32 v241, v241, v210\n"
    "v_cvt_pk_bf16_f32 v194, v208, v209\n"
    "v_mfma_f32_16x16x32_bf16 v[212:215], a[152:155], a[40:43], v[212:215]\n"
    "v_exp_f32 v211, v211\n"
    "v_mfma_f32_16x16x32_bf16 v[212:215], a[148:151], a[36:39], v[212:215]\n"
    "v_add_f32 v241, v241, v211\n"
    "v_cvt_pk_bf16_f32 v195, v210, v211\n"
    "v_mfma_f32_16x16x32_bf16 v[212:215], a[144:147], a[32:35], v[212:215]\n"
    "v_exp_f32 v196, v196\n"
    "v_mfma_f32_16x16x32_bf16 v[32:35], v[204:207], a[192:195], v[32:35]\n"
    "ds_read_b128 a[128:131], v246 offset:49536\n"
    "v_add_f32 v242, v242, v196\n"
    "v_mfma_f32_16x16x32_bf16 v[32:35], v[192:195], a[160:163], v[32:35]\n"
    "v_exp_f32 v197, v197\n"
    "v_mfma_f32_16x16x32_bf16 v[36:39], v[192:195], a[164:167], v[36:39]\n"
    "ds_read_b128 a[132:135], v246 offset:49792\n"
    "v_add_f32 v242, v242, v197\n"
    "v_mfma_f32_16x16x32_bf16 v[36:39], v[204:207], a[196:199], v[36:39]\n"
    "v_exp_f32 v198, v198\n"
    "v_mfma_f32_16x16x32_bf16 v[40:43], v[204:207], a[200:203], v[40:43]\n"
    "ds_read_b128 a[136:139], v246 offset:50048\n"
    "v_add_f32 v242, v242, v198\n"
    "v_mfma_f32_16x16x32_bf16 v[40:43], v[192:195], a[168:171], v[40:43]\n"
    "v_exp_f32 v199, v199\n"
    "v_mfma_f32_16x16x32_bf16 v[44:47], v[192:195], a[224:227], v[44:47]\n"
    "ds_read_b128 a[140:143], v246 offset:50304\n"
    "v_add_f32 v242, v242, v199\n"
    "v_mfma_f32_16x16x32_bf16 v[44:47], v[204:207], a[204:207], v[44:47]\n"
    "v_exp_f32 v212, v212\n"
    "v_mfma_f32_16x16x32_bf16 v[200:203], a[96:99], a[32:35], v[224:227]\n"
    "s_cmp_eq_u32 s63, 32\n"
    "v_add_f32 v242, v242, v212\n"
    "v_cvt_pk_bf16_f32 v196, v196, v197\n"
    "v_mfma_f32_16x16x32_bf16 v[200:203], a[100:103], a[36:39], v[200:203]\n"
    "s_cbranch_scc1 LMID_A\n"
    "v_exp_f32 v213, v213\n"
    "v_mfma_f32_16x16x32_bf16 v[200:203], a[104:107], a[40:43], v[200:203]\n"
    "v_add_f32 v242, v242, v213\n"
    "v_cvt_pk_bf16_f32 v197, v198, v199\n"
    "v_mfma_f32_16x16x32_bf16 v[200:203], a[108:111], a[44:47], v[200:203]\n"
    "v_exp_f32 v214, v214\n"
    "v_mfma_f32_16x16x32_bf16 v[208:211], a[124:127], a[44:47], v[224:227]\n"
    "v_add_f32 v242, v242, v214\n"
    "v_cvt_pk_bf16_f32 v198, v212, v213\n"
    "v_mfma_f32_16x16x32_bf16 v[208:211], a[120:123], a[40:43], v[208:211]\n"
    "v_exp_f32 v215, v215\n"
    "v_mfma_f32_16x16x32_bf16 v[208:211], a[116:119], a[36:39], v[208:211]\n"
    "v_add_f32 v242, v242, v215\n"
    "v_cvt_pk_bf16_f32 v199, v214, v215\n"
    "v_mfma_f32_16x16x32_bf16 v[208:211], a[112:115], a[32:35], v[208:211]\n"
    "v_exp_f32 v200, v200\n"
    "v_mfma_f32_16x16x32_bf16 v[48:51], v[204:207], a[208:211], v[48:51]\n"
    "ds_read_b128 a[156:159], v246 offset:58560\n"
    "v_add_f32 v242, v242, v200\n"
    "v_mfma_f32_16x16x32_bf16 v[48:51], v[192:195], a[176:179], v[48:51]\n"
    "v_exp_f32 v201, v201\n"
    "v_mfma_f32_16x16x32_bf16 v[52:55], v[192:195], a[180:183], v[52:55]\n"
    "ds_read_b128 a[152:155], v246 offset:58304\n"
    "v_add_f32 v242, v242, v201\n"
    "v_mfma_f32_16x16x32_bf16 v[52:55], v[204:207], a[212:215], v[52:55]\n"
    "v_exp_f32 v202, v202\n"
    "v_mfma_f32_16x16x32_bf16 v[56:59], v[204:207], a[216:219], v[56:59]\n"
    "ds_read_b128 a[148:151], v246 offset:58048\n"
    "v_add_f32 v242, v242, v202\n"
    "v_mfma_f32_16x16x32_bf16 v[56:59], v[192:195], a[228:231], v[56:59]\n"
    "v_exp_f32 v203, v203\n"
    "v_mfma_f32_16x16x32_bf16 v[60:63], v[192:195], a[232:235], v[60:63]\n"
    "ds_read_b128 a[144:147], v246 offset:57792\n"
    "v_add_f32 v242, v242, v203\n"
    "v_mfma_f32_16x16x32_bf16 v[60:63], v[204:207], a[220:223], v[60:63]\n"
    "v_exp_f32 v208, v208\n"
    "v_mfma_f32_16x16x32_bf16 v[204:207], a[96:99], a[48:51], v[228:231]\n"
    "s_cmp_eq_u32 s63, 0\n"
    "v_add_f32 v242, v242, v208\n"
    "v_cvt_pk_bf16_f32 v200, v200, v201\n"
    "v_mfma_f32_16x16x32_bf16 v[204:207], a[100:103], a[52:55], v[204:207]\n"
    "s_cbranch_scc1 LTAIL_A\n"
    "v_exp_f32 v209, v209\n"
    "v_mfma_f32_16x16x32_bf16 v[204:207], a[104:107], a[56:59], v[204:207]\n"
    "v_add_f32 v242, v242, v209\n"
    "v_cvt_pk_bf16_f32 v201, v202, v203\n"
    "v_mfma_f32_16x16x32_bf16 v[204:207], a[108:111], a[60:63], v[204:207]\n"
    "v_exp_f32 v210, v210\n"
    "v_mfma_f32_16x16x32_bf16 v[212:215], a[124:127], a[60:63], v[228:231]\n"
    "v_add_f32 v242, v242, v210\n"
    "v_cvt_pk_bf16_f32 v202, v208, v209\n"
    "v_mfma_f32_16x16x32_bf16 v[212:215], a[120:123], a[56:59], v[212:215]\n"
    "v_exp_f32 v211, v211\n"
    "v_mfma_f32_16x16x32_bf16 v[212:215], a[116:119], a[52:55], v[212:215]\n"
    "v_add_f32 v242, v242, v211\n"
    "v_cvt_pk_bf16_f32 v203, v210, v211\n"
    "v_mfma_f32_16x16x32_bf16 v[212:215], a[112:115], a[48:51], v[212:215]\n"
    "v_exp_f32 v204, v204\n"
    "v_mfma_f32_16x16x32_bf16 v[64:67], v[196:199], a[192:195], v[64:67]\n"
    "ds_read_b64_tr_b16 a[192:193], v247 offset:49536\n"
    "v_add_f32 v243, v243, v204\n"
    "v_mfma_f32_16x16x32_bf16 v[64:67], v[200:203], a[160:163], v[64:67]\n"
    "v_exp_f32 v205, v205\n"
    "v_mfma_f32_16x16x32_bf16 v[68:71], v[200:203], a[164:167], v[68:71]\n"
    "ds_read_b64_tr_b16 a[194:195], v247 offset:57792\n"
    "v_add_f32 v243, v243, v205\n"
    "v_mfma_f32_16x16x32_bf16 v[68:71], v[196:199], a[196:199], v[68:71]\n"
    "v_exp_f32 v206, v206\n"
    "v_mfma_f32_16x16x32_bf16 v[72:75], v[196:199], a[200:203], v[72:75]\n"
    "ds_read_b64_tr_b16 a[196:197], v247 offset:49544\n"
    "v_add_f32 v243, v243, v206\n"
    "v_mfma_f32_16x16x32_bf16 v[72:75], v[200:203], a[168:171], v[72:75]\n"
    "v_exp_f32 v207, v207\n"
    "v_mfma_f32_16x16x32_bf16 v[76:79], v[200:203], a[224:227], v[76:79]\n"
    "ds_read_b64_tr_b16 a[198:199], v247 offset:57800\n"
    "v_add_f32 v243, v243, v207\n"
    "v_mfma_f32_16x16x32_bf16 v[76:79], v[196:199], a[204:207], v[76:79]\n"
    "s_waitcnt lgkmcnt(3)\n"
    "v_exp_f32 v212, v212\n"
    "v_mfma_f32_16x16x32_bf16 v[192:195], a[128:131], a[48:51], v[228:231]\n"
    "v_add_f32 v243, v243, v212\n"
    "v_cvt_pk_bf16_f32 v204, v204, v205\n"
    "v_mfma_f32_16x16x32_bf16 v[192:195], a[132:135], a[52:55], v[192:195]\n"
    "v_exp_f32 v213, v213\n"
    "v_mfma_f32_16x16x32_bf16 v[192:195], a[136:139], a[56:59], v[192:195]\n"
    "v_add_f32 v243, v243, v213\n"
    "v_cvt_pk_bf16_f32 v205, v206, v207\n"
    "v_mfma_f32_16x16x32_bf16 v[192:195], a[140:143], a[60:63], v[192:195]\n"
    "v_exp_f32 v214, v214\n"
    "v_mfma_f32_16x16x32_bf16 v[208:211], a[156:159], a[60:63], v[228:231]\n"
    "v_add_f32 v243, v243, v214\n"
    "v_cvt_pk_bf16_f32 v206, v212, v213\n"
    "v_mfma_f32_16x16x32_bf16 v[208:211], a[152:155], a[56:59], v[208:211]\n"
    "v_exp_f32 v215, v215\n"
    "v_mfma_f32_16x16x32_bf16 v[208:211], a[148:151], a[52:55], v[208:211]\n"
    "v_add_f32 v243, v243, v215\n"
    "v_cvt_pk_bf16_f32 v207, v214, v215\n"
    "v_mfma_f32_16x16x32_bf16 v[208:211], a[144:147], a[48:51], v[208:211]\n"
    "v_exp_f32 v192, v192\n"
    "v_mfma_f32_16x16x32_bf16 v[80:83], v[196:199], a[208:211], v[80:83]\n"
    "ds_read_b64_tr_b16 a[200:201], v251 offset:49536\n"
    "v_add_f32 v243, v243, v192\n"
    "v_mfma_f32_16x16x32_bf16 v[80:83], v[200:203], a[176:179], v[80:83]\n"
    "v_exp_f32 v193, v193\n"
    "v_mfma_f32_16x16x32_bf16 v[84:87], v[200:203], a[180:183], v[84:87]\n"
    "ds_read_b64_tr_b16 a[202:203], v251 offset:57792\n"
    "v_add_f32 v243, v243, v193\n"
    "v_mfma_f32_16x16x32_bf16 v[84:87], v[196:199], a[212:215], v[84:87]\n"
    "v_exp_f32 v194, v194\n"
    "v_mfma_f32_16x16x32_bf16 v[88:91], v[196:199], a[216:219], v[88:91]\n"
    "ds_read_b64_tr_b16 a[244:245], v254 offset:49544\n"
    "v_add_f32 v243, v243, v194\n"
    "v_mfma_f32_16x16x32_bf16 v[88:91], v[200:203], a[228:231], v[88:91]\n"
    "v_exp_f32 v195, v195\n"
    "v_mfma_f32_16x16x32_bf16 v[92:95], v[200:203], a[232:235], v[92:95]\n"
    "ds_read_b64_tr_b16 a[246:247], v254 offset:57800\n"
    "v_add_f32 v243, v243, v195\n"
    "v_mfma_f32_16x16x32_bf16 v[92:95], v[196:199], a[220:223], v[92:95]\n"
    "v_exp_f32 v208, v208\n"
    "v_mfma_f32_16x16x32_bf16 v[196:199], a[96:99], a[64:67], v[232:235]\n"
    "v_add_f32 v243, v243, v208\n"
    "v_cvt_pk_bf16_f32 v192, v192, v193\n"
    "v_mfma_f32_16x16x32_bf16 v[196:199], a[100:103], a[68:71], v[196:199]\n"
    "v_exp_f32 v209, v209\n"
    "v_mfma_f32_16x16x32_bf16 v[196:199], a[104:107], a[72:75], v[196:199]\n"
    "v_add_f32 v243, v243, v209\n"
    "v_cvt_pk_bf16_f32 v193, v194, v195\n"
    "v_mfma_f32_16x16x32_bf16 v[196:199], a[108:111], a[76:79], v[196:199]\n"
    "v_exp_f32 v210, v210\n"
    "v_mfma_f32_16x16x32_bf16 v[212:215], a[124:127], a[76:79], v[232:235]\n"
    "v_add_f32 v243, v243, v210\n"
    "v_cvt_pk_bf16_f32 v194, v208, v209\n"
    "v_mfma_f32_16x16x32_bf16 v[212:215], a[120:123], a[72:75], v[212:215]\n"
    "v_exp_f32 v211, v211\n"
    "v_mfma_f32_16x16x32_bf16 v[212:215], a[116:119], a[68:71], v[212:215]\n"
    "v_add_f32 v243, v243, v211\n"
    "v_cvt_pk_bf16_f32 v195, v210, v211\n"
    "v_mfma_f32_16x16x32_bf16 v[212:215], a[112:115], a[64:67], v[212:215]\n"
    "v_exp_f32 v196, v196\n"
    "v_mfma_f32_16x16x32_bf16 v[96:99], v[204:207], a[160:163], v[96:99]\n"
    "s_waitcnt lgkmcnt(0)\n"
    "ds_read_b64_tr_b16 a[208:209], v252 offset:49536\n"
    "v_add_f32 v244, v244, v196\n"
    "v_mfma_f32_16x16x32_bf16 v[96:99], v[192:195], a[192:195], v[96:99]\n"
    "v_exp_f32 v197, v197\n"
    "v_mfma_f32_16x16x32_bf16 v[100:103], v[192:195], a[196:199], v[100:103]\n"
    "ds_read_b64_tr_b16 a[210:211], v252 offset:57792\n"
    "v_add_f32 v244, v244, v197\n"
    "v_mfma_f32_16x16x32_bf16 v[100:103], v[204:207], a[164:167], v[100:103]\n"
    "v_exp_f32 v198, v198\n"
    "v_mfma_f32_16x16x32_bf16 v[104:107], v[204:207], a[168:171], v[104:107]\n"
    "ds_read_b64_tr_b16 a[212:213], v252 offset:49544\n"
    "v_add_f32 v244, v244, v198\n"
    "v_mfma_f32_16x16x32_bf16 v[104:107], v[192:195], a[200:203], v[104:107]\n"
    "v_exp_f32 v199, v199\n"
    "v_mfma_f32_16x16x32_bf16 v[108:111], v[192:195], a[236:239], v[108:111]\n"
    "ds_read_b64_tr_b16 a[214:215], v252 offset:57800\n"
    "v_add_f32 v244, v244, v199\n"
    "v_mfma_f32_16x16x32_bf16 v[108:111], v[204:207], a[224:227], v[108:111]\n"
    "v_exp_f32 v212, v212\n"
    "v_mfma_f32_16x16x32_bf16 v[200:203], a[128:131], a[64:67], v[232:235]\n"
    "v_add_f32 v244, v244, v212\n"
    "v_cvt_pk_bf16_f32 v196, v196, v197\n"
    "v_mfma_f32_16x16x32_bf16 v[200:203], a[132:135], a[68:71], v[200:203]\n"
    "v_exp_f32 v213, v213\n"
    "v_mfma_f32_16x16x32_bf16 v[200:203], a[136:139], a[72:75], v[200:203]\n"
    "v_add_f32 v244, v244, v213\n"
    "v_cvt_pk_bf16_f32 v197, v198, v199\n"
    "v_mfma_f32_16x16x32_bf16 v[200:203], a[140:143], a[76:79], v[200:203]\n"
    "v_exp_f32 v214, v214\n"
    "v_mfma_f32_16x16x32_bf16 v[208:211], a[156:159], a[76:79], v[232:235]\n"
    "v_add_f32 v244, v244, v214\n"
    "v_cvt_pk_bf16_f32 v198, v212, v213\n"
    "v_mfma_f32_16x16x32_bf16 v[208:211], a[152:155], a[72:75], v[208:211]\n"
    "v_exp_f32 v215, v215\n"
    "v_mfma_f32_16x16x32_bf16 v[208:211], a[148:151], a[68:71], v[208:211]\n"
    "s_waitcnt vmcnt(63) lgkmcnt(0)\n"
    "v_add_f32 v244, v244, v215\n"
    "v_cvt_pk_bf16_f32 v199, v214, v215\n"
    "v_mfma_f32_16x16x32_bf16 v[208:211], a[144:147], a[64:67], v[208:211]\n"
    "s_barrier\n"
    "v_exp_f32 v200, v200\n"
    "v_mfma_f32_16x16x32_bf16 v[112:115], v[204:207], a[176:179], v[112:115]\n"
    "ds_read_b64_tr_b16 a[172:173], v251 offset:8\n"
    "v_add_f32 v244, v244, v200\n"
    "v_mfma_f32_16x16x32_bf16 v[112:115], v[192:195], a[208:211], v[112:115]\n"
    "v_exp_f32 v201, v201\n"
    "v_mfma_f32_16x16x32_bf16 v[116:119], v[192:195], a[212:215], v[116:119]\n"
    "ds_read_b64_tr_b16 a[174:175], v251 offset:8264\n"
    "v_add_f32 v244, v244, v201\n"
    "v_mfma_f32_16x16x32_bf16 v[116:119], v[204:207], a[180:183], v[116:119]\n"
    "v_exp_f32 v202, v202\n"
    "v_mfma_f32_16x16x32_bf16 v[120:123], v[204:207], a[228:231], v[120:123]\n"
    "ds_read_b64_tr_b16 a[184:185], v254 offset:0\n"
    "v_add_f32 v244, v244, v202\n"
    "v_mfma_f32_16x16x32_bf16 v[120:123], v[192:195], a[240:243], v[120:123]\n"
    "v_exp_f32 v203, v203\n"
    "v_mfma_f32_16x16x32_bf16 v[124:127], v[192:195], a[244:247], v[124:127]\n"
    "ds_read_b64_tr_b16 a[186:187], v254 offset:8256\n"
    "v_add_f32 v244, v244, v203\n"
    "v_mfma_f32_16x16x32_bf16 v[124:127], v[204:207], a[232:235], v[124:127]\n"
    "v_exp_f32 v208, v208\n"
    "v_mfma_f32_16x16x32_bf16 v[204:207], a[96:99], a[80:83], v[236:239]\n"
    "s_sub_u32 s63, s63, 1\n"
    "v_add_f32 v244, v244, v208\n"
    "v_cvt_pk_bf16_f32 v200, v200, v201\n"
    "v_mfma_f32_16x16x32_bf16 v[204:207], a[100:103], a[84:87], v[204:207]\n"
    "s_branch LOOP_A\n"
    "LMID_A:\n"
    "s_mov_b32 s63, 4\n"
    "v_exp_f32 v213, v213\n"
    "v_mfma_f32_16x16x32_bf16 v[200:203], a[104:107], a[40:43], v[200:203]\n"
    "v_add_f32 v242, v242, v213\n"
    "v_cvt_pk_bf16_f32 v197, v198, v199\n"
    "v_mfma_f32_16x16x32_bf16 v[200:203], a[108:111], a[44:47], v[200:203]\n"
    "v_exp_f32 v214, v214\n"
    "v_mfma_f32_16x16x32_bf16 v[208:211], a[124:127], a[44:47], v[224:227]\n"
    "v_add_f32 v242, v242, v214\n"
    "v_cvt_pk_bf16_f32 v198, v212, v213\n"
    "v_mfma_f32_16x16x32_bf16 v[208:211], a[120:123], a[40:43], v[208:211]\n"
    "v_exp_f32 v215, v215\n"
    "v_mfma_f32_16x16x32_bf16 v[208:211], a[116:119], a[36:39], v[208:211]\n"
    "v_add_f32 v242, v242, v215\n"
    "v_cvt_pk_bf16_f32 v199, v214, v215\n"
    "v_mfma_f32_16x16x32_bf16 v[208:211], a[112:115], a[32:35], v[208:211]\n"
    "v_exp_f32 v200, v200\n"
    "v_mfma_f32_16x16x32_bf16 v[48:51], v[204:207], a[208:211], v[48:51]\n"
    "ds_read_b64_tr_b16 a[244:245], v254 offset:49544\n"
    "v_add_f32 v242, v242, v200\n"
    "v_mfma_f32_16x16x32_bf16 v[48:51], v[192:195], a[176:179], v[48:51]\n"
    "v_exp_f32 v201, v201\n"
    "v_mfma_f32_16x16x32_bf16 v[52:55], v[192:195], a[180:183], v[52:55]\n"
    "ds_read_b64_tr_b16 a[246:247], v254 offset:57800\n"
    "v_add_f32 v242, v242, v201\n"
    "v_mfma_f32_16x16x32_bf16 v[52:55], v[204:207], a[212:215], v[52:55]\n"
    "v_exp_f32 v202, v202\n"
    "v_mfma_f32_16x16x32_bf16 v[56:59], v[204:207], a[216:219], v[56:59]\n"
    "ds_read_b128 a[156:159], v246 offset:58560\n"
    "v_add_f32 v242, v242, v202\n"
    "v_mfma_f32_16x16x32_bf16 v[56:59], v[192:195], a[228:231], v[56:59]\n"
    "v_exp_f32 v203, v203\n"
    "v_mfma_f32_16x16x32_bf16 v[60:63], v[192:195], a[232:235], v[60:63]\n"
    "ds_read_b128 a[152:155], v246 offset:58304\n"
    "v_add_f32 v242, v242, v203\n"
    "v_mfma_f32_16x16x32_bf16 v[60:63], v[204:207], a[220:223], v[60:63]\n"
    "v_exp_f32 v208, v208\n"
    "v_mfma_f32_16x16x32_bf16 v[204:207], a[96:99], a[48:51], v[228:231]\n"
    "v_add_f32 v242, v242, v208\n"
    "v_cvt_pk_bf16_f32 v200, v200, v201\n"
    "v_mfma_f32_16x16x32_bf16 v[204:207], a[100:103], a[52:55], v[204:207]\n"
    "v_exp_f32 v209, v209\n"
    "v_mfma_f32_16x16x32_bf16 v[204:207], a[104:107], a[56:59], v[204:207]\n"
    "v_add_f32 v242, v242, v209\n"
    "v_cvt_pk_bf16_f32 v201, v202, v203\n"
    "v_mfma_f32_16x16x32_bf16 v[204:207], a[108:111], a[60:63], v[204:207]\n"
    "v_exp_f32 v210, v210\n"
    "v_mfma_f32_16x16x32_bf16 v[212:215], a[124:127], a[60:63], v[228:231]\n"
    "v_add_f32 v242, v242, v210\n"
    "v_cvt_pk_bf16_f32 v202, v208, v209\n"
    "v_mfma_f32_16x16x32_bf16 v[212:215], a[120:123], a[56:59], v[212:215]\n"
    "v_exp_f32 v211, v211\n"
    "v_mfma_f32_16x16x32_bf16 v[212:215], a[116:119], a[52:55], v[212:215]\n"
    "v_add_f32 v242, v242, v211\n"
    "v_cvt_pk_bf16_f32 v203, v210, v211\n"
    "v_mfma_f32_16x16x32_bf16 v[212:215], a[112:115], a[48:51], v[212:215]\n"
    "v_exp_f32 v204, v204\n"
    "v_mfma_f32_16x16x32_bf16 v[64:67], v[196:199], a[192:195], v[64:67]\n"
    "ds_read_b128 a[148:151], v246 offset:58048\n"
    "v_add_f32 v243, v243, v204\n"
    "v_mfma_f32_16x16x32_bf16 v[64:67], v[200:203], a[160:163], v[64:67]\n"
    "v_exp_f32 v205, v205\n"
    "v_mfma_f32_16x16x32_bf16 v[68:71], v[200:203], a[164:167], v[68:71]\n"
    "ds_read_b128 a[144:147], v246 offset:57792\n"
    "v_add_f32 v243, v243, v205\n"
    "v_mfma_f32_16x16x32_bf16 v[68:71], v[196:199], a[196:199], v[68:71]\n"
    "v_exp_f32 v206, v206\n"
    "v_mfma_f32_16x16x32_bf16 v[72:75], v[196:199], a[200:203], v[72:75]\n"
    "ds_read_b64_tr_b16 a[192:193], v247 offset:49536\n"
    "v_add_f32 v243, v243, v206\n"
    "v_mfma_f32_16x16x32_bf16 v[72:75], v[200:203], a[168:171], v[72:75]\n"
    "v_exp_f32 v207, v207\n"
    "v_mfma_f32_16x16x32_bf16 v[76:79], v[200:203], a[224:227], v[76:79]\n"
    "ds_read_b64_tr_b16 a[194:195], v247 offset:57792\n"
    "v_add_f32 v243, v243, v207\n"
    "v_mfma_f32_16x16x32_bf16 v[76:79], v[196:199], a[204:207], v[76:79]\n"
    "v_exp_f32 v212, v212\n"
    "v_mfma_f32_16x16x32_bf16 v[192:195], a[96:99], a[64:67], v[232:235]\n"
    "v_add_f32 v243, v243, v212\n"
    "v_cvt_pk_bf16_f32 v204, v204, v205\n"
    "v_mfma_f32_16x16x32_bf16 v[192:195], a[100:103], a[68:71], v[192:195]\n"
    "v_exp_f32 v213, v213\n"
    "v_mfma_f32_16x16x32_bf16 v[192:195], a[104:107], a[72:75], v[192:195]\n"
    "v_add_f32 v243, v243, v213\n"
    "v_cvt_pk_bf16_f32 v205, v206, v207\n"
    "v_mfma_f32_16x16x32_bf16 v[192:195], a[108:111], a[76:79], v[192:195]\n"
    "v_exp_f32 v214, v214\n"
    "v_mfma_f32_16x16x32_bf16 v[208:211], a[124:127], a[76:79], v[232:235]\n"
    "v_add_f32 v243, v243, v214\n"
    "v_cvt_pk_bf16_f32 v206, v212, v213\n"
    "v_mfma_f32_16x16x32_bf16 v[208:211], a[120:123], a[72:75], v[208:211]\n"
    "v_exp_f32 v215, v215\n"
    "v_mfma_f32_16x16x32_bf16 v[208:211], a[116:119], a[68:71], v[208:211]\n"
    "v_add_f32 v243, v243, v215\n"
    "v_cvt_pk_bf16_f32 v207, v214, v215\n"
    "v_mfma_f32_16x16x32_bf16 v[208:211], a[112:115], a[64:67], v[208:211]\n"
    "LPG4:\n"
    "s_cbranch_execz LPG5\n"
    "v_exp_f32 v192, v192\n"
    "v_mfma_f32_16x16x32_bf16 v[80:83], v[196:199], a[208:211], v[80:83]\n"
    "ds_read_b64_tr_b16 a[196:197], v247 offset:49544\n"
    "v_add_f32 v244, v244, v192\n"
    "v_mfma_f32_16x16x32_bf16 v[80:83], v[200:203], a[176:179], v[80:83]\n"
    "v_exp_f32 v193, v193\n"
    "v_mfma_f32_16x16x32_bf16 v[84:87], v[200:203], a[180:183], v[84:87]\n"
    "ds_read_b64_tr_b16 a[198:199], v247 offset:57800\n"
    "v_add_f32 v244, v244, v193\n"
    "v_mfma_f32_16x16x32_bf16 v[84:87], v[196:199], a[212:215], v[84:87]\n"
    "v_exp_f32 v194, v194\n"
    "v_mfma_f32_16x16x32_bf16 v[88:91], v[196:199], a[216:219], v[88:91]\n"
    "ds_read_b64_tr_b16 a[200:201], v251 offset:49536\n"
    "v_add_f32 v244, v244, v194\n"
    "v_mfma_f32_16x16x32_bf16 v[88:91], v[200:203], a[228:231], v[88:91]\n"
    "v_exp_f32 v195, v195\n"
    "v_mfma_f32_16x16x32_bf16 v[92:95], v[200:203], a[232:235], v[92:95]\n"
    "ds_read_b64_tr_b16 a[202:203], v251 offset:57792\n"
    "v_add_f32 v244, v244, v195\n"
    "v_mfma_f32_16x16x32_bf16 v[92:95], v[196:199], a[220:223], v[92:95]\n"
    "v_exp_f32 v208, v208\n"
    "v_and_b32 v248, s76, v243\n"
    "v_sub_f32 v243, v243, v248\n"
    "v_cvt_pk_bf16_f32 v248, v248, v243\n"
    "ds_read_b128 a[48:51], v250 offset:12384\n"
    "ds_read_b128 a[52:55], v250 offset:12640\n"
    "ds_read_b128 a[56:59], v250 offset:12896\n"
    "ds_read_b128 a[60:63], v250 offset:13152\n"
    "v_mfma_f32_16x16x16_bf16 v[228:231], v[248:249], a[252:253], 0\n"
    "v_mfma_f32_16x16x32_bf16 v[196:199], a[96:99], a[80:83], v[236:239]\n"
    "v_add_f32 v244, v244, v208\n"
    "v_cvt_pk_bf16_f32 v192, v192, v193\n"
    "v_mfma_f32_16x16x32_bf16 v[196:199], a[100:103], a[84:87], v[196:199]\n"
    "v_exp_f32 v209, v209\n"
    "v_mfma_f32_16x16x32_bf16 v[196:199], a[104:107], a[88:91], v[196:199]\n"
    "v_add_f32 v244, v244, v209\n"
    "v_cvt_pk_bf16_f32 v193, v194, v195\n"
    "v_mfma_f32_16x16x32_bf16 v[196:199], a[108:111], a[92:95], v[196:199]\n"
    "v_exp_f32 v210, v210\n"
    "v_mfma_f32_16x16x32_bf16 v[212:215], a[124:127], a[92:95], v[236:239]\n"
    "v_add_f32 v244, v244, v210\n"
    "v_cvt_pk_bf16_f32 v194, v208, v209\n"
    "v_mfma_f32_16x16x32_bf16 v[212:215], a[120:123], a[88:91], v[212:215]\n"
    "v_exp_f32 v211, v211\n"
    "v_mfma_f32_16x16x32_bf16 v[212:215], a[116:119], a[84:87], v[212:215]\n"
    "v_add_f32 v244, v244, v211\n"
    "v_cvt_pk_bf16_f32 v195, v210, v211\n"
    "v_mfma_f32_16x16x32_bf16 v[212:215], a[112:115], a[80:83], v[212:215]\n"
    "v_exp_f32 v196, v196\n"
    "v_mfma_f32_16x16x32_bf16 v[96:99], v[204:207], a[160:163], v[96:99]\n"
    "ds_read_b64_tr_b16 a[208:209], v252 offset:49536\n"
    "v_add_f32 v245, v245, v196\n"
    "v_mfma_f32_16x16x32_bf16 v[100:103], v[204:207], a[164:167], v[100:103]\n"
    "v_exp_f32 v197, v197\n"
    "v_mfma_f32_16x16x32_bf16 v[104:107], v[204:207], a[168:171], v[104:107]\n"
    "ds_read_b64_tr_b16 a[210:211], v252 offset:57792\n"
    "v_add_f32 v245, v245, v197\n"
    "v_mfma_f32_16x16x32_bf16 v[108:111], v[204:207], a[224:227], v[108:111]\n"
    "v_exp_f32 v198, v198\n"
    "v_mfma_f32_16x16x32_bf16 v[112:115], v[204:207], a[176:179], v[112:115]\n"
    "ds_read_b64_tr_b16 a[212:213], v252 offset:49544\n"
    "v_add_f32 v245, v245, v198\n"
    "v_mfma_f32_16x16x32_bf16 v[116:119], v[204:207], a[180:183], v[116:119]\n"
    "v_exp_f32 v199, v199\n"
    "v_mfma_f32_16x16x32_bf16 v[120:123], v[204:207], a[228:231], v[120:123]\n"
    "ds_read_b64_tr_b16 a[214:215], v252 offset:57800\n"
    "v_add_f32 v245, v245, v199\n"
    "v_mfma_f32_16x16x32_bf16 v[124:127], v[204:207], a[232:235], v[124:127]\n"
    "v_exp_f32 v212, v212\n"
    "v_maximum3_f32 v253, v228, v229, v228\n"
    "v_maximum3_f32 v253, v230, v231, v253\n"
    "v_rcp_f32 v228, v228\n"
    "v_rcp_f32 v229, v229\n"
    "v_rcp_f32 v230, v230\n"
    "v_rcp_f32 v231, v231\n"
    "v_and_b32 v248, s76, v240\n"
    "v_sub_f32 v240, v240, v248\n"
    "v_cvt_pk_bf16_f32 v248, v248, v240\n"
    "v_maximum3_f32 v253, v228, v229, v253\n"
    "v_maximum3_f32 v253, v230, v231, v253\n"
    "v_mbcnt_lo_u32_b32 v240, -1, 0\n"
    "v_mbcnt_hi_u32_b32 v240, -1, v240\n"
    "v_lshl_add_u32 v240, v240, 4, s68\n"
    "v_pk_mul_f32 v[96:97], v[96:97], v[228:229]\n"
    "v_pk_mul_f32 v[98:99], v[98:99], v[230:231]\n"
    "v_pk_mul_f32 v[104:105], v[104:105], v[228:229]\n"
    "v_pk_mul_f32 v[106:107], v[106:107], v[230:231]\n"
    "v_cvt_pk_bf16_f32 v96, v96, v104\n"
    "v_cvt_pk_bf16_f32 v98, v98, v106\n"
    "v_cvt_pk_bf16_f32 v104, v97, v105\n"
    "v_cvt_pk_bf16_f32 v106, v99, v107\n"
    "v_pk_mul_f32 v[112:113], v[112:113], v[228:229]\n"
    "v_pk_mul_f32 v[114:115], v[114:115], v[230:231]\n"
    "v_pk_mul_f32 v[120:121], v[120:121], v[228:229]\n"
    "v_pk_mul_f32 v[122:123], v[122:123], v[230:231]\n"
    "v_cvt_pk_bf16_f32 v97, v112, v120\n"
    "v_cvt_pk_bf16_f32 v99, v114, v122\n"
    "v_cvt_pk_bf16_f32 v105, v113, v121\n"
    "v_cvt_pk_bf16_f32 v107, v115, v123\n"
    "v_pk_mul_f32 v[100:101], v[100:101], v[228:229]\n"
    "v_pk_mul_f32 v[102:103], v[102:103], v[230:231]\n"
    "v_pk_mul_f32 v[108:109], v[108:109], v[228:229]\n"
    "v_pk_mul_f32 v[110:111], v[110:111], v[230:231]\n"
    "v_cvt_pk_bf16_f32 v100, v100, v108\n"
    "v_cvt_pk_bf16_f32 v102, v102, v110\n"
    "v_cvt_pk_bf16_f32 v108, v101, v109\n"
    "v_cvt_pk_bf16_f32 v110, v103, v111\n"
    "v_pk_mul_f32 v[116:117], v[116:117], v[228:229]\n"
    "v_pk_mul_f32 v[118:119], v[118:119], v[230:231]\n"
    "v_pk_mul_f32 v[124:125], v[124:125], v[228:229]\n"
    "v_pk_mul_f32 v[126:127], v[126:127], v[230:231]\n"
    "v_mfma_f32_16x16x16_bf16 v[216:219], v[248:249], a[252:253], 0\n"
    "v_cvt_pk_bf16_f32 v101, v116, v124\n"
    "v_cvt_pk_bf16_f32 v103, v118, v126\n"
    "v_cvt_pk_bf16_f32 v109, v117, v125\n"
    "s_waitcnt lgkmcnt(0)\n"
    "v_mfma_f32_4x4x4_16b_bf16 v[112:115], a[248:249], a[48:49], 0\n"
    "ds_write_b128 v240, v[96:99] offset:12384\n"
    "v_cvt_pk_bf16_f32 v111, v119, v127\n"
    "v_mfma_f32_4x4x4_16b_bf16 v[112:115], a[250:251], a[48:49], v[112:115]\n"
    "v_mfma_f32_4x4x4_16b_bf16 v[120:123], a[248:249], a[50:51], 0\n"
    "s_nop 1\n"
    "v_mfma_f32_4x4x4_16b_bf16 v[120:123], a[250:251], a[50:51], v[120:123]\n"
    "v_nop_e64\n"
    "v_cvt_pk_bf16_f32 v112, v112, v113\n"
    "v_mfma_f32_4x4x4_16b_bf16 v[116:119], a[248:249], a[52:53], 0\n"
    "ds_write_b128 v240, v[104:107] offset:14432\n"
    "v_cvt_pk_bf16_f32 v113, v114, v115\n"
    "v_accvgpr_write_b32 a48, v112\n"
    "v_mfma_f32_4x4x4_16b_bf16 v[116:119], a[250:251], a[52:53], v[116:119]\n"
    "v_cvt_pk_bf16_f32 v120, v120, v121\n"
    "v_accvgpr_write_b32 a49, v113\n"
    "v_mfma_f32_4x4x4_16b_bf16 v[112:115], a[248:249], a[54:55], 0\n"
    "v_cvt_pk_bf16_f32 v121, v122, v123\n"
    "v_accvgpr_write_b32 a50, v120\n"
    "v_mfma_f32_4x4x4_16b_bf16 v[112:115], a[250:251], a[54:55], v[112:115]\n"
    "v_cvt_pk_bf16_f32 v116, v116, v117\n"
    "v_accvgpr_write_b32 a51, v121\n"
    "v_mfma_f32_4x4x4_16b_bf16 v[120:123], a[248:249], a[56:57], 0\n"
    "ds_write_b128 v240, v[100:103] offset:13408\n"
    "v_cvt_pk_bf16_f32 v117, v118, v119\n"
    "v_accvgpr_write_b32 a52, v116\n"
    "v_mfma_f32_4x4x4_16b_bf16 v[120:123], a[250:251], a[56:57], v[120:123]\n"
    "v_cvt_pk_bf16_f32 v112, v112, v113\n"
    "v_accvgpr_write_b32 a53, v117\n"
    "v_mfma_f32_4x4x4_16b_bf16 v[116:119], a[248:249], a[58:59], 0\n"
    "v_cvt_pk_bf16_f32 v113, v114, v115\n"
    "v_accvgpr_write_b32 a54, v112\n"
    "v_mfma_f32_4x4x4_16b_bf16 v[116:119], a[250:251], a[58:59], v[116:119]\n"
    "v_cvt_pk_bf16_f32 v120, v120, v121\n"
    "v_accvgpr_write_b32 a55, v113\n"
    "v_mfma_f32_4x4x4_16b_bf16 v[112:115], a[248:249], a[60:61], 0\n"
    "ds_write_b128 v240, v[108:111] offset:15456\n"
    "v_cvt_pk_bf16_f32 v121, v122, v123\n"
    "v_accvgpr_write_b32 a56, v120\n"
    "v_mfma_f32_4x4x4_16b_bf16 v[112:115], a[250:251], a[60:61], v[112:115]\n"
    "v_cvt_pk_bf16_f32 v116, v116, v117\n"
    "v_accvgpr_write_b32 a57, v121\n"
    "v_mfma_f32_4x4x4_16b_bf16 v[120:123], a[248:249], a[62:63], 0\n"
    "v_cvt_pk_bf16_f32 v117, v118, v119\n"
    "v_accvgpr_write_b32 a58, v116\n"
    "v_mfma_f32_4x4x4_16b_bf16 v[120:123], a[250:251], a[62:63], v[120:123]\n"
    "v_cvt_pk_bf16_f32 v112, v112, v113\n"
    "v_accvgpr_write_b32 a59, v117\n"
    "v_mfma_f32_16x16x32_bf16 v[124:127], a[128:131], a[48:51], 0\n"
    "ds_read_b128 a[0:3], v250 offset:0\n"
    "ds_read_b128 a[4:7], v250 offset:256\n"
    "v_cvt_pk_bf16_f32 v113, v114, v115\n"
    "v_accvgpr_write_b32 a60, v112\n"
    "v_mfma_f32_16x16x32_bf16 v[124:127], a[132:135], a[52:55], v[124:127]\n"
    "ds_read_b128 a[8:11], v250 offset:512\n"
    "ds_read_b128 a[12:15], v250 offset:768\n"
    "v_cvt_pk_bf16_f32 v120, v120, v121\n"
    "v_accvgpr_write_b32 a61, v113\n"
    "v_mfma_f32_16x16x32_bf16 v[124:127], a[136:139], a[56:59], v[124:127]\n"
    "v_cvt_pk_bf16_f32 v121, v122, v123\n"
    "v_accvgpr_write_b32 a62, v120\n"
    "v_accvgpr_write_b32 a63, v121\n"
    "v_maximum3_f32 v253, v216, v217, v253\n"
    "v_maximum3_f32 v253, v218, v219, v253\n"
    "v_mfma_f32_16x16x32_bf16 v[124:127], a[140:143], a[60:63], v[124:127]\n"
    "v_rcp_f32 v216, v216\n"
    "v_rcp_f32 v217, v217\n"
    "v_rcp_f32 v218, v218\n"
    "v_rcp_f32 v219, v219\n"
    "v_and_b32 v248, s76, v244\n"
    "v_sub_f32 v244, v244, v248\n"
    "v_cvt_pk_bf16_f32 v248, v248, v244\n"
    "v_maximum3_f32 v253, v216, v217, v253\n"
    "v_maximum3_f32 v253, v218, v219, v253\n"
    "v_pk_mul_f32 v[0:1], v[0:1], v[216:217]\n"
    "v_pk_mul_f32 v[2:3], v[2:3], v[218:219]\n"
    "v_pk_mul_f32 v[8:9], v[8:9], v[216:217]\n"
    "v_pk_mul_f32 v[10:11], v[10:11], v[218:219]\n"
    "v_cvt_pk_bf16_f32 v0, v0, v8\n"
    "v_cvt_pk_bf16_f32 v2, v2, v10\n"
    "v_cvt_pk_bf16_f32 v8, v1, v9\n"
    "v_cvt_pk_bf16_f32 v10, v3, v11\n"
    "v_pk_mul_f32 v[16:17], v[16:17], v[216:217]\n"
    "v_pk_mul_f32 v[18:19], v[18:19], v[218:219]\n"
    "v_pk_mul_f32 v[24:25], v[24:25], v[216:217]\n"
    "v_pk_mul_f32 v[26:27], v[26:27], v[218:219]\n"
    "v_cvt_pk_bf16_f32 v1, v16, v24\n"
    "v_cvt_pk_bf16_f32 v3, v18, v26\n"
    "v_cvt_pk_bf16_f32 v9, v17, v25\n"
    "v_cvt_pk_bf16_f32 v11, v19, v27\n"
    "v_pk_mul_f32 v[4:5], v[4:5], v[216:217]\n"
    "v_pk_mul_f32 v[6:7], v[6:7], v[218:219]\n"
    "v_pk_mul_f32 v[12:13], v[12:13], v[216:217]\n"
    "v_pk_mul_f32 v[14:15], v[14:15], v[218:219]\n"
    "v_cvt_pk_bf16_f32 v4, v4, v12\n"
    "v_cvt_pk_bf16_f32 v6, v6, v14\n"
    "v_cvt_pk_bf16_f32 v12, v5, v13\n"
    "v_cvt_pk_bf16_f32 v14, v7, v15\n"
    "v_pk_mul_f32 v[20:21], v[20:21], v[216:217]\n"
    "v_pk_mul_f32 v[22:23], v[22:23], v[218:219]\n"
    "v_pk_mul_f32 v[28:29], v[28:29], v[216:217]\n"
    "v_pk_mul_f32 v[30:31], v[30:31], v[218:219]\n"
    "v_mfma_f32_16x16x16_bf16 v[232:235], v[248:249], a[252:253], 0\n"
    "v_cvt_pk_bf16_f32 v5, v20, v28\n"
    "v_cvt_pk_bf16_f32 v7, v22, v30\n"
    "v_cvt_pk_bf16_f32 v13, v21, v29\n"
    "s_waitcnt lgkmcnt(0)\n"
    "v_mfma_f32_4x4x4_16b_bf16 v[16:19], a[248:249], a[0:1], 0\n"
    "ds_write_b128 v240, v[0:3] offset:0\n"
    "v_cvt_pk_bf16_f32 v15, v23, v31\n"
    "v_max3_f32 v248, v124, v125, v126\n"
    "v_mfma_f32_4x4x4_16b_bf16 v[16:19], a[250:251], a[0:1], v[16:19]\n"
    "v_max_f32 v248, v248, v127\n"
    "v_mul_f32 v248, 0xbe800000, v248\n"
    "v_mfma_f32_4x4x4_16b_bf16 v[24:27], a[248:249], a[2:3], 0\n"
    "v_cvt_pk_bf16_f32 v248, v248, s80\n"
    "v_nop_e64\n"
    "v_mfma_f32_4x4x4_16b_bf16 v[24:27], a[250:251], a[2:3], v[24:27]\n"
    "v_mfma_f32_16x16x16_bf16 v[228:231], a[252:253], v[248:249], 0\n"
    "v_cvt_pk_bf16_f32 v16, v16, v17\n"
    "v_mfma_f32_4x4x4_16b_bf16 v[20:23], a[248:249], a[4:5], 0\n"
    "ds_write_b128 v240, v[8:11] offset:2048\n"
    "v_cvt_pk_bf16_f32 v17, v18, v19\n"
    "v_accvgpr_write_b32 a0, v16\n"
    "v_mfma_f32_4x4x4_16b_bf16 v[20:23], a[250:251], a[4:5], v[20:23]\n"
    "v_cvt_pk_bf16_f32 v24, v24, v25\n"
    "v_accvgpr_write_b32 a1, v17\n"
    "v_mfma_f32_4x4x4_16b_bf16 v[16:19], a[248:249], a[6:7], 0\n"
    "v_cvt_pk_bf16_f32 v25, v26, v27\n"
    "v_accvgpr_write_b32 a2, v24\n"
    "v_mfma_f32_4x4x4_16b_bf16 v[16:19], a[250:251], a[6:7], v[16:19]\n"
    "v_cvt_pk_bf16_f32 v20, v20, v21\n"
    "v_accvgpr_write_b32 a3, v25\n"
    "v_mfma_f32_4x4x4_16b_bf16 v[24:27], a[248:249], a[8:9], 0\n"
    "ds_write_b128 v240, v[4:7] offset:1024\n"
    "v_cvt_pk_bf16_f32 v21, v22, v23\n"
    "v_accvgpr_write_b32 a4, v20\n"
    "v_mfma_f32_4x4x4_16b_bf16 v[24:27], a[250:251], a[8:9], v[24:27]\n"
    "v_cvt_pk_bf16_f32 v16, v16, v17\n"
    "v_accvgpr_write_b32 a5, v21\n"
    "v_mfma_f32_4x4x4_16b_bf16 v[20:23], a[248:249], a[10:11], 0\n"
    "v_cvt_pk_bf16_f32 v17, v18, v19\n"
    "v_accvgpr_write_b32 a6, v16\n"
    "v_mfma_f32_4x4x4_16b_bf16 v[20:23], a[250:251], a[10:11], v[20:23]\n"
    "v_cvt_pk_bf16_f32 v24, v24, v25\n"
    "v_accvgpr_write_b32 a7, v17\n"
    "v_mfma_f32_4x4x4_16b_bf16 v[16:19], a[248:249], a[12:13], 0\n"
    "ds_write_b128 v240, v[12:15] offset:3072\n"
    "v_cvt_pk_bf16_f32 v25, v26, v27\n"
    "v_accvgpr_write_b32 a8, v24\n"
    "v_mfma_f32_4x4x4_16b_bf16 v[16:19], a[250:251], a[12:13], v[16:19]\n"
    "v_cvt_pk_bf16_f32 v20, v20, v21\n"
    "v_accvgpr_write_b32 a9, v25\n"
    "v_mfma_f32_4x4x4_16b_bf16 v[24:27], a[248:249], a[14:15], 0\n"
    "v_cvt_pk_bf16_f32 v21, v22, v23\n"
    "v_accvgpr_write_b32 a10, v20\n"
    "v_mfma_f32_4x4x4_16b_bf16 v[24:27], a[250:251], a[14:15], v[24:27]\n"
    "v_cvt_pk_bf16_f32 v16, v16, v17\n"
    "v_accvgpr_write_b32 a11, v21\n"
    "v_mfma_f32_16x16x32_bf16 v[28:31], a[128:131], a[0:3], 0\n"
    "ds_read_b128 a[64:67], v250 offset:16512\n"
    "ds_read_b128 a[68:71], v250 offset:16768\n"
    "v_cvt_pk_bf16_f32 v17, v18, v19\n"
    "v_accvgpr_write_b32 a12, v16\n"
    "v_mfma_f32_16x16x32_bf16 v[28:31], a[132:135], a[4:7], v[28:31]\n"
    "ds_read_b128 a[72:75], v250 offset:17024\n"
    "ds_read_b128 a[76:79], v250 offset:17280\n"
    "v_cvt_pk_bf16_f32 v24, v24, v25\n"
    "v_accvgpr_write_b32 a13, v17\n"
    "v_mfma_f32_16x16x32_bf16 v[28:31], a[136:139], a[8:11], v[28:31]\n"
    "v_cvt_pk_bf16_f32 v25, v26, v27\n"
    "v_accvgpr_write_b32 a14, v24\n"
    "v_accvgpr_write_b32 a15, v25\n"
    "v_add_f32 v245, v245, v212\n"
    "v_cvt_pk_bf16_f32 v196, v196, v197\n"
    "v_mfma_f32_16x16x32_bf16 v[28:31], a[140:143], a[12:15], v[28:31]\n"
    "v_exp_f32 v213, v213\n"
    "v_cvt_pk_bf16_f32 v197, v198, v199\n"
    "v_add_f32 v245, v245, v213\n"
    "v_exp_f32 v214, v214\n"
    "v_add_f32 v200, v124, v228\n"
    "v_add_f32 v201, v125, v229\n"
    "v_add_f32 v202, v126, v230\n"
    "v_add_f32 v203, v127, v231\n"
    "v_mfma_f32_16x16x32_bf16 v[208:211], a[156:159], a[60:63], v[228:231]\n"
    "v_add_f32 v245, v245, v214\n"
    "v_cvt_pk_bf16_f32 v198, v212, v213\n"
    "v_mfma_f32_16x16x32_bf16 v[208:211], a[152:155], a[56:59], v[208:211]\n"
    "v_exp_f32 v215, v215\n"
    "v_mfma_f32_16x16x32_bf16 v[208:211], a[148:151], a[52:55], v[208:211]\n"
    "s_waitcnt vmcnt(63) lgkmcnt(0)\n"
    "v_add_f32 v245, v245, v215\n"
    "v_cvt_pk_bf16_f32 v199, v214, v215\n"
    "v_mfma_f32_16x16x32_bf16 v[208:211], a[144:147], a[48:51], v[208:211]\n"
    "s_barrier\n"
    "v_exp_f32 v200, v200\n"
    "v_mfma_f32_16x16x32_bf16 v[128:131], v[192:195], a[160:163], v[128:131]\n"
    "ds_read_b128 a[96:99], v246 offset:0\n"
    "v_mov_b32 v243, v200\n"
    "v_mfma_f32_16x16x32_bf16 v[132:135], v[192:195], a[164:167], v[132:135]\n"
    "v_exp_f32 v201, v201\n"
    "v_mfma_f32_16x16x32_bf16 v[136:139], v[192:195], a[168:171], v[136:139]\n"
    "ds_read_b128 a[100:103], v246 offset:256\n"
    "v_add_f32 v243, v243, v201\n"
    "v_mfma_f32_16x16x32_bf16 v[140:143], v[192:195], a[224:227], v[140:143]\n"
    "v_exp_f32 v202, v202\n"
    "v_mfma_f32_16x16x32_bf16 v[144:147], v[192:195], a[176:179], v[144:147]\n"
    "ds_read_b128 a[104:107], v246 offset:512\n"
    "v_add_f32 v243, v243, v202\n"
    "v_mfma_f32_16x16x32_bf16 v[148:151], v[192:195], a[180:183], v[148:151]\n"
    "v_exp_f32 v203, v203\n"
    "v_mfma_f32_16x16x32_bf16 v[152:155], v[192:195], a[228:231], v[152:155]\n"
    "ds_read_b128 a[108:111], v246 offset:768\n"
    "v_add_f32 v243, v243, v203\n"
    "v_mfma_f32_16x16x32_bf16 v[156:159], v[192:195], a[232:235], v[156:159]\n"
    "v_exp_f32 v208, v208\n"
    "v_maximum3_f32 v253, v232, v233, v253\n"
    "v_maximum3_f32 v253, v234, v235, v253\n"
    "v_rcp_f32 v232, v232\n"
    "v_rcp_f32 v233, v233\n"
    "v_rcp_f32 v234, v234\n"
    "v_rcp_f32 v235, v235\n"
    "v_and_b32 v248, s76, v241\n"
    "v_sub_f32 v241, v241, v248\n"
    "v_cvt_pk_bf16_f32 v248, v248, v241\n"
    "v_maximum3_f32 v253, v232, v233, v253\n"
    "v_maximum3_f32 v253, v234, v235, v253\n"
    "v_pk_mul_f32 v[128:129], v[128:129], v[232:233]\n"
    "v_pk_mul_f32 v[130:131], v[130:131], v[234:235]\n"
    "v_pk_mul_f32 v[136:137], v[136:137], v[232:233]\n"
    "v_pk_mul_f32 v[138:139], v[138:139], v[234:235]\n"
    "v_cvt_pk_bf16_f32 v128, v128, v136\n"
    "v_cvt_pk_bf16_f32 v130, v130, v138\n"
    "v_cvt_pk_bf16_f32 v136, v129, v137\n"
    "v_cvt_pk_bf16_f32 v138, v131, v139\n"
    "v_pk_mul_f32 v[144:145], v[144:145], v[232:233]\n"
    "v_pk_mul_f32 v[146:147], v[146:147], v[234:235]\n"
    "v_pk_mul_f32 v[152:153], v[152:153], v[232:233]\n"
    "v_pk_mul_f32 v[154:155], v[154:155], v[234:235]\n"
    "v_cvt_pk_bf16_f32 v129, v144, v152\n"
    "v_cvt_pk_bf16_f32 v131, v146, v154\n"
    "v_cvt_pk_bf16_f32 v137, v145, v153\n"
    "v_cvt_pk_bf16_f32 v139, v147, v155\n"
    "v_pk_mul_f32 v[132:133], v[132:133], v[232:233]\n"
    "v_pk_mul_f32 v[134:135], v[134:135], v[234:235]\n"
    "v_pk_mul_f32 v[140:141], v[140:141], v[232:233]\n"
    "v_pk_mul_f32 v[142:143], v[142:143], v[234:235]\n"
    "v_cvt_pk_bf16_f32 v132, v132, v140\n"
    "v_cvt_pk_bf16_f32 v134, v134, v142\n"
    "v_cvt_pk_bf16_f32 v140, v133, v141\n"
    "v_cvt_pk_bf16_f32 v142, v135, v143\n"
    "v_pk_mul_f32 v[148:149], v[148:149], v[232:233]\n"
    "v_pk_mul_f32 v[150:151], v[150:151], v[234:235]\n"
    "v_pk_mul_f32 v[156:157], v[156:157], v[232:233]\n"
    "v_pk_mul_f32 v[158:159], v[158:159], v[234:235]\n"
    "v_mfma_f32_16x16x16_bf16 v[220:223], v[248:249], a[252:253], 0\n"
    "v_cvt_pk_bf16_f32 v133, v148, v156\n"
    "v_cvt_pk_bf16_f32 v135, v150, v158\n"
    "v_cvt_pk_bf16_f32 v141, v149, v157\n"
    "v_mfma_f32_4x4x4_16b_bf16 v[144:147], a[248:249], a[64:65], 0\n"
    "ds_write_b128 v240, v[128:131] offset:16512\n"
    "v_cvt_pk_bf16_f32 v143, v151, v159\n"
    "v_max3_f32 v248, v28, v29, v30\n"
    "v_mfma_f32_4x4x4_16b_bf16 v[144:147], a[250:251], a[64:65], v[144:147]\n"
    "v_max_f32 v248, v248, v31\n"
    "v_mul_f32 v248, 0xbe800000, v248\n"
    "v_mfma_f32_4x4x4_16b_bf16 v[152:155], a[248:249], a[66:67], 0\n"
    "v_cvt_pk_bf16_f32 v248, v248, s80\n"
    "s_nop 0\n"
    "v_mfma_f32_4x4x4_16b_bf16 v[152:155], a[250:251], a[66:67], v[152:155]\n"
    "v_mfma_f32_16x16x16_bf16 v[216:219], a[252:253], v[248:249], 0\n"
    "v_cvt_pk_bf16_f32 v144, v144, v145\n"
    "v_mfma_f32_4x4x4_16b_bf16 v[148:151], a[248:249], a[68:69], 0\n"
    "ds_write_b128 v240, v[136:139] offset:18560\n"
    "v_cvt_pk_bf16_f32 v145, v146, v147\n"
    "v_accvgpr_write_b32 a64, v144\n"
    "v_mfma_f32_4x4x4_16b_bf16 v[148:151], a[250:251], a[68:69], v[148:151]\n"
    "v_cvt_pk_bf16_f32 v152, v152, v153\n"
    "v_accvgpr_write_b32 a65, v145\n"
    "v_mfma_f32_4x4x4_16b_bf16 v[144:147], a[248:249], a[70:71], 0\n"
    "v_cvt_pk_bf16_f32 v153, v154, v155\n"
    "v_accvgpr_write_b32 a66, v152\n"
    "v_mfma_f32_4x4x4_16b_bf16 v[144:147], a[250:251], a[70:71], v[144:147]\n"
    "v_cvt_pk_bf16_f32 v148, v148, v149\n"
    "v_accvgpr_write_b32 a67, v153\n"
    "v_mfma_f32_4x4x4_16b_bf16 v[152:155], a[248:249], a[72:73], 0\n"
    "ds_write_b128 v240, v[132:135] offset:17536\n"
    "v_cvt_pk_bf16_f32 v149, v150, v151\n"
    "v_accvgpr_write_b32 a68, v148\n"
    "v_mfma_f32_4x4x4_16b_bf16 v[152:155], a[250:251], a[72:73], v[152:155]\n"
    "v_cvt_pk_bf16_f32 v144, v144, v145\n"
    "v_accvgpr_write_b32 a69, v149\n"
    "v_mfma_f32_4x4x4_16b_bf16 v[148:151], a[248:249], a[74:75], 0\n"
    "v_cvt_pk_bf16_f32 v145, v146, v147\n"
    "v_accvgpr_write_b32 a70, v144\n"
    "v_mfma_f32_4x4x4_16b_bf16 v[148:151], a[250:251], a[74:75], v[148:151]\n"
    "v_cvt_pk_bf16_f32 v152, v152, v153\n"
    "v_accvgpr_write_b32 a71, v145\n"
    "v_mfma_f32_4x4x4_16b_bf16 v[144:147], a[248:249], a[76:77], 0\n"
    "ds_write_b128 v240, v[140:143] offset:19584\n"
    "v_cvt_pk_bf16_f32 v153, v154, v155\n"
    "v_accvgpr_write_b32 a72, v152\n"
    "v_mfma_f32_4x4x4_16b_bf16 v[144:147], a[250:251], a[76:77], v[144:147]\n"
    "v_cvt_pk_bf16_f32 v148, v148, v149\n"
    "v_accvgpr_write_b32 a73, v153\n"
    "v_mfma_f32_4x4x4_16b_bf16 v[152:155], a[248:249], a[78:79], 0\n"
    "v_cvt_pk_bf16_f32 v149, v150, v151\n"
    "v_accvgpr_write_b32 a74, v148\n"
    "v_mfma_f32_4x4x4_16b_bf16 v[152:155], a[250:251], a[78:79], v[152:155]\n"
    "v_cvt_pk_bf16_f32 v144, v144, v145\n"
    "v_accvgpr_write_b32 a75, v149\n"
    "v_mfma_f32_16x16x32_bf16 v[156:159], a[128:131], a[64:67], 0\n"
    "ds_read_b128 a[16:19], v250 offset:4128\n"
    "ds_read_b128 a[20:23], v250 offset:4384\n"
    "v_cvt_pk_bf16_f32 v145, v146, v147\n"
    "v_accvgpr_write_b32 a76, v144\n"
    "v_mfma_f32_16x16x32_bf16 v[156:159], a[132:135], a[68:71], v[156:159]\n"
    "ds_read_b128 a[24:27], v250 offset:4640\n"
    "ds_read_b128 a[28:31], v250 offset:4896\n"
    "v_cvt_pk_bf16_f32 v152, v152, v153\n"
    "v_accvgpr_write_b32 a77, v145\n"
    "v_mfma_f32_16x16x32_bf16 v[156:159], a[136:139], a[72:75], v[156:159]\n"
    "v_cvt_pk_bf16_f32 v153, v154, v155\n"
    "v_accvgpr_write_b32 a78, v152\n"
    "v_accvgpr_write_b32 a79, v153\n"
    "v_maximum3_f32 v253, v220, v221, v253\n"
    "v_maximum3_f32 v253, v222, v223, v253\n"
    "v_mfma_f32_16x16x32_bf16 v[156:159], a[140:143], a[76:79], v[156:159]\n"
    "v_rcp_f32 v220, v220\n"
    "v_rcp_f32 v221, v221\n"
    "v_rcp_f32 v222, v222\n"
    "v_rcp_f32 v223, v223\n"
    "v_and_b32 v248, s76, v245\n"
    "v_sub_f32 v245, v245, v248\n"
    "v_cvt_pk_bf16_f32 v248, v248, v245\n"
    "v_maximum3_f32 v253, v220, v221, v253\n"
    "v_maximum3_f32 v253, v222, v223, v253\n"
    "v_pk_mul_f32 v[32:33], v[32:33], v[220:221]\n"
    "v_pk_mul_f32 v[34:35], v[34:35], v[222:223]\n"
    "v_pk_mul_f32 v[40:41], v[40:41], v[220:221]\n"
    "v_pk_mul_f32 v[42:43], v[42:43], v[222:223]\n"
    "v_cvt_pk_bf16_f32 v32, v32, v40\n"
    "v_cvt_pk_bf16_f32 v34, v34, v42\n"
    "v_cvt_pk_bf16_f32 v40, v33, v41\n"
    "v_cvt_pk_bf16_f32 v42, v35, v43\n"
    "v_pk_mul_f32 v[48:49], v[48:49], v[220:221]\n"
    "v_pk_mul_f32 v[50:51], v[50:51], v[222:223]\n"
    "v_pk_mul_f32 v[56:57], v[56:57], v[220:221]\n"
    "v_pk_mul_f32 v[58:59], v[58:59], v[222:223]\n"
    "v_cvt_pk_bf16_f32 v33, v48, v56\n"
    "v_cvt_pk_bf16_f32 v35, v50, v58\n"
    "v_cvt_pk_bf16_f32 v41, v49, v57\n"
    "v_cvt_pk_bf16_f32 v43, v51, v59\n"
    "v_pk_mul_f32 v[36:37], v[36:37], v[220:221]\n"
    "v_pk_mul_f32 v[38:39], v[38:39], v[222:223]\n"
    "v_pk_mul_f32 v[44:45], v[44:45], v[220:221]\n"
    "v_pk_mul_f32 v[46:47], v[46:47], v[222:223]\n"
    "v_cvt_pk_bf16_f32 v36, v36, v44\n"
    "v_cvt_pk_bf16_f32 v38, v38, v46\n"
    "v_cvt_pk_bf16_f32 v44, v37, v45\n"
    "v_cvt_pk_bf16_f32 v46, v39, v47\n"
    "v_pk_mul_f32 v[52:53], v[52:53], v[220:221]\n"
    "v_pk_mul_f32 v[54:55], v[54:55], v[222:223]\n"
    "v_pk_mul_f32 v[60:61], v[60:61], v[220:221]\n"
    "v_pk_mul_f32 v[62:63], v[62:63], v[222:223]\n"
    "v_mfma_f32_16x16x16_bf16 v[236:239], v[248:249], a[252:253], 0\n"
    "v_cvt_pk_bf16_f32 v37, v52, v60\n"
    "v_cvt_pk_bf16_f32 v39, v54, v62\n"
    "v_cvt_pk_bf16_f32 v45, v53, v61\n"
    "s_waitcnt lgkmcnt(0)\n"
    "v_mfma_f32_4x4x4_16b_bf16 v[48:51], a[248:249], a[16:17], 0\n"
    "ds_write_b128 v240, v[32:35] offset:4128\n"
    "v_cvt_pk_bf16_f32 v47, v55, v63\n"
    "v_max3_f32 v248, v156, v157, v158\n"
    "v_mfma_f32_4x4x4_16b_bf16 v[48:51], a[250:251], a[16:17], v[48:51]\n"
    "v_max_f32 v248, v248, v159\n"
    "v_mul_f32 v248, 0xbe800000, v248\n"
    "v_mfma_f32_4x4x4_16b_bf16 v[56:59], a[248:249], a[18:19], 0\n"
    "v_cvt_pk_bf16_f32 v248, v248, s80\n"
    "v_nop_e64\n"
    "v_mfma_f32_4x4x4_16b_bf16 v[56:59], a[250:251], a[18:19], v[56:59]\n"
    "v_mfma_f32_16x16x16_bf16 v[232:235], a[252:253], v[248:249], 0\n"
    "v_cvt_pk_bf16_f32 v48, v48, v49\n"
    "v_mfma_f32_4x4x4_16b_bf16 v[52:55], a[248:249], a[20:21], 0\n"
    "ds_write_b128 v240, v[40:43] offset:6176\n"
    "v_cvt_pk_bf16_f32 v49, v50, v51\n"
    "v_accvgpr_write_b32 a16, v48\n"
    "v_mfma_f32_4x4x4_16b_bf16 v[52:55], a[250:251], a[20:21], v[52:55]\n"
    "v_cvt_pk_bf16_f32 v56, v56, v57\n"
    "v_accvgpr_write_b32 a17, v49\n"
    "v_mfma_f32_4x4x4_16b_bf16 v[48:51], a[248:249], a[22:23], 0\n"
    "v_cvt_pk_bf16_f32 v57, v58, v59\n"
    "v_accvgpr_write_b32 a18, v56\n"
    "v_mfma_f32_4x4x4_16b_bf16 v[48:51], a[250:251], a[22:23], v[48:51]\n"
    "v_cvt_pk_bf16_f32 v52, v52, v53\n"
    "v_accvgpr_write_b32 a19, v57\n"
    "v_mfma_f32_4x4x4_16b_bf16 v[56:59], a[248:249], a[24:25], 0\n"
    "ds_write_b128 v240, v[36:39] offset:5152\n"
    "v_cvt_pk_bf16_f32 v53, v54, v55\n"
    "v_accvgpr_write_b32 a20, v52\n"
    "v_mfma_f32_4x4x4_16b_bf16 v[56:59], a[250:251], a[24:25], v[56:59]\n"
    "v_cvt_pk_bf16_f32 v48, v48, v49\n"
    "v_accvgpr_write_b32 a21, v53\n"
    "v_mfma_f32_4x4x4_16b_bf16 v[52:55], a[248:249], a[26:27], 0\n"
    "v_cvt_pk_bf16_f32 v49, v50, v51\n"
    "v_accvgpr_write_b32 a22, v48\n"
    "v_mfma_f32_4x4x4_16b_bf16 v[52:55], a[250:251], a[26:27], v[52:55]\n"
    "v_cvt_pk_bf16_f32 v56, v56, v57\n"
    "v_accvgpr_write_b32 a23, v49\n"
    "v_mfma_f32_4x4x4_16b_bf16 v[48:51], a[248:249], a[28:29], 0\n"
    "ds_write_b128 v240, v[44:47] offset:7200\n"
    "v_cvt_pk_bf16_f32 v57, v58, v59\n"
    "v_accvgpr_write_b32 a24, v56\n"
    "v_mfma_f32_4x4x4_16b_bf16 v[48:51], a[250:251], a[28:29], v[48:51]\n"
    "v_cvt_pk_bf16_f32 v52, v52, v53\n"
    "v_accvgpr_write_b32 a25, v57\n"
    "v_mfma_f32_4x4x4_16b_bf16 v[56:59], a[248:249], a[30:31], 0\n"
    "v_cvt_pk_bf16_f32 v53, v54, v55\n"
    "v_accvgpr_write_b32 a26, v52\n"
    "v_mfma_f32_4x4x4_16b_bf16 v[56:59], a[250:251], a[30:31], v[56:59]\n"
    "v_cvt_pk_bf16_f32 v48, v48, v49\n"
    "v_accvgpr_write_b32 a27, v53\n"
    "v_mfma_f32_16x16x32_bf16 v[60:63], a[128:131], a[16:19], 0\n"
    "ds_read_b128 a[80:83], v250 offset:20640\n"
    "ds_read_b128 a[84:87], v250 offset:20896\n"
    "v_cvt_pk_bf16_f32 v49, v50, v51\n"
    "v_accvgpr_write_b32 a28, v48\n"
    "v_mfma_f32_16x16x32_bf16 v[60:63], a[132:135], a[20:23], v[60:63]\n"
    "ds_read_b128 a[88:91], v250 offset:21152\n"
    "ds_read_b128 a[92:95], v250 offset:21408\n"
    "v_cvt_pk_bf16_f32 v56, v56, v57\n"
    "v_accvgpr_write_b32 a29, v49\n"
    "v_mfma_f32_16x16x32_bf16 v[60:63], a[136:139], a[24:27], v[60:63]\n"
    "v_cvt_pk_bf16_f32 v57, v58, v59\n"
    "v_accvgpr_write_b32 a30, v56\n"
    "v_accvgpr_write_b32 a31, v57\n"
    "v_add_f32 v243, v243, v208\n"
    "v_cvt_pk_bf16_f32 v200, v200, v201\n"
    "v_mfma_f32_16x16x32_bf16 v[60:63], a[140:143], a[28:31], v[60:63]\n"
    "LPG5:\n"
    "s_cbranch_execz LPG6\n"
    "v_exp_f32 v209, v209\n"
    "v_cvt_pk_bf16_f32 v201, v202, v203\n"
    "v_add_f32 v243, v243, v209\n"
    "v_exp_f32 v210, v210\n"
    "v_add_f32 v204, v156, v232\n"
    "v_add_f32 v205, v157, v233\n"
    "v_add_f32 v206, v158, v234\n"
    "v_add_f32 v207, v159, v235\n"
    "v_mfma_f32_16x16x32_bf16 v[212:215], a[156:159], a[76:79], v[232:235]\n"
    "v_add_f32 v243, v243, v210\n"
    "v_cvt_pk_bf16_f32 v202, v208, v209\n"
    "v_mfma_f32_16x16x32_bf16 v[212:215], a[152:155], a[72:75], v[212:215]\n"
    "v_exp_f32 v211, v211\n"
    "v_mfma_f32_16x16x32_bf16 v[212:215], a[148:151], a[68:71], v[212:215]\n"
    "v_add_f32 v243, v243, v211\n"
    "v_cvt_pk_bf16_f32 v203, v210, v211\n"
    "v_mfma_f32_16x16x32_bf16 v[212:215], a[144:147], a[64:67], v[212:215]\n"
    "v_exp_f32 v204, v204\n"
    "v_mfma_f32_16x16x32_bf16 v[160:163], v[196:199], a[160:163], v[160:163]\n"
    "ds_read_b128 a[124:127], v246 offset:9024\n"
    "v_mov_b32 v244, v204\n"
    "v_mfma_f32_16x16x32_bf16 v[164:167], v[196:199], a[164:167], v[164:167]\n"
    "v_exp_f32 v205, v205\n"
    "v_mfma_f32_16x16x32_bf16 v[168:171], v[196:199], a[168:171], v[168:171]\n"
    "ds_read_b128 a[120:123], v246 offset:8768\n"
    "v_add_f32 v244, v244, v205\n"
    "v_mfma_f32_16x16x32_bf16 v[172:175], v[196:199], a[224:227], v[172:175]\n"
    "v_exp_f32 v206, v206\n"
    "v_mfma_f32_16x16x32_bf16 v[176:179], v[196:199], a[176:179], v[176:179]\n"
    "ds_read_b128 a[116:119], v246 offset:8512\n"
    "v_add_f32 v244, v244, v206\n"
    "v_mfma_f32_16x16x32_bf16 v[180:183], v[196:199], a[180:183], v[180:183]\n"
    "v_exp_f32 v207, v207\n"
    "v_mfma_f32_16x16x32_bf16 v[184:187], v[196:199], a[228:231], v[184:187]\n"
    "ds_read_b128 a[112:115], v246 offset:8256\n"
    "v_add_f32 v244, v244, v207\n"
    "v_mfma_f32_16x16x32_bf16 v[188:191], v[196:199], a[232:235], v[188:191]\n"
    "v_exp_f32 v212, v212\n"
    "v_maximum3_f32 v253, v236, v237, v253\n"
    "v_maximum3_f32 v253, v238, v239, v253\n"
    "v_rcp_f32 v236, v236\n"
    "v_rcp_f32 v237, v237\n"
    "v_rcp_f32 v238, v238\n"
    "v_rcp_f32 v239, v239\n"
    "v_and_b32 v248, s76, v242\n"
    "v_sub_f32_e64 v242, v242, v248\n"
    "v_cvt_pk_bf16_f32 v248, v248, v242\n"
    "v_maximum3_f32 v253, v236, v237, v253\n"
    "v_maximum3_f32 v253, v238, v239, v253\n"
    "v_pk_mul_f32 v[160:161], v[160:161], v[236:237]\n"
    "v_pk_mul_f32 v[162:163], v[162:163], v[238:239]\n"
    "v_pk_mul_f32 v[168:169], v[168:169], v[236:237]\n"
    "v_pk_mul_f32 v[170:171], v[170:171], v[238:239]\n"
    "v_cvt_pk_bf16_f32 v160, v160, v168\n"
    "v_cvt_pk_bf16_f32 v162, v162, v170\n"
    "v_cvt_pk_bf16_f32 v168, v161, v169\n"
    "v_cvt_pk_bf16_f32 v170, v163, v171\n"
    "v_pk_mul_f32 v[176:177], v[176:177], v[236:237]\n"
    "v_pk_mul_f32 v[178:179], v[178:179], v[238:239]\n"
    "v_pk_mul_f32 v[184:185], v[184:185], v[236:237]\n"
    "v_pk_mul_f32 v[186:187], v[186:187], v[238:239]\n"
    "v_cvt_pk_bf16_f32 v161, v176, v184\n"
    "v_cvt_pk_bf16_f32 v163, v178, v186\n"
    "v_cvt_pk_bf16_f32 v169, v177, v185\n"
    "v_cvt_pk_bf16_f32 v171, v179, v187\n"
    "v_pk_mul_f32 v[164:165], v[164:165], v[236:237]\n"
    "v_pk_mul_f32 v[166:167], v[166:167], v[238:239]\n"
    "v_pk_mul_f32 v[172:173], v[172:173], v[236:237]\n"
    "v_pk_mul_f32 v[174:175], v[174:175], v[238:239]\n"
    "v_cvt_pk_bf16_f32 v164, v164, v172\n"
    "v_cvt_pk_bf16_f32 v166, v166, v174\n"
    "v_cvt_pk_bf16_f32 v172, v165, v173\n"
    "v_cvt_pk_bf16_f32 v174, v167, v175\n"
    "v_pk_mul_f32 v[180:181], v[180:181], v[236:237]\n"
    "v_pk_mul_f32 v[182:183], v[182:183], v[238:239]\n"
    "v_pk_mul_f32 v[188:189], v[188:189], v[236:237]\n"
    "v_pk_mul_f32 v[190:191], v[190:191], v[238:239]\n"
    "v_mfma_f32_16x16x16_bf16 v[224:227], v[248:249], a[252:253], 0\n"
    "v_cvt_pk_bf16_f32 v165, v180, v188\n"
    "v_cvt_pk_bf16_f32 v167, v182, v190\n"
    "v_cvt_pk_bf16_f32 v173, v181, v189\n"
    "s_waitcnt lgkmcnt(0)\n"
    "v_mfma_f32_4x4x4_16b_bf16 v[176:179], a[248:249], a[80:81], 0\n"
    "ds_write_b128 v240, v[160:163] offset:20640\n"
    "v_cvt_pk_bf16_f32 v175, v183, v191\n"
    "v_max3_f32 v248, v60, v61, v62\n"
    "v_mfma_f32_4x4x4_16b_bf16 v[176:179], a[250:251], a[80:81], v[176:179]\n"
    "v_max_f32 v248, v248, v63\n"
    "v_mul_f32 v248, 0xbe800000, v248\n"
    "v_mfma_f32_4x4x4_16b_bf16 v[184:187], a[248:249], a[82:83], 0\n"
    "v_cvt_pk_bf16_f32 v248, v248, s80\n"
    "v_nop_e64\n"
    "v_mfma_f32_4x4x4_16b_bf16 v[184:187], a[250:251], a[82:83], v[184:187]\n"
    "v_mfma_f32_16x16x16_bf16 v[220:223], a[252:253], v[248:249], 0\n"
    "v_cvt_pk_bf16_f32 v176, v176, v177\n"
    "v_mfma_f32_4x4x4_16b_bf16 v[180:183], a[248:249], a[84:85], 0\n"
    "ds_write_b128 v240, v[168:171] offset:22688\n"
    "v_cvt_pk_bf16_f32 v177, v178, v179\n"
    "v_accvgpr_write_b32 a80, v176\n"
    "v_mfma_f32_4x4x4_16b_bf16 v[180:183], a[250:251], a[84:85], v[180:183]\n"
    "v_cvt_pk_bf16_f32 v184, v184, v185\n"
    "v_accvgpr_write_b32 a81, v177\n"
    "v_mfma_f32_4x4x4_16b_bf16 v[176:179], a[248:249], a[86:87], 0\n"
    "v_cvt_pk_bf16_f32 v185, v186, v187\n"
    "v_accvgpr_write_b32 a82, v184\n"
    "v_mfma_f32_4x4x4_16b_bf16 v[176:179], a[250:251], a[86:87], v[176:179]\n"
    "v_cvt_pk_bf16_f32 v180, v180, v181\n"
    "v_accvgpr_write_b32 a83, v185\n"
    "v_mfma_f32_4x4x4_16b_bf16 v[184:187], a[248:249], a[88:89], 0\n"
    "ds_write_b128 v240, v[164:167] offset:21664\n"
    "v_cvt_pk_bf16_f32 v181, v182, v183\n"
    "v_accvgpr_write_b32 a84, v180\n"
    "v_mfma_f32_4x4x4_16b_bf16 v[184:187], a[250:251], a[88:89], v[184:187]\n"
    "v_cvt_pk_bf16_f32 v176, v176, v177\n"
    "v_accvgpr_write_b32 a85, v181\n"
    "v_mfma_f32_4x4x4_16b_bf16 v[180:183], a[248:249], a[90:91], 0\n"
    "v_cvt_pk_bf16_f32 v177, v178, v179\n"
    "v_accvgpr_write_b32 a86, v176\n"
    "v_mfma_f32_4x4x4_16b_bf16 v[180:183], a[250:251], a[90:91], v[180:183]\n"
    "v_cvt_pk_bf16_f32 v184, v184, v185\n"
    "v_accvgpr_write_b32 a87, v177\n"
    "v_mfma_f32_4x4x4_16b_bf16 v[176:179], a[248:249], a[92:93], 0\n"
    "ds_write_b128 v240, v[172:175] offset:23712\n"
    "v_cvt_pk_bf16_f32 v185, v186, v187\n"
    "v_accvgpr_write_b32 a88, v184\n"
    "v_mfma_f32_4x4x4_16b_bf16 v[176:179], a[250:251], a[92:93], v[176:179]\n"
    "v_cvt_pk_bf16_f32 v180, v180, v181\n"
    "v_accvgpr_write_b32 a89, v185\n"
    "v_mfma_f32_4x4x4_16b_bf16 v[184:187], a[248:249], a[94:95], 0\n"
    "v_cvt_pk_bf16_f32 v181, v182, v183\n"
    "v_accvgpr_write_b32 a90, v180\n"
    "v_mfma_f32_4x4x4_16b_bf16 v[184:187], a[250:251], a[94:95], v[184:187]\n"
    "v_cvt_pk_bf16_f32 v176, v176, v177\n"
    "v_accvgpr_write_b32 a91, v181\n"
    "v_mfma_f32_16x16x32_bf16 v[188:191], a[128:131], a[80:83], 0\n"
    "ds_read_b128 a[32:35], v250 offset:8256\n"
    "ds_read_b128 a[36:39], v250 offset:8512\n"
    "v_cvt_pk_bf16_f32 v177, v178, v179\n"
    "v_accvgpr_write_b32 a92, v176\n"
    "v_mfma_f32_16x16x32_bf16 v[188:191], a[132:135], a[84:87], v[188:191]\n"
    "ds_read_b128 a[40:43], v250 offset:8768\n"
    "ds_read_b128 a[44:47], v250 offset:9024\n"
    "v_cvt_pk_bf16_f32 v184, v184, v185\n"
    "v_accvgpr_write_b32 a93, v177\n"
    "v_mfma_f32_16x16x32_bf16 v[188:191], a[136:139], a[88:91], v[188:191]\n"
    "v_cvt_pk_bf16_f32 v185, v186, v187\n"
    "v_accvgpr_write_b32 a94, v184\n"
    "v_accvgpr_write_b32 a95, v185\n"
    "v_maximum3_f32 v253, v224, v225, v253\n"
    "v_maximum3_f32 v253, v226, v227, v253\n"
    "v_mfma_f32_16x16x32_bf16 v[188:191], a[140:143], a[92:95], v[188:191]\n"
    "v_rcp_f32 v224, v224\n"
    "v_rcp_f32 v225, v225\n"
    "v_rcp_f32 v226, v226\n"
    "v_rcp_f32 v227, v227\n"
    "v_maximum3_f32 v253, v224, v225, v253\n"
    "v_maximum3_f32 v253, v226, v227, v253\n"
    "v_pk_mul_f32 v[64:65], v[64:65], v[224:225]\n"
    "v_pk_mul_f32 v[66:67], v[66:67], v[226:227]\n"
    "v_pk_mul_f32 v[72:73], v[72:73], v[224:225]\n"
    "v_pk_mul_f32 v[74:75], v[74:75], v[226:227]\n"
    "v_cvt_pk_bf16_f32 v64, v64, v72\n"
    "v_cvt_pk_bf16_f32 v66, v66, v74\n"
    "v_cvt_pk_bf16_f32 v72, v65, v73\n"
    "v_cvt_pk_bf16_f32 v74, v67, v75\n"
    "v_pk_mul_f32 v[80:81], v[80:81], v[224:225]\n"
    "v_pk_mul_f32 v[82:83], v[82:83], v[226:227]\n"
    "v_pk_mul_f32 v[88:89], v[88:89], v[224:225]\n"
    "v_pk_mul_f32 v[90:91], v[90:91], v[226:227]\n"
    "v_cvt_pk_bf16_f32 v65, v80, v88\n"
    "v_cvt_pk_bf16_f32 v67, v82, v90\n"
    "v_cvt_pk_bf16_f32 v73, v81, v89\n"
    "v_cvt_pk_bf16_f32 v75, v83, v91\n"
    "v_pk_mul_f32 v[68:69], v[68:69], v[224:225]\n"
    "v_pk_mul_f32 v[70:71], v[70:71], v[226:227]\n"
    "v_pk_mul_f32 v[76:77], v[76:77], v[224:225]\n"
    "v_pk_mul_f32 v[78:79], v[78:79], v[226:227]\n"
    "v_cvt_pk_bf16_f32 v68, v68, v76\n"
    "v_cvt_pk_bf16_f32 v70, v70, v78\n"
    "v_cvt_pk_bf16_f32 v76, v69, v77\n"
    "v_cvt_pk_bf16_f32 v78, v71, v79\n"
    "v_pk_mul_f32 v[84:85], v[84:85], v[224:225]\n"
    "v_pk_mul_f32 v[86:87], v[86:87], v[226:227]\n"
    "v_pk_mul_f32 v[92:93], v[92:93], v[224:225]\n"
    "v_pk_mul_f32 v[94:95], v[94:95], v[226:227]\n"
    "v_cvt_pk_bf16_f32 v69, v84, v92\n"
    "v_cvt_pk_bf16_f32 v71, v86, v94\n"
    "v_cvt_pk_bf16_f32 v77, v85, v93\n"
    "s_waitcnt lgkmcnt(0)\n"
    "v_mfma_f32_4x4x4_16b_bf16 v[80:83], a[248:249], a[32:33], 0\n"
    "ds_write_b128 v240, v[64:67] offset:8256\n"
    "v_cvt_pk_bf16_f32 v79, v87, v95\n"
    "v_max3_f32 v248, v188, v189, v190\n"
    "v_mfma_f32_4x4x4_16b_bf16 v[80:83], a[250:251], a[32:33], v[80:83]\n"
    "v_max_f32 v248, v248, v191\n"
    "v_mul_f32 v248, 0xbe800000, v248\n"
    "v_mfma_f32_4x4x4_16b_bf16 v[88:91], a[248:249], a[34:35], 0\n"
    "v_cvt_pk_bf16_f32 v248, v248, s80\n"
    "v_nop_e64\n"
    "v_mfma_f32_4x4x4_16b_bf16 v[88:91], a[250:251], a[34:35], v[88:91]\n"
    "v_mfma_f32_16x16x16_bf16 v[236:239], a[252:253], v[248:249], 0\n"
    "v_cvt_pk_bf16_f32 v80, v80, v81\n"
    "v_mfma_f32_4x4x4_16b_bf16 v[84:87], a[248:249], a[36:37], 0\n"
    "ds_write_b128 v240, v[72:75] offset:10304\n"
    "v_cvt_pk_bf16_f32 v81, v82, v83\n"
    "v_accvgpr_write_b32 a32, v80\n"
    "v_mfma_f32_4x4x4_16b_bf16 v[84:87], a[250:251], a[36:37], v[84:87]\n"
    "v_cvt_pk_bf16_f32 v88, v88, v89\n"
    "v_accvgpr_write_b32 a33, v81\n"
    "v_mfma_f32_4x4x4_16b_bf16 v[80:83], a[248:249], a[38:39], 0\n"
    "v_cvt_pk_bf16_f32 v89, v90, v91\n"
    "v_accvgpr_write_b32 a34, v88\n"
    "v_mfma_f32_4x4x4_16b_bf16 v[80:83], a[250:251], a[38:39], v[80:83]\n"
    "v_cvt_pk_bf16_f32 v84, v84, v85\n"
    "v_accvgpr_write_b32 a35, v89\n"
    "v_mfma_f32_4x4x4_16b_bf16 v[88:91], a[248:249], a[40:41], 0\n"
    "ds_write_b128 v240, v[68:71] offset:9280\n"
    "v_cvt_pk_bf16_f32 v85, v86, v87\n"
    "v_accvgpr_write_b32 a36, v84\n"
    "v_mfma_f32_4x4x4_16b_bf16 v[88:91], a[250:251], a[40:41], v[88:91]\n"
    "v_cvt_pk_bf16_f32 v80, v80, v81\n"
    "v_accvgpr_write_b32 a37, v85\n"
    "v_mfma_f32_4x4x4_16b_bf16 v[84:87], a[248:249], a[42:43], 0\n"
    "v_cvt_pk_bf16_f32 v81, v82, v83\n"
    "v_accvgpr_write_b32 a38, v80\n"
    "v_mfma_f32_4x4x4_16b_bf16 v[84:87], a[250:251], a[42:43], v[84:87]\n"
    "v_cvt_pk_bf16_f32 v88, v88, v89\n"
    "v_accvgpr_write_b32 a39, v81\n"
    "v_mfma_f32_4x4x4_16b_bf16 v[80:83], a[248:249], a[44:45], 0\n"
    "ds_write_b128 v240, v[76:79] offset:11328\n"
    "v_cvt_pk_bf16_f32 v89, v90, v91\n"
    "v_accvgpr_write_b32 a40, v88\n"
    "v_mfma_f32_4x4x4_16b_bf16 v[80:83], a[250:251], a[44:45], v[80:83]\n"
    "v_cvt_pk_bf16_f32 v84, v84, v85\n"
    "v_accvgpr_write_b32 a41, v89\n"
    "v_mfma_f32_4x4x4_16b_bf16 v[88:91], a[248:249], a[46:47], 0\n"
    "v_cvt_pk_bf16_f32 v85, v86, v87\n"
    "v_accvgpr_write_b32 a42, v84\n"
    "v_mfma_f32_4x4x4_16b_bf16 v[88:91], a[250:251], a[46:47], v[88:91]\n"
    "v_cvt_pk_bf16_f32 v80, v80, v81\n"
    "v_accvgpr_write_b32 a43, v85\n"
    "v_mfma_f32_16x16x32_bf16 v[92:95], a[128:131], a[32:35], 0\n"
    "v_cvt_pk_bf16_f32 v81, v82, v83\n"
    "v_accvgpr_write_b32 a44, v80\n"
    "v_mfma_f32_16x16x32_bf16 v[92:95], a[132:135], a[36:39], v[92:95]\n"
    "v_cvt_pk_bf16_f32 v88, v88, v89\n"
    "v_accvgpr_write_b32 a45, v81\n"
    "v_mfma_f32_16x16x32_bf16 v[92:95], a[136:139], a[40:43], v[92:95]\n"
    "v_cvt_pk_bf16_f32 v89, v90, v91\n"
    "v_accvgpr_write_b32 a46, v88\n"
    "v_accvgpr_write_b32 a47, v89\n"
    "v_cmp_ngt_f32 vcc, s72, v253\n"
    "s_nop 0\n"
    "v_mfma_f32_16x16x32_bf16 v[92:95], a[140:143], a[44:47], v[92:95]\n"
    "s_cmp_lg_u64 vcc, 0\n"
    "s_cselect_b32 s69, 1, 0\n"
    "s_or_b32 s65, s65, s69\n"
    "v_add_f32 v244, v244, v212\n"
    "v_cvt_pk_bf16_f32 v204, v204, v205\n"
    "v_exp_f32 v213, v213\n"
    "v_cvt_pk_bf16_f32 v205, v206, v207\n"
    "v_add_f32 v244, v244, v213\n"
    "v_exp_f32 v214, v214\n"
    "v_add_f32 v192, v188, v236\n"
    "v_add_f32 v193, v189, v237\n"
    "v_add_f32 v194, v190, v238\n"
    "v_add_f32 v195, v191, v239\n"
    "v_mfma_f32_16x16x32_bf16 v[208:211], a[156:159], a[92:95], v[236:239]\n"
    "v_add_f32 v244, v244, v214\n"
    "v_cvt_pk_bf16_f32 v206, v212, v213\n"
    "v_mfma_f32_16x16x32_bf16 v[208:211], a[152:155], a[88:91], v[208:211]\n"
    "v_exp_f32 v215, v215\n"
    "v_mfma_f32_16x16x32_bf16 v[208:211], a[148:151], a[84:87], v[208:211]\n"
    "v_add_f32 v244, v244, v215\n"
    "v_cvt_pk_bf16_f32 v207, v214, v215\n"
    "v_mfma_f32_16x16x32_bf16 v[208:211], a[144:147], a[80:83], v[208:211]\n"
    "v_exp_f32 v192, v192\n"
    "v_mfma_f32_16x16x32_bf16 v[96:99], v[200:203], a[192:195], 0\n"
    "ds_read_b64_tr_b16 a[160:161], v247 offset:0\n"
    "v_mov_b32 v245, v192\n"
    "v_mfma_f32_16x16x32_bf16 v[100:103], v[200:203], a[196:199], 0\n"
    "v_exp_f32 v193, v193\n"
    "v_mfma_f32_16x16x32_bf16 v[104:107], v[200:203], a[200:203], 0\n"
    "ds_read_b64_tr_b16 a[162:163], v247 offset:8256\n"
    "v_add_f32 v245, v245, v193\n"
    "v_mfma_f32_16x16x32_bf16 v[108:111], v[200:203], a[236:239], 0\n"
    "v_exp_f32 v194, v194\n"
    "v_mfma_f32_16x16x32_bf16 v[112:115], v[200:203], a[208:211], 0\n"
    "ds_read_b64_tr_b16 a[164:165], v247 offset:8\n"
    "v_add_f32 v245, v245, v194\n"
    "v_mfma_f32_16x16x32_bf16 v[116:119], v[200:203], a[212:215], 0\n"
    "v_exp_f32 v195, v195\n"
    "v_mfma_f32_16x16x32_bf16 v[120:123], v[200:203], a[240:243], 0\n"
    "ds_read_b64_tr_b16 a[166:167], v247 offset:8264\n"
    "v_add_f32 v245, v245, v195\n"
    "v_mfma_f32_16x16x32_bf16 v[124:127], v[200:203], a[244:247], 0\n"
    "v_exp_f32 v208, v208\n"
    "v_max3_f32 v248, v92, v93, v94\n"
    "v_max_f32 v248, v248, v95\n"
    "v_mul_f32 v248, 0xbe800000, v248\n"
    "v_cvt_pk_bf16_f32 v248, v248, s80\n"
    "v_add_f32 v245, v245, v208\n"
    "v_cvt_pk_bf16_f32 v192, v192, v193\n"
    "v_mfma_f32_16x16x16_bf16 v[224:227], a[252:253], v[248:249], 0\n"
    "v_exp_f32 v209, v209\n"
    "v_cvt_pk_bf16_f32 v193, v194, v195\n"
    "v_add_f32 v245, v245, v209\n"
    "v_exp_f32 v210, v210\n"
    "v_add_f32 v196, v28, v216\n"
    "v_add_f32 v197, v29, v217\n"
    "v_add_f32 v198, v30, v218\n"
    "v_add_f32 v199, v31, v219\n"
    "v_mfma_f32_16x16x32_bf16 v[212:215], a[156:159], a[12:15], v[216:219]\n"
    "v_add_f32 v245, v245, v210\n"
    "v_cvt_pk_bf16_f32 v194, v208, v209\n"
    "v_mfma_f32_16x16x32_bf16 v[212:215], a[152:155], a[8:11], v[212:215]\n"
    "v_exp_f32 v211, v211\n"
    "v_mfma_f32_16x16x32_bf16 v[212:215], a[148:151], a[4:7], v[212:215]\n"
    "v_add_f32 v245, v245, v211\n"
    "v_cvt_pk_bf16_f32 v195, v210, v211\n"
    "v_mfma_f32_16x16x32_bf16 v[212:215], a[144:147], a[0:3], v[212:215]\n"
    "v_exp_f32 v196, v196\n"
    "v_mfma_f32_16x16x32_bf16 v[128:131], v[204:207], a[192:195], 0\n"
    "ds_read_b64_tr_b16 a[168:169], v251 offset:0\n"
    "v_mov_b32 v240, v196\n"
    "v_mfma_f32_16x16x32_bf16 v[132:135], v[204:207], a[196:199], 0\n"
    "v_exp_f32 v197, v197\n"
    "v_mfma_f32_16x16x32_bf16 v[136:139], v[204:207], a[200:203], 0\n"
    "ds_read_b64_tr_b16 a[170:171], v251 offset:8256\n"
    "v_add_f32 v240, v240, v197\n"
    "v_mfma_f32_16x16x32_bf16 v[140:143], v[204:207], a[236:239], 0\n"
    "v_exp_f32 v198, v198\n"
    "v_mfma_f32_16x16x32_bf16 v[144:147], v[204:207], a[208:211], 0\n"
    "ds_read_b64_tr_b16 a[172:173], v251 offset:8\n"
    "v_add_f32 v240, v240, v198\n"
    "v_mfma_f32_16x16x32_bf16 v[148:151], v[204:207], a[212:215], 0\n"
    "v_exp_f32 v199, v199\n"
    "v_mfma_f32_16x16x32_bf16 v[152:155], v[204:207], a[240:243], 0\n"
    "ds_read_b64_tr_b16 a[174:175], v251 offset:8264\n"
    "v_add_f32 v240, v240, v199\n"
    "v_mfma_f32_16x16x32_bf16 v[156:159], v[204:207], a[244:247], 0\n"
    "v_exp_f32 v212, v212\n"
    "v_mfma_f32_16x16x32_bf16 v[200:203], a[96:99], a[0:3], v[216:219]\n"
    "v_add_f32 v240, v240, v212\n"
    "v_cvt_pk_bf16_f32 v196, v196, v197\n"
    "v_mfma_f32_16x16x32_bf16 v[200:203], a[100:103], a[4:7], v[200:203]\n"
    "v_exp_f32 v213, v213\n"
    "v_mfma_f32_16x16x32_bf16 v[200:203], a[104:107], a[8:11], v[200:203]\n"
    "v_add_f32 v240, v240, v213\n"
    "v_cvt_pk_bf16_f32 v197, v198, v199\n"
    "v_mfma_f32_16x16x32_bf16 v[200:203], a[108:111], a[12:15], v[200:203]\n"
    "v_exp_f32 v214, v214\n"
    "v_mfma_f32_16x16x32_bf16 v[208:211], a[124:127], a[12:15], v[216:219]\n"
    "v_add_f32 v240, v240, v214\n"
    "v_cvt_pk_bf16_f32 v198, v212, v213\n"
    "v_mfma_f32_16x16x32_bf16 v[208:211], a[120:123], a[8:11], v[208:211]\n"
    "v_exp_f32 v215, v215\n"
    "v_mfma_f32_16x16x32_bf16 v[208:211], a[116:119], a[4:7], v[208:211]\n"
    "v_add_f32 v240, v240, v215\n"
    "v_cvt_pk_bf16_f32 v199, v214, v215\n"
    "v_mfma_f32_16x16x32_bf16 v[208:211], a[112:115], a[0:3], v[208:211]\n"
    "v_exp_f32 v200, v200\n"
    "v_mfma_f32_16x16x32_bf16 v[160:163], v[192:195], a[192:195], 0\n"
    "ds_read_b64_tr_b16 a[184:185], v254 offset:0\n"
    "v_add_f32 v240, v240, v200\n"
    "v_mfma_f32_16x16x32_bf16 v[164:167], v[192:195], a[196:199], 0\n"
    "v_exp_f32 v201, v201\n"
    "v_mfma_f32_16x16x32_bf16 v[168:171], v[192:195], a[200:203], 0\n"
    "ds_read_b64_tr_b16 a[186:187], v254 offset:8256\n"
    "v_add_f32 v240, v240, v201\n"
    "v_mfma_f32_16x16x32_bf16 v[172:175], v[192:195], a[236:239], 0\n"
    "v_exp_f32 v202, v202\n"
    "v_mfma_f32_16x16x32_bf16 v[176:179], v[192:195], a[208:211], 0\n"
    "ds_read_b64_tr_b16 a[188:189], v254 offset:8\n"
    "v_add_f32 v240, v240, v202\n"
    "v_mfma_f32_16x16x32_bf16 v[180:183], v[192:195], a[212:215], 0\n"
    "v_exp_f32 v203, v203\n"
    "v_mfma_f32_16x16x32_bf16 v[184:187], v[192:195], a[240:243], 0\n"
    "ds_read_b64_tr_b16 a[190:191], v254 offset:8264\n"
    "v_add_f32 v240, v240, v203\n"
    "v_mfma_f32_16x16x32_bf16 v[188:191], v[192:195], a[244:247], 0\n"
    "v_exp_f32 v208, v208\n"
    "v_cvt_pk_bf16_f32 v200, v200, v201\n"
    "v_add_f32 v240, v240, v208\n"
    "v_exp_f32 v209, v209\n"
    "v_cvt_pk_bf16_f32 v201, v202, v203\n"
    "v_add_f32 v240, v240, v209\n"
    "v_exp_f32 v210, v210\n"
    "v_add_f32 v204, v60, v220\n"
    "v_add_f32 v205, v61, v221\n"
    "v_add_f32 v206, v62, v222\n"
    "v_add_f32 v207, v63, v223\n"
    "v_mfma_f32_16x16x32_bf16 v[212:215], a[156:159], a[28:31], v[220:223]\n"
    "v_add_f32 v240, v240, v210\n"
    "v_cvt_pk_bf16_f32 v202, v208, v209\n"
    "v_mfma_f32_16x16x32_bf16 v[212:215], a[152:155], a[24:27], v[212:215]\n"
    "v_exp_f32 v211, v211\n"
    "v_mfma_f32_16x16x32_bf16 v[212:215], a[148:151], a[20:23], v[212:215]\n"
    "v_add_f32 v240, v240, v211\n"
    "v_cvt_pk_bf16_f32 v203, v210, v211\n"
    "v_mfma_f32_16x16x32_bf16 v[212:215], a[144:147], a[16:19], v[212:215]\n"
    "v_exp_f32 v204, v204\n"
    "v_mfma_f32_16x16x32_bf16 v[0:3], v[196:199], a[192:195], 0\n"
    "s_waitcnt lgkmcnt(0)\n"
    "ds_read_b64_tr_b16 a[176:177], v252 offset:0\n"
    "v_mov_b32 v241, v204\n"
    "v_mfma_f32_16x16x32_bf16 v[0:3], v[200:203], a[160:163], v[0:3]\n"
    "v_exp_f32 v205, v205\n"
    "v_mfma_f32_16x16x32_bf16 v[4:7], v[200:203], a[164:167], 0\n"
    "ds_read_b64_tr_b16 a[178:179], v252 offset:8256\n"
    "v_add_f32 v241, v241, v205\n"
    "v_mfma_f32_16x16x32_bf16 v[4:7], v[196:199], a[196:199], v[4:7]\n"
    "v_exp_f32 v206, v206\n"
    "v_mfma_f32_16x16x32_bf16 v[8:11], v[196:199], a[200:203], 0\n"
    "ds_read_b64_tr_b16 a[180:181], v252 offset:8\n"
    "v_add_f32 v241, v241, v206\n"
    "v_mfma_f32_16x16x32_bf16 v[8:11], v[200:203], a[168:171], v[8:11]\n"
    "v_exp_f32 v207, v207\n"
    "v_mfma_f32_16x16x32_bf16 v[12:15], v[200:203], a[172:175], 0\n"
    "ds_read_b64_tr_b16 a[182:183], v252 offset:8264\n"
    "v_add_f32 v241, v241, v207\n"
    "v_mfma_f32_16x16x32_bf16 v[12:15], v[196:199], a[236:239], v[12:15]\n"
    "v_exp_f32 v212, v212\n"
    "v_mfma_f32_16x16x32_bf16 v[192:195], a[96:99], a[16:19], v[220:223]\n"
    "v_add_f32 v241, v241, v212\n"
    "v_cvt_pk_bf16_f32 v204, v204, v205\n"
    "v_mfma_f32_16x16x32_bf16 v[192:195], a[100:103], a[20:23], v[192:195]\n"
    "v_exp_f32 v213, v213\n"
    "v_mfma_f32_16x16x32_bf16 v[192:195], a[104:107], a[24:27], v[192:195]\n"
    "v_add_f32 v241, v241, v213\n"
    "v_cvt_pk_bf16_f32 v205, v206, v207\n"
    "v_mfma_f32_16x16x32_bf16 v[192:195], a[108:111], a[28:31], v[192:195]\n"
    "v_exp_f32 v214, v214\n"
    "v_mfma_f32_16x16x32_bf16 v[208:211], a[124:127], a[28:31], v[220:223]\n"
    "v_add_f32 v241, v241, v214\n"
    "v_cvt_pk_bf16_f32 v206, v212, v213\n"
    "v_mfma_f32_16x16x32_bf16 v[208:211], a[120:123], a[24:27], v[208:211]\n"
    "v_exp_f32 v215, v215\n"
    "v_mfma_f32_16x16x32_bf16 v[208:211], a[116:119], a[20:23], v[208:211]\n"
    "s_waitcnt vmcnt(63) lgkmcnt(0)\n"
    "v_add_f32 v241, v241, v215\n"
    "v_cvt_pk_bf16_f32 v207, v214, v215\n"
    "v_mfma_f32_16x16x32_bf16 v[208:211], a[112:115], a[16:19], v[208:211]\n"
    "s_barrier\n"
    "v_exp_f32 v192, v192\n"
    "v_mfma_f32_16x16x32_bf16 v[16:19], v[196:199], a[208:211], 0\n"
    "ds_read_b64_tr_b16 a[204:205], v251 offset:16520\n"
    "v_add_f32 v241, v241, v192\n"
    "v_mfma_f32_16x16x32_bf16 v[16:19], v[200:203], a[176:179], v[16:19]\n"
    "v_exp_f32 v193, v193\n"
    "v_mfma_f32_16x16x32_bf16 v[20:23], v[200:203], a[180:183], 0\n"
    "ds_read_b64_tr_b16 a[206:207], v251 offset:24776\n"
    "v_add_f32 v241, v241, v193\n"
    "v_mfma_f32_16x16x32_bf16 v[20:23], v[196:199], a[212:215], v[20:23]\n"
    "v_exp_f32 v194, v194\n"
    "v_mfma_f32_16x16x32_bf16 v[24:27], v[196:199], a[240:243], 0\n"
    "ds_read_b64_tr_b16 a[216:217], v254 offset:16512\n"
    "v_add_f32 v241, v241, v194\n"
    "v_mfma_f32_16x16x32_bf16 v[24:27], v[200:203], a[184:187], v[24:27]\n"
    "v_exp_f32 v195, v195\n"
    "v_mfma_f32_16x16x32_bf16 v[28:31], v[200:203], a[188:191], 0\n"
    "ds_read_b64_tr_b16 a[218:219], v254 offset:24768\n"
    "v_add_f32 v241, v241, v195\n"
    "v_mfma_f32_16x16x32_bf16 v[28:31], v[196:199], a[244:247], v[28:31]\n"
    "v_exp_f32 v208, v208\n"
    "v_cvt_pk_bf16_f32 v192, v192, v193\n"
    "v_add_f32 v241, v241, v208\n"
    "v_exp_f32 v209, v209\n"
    "v_cvt_pk_bf16_f32 v193, v194, v195\n"
    "v_add_f32 v241, v241, v209\n"
    "v_exp_f32 v210, v210\n"
    "v_add_f32 v196, v92, v224\n"
    "v_add_f32 v197, v93, v225\n"
    "v_add_f32 v198, v94, v226\n"
    "v_add_f32 v199, v95, v227\n"
    "v_mfma_f32_16x16x32_bf16 v[212:215], a[156:159], a[44:47], v[224:227]\n"
    "v_add_f32 v241, v241, v210\n"
    "v_cvt_pk_bf16_f32 v194, v208, v209\n"
    "v_mfma_f32_16x16x32_bf16 v[212:215], a[152:155], a[40:43], v[212:215]\n"
    "v_exp_f32 v211, v211\n"
    "v_mfma_f32_16x16x32_bf16 v[212:215], a[148:151], a[36:39], v[212:215]\n"
    "v_add_f32 v241, v241, v211\n"
    "v_cvt_pk_bf16_f32 v195, v210, v211\n"
    "v_mfma_f32_16x16x32_bf16 v[212:215], a[144:147], a[32:35], v[212:215]\n"
    "v_exp_f32 v196, v196\n"
    "v_mfma_f32_16x16x32_bf16 v[32:35], v[204:207], a[192:195], 0\n"
    "ds_read_b128 a[128:131], v246 offset:16512\n"
    "v_mov_b32 v242, v196\n"
    "v_mfma_f32_16x16x32_bf16 v[32:35], v[192:195], a[160:163], v[32:35]\n"
    "v_exp_f32 v197, v197\n"
    "v_mfma_f32_16x16x32_bf16 v[36:39], v[192:195], a[164:167], 0\n"
    "ds_read_b128 a[132:135], v246 offset:16768\n"
    "v_add_f32 v242, v242, v197\n"
    "v_mfma_f32_16x16x32_bf16 v[36:39], v[204:207], a[196:199], v[36:39]\n"
    "v_exp_f32 v198, v198\n"
    "v_mfma_f32_16x16x32_bf16 v[40:43], v[204:207], a[200:203], 0\n"
    "ds_read_b128 a[136:139], v246 offset:17024\n"
    "v_add_f32 v242, v242, v198\n"
    "v_mfma_f32_16x16x32_bf16 v[40:43], v[192:195], a[168:171], v[40:43]\n"
    "v_exp_f32 v199, v199\n"
    "v_mfma_f32_16x16x32_bf16 v[44:47], v[192:195], a[172:175], 0\n"
    "ds_read_b128 a[140:143], v246 offset:17280\n"
    "v_add_f32 v242, v242, v199\n"
    "v_mfma_f32_16x16x32_bf16 v[44:47], v[204:207], a[236:239], v[44:47]\n"
    "v_exp_f32 v212, v212\n"
    "v_mfma_f32_16x16x32_bf16 v[200:203], a[96:99], a[32:35], v[224:227]\n"
    "v_add_f32 v242, v242, v212\n"
    "v_cvt_pk_bf16_f32 v196, v196, v197\n"
    "v_mfma_f32_16x16x32_bf16 v[200:203], a[100:103], a[36:39], v[200:203]\n"
    "v_exp_f32 v213, v213\n"
    "v_mfma_f32_16x16x32_bf16 v[200:203], a[104:107], a[40:43], v[200:203]\n"
    "v_add_f32 v242, v242, v213\n"
    "v_cvt_pk_bf16_f32 v197, v198, v199\n"
    "v_mfma_f32_16x16x32_bf16 v[200:203], a[108:111], a[44:47], v[200:203]\n"
    "v_exp_f32 v214, v214\n"
    "v_mfma_f32_16x16x32_bf16 v[208:211], a[124:127], a[44:47], v[224:227]\n"
    "v_add_f32 v242, v242, v214\n"
    "v_cvt_pk_bf16_f32 v198, v212, v213\n"
    "v_mfma_f32_16x16x32_bf16 v[208:211], a[120:123], a[40:43], v[208:211]\n"
    "v_exp_f32 v215, v215\n"
    "v_mfma_f32_16x16x32_bf16 v[208:211], a[116:119], a[36:39], v[208:211]\n"
    "v_add_f32 v242, v242, v215\n"
    "v_cvt_pk_bf16_f32 v199, v214, v215\n"
    "v_mfma_f32_16x16x32_bf16 v[208:211], a[112:115], a[32:35], v[208:211]\n"
    "v_exp_f32 v200, v200\n"
    "v_mfma_f32_16x16x32_bf16 v[48:51], v[204:207], a[208:211], 0\n"
    "ds_read_b128 a[156:159], v246 offset:25536\n"
    "v_add_f32 v242, v242, v200\n"
    "v_mfma_f32_16x16x32_bf16 v[48:51], v[192:195], a[176:179], v[48:51]\n"
    "v_exp_f32 v201, v201\n"
    "v_mfma_f32_16x16x32_bf16 v[52:55], v[192:195], a[180:183], 0\n"
    "ds_read_b128 a[152:155], v246 offset:25280\n"
    "v_add_f32 v242, v242, v201\n"
    "v_mfma_f32_16x16x32_bf16 v[52:55], v[204:207], a[212:215], v[52:55]\n"
    "v_exp_f32 v202, v202\n"
    "v_mfma_f32_16x16x32_bf16 v[56:59], v[204:207], a[240:243], 0\n"
    "ds_read_b128 a[148:151], v246 offset:25024\n"
    "v_add_f32 v242, v242, v202\n"
    "v_mfma_f32_16x16x32_bf16 v[56:59], v[192:195], a[184:187], v[56:59]\n"
    "v_exp_f32 v203, v203\n"
    "v_mfma_f32_16x16x32_bf16 v[60:63], v[192:195], a[188:191], 0\n"
    "ds_read_b128 a[144:147], v246 offset:24768\n"
    "v_add_f32 v242, v242, v203\n"
    "v_mfma_f32_16x16x32_bf16 v[60:63], v[204:207], a[244:247], v[60:63]\n"
    "v_exp_f32 v208, v208\n"
    "v_mfma_f32_16x16x32_bf16 v[204:207], a[96:99], a[48:51], v[228:231]\n"
    "v_add_f32 v242, v242, v208\n"
    "v_cvt_pk_bf16_f32 v200, v200, v201\n"
    "v_mfma_f32_16x16x32_bf16 v[204:207], a[100:103], a[52:55], v[204:207]\n"
    "v_exp_f32 v209, v209\n"
    "v_mfma_f32_16x16x32_bf16 v[204:207], a[104:107], a[56:59], v[204:207]\n"
    "v_add_f32 v242, v242, v209\n"
    "v_cvt_pk_bf16_f32 v201, v202, v203\n"
    "v_mfma_f32_16x16x32_bf16 v[204:207], a[108:111], a[60:63], v[204:207]\n"
    "v_exp_f32 v210, v210\n"
    "v_mfma_f32_16x16x32_bf16 v[212:215], a[124:127], a[60:63], v[228:231]\n"
    "v_add_f32 v242, v242, v210\n"
    "v_cvt_pk_bf16_f32 v202, v208, v209\n"
    "v_mfma_f32_16x16x32_bf16 v[212:215], a[120:123], a[56:59], v[212:215]\n"
    "v_exp_f32 v211, v211\n"
    "v_mfma_f32_16x16x32_bf16 v[212:215], a[116:119], a[52:55], v[212:215]\n"
    "v_add_f32 v242, v242, v211\n"
    "v_cvt_pk_bf16_f32 v203, v210, v211\n"
    "v_mfma_f32_16x16x32_bf16 v[212:215], a[112:115], a[48:51], v[212:215]\n"
    "v_exp_f32 v204, v204\n"
    "v_mfma_f32_16x16x32_bf16 v[64:67], v[196:199], a[192:195], 0\n"
    "ds_read_b64_tr_b16 a[192:193], v247 offset:16512\n"
    "v_add_f32 v243, v243, v204\n"
    "v_mfma_f32_16x16x32_bf16 v[64:67], v[200:203], a[160:163], v[64:67]\n"
    "v_exp_f32 v205, v205\n"
    "v_mfma_f32_16x16x32_bf16 v[68:71], v[200:203], a[164:167], 0\n"
    "ds_read_b64_tr_b16 a[194:195], v247 offset:24768\n"
    "v_add_f32 v243, v243, v205\n"
    "v_mfma_f32_16x16x32_bf16 v[68:71], v[196:199], a[196:199], v[68:71]\n"
    "v_exp_f32 v206, v206\n"
    "v_mfma_f32_16x16x32_bf16 v[72:75], v[196:199], a[200:203], 0\n"
    "ds_read_b64_tr_b16 a[196:197], v247 offset:16520\n"
    "v_add_f32 v243, v243, v206\n"
    "v_mfma_f32_16x16x32_bf16 v[72:75], v[200:203], a[168:171], v[72:75]\n"
    "v_exp_f32 v207, v207\n"
    "v_mfma_f32_16x16x32_bf16 v[76:79], v[200:203], a[172:175], 0\n"
    "ds_read_b64_tr_b16 a[198:199], v247 offset:24776\n"
    "v_add_f32 v243, v243, v207\n"
    "v_mfma_f32_16x16x32_bf16 v[76:79], v[196:199], a[236:239], v[76:79]\n"
    "s_waitcnt lgkmcnt(3)\n"
    "v_exp_f32 v212, v212\n"
    "v_mfma_f32_16x16x32_bf16 v[192:195], a[128:131], a[48:51], v[228:231]\n"
    "v_add_f32 v243, v243, v212\n"
    "v_cvt_pk_bf16_f32 v204, v204, v205\n"
    "v_mfma_f32_16x16x32_bf16 v[192:195], a[132:135], a[52:55], v[192:195]\n"
    "v_exp_f32 v213, v213\n"
    "v_mfma_f32_16x16x32_bf16 v[192:195], a[136:139], a[56:59], v[192:195]\n"
    "v_add_f32 v243, v243, v213\n"
    "v_cvt_pk_bf16_f32 v205, v206, v207\n"
    "v_mfma_f32_16x16x32_bf16 v[192:195], a[140:143], a[60:63], v[192:195]\n"
    "v_exp_f32 v214, v214\n"
    "v_mfma_f32_16x16x32_bf16 v[208:211], a[156:159], a[60:63], v[228:231]\n"
    "v_add_f32 v243, v243, v214\n"
    "v_cvt_pk_bf16_f32 v206, v212, v213\n"
    "v_mfma_f32_16x16x32_bf16 v[208:211], a[152:155], a[56:59], v[208:211]\n"
    "v_exp_f32 v215, v215\n"
    "v_mfma_f32_16x16x32_bf16 v[208:211], a[148:151], a[52:55], v[208:211]\n"
    "v_add_f32 v243, v243, v215\n"
    "v_cvt_pk_bf16_f32 v207, v214, v215\n"
    "v_mfma_f32_16x16x32_bf16 v[208:211], a[144:147], a[48:51], v[208:211]\n"
    "LPG6:\n"
    "s_cbranch_execz LWRET3\n"
    "v_exp_f32 v192, v192\n"
    "v_mfma_f32_16x16x32_bf16 v[80:83], v[196:199], a[208:211], 0\n"
    "ds_read_b64_tr_b16 a[200:201], v251 offset:16512\n"
    "v_add_f32 v243, v243, v192\n"
    "v_mfma_f32_16x16x32_bf16 v[80:83], v[200:203], a[176:179], v[80:83]\n"
    "v_exp_f32 v193, v193\n"
    "v_mfma_f32_16x16x32_bf16 v[84:87], v[200:203], a[180:183], 0\n"
    "ds_read_b64_tr_b16 a[202:203], v251 offset:24768\n"
    "v_add_f32 v243, v243, v193\n"
    "v_mfma_f32_16x16x32_bf16 v[84:87], v[196:199], a[212:215], v[84:87]\n"
    "v_exp_f32 v194, v194\n"
    "v_mfma_f32_16x16x32_bf16 v[88:91], v[196:199], a[240:243], 0\n"
    "ds_read_b64_tr_b16 a[220:221], v254 offset:16520\n"
    "v_add_f32 v243, v243, v194\n"
    "v_mfma_f32_16x16x32_bf16 v[88:91], v[200:203], a[184:187], v[88:91]\n"
    "v_exp_f32 v195, v195\n"
    "v_mfma_f32_16x16x32_bf16 v[92:95], v[200:203], a[188:191], 0\n"
    "ds_read_b64_tr_b16 a[222:223], v254 offset:24776\n"
    "v_add_f32 v243, v243, v195\n"
    "v_mfma_f32_16x16x32_bf16 v[92:95], v[196:199], a[244:247], v[92:95]\n"
    "v_exp_f32 v208, v208\n"
    "v_mfma_f32_16x16x32_bf16 v[196:199], a[96:99], a[64:67], v[232:235]\n"
    "v_add_f32 v243, v243, v208\n"
    "v_cvt_pk_bf16_f32 v192, v192, v193\n"
    "v_mfma_f32_16x16x32_bf16 v[196:199], a[100:103], a[68:71], v[196:199]\n"
    "s_branch LE2_A\n"
    "LTAIL_A:\n"
    "v_exp_f32 v209, v209\n"
    "v_mfma_f32_16x16x32_bf16 v[204:207], a[104:107], a[56:59], v[204:207]\n"
    "v_add_f32 v242, v242, v209\n"
    "v_cvt_pk_bf16_f32 v201, v202, v203\n"
    "v_mfma_f32_16x16x32_bf16 v[204:207], a[108:111], a[60:63], v[204:207]\n"
    "v_exp_f32 v210, v210\n"
    "v_mfma_f32_16x16x32_bf16 v[212:215], a[124:127], a[60:63], v[228:231]\n"
    "v_add_f32 v242, v242, v210\n"
    "v_cvt_pk_bf16_f32 v202, v208, v209\n"
    "v_mfma_f32_16x16x32_bf16 v[212:215], a[120:123], a[56:59], v[212:215]\n"
    "v_exp_f32 v211, v211\n"
    "v_mfma_f32_16x16x32_bf16 v[212:215], a[116:119], a[52:55], v[212:215]\n"
    "v_add_f32 v242, v242, v211\n"
    "v_cvt_pk_bf16_f32 v203, v210, v211\n"
    "v_mfma_f32_16x16x32_bf16 v[212:215], a[112:115], a[48:51], v[212:215]\n"
    "v_exp_f32 v204, v204\n"
    "v_mfma_f32_16x16x32_bf16 v[64:67], v[196:199], a[192:195], v[64:67]\n"
    "ds_read_b64_tr_b16 a[192:193], v247 offset:49536\n"
    "v_add_f32 v243, v243, v204\n"
    "v_mfma_f32_16x16x32_bf16 v[64:67], v[200:203], a[160:163], v[64:67]\n"
    "v_exp_f32 v205, v205\n"
    "v_mfma_f32_16x16x32_bf16 v[68:71], v[200:203], a[164:167], v[68:71]\n"
    "ds_read_b64_tr_b16 a[194:195], v247 offset:57792\n"
    "v_add_f32 v243, v243, v205\n"
    "v_mfma_f32_16x16x32_bf16 v[68:71], v[196:199], a[196:199], v[68:71]\n"
    "v_exp_f32 v206, v206\n"
    "v_mfma_f32_16x16x32_bf16 v[72:75], v[196:199], a[200:203], v[72:75]\n"
    "ds_read_b64_tr_b16 a[196:197], v247 offset:49544\n"
    "v_add_f32 v243, v243, v206\n"
    "v_mfma_f32_16x16x32_bf16 v[72:75], v[200:203], a[168:171], v[72:75]\n"
    "v_exp_f32 v207, v207\n"
    "v_mfma_f32_16x16x32_bf16 v[76:79], v[200:203], a[224:227], v[76:79]\n"
    "ds_read_b64_tr_b16 a[198:199], v247 offset:57800\n"
    "v_add_f32 v243, v243, v207\n"
    "v_mfma_f32_16x16x32_bf16 v[76:79], v[196:199], a[204:207], v[76:79]\n"
    "v_exp_f32 v212, v212\n"
    "v_mfma_f32_16x16x32_bf16 v[192:195], a[96:99], a[64:67], v[232:235]\n"
    "v_add_f32 v243, v243, v212\n"
    "v_cvt_pk_bf16_f32 v204, v204, v205\n"
    "v_mfma_f32_16x16x32_bf16 v[192:195], a[100:103], a[68:71], v[192:195]\n"
    "v_exp_f32 v213, v213\n"
    "v_mfma_f32_16x16x32_bf16 v[192:195], a[104:107], a[72:75], v[192:195]\n"
    "v_add_f32 v243, v243, v213\n"
    "v_cvt_pk_bf16_f32 v205, v206, v207\n"
    "v_mfma_f32_16x16x32_bf16 v[192:195], a[108:111], a[76:79], v[192:195]\n"
    "v_exp_f32 v214, v214\n"
    "v_mfma_f32_16x16x32_bf16 v[208:211], a[124:127], a[76:79], v[232:235]\n"
    "v_add_f32 v243, v243, v214\n"
    "v_cvt_pk_bf16_f32 v206, v212, v213\n"
    "v_mfma_f32_16x16x32_bf16 v[208:211], a[120:123], a[72:75], v[208:211]\n"
    "v_exp_f32 v215, v215\n"
    "v_mfma_f32_16x16x32_bf16 v[208:211], a[116:119], a[68:71], v[208:211]\n"
    "v_add_f32 v243, v243, v215\n"
    "v_cvt_pk_bf16_f32 v207, v214, v215\n"
    "v_mfma_f32_16x16x32_bf16 v[208:211], a[112:115], a[64:67], v[208:211]\n"
    "v_exp_f32 v192, v192\n"
    "v_mfma_f32_16x16x32_bf16 v[80:83], v[196:199], a[208:211], v[80:83]\n"
    "ds_read_b64_tr_b16 a[200:201], v251 offset:49536\n"
    "v_add_f32 v244, v244, v192\n"
    "v_mfma_f32_16x16x32_bf16 v[80:83], v[200:203], a[176:179], v[80:83]\n"
    "ds_read_b64_tr_b16 a[246:247], v254 offset:57800\n"
    "v_exp_f32 v193, v193\n"
    "v_mfma_f32_16x16x32_bf16 v[84:87], v[200:203], a[180:183], v[84:87]\n"
    "ds_read_b64_tr_b16 a[202:203], v251 offset:57792\n"
    "v_add_f32 v244, v244, v193\n"
    "v_mfma_f32_16x16x32_bf16 v[84:87], v[196:199], a[212:215], v[84:87]\n"
    "v_exp_f32 v194, v194\n"
    "v_mfma_f32_16x16x32_bf16 v[88:91], v[196:199], a[216:219], v[88:91]\n"
    "ds_read_b64_tr_b16 a[208:209], v252 offset:49536\n"
    "v_add_f32 v244, v244, v194\n"
    "v_mfma_f32_16x16x32_bf16 v[88:91], v[200:203], a[228:231], v[88:91]\n"
    "v_exp_f32 v195, v195\n"
    "v_mfma_f32_16x16x32_bf16 v[92:95], v[200:203], a[232:235], v[92:95]\n"
    "ds_read_b64_tr_b16 a[210:211], v252 offset:57792\n"
    "v_add_f32 v244, v244, v195\n"
    "v_mfma_f32_16x16x32_bf16 v[92:95], v[196:199], a[220:223], v[92:95]\n"
    "v_exp_f32 v208, v208\n"
    "v_mfma_f32_16x16x32_bf16 v[196:199], a[96:99], a[80:83], v[236:239]\n"
    "v_add_f32 v244, v244, v208\n"
    "v_cvt_pk_bf16_f32 v192, v192, v193\n"
    "v_mfma_f32_16x16x32_bf16 v[196:199], a[100:103], a[84:87], v[196:199]\n"
    "v_exp_f32 v209, v209\n"
    "v_mfma_f32_16x16x32_bf16 v[196:199], a[104:107], a[88:91], v[196:199]\n"
    "ds_read_b64_tr_b16 a[212:213], v252 offset:49544\n"
    "v_add_f32 v244, v244, v209\n"
    "v_cvt_pk_bf16_f32 v193, v194, v195\n"
    "v_mfma_f32_16x16x32_bf16 v[196:199], a[108:111], a[92:95], v[196:199]\n"
    "ds_read_b64_tr_b16 a[214:215], v252 offset:57800\n"
    "v_exp_f32 v210, v210\n"
    "v_mfma_f32_16x16x32_bf16 v[212:215], a[124:127], a[92:95], v[236:239]\n"
    "ds_read_b64_tr_b16 a[244:245], v254 offset:49544\n"
    "v_add_f32 v244, v244, v210\n"
    "v_cvt_pk_bf16_f32 v194, v208, v209\n"
    "v_mfma_f32_16x16x32_bf16 v[212:215], a[120:123], a[88:91], v[212:215]\n"
    "v_exp_f32 v211, v211\n"
    "v_mfma_f32_16x16x32_bf16 v[212:215], a[116:119], a[84:87], v[212:215]\n"
    "s_waitcnt vmcnt(63)\n"
    "v_add_f32 v244, v244, v211\n"
    "v_cvt_pk_bf16_f32 v195, v210, v211\n"
    "v_mfma_f32_16x16x32_bf16 v[212:215], a[112:115], a[80:83], v[212:215]\n"
    "s_barrier\n"
    "v_exp_f32 v196, v196\n"
    "v_mfma_f32_16x16x32_bf16 v[96:99], v[204:207], a[160:163], v[96:99]\n"
    "ds_read_b128 a[96:99], v246 offset:0\n"
    "v_add_f32 v245, v245, v196\n"
    "v_mfma_f32_16x16x32_bf16 v[100:103], v[204:207], a[164:167], v[100:103]\n"
    "ds_read_b64_tr_b16 a[204:205], v251 offset:16520\n"
    "v_exp_f32 v197, v197\n"
    "v_mfma_f32_16x16x32_bf16 v[104:107], v[204:207], a[168:171], v[104:107]\n"
    "ds_read_b128 a[100:103], v246 offset:256\n"
    "v_add_f32 v245, v245, v197\n"
    "v_mfma_f32_16x16x32_bf16 v[108:111], v[204:207], a[224:227], v[108:111]\n"
    "v_exp_f32 v198, v198\n"
    "v_mfma_f32_16x16x32_bf16 v[112:115], v[204:207], a[176:179], v[112:115]\n"
    "ds_read_b128 a[104:107], v246 offset:512\n"
    "v_add_f32 v245, v245, v198\n"
    "v_mfma_f32_16x16x32_bf16 v[116:119], v[204:207], a[180:183], v[116:119]\n"
    "ds_read_b64_tr_b16 a[206:207], v251 offset:24776\n"
    "v_exp_f32 v199, v199\n"
    "v_mfma_f32_16x16x32_bf16 v[120:123], v[204:207], a[228:231], v[120:123]\n"
    "ds_read_b128 a[108:111], v246 offset:768\n"
    "v_add_f32 v245, v245, v199\n"
    "v_mfma_f32_16x16x32_bf16 v[124:127], v[204:207], a[232:235], v[124:127]\n"
    "s_waitcnt lgkmcnt(5)\n"
    "v_exp_f32 v212, v212\n"
    "v_mfma_f32_16x16x32_bf16 v[200:203], a[128:131], a[0:3], v[216:219]\n"
    "v_add_f32 v245, v245, v212\n"
    "v_cvt_pk_bf16_f32 v196, v196, v197\n"
    "v_mfma_f32_16x16x32_bf16 v[200:203], a[132:135], a[4:7], v[200:203]\n"
    "v_exp_f32 v213, v213\n"
    "v_mfma_f32_16x16x32_bf16 v[200:203], a[136:139], a[8:11], v[200:203]\n"
    "ds_read_b64_tr_b16 a[174:175], v251 offset:8264\n"
    "v_add_f32 v245, v245, v213\n"
    "v_cvt_pk_bf16_f32 v197, v198, v199\n"
    "v_mfma_f32_16x16x32_bf16 v[200:203], a[140:143], a[12:15], v[200:203]\n"
    "ds_read_b128 a[140:143], v246 offset:17280\n"
    "v_exp_f32 v214, v214\n"
    "v_mfma_f32_16x16x32_bf16 v[208:211], a[156:159], a[12:15], v[216:219]\n"
    "ds_read_b64_tr_b16 a[218:219], v254 offset:24768\n"
    "v_add_f32 v245, v245, v214\n"
    "v_cvt_pk_bf16_f32 v198, v212, v213\n"
    "v_mfma_f32_16x16x32_bf16 v[208:211], a[152:155], a[8:11], v[208:211]\n"
    "v_exp_f32 v215, v215\n"
    "v_mfma_f32_16x16x32_bf16 v[208:211], a[148:151], a[4:7], v[208:211]\n"
    "v_add_f32 v245, v245, v215\n"
    "v_cvt_pk_bf16_f32 v199, v214, v215\n"
    "v_mfma_f32_16x16x32_bf16 v[208:211], a[144:147], a[0:3], v[208:211]\n"
    "ds_read_b128 a[144:147], v246 offset:24768\n"
    "v_exp_f32 v200, v200\n"
    "v_mfma_f32_16x16x32_bf16 v[128:131], v[192:195], a[160:163], v[128:131]\n"
    "ds_read_b128 a[124:127], v246 offset:9024\n"
    "v_add_f32 v240, v240, v200\n"
    "v_mfma_f32_16x16x32_bf16 v[132:135], v[192:195], a[164:167], v[132:135]\n"
    "v_exp_f32 v201, v201\n"
    "v_mfma_f32_16x16x32_bf16 v[136:139], v[192:195], a[168:171], v[136:139]\n"
    "ds_read_b128 a[120:123], v246 offset:8768\n"
    "v_add_f32 v240, v240, v201\n"
    "v_mfma_f32_16x16x32_bf16 v[140:143], v[192:195], a[224:227], v[140:143]\n"
    "v_exp_f32 v202, v202\n"
    "v_mfma_f32_16x16x32_bf16 v[144:147], v[192:195], a[176:179], v[144:147]\n"
    "ds_read_b128 a[116:119], v246 offset:8512\n"
    "v_add_f32 v240, v240, v202\n"
    "v_mfma_f32_16x16x32_bf16 v[148:151], v[192:195], a[180:183], v[148:151]\n"
    "ds_read_b64_tr_b16 a[220:221], v254 offset:16520\n"
    "v_exp_f32 v203, v203\n"
    "v_mfma_f32_16x16x32_bf16 v[152:155], v[192:195], a[228:231], v[152:155]\n"
    "ds_read_b128 a[112:115], v246 offset:8256\n"
    "v_add_f32 v240, v240, v203\n"
    "v_mfma_f32_16x16x32_bf16 v[156:159], v[192:195], a[232:235], v[156:159]\n"
    "v_exp_f32 v208, v208\n"
    "v_mfma_f32_16x16x32_bf16 v[204:207], a[96:99], a[0:3], v[216:219]\n"
    "s_waitcnt lgkmcnt(4)\n"
    "v_add_f32 v240, v240, v208\n"
    "v_cvt_pk_bf16_f32 v200, v200, v201\n"
    "v_mfma_f32_16x16x32_bf16 v[204:207], a[100:103], a[4:7], v[204:207]\n"
    "v_exp_f32 v209, v209\n"
    "v_mfma_f32_16x16x32_bf16 v[204:207], a[104:107], a[8:11], v[204:207]\n"
    "ds_read_b128 a[152:155], v246 offset:25280\n"
    "v_add_f32 v240, v240, v209\n"
    "v_cvt_pk_bf16_f32 v201, v202, v203\n"
    "v_mfma_f32_16x16x32_bf16 v[204:207], a[108:111], a[12:15], v[204:207]\n"
    "ds_read_b128 a[148:151], v246 offset:25024\n"
    "v_exp_f32 v210, v210\n"
    "v_mfma_f32_16x16x32_bf16 v[212:215], a[124:127], a[12:15], v[216:219]\n"
    "s_waitcnt lgkmcnt(2)\n"
    "ds_read_b64_tr_b16 a[186:187], v254 offset:8256\n"
    "v_add_f32 v240, v240, v210\n"
    "v_cvt_pk_bf16_f32 v202, v208, v209\n"
    "v_mfma_f32_16x16x32_bf16 v[212:215], a[120:123], a[8:11], v[212:215]\n"
    "v_exp_f32 v211, v211\n"
    "v_mfma_f32_16x16x32_bf16 v[212:215], a[116:119], a[4:7], v[212:215]\n"
    "v_add_f32 v240, v240, v211\n"
    "v_cvt_pk_bf16_f32 v203, v210, v211\n"
    "v_mfma_f32_16x16x32_bf16 v[212:215], a[112:115], a[0:3], v[212:215]\n"
    "v_exp_f32 v204, v204\n"
    "v_mfma_f32_16x16x32_bf16 v[160:163], v[196:199], a[160:163], v[160:163]\n"
    "ds_read_b128 a[128:131], v246 offset:16512\n"
    "v_add_f32 v240, v240, v204\n"
    "v_mfma_f32_16x16x32_bf16 v[164:167], v[196:199], a[164:167], v[164:167]\n"
    "ds_read_b64_tr_b16 a[166:167], v247 offset:8264\n"
    "v_exp_f32 v205, v205\n"
    "v_mfma_f32_16x16x32_bf16 v[168:171], v[196:199], a[168:171], v[168:171]\n"
    "ds_read_b128 a[132:135], v246 offset:16768\n"
    "v_add_f32 v240, v240, v205\n"
    "v_mfma_f32_16x16x32_bf16 v[172:175], v[196:199], a[224:227], v[172:175]\n"
    "v_exp_f32 v206, v206\n"
    "v_mfma_f32_16x16x32_bf16 v[176:179], v[196:199], a[176:179], v[176:179]\n"
    "ds_read_b128 a[136:139], v246 offset:17024\n"
    "v_add_f32 v240, v240, v206\n"
    "v_mfma_f32_16x16x32_bf16 v[180:183], v[196:199], a[180:183], v[180:183]\n"
    "ds_read_b64_tr_b16 a[168:169], v251 offset:0\n"
    "v_exp_f32 v207, v207\n"
    "v_mfma_f32_16x16x32_bf16 v[184:187], v[196:199], a[228:231], v[184:187]\n"
    "ds_read_b128 a[156:159], v246 offset:25536\n"
    "v_add_f32 v240, v240, v207\n"
    "v_mfma_f32_16x16x32_bf16 v[188:191], v[196:199], a[232:235], v[188:191]\n"
    "s_waitcnt lgkmcnt(5)\n"
    "v_exp_f32 v212, v212\n"
    "v_mfma_f32_16x16x32_bf16 v[192:195], a[128:131], a[0:3], v[216:219]\n"
    "s_waitcnt lgkmcnt(3)\n"
    "v_add_f32 v240, v240, v212\n"
    "v_cvt_pk_bf16_f32 v204, v204, v205\n"
    "v_mfma_f32_16x16x32_bf16 v[192:195], a[132:135], a[4:7], v[192:195]\n"
    "s_waitcnt lgkmcnt(2)\n"
    "v_exp_f32 v213, v213\n"
    "v_mfma_f32_16x16x32_bf16 v[192:195], a[136:139], a[8:11], v[192:195]\n"
    "ds_read_b64_tr_b16 a[160:161], v247 offset:0\n"
    "v_add_f32 v240, v240, v213\n"
    "v_cvt_pk_bf16_f32 v205, v206, v207\n"
    "v_mfma_f32_16x16x32_bf16 v[192:195], a[140:143], a[12:15], v[192:195]\n"
    "s_waitcnt lgkmcnt(1)\n"
    "ds_read_b64_tr_b16 a[162:163], v247 offset:8256\n"
    "v_exp_f32 v214, v214\n"
    "v_mfma_f32_16x16x32_bf16 v[208:211], a[156:159], a[12:15], v[216:219]\n"
    "ds_read_b64_tr_b16 a[164:165], v247 offset:8\n"
    "v_add_f32 v240, v240, v214\n"
    "v_cvt_pk_bf16_f32 v206, v212, v213\n"
    "v_mfma_f32_16x16x32_bf16 v[208:211], a[152:155], a[8:11], v[208:211]\n"
    "v_exp_f32 v215, v215\n"
    "v_mfma_f32_16x16x32_bf16 v[208:211], a[148:151], a[4:7], v[208:211]\n"
    "ds_read_b64_tr_b16 a[170:171], v251 offset:8256\n"
    "v_add_f32 v240, v240, v215\n"
    "v_cvt_pk_bf16_f32 v207, v214, v215\n"
    "v_mfma_f32_16x16x32_bf16 v[208:211], a[144:147], a[0:3], v[208:211]\n"
    "v_exp_f32 v192, v192\n"
    "v_mfma_f32_16x16x32_bf16 v[0:3], v[200:203], a[192:195], v[0:3]\n"
    "s_waitcnt lgkmcnt(2)\n"
    "ds_read_b64_tr_b16 a[172:173], v251 offset:8\n"
    "v_add_f32 v240, v240, v192\n"
    "v_mfma_f32_16x16x32_bf16 v[0:3], v[204:207], a[160:163], v[0:3]\n"
    "s_waitcnt lgkmcnt(2)\n"
    "v_exp_f32 v193, v193\n"
    "v_mfma_f32_16x16x32_bf16 v[4:7], v[204:207], a[164:167], v[4:7]\n"
    "ds_read_b64_tr_b16 a[176:177], v252 offset:0\n"
    "v_add_f32 v240, v240, v193\n"
    "v_mfma_f32_16x16x32_bf16 v[4:7], v[200:203], a[196:199], v[4:7]\n"
    "v_exp_f32 v194, v194\n"
    "v_mfma_f32_16x16x32_bf16 v[8:11], v[200:203], a[200:203], v[8:11]\n"
    "s_waitcnt lgkmcnt(2)\n"
    "ds_read_b64_tr_b16 a[178:179], v252 offset:8256\n"
    "v_add_f32 v240, v240, v194\n"
    "v_mfma_f32_16x16x32_bf16 v[8:11], v[204:207], a[168:171], v[8:11]\n"
    "s_waitcnt lgkmcnt(2)\n"
    "ds_read_b64_tr_b16 a[222:223], v254 offset:24776\n"
    "v_exp_f32 v195, v195\n"
    "v_mfma_f32_16x16x32_bf16 v[12:15], v[204:207], a[172:175], v[12:15]\n"
    "ds_read_b64_tr_b16 a[180:181], v252 offset:8\n"
    "v_add_f32 v240, v240, v195\n"
    "v_mfma_f32_16x16x32_bf16 v[12:15], v[200:203], a[236:239], v[12:15]\n"
    "v_exp_f32 v208, v208\n"
    "v_mfma_f32_16x16x32_bf16 v[196:199], a[128:131], a[16:19], v[220:223]\n"
    "v_add_f32 v240, v240, v208\n"
    "v_cvt_pk_bf16_f32 v192, v192, v193\n"
    "v_mfma_f32_16x16x32_bf16 v[196:199], a[132:135], a[20:23], v[196:199]\n"
    "v_exp_f32 v209, v209\n"
    "v_mfma_f32_16x16x32_bf16 v[196:199], a[136:139], a[24:27], v[196:199]\n"
    "ds_read_b64_tr_b16 a[182:183], v252 offset:8264\n"
    "v_add_f32 v240, v240, v209\n"
    "v_cvt_pk_bf16_f32 v193, v194, v195\n"
    "v_mfma_f32_16x16x32_bf16 v[196:199], a[140:143], a[28:31], v[196:199]\n"
    "ds_read_b64_tr_b16 a[184:185], v254 offset:0\n"
    "v_exp_f32 v210, v210\n"
    "v_mfma_f32_16x16x32_bf16 v[212:215], a[156:159], a[28:31], v[220:223]\n"
    "ds_read_b64_tr_b16 a[190:191], v254 offset:8264\n"
    "v_add_f32 v240, v240, v210\n"
    "v_cvt_pk_bf16_f32 v194, v208, v209\n"
    "v_mfma_f32_16x16x32_bf16 v[212:215], a[152:155], a[24:27], v[212:215]\n"
    "ds_read_b128 a[132:135], v246 offset:49792\n"
    "v_exp_f32 v211, v211\n"
    "v_mfma_f32_16x16x32_bf16 v[212:215], a[148:151], a[20:23], v[212:215]\n"
    "v_add_f32 v240, v240, v211\n"
    "v_cvt_pk_bf16_f32 v195, v210, v211\n"
    "v_mfma_f32_16x16x32_bf16 v[212:215], a[144:147], a[16:19], v[212:215]\n"
    "ds_read_b128 a[144:147], v246 offset:57792\n"
    "v_exp_f32 v196, v196\n"
    "v_mfma_f32_16x16x32_bf16 v[16:19], v[200:203], a[208:211], v[16:19]\n"
    "s_waitcnt lgkmcnt(4)\n"
    "ds_read_b64_tr_b16 a[188:189], v254 offset:8\n"
    "v_add_f32 v241, v241, v196\n"
    "v_mfma_f32_16x16x32_bf16 v[16:19], v[204:207], a[176:179], v[16:19]\n"
    "ds_read_b64_tr_b16 a[208:209], v252 offset:16512\n"
    "v_exp_f32 v197, v197\n"
    "v_mfma_f32_16x16x32_bf16 v[20:23], v[204:207], a[180:183], v[20:23]\n"
    "ds_read_b64_tr_b16 a[192:193], v247 offset:16512\n"
    "v_add_f32 v241, v241, v197\n"
    "v_and_b32 v248, s76, v240\n"
    "v_mfma_f32_16x16x32_bf16 v[20:23], v[200:203], a[212:215], v[20:23]\n"
    "v_exp_f32 v198, v198\n"
    "v_mfma_f32_16x16x32_bf16 v[24:27], v[200:203], a[240:243], v[24:27]\n"
    "s_waitcnt lgkmcnt(4)\n"
    "ds_read_b64_tr_b16 a[194:195], v247 offset:24768\n"
    "v_add_f32 v241, v241, v198\n"
    "v_sub_f32 v240, v240, v248\n"
    "v_mfma_f32_16x16x32_bf16 v[24:27], v[204:207], a[184:187], v[24:27]\n"
    "s_waitcnt lgkmcnt(3)\n"
    "ds_read_b64_tr_b16 a[210:211], v252 offset:24768\n"
    "v_exp_f32 v199, v199\n"
    "v_mfma_f32_16x16x32_bf16 v[28:31], v[204:207], a[188:191], v[28:31]\n"
    "ds_read_b64_tr_b16 a[196:197], v247 offset:16520\n"
    "v_add_f32 v241, v241, v199\n"
    "v_cvt_pk_bf16_f32 v248, v248, v240\n"
    "v_mfma_f32_16x16x32_bf16 v[28:31], v[200:203], a[244:247], v[28:31]\n"
    "ds_read_b64_tr_b16 a[214:215], v252 offset:24776\n"
    "v_exp_f32 v212, v212\n"
    "v_mfma_f32_16x16x16_bf16 v[216:219], v[248:249], a[252:253], 0\n"
    "v_mfma_f32_16x16x32_bf16 v[200:203], a[96:99], a[16:19], v[220:223]\n"
    "v_add_f32 v241, v241, v212\n"
    "v_cvt_pk_bf16_f32 v196, v196, v197\n"
    "v_mfma_f32_16x16x32_bf16 v[200:203], a[100:103], a[20:23], v[200:203]\n"
    "v_exp_f32 v213, v213\n"
    "v_mfma_f32_16x16x32_bf16 v[200:203], a[104:107], a[24:27], v[200:203]\n"
    "ds_read_b64_tr_b16 a[198:199], v247 offset:24776\n"
    "v_add_f32 v241, v241, v213\n"
    "v_cvt_pk_bf16_f32 v197, v198, v199\n"
    "v_mfma_f32_16x16x32_bf16 v[200:203], a[108:111], a[28:31], v[200:203]\n"
    "ds_read_b64_tr_b16 a[200:201], v251 offset:16512\n"
    "v_exp_f32 v214, v214\n"
    "v_mfma_f32_16x16x32_bf16 v[208:211], a[124:127], a[28:31], v[220:223]\n"
    "ds_read_b64_tr_b16 a[202:203], v251 offset:24768\n"
    "v_add_f32 v241, v241, v214\n"
    "v_cvt_pk_bf16_f32 v198, v212, v213\n"
    "v_mfma_f32_16x16x32_bf16 v[208:211], a[120:123], a[24:27], v[208:211]\n"
    "v_exp_f32 v215, v215\n"
    "v_mfma_f32_16x16x32_bf16 v[208:211], a[116:119], a[20:23], v[208:211]\n"
    "ds_read_b64_tr_b16 a[212:213], v252 offset:16520\n"
    "v_add_f32 v241, v241, v215\n"
    "v_cvt_pk_bf16_f32 v199, v214, v215\n"
    "v_mfma_f32_16x16x32_bf16 v[208:211], a[112:115], a[16:19], v[208:211]\n"
    "s_waitcnt lgkmcnt(4)\n"
    "ds_read_b128 a[148:151], v246 offset:58048\n"
    "v_exp_f32 v200, v200\n"
    "v_mfma_f32_16x16x32_bf16 v[0:3], v[192:195], a[192:195], v[0:3]\n"
    "s_waitcnt lgkmcnt(4)\n"
    "ds_read_b64_tr_b16 a[216:217], v254 offset:16512\n"
    "v_add_f32 v241, v241, v200\n"
    "v_maximum3_f32 v253, v216, v217, v216\n"
    "v_mfma_f32_16x16x32_bf16 v[4:7], v[192:195], a[196:199], v[4:7]\n"
    "s_waitcnt lgkmcnt(3)\n"
    "v_exp_f32 v201, v201\n"
    "v_mfma_f32_16x16x32_bf16 v[8:11], v[192:195], a[200:203], v[8:11]\n"
    "ds_read_b128 a[128:131], v246 offset:49536\n"
    "v_add_f32 v241, v241, v201\n"
    "v_maximum3_f32 v253, v218, v219, v253\n"
    "v_mfma_f32_16x16x32_bf16 v[12:15], v[192:195], a[204:207], v[12:15]\n"
    "v_exp_f32 v202, v202\n"
    "v_mfma_f32_16x16x32_bf16 v[16:19], v[192:195], a[208:211], v[16:19]\n"
    "s_waitcnt lgkmcnt(3)\n"
    "ds_read_b128 a[136:139], v246 offset:50048\n"
    "v_add_f32 v241, v241, v202\n"
    "v_mfma_f32_16x16x32_bf16 v[20:23], v[192:195], a[212:215], v[20:23]\n"
    "s_waitcnt lgkmcnt(2)\n"
    "ds_read_b128 a[140:143], v246 offset:50304\n"
    "v_exp_f32 v203, v203\n"
    "v_mfma_f32_16x16x32_bf16 v[24:27], v[192:195], a[216:219], v[24:27]\n"
    "ds_read_b128 a[156:159], v246 offset:58560\n"
    "v_add_f32 v241, v241, v203\n"
    "v_mfma_f32_16x16x32_bf16 v[28:31], v[192:195], a[220:223], v[28:31]\n"
    "ds_read_b128 a[152:155], v246 offset:58304\n"
    "v_exp_f32 v208, v208\n"
    "v_rcp_f32 v216, v216\n"
    "v_rcp_f32 v217, v217\n"
    "v_rcp_f32 v218, v218\n"
    "v_rcp_f32_e64 v219, v219\n"
    "v_maximum3_f32 v253, v216, v217, v253\n"
    "v_maximum3_f32 v253, v218, v219, v253\n"
    "v_pk_mul_f32 v[0:1], v[0:1], v[216:217]\n"
    "v_pk_mul_f32 v[2:3], v[2:3], v[218:219]\n"
    "v_pk_mul_f32 v[8:9], v[8:9], v[216:217]\n"
    "v_pk_mul_f32 v[10:11], v[10:11], v[218:219]\n"
    "v_cvt_pk_bf16_f32 v0, v0, v8\n"
    "v_cvt_pk_bf16_f32 v2, v2, v10\n"
    "v_cvt_pk_bf16_f32 v8, v1, v9\n"
    "v_cvt_pk_bf16_f32 v10, v3, v11\n"
    "v_pk_mul_f32 v[16:17], v[16:17], v[216:217]\n"
    "v_pk_mul_f32 v[18:19], v[18:19], v[218:219]\n"
    "v_pk_mul_f32 v[24:25], v[24:25], v[216:217]\n"
    "v_pk_mul_f32 v[26:27], v[26:27], v[218:219]\n"
    "v_cvt_pk_bf16_f32 v1, v16, v24\n"
    "v_cvt_pk_bf16_f32 v3, v18, v26\n"
    "v_cvt_pk_bf16_f32 v9, v17, v25\n"
    "v_cvt_pk_bf16_f32 v11, v19, v27\n"
    "v_pk_mul_f32 v[4:5], v[4:5], v[216:217]\n"
    "v_pk_mul_f32 v[6:7], v[6:7], v[218:219]\n"
    "v_pk_mul_f32 v[12:13], v[12:13], v[216:217]\n"
    "v_pk_mul_f32 v[14:15], v[14:15], v[218:219]\n"
    "v_cvt_pk_bf16_f32 v4, v4, v12\n"
    "v_cvt_pk_bf16_f32 v6, v6, v14\n"
    "v_cvt_pk_bf16_f32 v12, v5, v13\n"
    "v_cvt_pk_bf16_f32 v14, v7, v15\n"
    "v_pk_mul_f32 v[20:21], v[20:21], v[216:217]\n"
    "v_pk_mul_f32 v[22:23], v[22:23], v[218:219]\n"
    "v_pk_mul_f32 v[28:29], v[28:29], v[216:217]\n"
    "v_pk_mul_f32 v[30:31], v[30:31], v[218:219]\n"
    "v_mfma_f32_4x4x4_16b_bf16 v[16:19], v[0:1], a[254:255], 0\n"
    "v_cvt_pk_bf16_f32 v5, v20, v28\n"
    "v_cvt_pk_bf16_f32 v7, v22, v30\n"
    "v_mfma_f32_4x4x4_16b_bf16 v[24:27], v[2:3], a[254:255], 0\n"
    "v_cvt_pk_bf16_f32 v13, v21, v29\n"
    "v_cvt_pk_bf16_f32 v15, v23, v31\n"
    "v_mfma_f32_4x4x4_16b_bf16 v[20:23], v[4:5], a[254:255], 0\n"
    "v_cvt_pk_bf16_f32 v16, v16, v17\n"
    "v_cvt_pk_bf16_f32 v17, v18, v19\n"
    "v_mfma_f32_4x4x4_16b_bf16 v[28:31], v[6:7], a[254:255], 0\n"
    "v_cvt_pk_bf16_f32 v24, v24, v25\n"
    "v_cvt_pk_bf16_f32 v25, v26, v27\n"
    "v_mfma_f32_4x4x4_16b_bf16 v[0:3], v[8:9], a[254:255], 0\n"
    "v_cvt_pk_bf16_f32 v18, v20, v21\n"
    "v_cvt_pk_bf16_f32 v19, v22, v23\n"
    "v_mfma_f32_4x4x4_16b_bf16 v[4:7], v[12:13], a[254:255], 0\n"
    "v_cvt_pk_bf16_f32 v26, v28, v29\n"
    "v_cvt_pk_bf16_f32 v27, v30, v31\n"
    "v_mfma_f32_4x4x4_16b_bf16 v[20:23], v[10:11], a[254:255], 0\n"
    "v_cvt_pk_bf16_f32 v0, v0, v1\n"
    "v_cvt_pk_bf16_f32 v1, v2, v3\n"
    "v_mfma_f32_4x4x4_16b_bf16 v[28:31], v[14:15], a[254:255], 0\n"
    "v_cvt_pk_bf16_f32 v2, v4, v5\n"
    "v_cvt_pk_bf16_f32 v3, v6, v7\n"
    "v_cvt_pk_bf16_f32 v20, v20, v21\n"
    "v_cvt_pk_bf16_f32 v21, v22, v23\n"
    "s_nop 0\n"
    "v_cvt_pk_bf16_f32 v22, v28, v29\n"
    "v_cvt_pk_bf16_f32 v23, v30, v31\n"
    "s_waitcnt lgkmcnt(0)\n"
    "v_mfma_f32_16x16x32_bf16 v[204:207], a[128:131], a[16:19], v[220:223]\n"
    "v_add_f32 v241, v241, v208\n"
    "v_cvt_pk_bf16_f32 v200, v200, v201\n"
    "v_mfma_f32_16x16x32_bf16 v[204:207], a[132:135], a[20:23], v[204:207]\n"
    "v_exp_f32 v209, v209\n"
    "v_mfma_f32_16x16x32_bf16 v[204:207], a[136:139], a[24:27], v[204:207]\n"
    "s_add_u32 s67, s66, 24117248\n"
    "v_add_f32 v241, v241, v209\n"
    "v_cvt_pk_bf16_f32 v201, v202, v203\n"
    "v_mfma_f32_16x16x32_bf16 v[204:207], a[140:143], a[28:31], v[204:207]\n"
    "buffer_store_dwordx4 v[16:19], v249, s[56:59], s67 offen offset:0 sc1\n"
    "v_exp_f32 v210, v210\n"
    "v_mfma_f32_16x16x32_bf16 v[212:215], a[156:159], a[28:31], v[220:223]\n"
    "v_add_f32 v241, v241, v210\n"
    "v_cvt_pk_bf16_f32 v202, v208, v209\n"
    "v_mfma_f32_16x16x32_bf16 v[212:215], a[152:155], a[24:27], v[212:215]\n"
    "v_exp_f32 v211, v211\n"
    "v_mfma_f32_16x16x32_bf16 v[212:215], a[148:151], a[20:23], v[212:215]\n"
    "v_add_f32 v241, v241, v211\n"
    "v_cvt_pk_bf16_f32 v203, v210, v211\n"
    "v_mfma_f32_16x16x32_bf16 v[212:215], a[144:147], a[16:19], v[212:215]\n"
    "v_exp_f32 v204, v204\n"
    "v_mfma_f32_16x16x32_bf16 v[32:35], v[196:199], a[192:195], v[32:35]\n"
    "ds_read_b64_tr_b16 a[192:193], v247 offset:49536\n"
    "v_add_f32 v241, v241, v204\n"
    "v_mfma_f32_16x16x32_bf16 v[32:35], v[200:203], a[160:163], v[32:35]\n"
    "v_exp_f32 v205, v205\n"
    "v_mfma_f32_16x16x32_bf16 v[36:39], v[200:203], a[164:167], v[36:39]\n"
    "ds_read_b64_tr_b16 a[194:195], v247 offset:57792\n"
    "v_add_f32 v241, v241, v205\n"
    "v_mfma_f32_16x16x32_bf16 v[36:39], v[196:199], a[196:199], v[36:39]\n"
    "v_exp_f32 v206, v206\n"
    "v_mfma_f32_16x16x32_bf16 v[40:43], v[196:199], a[200:203], v[40:43]\n"
    "ds_read_b64_tr_b16 a[196:197], v247 offset:49544\n"
    "v_add_f32 v241, v241, v206\n"
    "v_mfma_f32_16x16x32_bf16 v[40:43], v[200:203], a[168:171], v[40:43]\n"
    "buffer_store_dwordx4 v[24:27], v249, s[56:59], s67 offen offset:512 sc1\n"
    "v_exp_f32 v207, v207\n"
    "v_mfma_f32_16x16x32_bf16 v[44:47], v[200:203], a[172:175], v[44:47]\n"
    "ds_read_b64_tr_b16 a[198:199], v247 offset:57800\n"
    "v_add_f32 v241, v241, v207\n"
    "v_mfma_f32_16x16x32_bf16 v[44:47], v[196:199], a[204:207], v[44:47]\n"
    "v_exp_f32 v212, v212\n"
    "v_mfma_f32_16x16x32_bf16 v[192:195], a[128:131], a[32:35], v[224:227]\n"
    "v_add_f32 v241, v241, v212\n"
    "v_cvt_pk_bf16_f32 v204, v204, v205\n"
    "v_mfma_f32_16x16x32_bf16 v[192:195], a[132:135], a[36:39], v[192:195]\n"
    "v_exp_f32 v213, v213\n"
    "v_mfma_f32_16x16x32_bf16 v[192:195], a[136:139], a[40:43], v[192:195]\n"
    "v_add_f32 v241, v241, v213\n"
    "v_cvt_pk_bf16_f32 v205, v206, v207\n"
    "v_mfma_f32_16x16x32_bf16 v[192:195], a[140:143], a[44:47], v[192:195]\n"
    "ds_read_b128 a[140:143], v246 offset:17280\n"
    "v_exp_f32 v214, v214\n"
    "v_mfma_f32_16x16x32_bf16 v[208:211], a[156:159], a[44:47], v[224:227]\n"
    "v_add_f32 v241, v241, v214\n"
    "v_cvt_pk_bf16_f32 v206, v212, v213\n"
    "v_mfma_f32_16x16x32_bf16 v[208:211], a[152:155], a[40:43], v[208:211]\n"
    "ds_read_b128 a[152:155], v246 offset:25280\n"
    "v_exp_f32 v215, v215\n"
    "v_mfma_f32_16x16x32_bf16 v[208:211], a[148:151], a[36:39], v[208:211]\n"
    "v_add_f32 v241, v241, v215\n"
    "v_cvt_pk_bf16_f32 v207, v214, v215\n"
    "v_mfma_f32_16x16x32_bf16 v[208:211], a[144:147], a[32:35], v[208:211]\n"
    "v_exp_f32 v192, v192\n"
    "v_mfma_f32_16x16x32_bf16 v[48:51], v[196:199], a[208:211], v[48:51]\n"
    "ds_read_b64_tr_b16 a[200:201], v251 offset:49536\n"
    "v_add_f32 v242, v242, v192\n"
    "v_mfma_f32_16x16x32_bf16 v[48:51], v[200:203], a[176:179], v[48:51]\n"
    "buffer_store_dwordx4 v[0:3], v249, s[56:59], s67 offen offset:256 sc1\n"
    "v_exp_f32 v193, v193\n"
    "v_mfma_f32_16x16x32_bf16 v[52:55], v[200:203], a[180:183], v[52:55]\n"
    "ds_read_b64_tr_b16 a[202:203], v251 offset:57792\n"
    "v_add_f32 v242, v242, v193\n"
    "v_and_b32 v248, s76, v241\n"
    "v_mfma_f32_16x16x32_bf16 v[52:55], v[196:199], a[212:215], v[52:55]\n"
    "v_exp_f32 v194, v194\n"
    "v_mfma_f32_16x16x32_bf16 v[56:59], v[196:199], a[216:219], v[56:59]\n"
    "ds_read_b64_tr_b16 a[208:209], v252 offset:49536\n"
    "v_add_f32 v242, v242, v194\n"
    "v_sub_f32 v241, v241, v248\n"
    "v_mfma_f32_16x16x32_bf16 v[56:59], v[200:203], a[184:187], v[56:59]\n"
    "v_exp_f32 v195, v195\n"
    "v_mfma_f32_16x16x32_bf16 v[60:63], v[200:203], a[188:191], v[60:63]\n"
    "ds_read_b64_tr_b16 a[210:211], v252 offset:57792\n"
    "v_add_f32 v242, v242, v195\n"
    "v_cvt_pk_bf16_f32 v248, v248, v241\n"
    "v_mfma_f32_16x16x32_bf16 v[60:63], v[196:199], a[220:223], v[60:63]\n"
    "v_exp_f32 v208, v208\n"
    "v_mfma_f32_16x16x16_bf16 v[220:223], v[248:249], a[252:253], 0\n"
    "v_mfma_f32_16x16x32_bf16 v[196:199], a[96:99], a[32:35], v[224:227]\n"
    "v_add_f32 v242, v242, v208\n"
    "v_cvt_pk_bf16_f32 v192, v192, v193\n"
    "v_mfma_f32_16x16x32_bf16 v[196:199], a[100:103], a[36:39], v[196:199]\n"
    "v_exp_f32 v209, v209\n"
    "v_mfma_f32_16x16x32_bf16 v[196:199], a[104:107], a[40:43], v[196:199]\n"
    "ds_read_b64_tr_b16 a[212:213], v252 offset:49544\n"
    "v_add_f32 v242, v242, v209\n"
    "v_cvt_pk_bf16_f32 v193, v194, v195\n"
    "v_mfma_f32_16x16x32_bf16 v[196:199], a[108:111], a[44:47], v[196:199]\n"
    "ds_read_b64_tr_b16 a[214:215], v252 offset:57800\n"
    "v_exp_f32 v210, v210\n"
    "v_mfma_f32_16x16x32_bf16 v[212:215], a[124:127], a[44:47], v[224:227]\n"
    "ds_read_b128 a[148:151], v246 offset:25024\n"
    "v_add_f32 v242, v242, v210\n"
    "v_cvt_pk_bf16_f32 v194, v208, v209\n"
    "v_mfma_f32_16x16x32_bf16 v[212:215], a[120:123], a[40:43], v[212:215]\n"
    "buffer_store_dwordx4 v[20:23], v249, s[56:59], s67 offen offset:768 sc1\n"
    "v_exp_f32 v211, v211\n"
    "v_mfma_f32_16x16x32_bf16 v[212:215], a[116:119], a[36:39], v[212:215]\n"
    "v_add_f32 v242, v242, v211\n"
    "v_cvt_pk_bf16_f32 v195, v210, v211\n"
    "v_mfma_f32_16x16x32_bf16 v[212:215], a[112:115], a[32:35], v[212:215]\n"
    "s_waitcnt lgkmcnt(3)\n"
    "v_exp_f32 v196, v196\n"
    "v_mfma_f32_16x16x32_bf16 v[32:35], v[204:207], a[192:195], v[32:35]\n"
    "ds_read_b128 a[128:131], v246 offset:16512\n"
    "v_add_f32 v242, v242, v196\n"
    "v_maximum3_f32 v253, v220, v221, v253\n"
    "v_mfma_f32_16x16x32_bf16 v[36:39], v[204:207], a[196:199], v[36:39]\n"
    "v_exp_f32 v197, v197\n"
    "v_mfma_f32_16x16x32_bf16 v[40:43], v[204:207], a[200:203], v[40:43]\n"
    "ds_read_b128 a[132:135], v246 offset:16768\n"
    "v_add_f32 v242, v242, v197\n"
    "v_maximum3_f32 v253, v222, v223, v253\n"
    "v_mfma_f32_16x16x32_bf16 v[44:47], v[204:207], a[236:239], v[44:47]\n"
    "v_exp_f32 v198, v198\n"
    "v_mfma_f32_16x16x32_bf16 v[48:51], v[204:207], a[208:211], v[48:51]\n"
    "s_waitcnt lgkmcnt(2)\n"
    "ds_read_b128 a[136:139], v246 offset:17024\n"
    "v_add_f32 v242, v242, v198\n"
    "v_mfma_f32_16x16x32_bf16 v[52:55], v[204:207], a[212:215], v[52:55]\n"
    "v_exp_f32 v199, v199\n"
    "v_mfma_f32_16x16x32_bf16 v[56:59], v[204:207], a[240:243], v[56:59]\n"
    "ds_read_b128 a[156:159], v246 offset:25536\n"
    "v_add_f32 v242, v242, v199\n"
    "v_mfma_f32_16x16x32_bf16 v[60:63], v[204:207], a[244:247], v[60:63]\n"
    "ds_read_b128 a[144:147], v246 offset:24768\n"
    "v_exp_f32 v212, v212\n"
    "v_rcp_f32 v220, v220\n"
    "v_rcp_f32 v221, v221\n"
    "v_rcp_f32 v222, v222\n"
    "v_rcp_f32 v223, v223\n"
    "v_maximum3_f32 v253, v220, v221, v253\n"
    "v_maximum3_f32 v253, v222, v223, v253\n"
    "v_pk_mul_f32 v[32:33], v[32:33], v[220:221]\n"
    "v_pk_mul_f32 v[34:35], v[34:35], v[222:223]\n"
    "v_pk_mul_f32 v[40:41], v[40:41], v[220:221]\n"
    "v_pk_mul_f32 v[42:43], v[42:43], v[222:223]\n"
    "v_cvt_pk_bf16_f32 v32, v32, v40\n"
    "v_cvt_pk_bf16_f32 v34, v34, v42\n"
    "v_cvt_pk_bf16_f32 v40, v33, v41\n"
    "v_cvt_pk_bf16_f32 v42, v35, v43\n"
    "v_pk_mul_f32 v[48:49], v[48:49], v[220:221]\n"
    "v_pk_mul_f32 v[50:51], v[50:51], v[222:223]\n"
    "v_pk_mul_f32 v[56:57], v[56:57], v[220:221]\n"
    "v_pk_mul_f32 v[58:59], v[58:59], v[222:223]\n"
    "v_cvt_pk_bf16_f32 v33, v48, v56\n"
    "LPG7:\n"
    "s_cbranch_execz LPG8\n"
    "s_nop 0\n"
    "v_cvt_pk_bf16_f32 v35, v50, v58\n"
    "v_cvt_pk_bf16_f32 v41, v49, v57\n"
    "v_cvt_pk_bf16_f32 v43, v51, v59\n"
    "v_pk_mul_f32 v[36:37], v[36:37], v[220:221]\n"
    "v_pk_mul_f32 v[38:39], v[38:39], v[222:223]\n"
    "v_pk_mul_f32 v[44:45], v[44:45], v[220:221]\n"
    "v_pk_mul_f32 v[46:47], v[46:47], v[222:223]\n"
    "v_cvt_pk_bf16_f32 v36, v36, v44\n"
    "v_cvt_pk_bf16_f32 v38, v38, v46\n"
    "v_cvt_pk_bf16_f32 v44, v37, v45\n"
    "v_cvt_pk_bf16_f32 v46, v39, v47\n"
    "v_pk_mul_f32 v[52:53], v[52:53], v[220:221]\n"
    "v_pk_mul_f32 v[54:55], v[54:55], v[222:223]\n"
    "v_pk_mul_f32 v[60:61], v[60:61], v[220:221]\n"
    "v_pk_mul_f32 v[62:63], v[62:63], v[222:223]\n"
    "v_mfma_f32_4x4x4_16b_bf16 v[48:51], v[32:33], a[254:255], 0\n"
    "v_cvt_pk_bf16_f32 v37, v52, v60\n"
    "v_cvt_pk_bf16_f32 v39, v54, v62\n"
    "v_mfma_f32_4x4x4_16b_bf16 v[56:59], v[34:35], a[254:255], 0\n"
    "v_cvt_pk_bf16_f32 v45, v53, v61\n"
    "v_cvt_pk_bf16_f32 v47, v55, v63\n"
    "v_mfma_f32_4x4x4_16b_bf16 v[52:55], v[36:37], a[254:255], 0\n"
    "v_cvt_pk_bf16_f32 v48, v48, v49\n"
    "v_cvt_pk_bf16_f32 v49, v50, v51\n"
    "v_mfma_f32_4x4x4_16b_bf16 v[60:63], v[38:39], a[254:255], 0\n"
    "v_cvt_pk_bf16_f32 v56, v56, v57\n"
    "v_cvt_pk_bf16_f32 v57, v58, v59\n"
    "v_mfma_f32_4x4x4_16b_bf16 v[32:35], v[40:41], a[254:255], 0\n"
    "v_cvt_pk_bf16_f32 v50, v52, v53\n"
    "v_cvt_pk_bf16_f32 v51, v54, v55\n"
    "v_mfma_f32_4x4x4_16b_bf16 v[36:39], v[44:45], a[254:255], 0\n"
    "v_cvt_pk_bf16_f32 v58, v60, v61\n"
    "v_cvt_pk_bf16_f32 v59, v62, v63\n"
    "v_mfma_f32_4x4x4_16b_bf16 v[52:55], v[42:43], a[254:255], 0\n"
    "v_cvt_pk_bf16_f32 v32, v32, v33\n"
    "v_cvt_pk_bf16_f32 v33, v34, v35\n"
    "v_mfma_f32_4x4x4_16b_bf16 v[60:63], v[46:47], a[254:255], 0\n"
    "v_cvt_pk_bf16_f32 v34, v36, v37\n"
    "v_cvt_pk_bf16_f32 v35, v38, v39\n"
    "v_cvt_pk_bf16_f32 v52, v52, v53\n"
    "v_cvt_pk_bf16_f32 v53, v54, v55\n"
    "s_nop 0\n"
    "v_cvt_pk_bf16_f32 v54, v60, v61\n"
    "v_cvt_pk_bf16_f32 v55, v62, v63\n"
    "s_waitcnt lgkmcnt(0)\n"
    "v_mfma_f32_16x16x32_bf16 v[200:203], a[128:131], a[32:35], v[224:227]\n"
    "v_add_f32 v242, v242, v212\n"
    "v_cvt_pk_bf16_f32 v196, v196, v197\n"
    "v_mfma_f32_16x16x32_bf16 v[200:203], a[132:135], a[36:39], v[200:203]\n"
    "v_exp_f32 v213, v213\n"
    "v_mfma_f32_16x16x32_bf16 v[200:203], a[136:139], a[40:43], v[200:203]\n"
    "s_add_u32 s67, s66, 24121344\n"
    "v_add_f32 v242, v242, v213\n"
    "v_cvt_pk_bf16_f32 v197, v198, v199\n"
    "v_mfma_f32_16x16x32_bf16 v[200:203], a[140:143], a[44:47], v[200:203]\n"
    "buffer_store_dwordx4 v[48:51], v249, s[56:59], s67 offen offset:0 sc1\n"
    "v_exp_f32 v214, v214\n"
    "v_mfma_f32_16x16x32_bf16 v[208:211], a[156:159], a[44:47], v[224:227]\n"
    "v_add_f32 v242, v242, v214\n"
    "v_cvt_pk_bf16_f32 v198, v212, v213\n"
    "v_mfma_f32_16x16x32_bf16 v[208:211], a[152:155], a[40:43], v[208:211]\n"
    "v_exp_f32 v215, v215\n"
    "v_mfma_f32_16x16x32_bf16 v[208:211], a[148:151], a[36:39], v[208:211]\n"
    "v_add_f32 v242, v242, v215\n"
    "v_cvt_pk_bf16_f32 v199, v214, v215\n"
    "v_mfma_f32_16x16x32_bf16 v[208:211], a[144:147], a[32:35], v[208:211]\n"
    "v_exp_f32 v200, v200\n"
    "v_mfma_f32_16x16x32_bf16 v[64:67], v[192:195], a[192:195], v[64:67]\n"
    "ds_read_b64_tr_b16 a[192:193], v247 offset:16512\n"
    "v_add_f32 v242, v242, v200\n"
    "v_mfma_f32_16x16x32_bf16 v[64:67], v[196:199], a[160:163], v[64:67]\n"
    "v_exp_f32 v201, v201\n"
    "v_mfma_f32_16x16x32_bf16 v[68:71], v[196:199], a[164:167], v[68:71]\n"
    "ds_read_b64_tr_b16 a[194:195], v247 offset:24768\n"
    "v_add_f32 v242, v242, v201\n"
    "v_mfma_f32_16x16x32_bf16 v[68:71], v[192:195], a[196:199], v[68:71]\n"
    "v_exp_f32 v202, v202\n"
    "v_mfma_f32_16x16x32_bf16 v[72:75], v[192:195], a[200:203], v[72:75]\n"
    "ds_read_b64_tr_b16 a[196:197], v247 offset:16520\n"
    "v_add_f32 v242, v242, v202\n"
    "v_mfma_f32_16x16x32_bf16 v[72:75], v[196:199], a[168:171], v[72:75]\n"
    "buffer_store_dwordx4 v[56:59], v249, s[56:59], s67 offen offset:512 sc1\n"
    "v_exp_f32 v203, v203\n"
    "v_mfma_f32_16x16x32_bf16 v[76:79], v[196:199], a[172:175], v[76:79]\n"
    "ds_read_b64_tr_b16 a[198:199], v247 offset:24776\n"
    "v_add_f32 v242, v242, v203\n"
    "v_mfma_f32_16x16x32_bf16 v[76:79], v[192:195], a[236:239], v[76:79]\n"
    "v_exp_f32 v208, v208\n"
    "v_mfma_f32_16x16x32_bf16 v[204:207], a[128:131], a[48:51], v[228:231]\n"
    "v_add_f32 v242, v242, v208\n"
    "v_cvt_pk_bf16_f32 v200, v200, v201\n"
    "v_mfma_f32_16x16x32_bf16 v[204:207], a[132:135], a[52:55], v[204:207]\n"
    "v_exp_f32 v209, v209\n"
    "v_mfma_f32_16x16x32_bf16 v[204:207], a[136:139], a[56:59], v[204:207]\n"
    "v_add_f32 v242, v242, v209\n"
    "v_cvt_pk_bf16_f32 v201, v202, v203\n"
    "v_mfma_f32_16x16x32_bf16 v[204:207], a[140:143], a[60:63], v[204:207]\n"
    "ds_read_b128 a[140:143], v246 offset:50304\n"
    "v_exp_f32 v210, v210\n"
    "v_mfma_f32_16x16x32_bf16 v[212:215], a[156:159], a[60:63], v[228:231]\n"
    "v_add_f32 v242, v242, v210\n"
    "v_cvt_pk_bf16_f32 v202, v208, v209\n"
    "v_mfma_f32_16x16x32_bf16 v[212:215], a[152:155], a[56:59], v[212:215]\n"
    "ds_read_b128 a[152:155], v246 offset:58304\n"
    "v_exp_f32 v211, v211\n"
    "v_mfma_f32_16x16x32_bf16 v[212:215], a[148:151], a[52:55], v[212:215]\n"
    "v_add_f32 v242, v242, v211\n"
    "v_cvt_pk_bf16_f32 v203, v210, v211\n"
    "v_mfma_f32_16x16x32_bf16 v[212:215], a[144:147], a[48:51], v[212:215]\n"
    "v_exp_f32 v204, v204\n"
    "v_mfma_f32_16x16x32_bf16 v[80:83], v[192:195], a[208:211], v[80:83]\n"
    "ds_read_b64_tr_b16 a[200:201], v251 offset:16512\n"
    "v_add_f32 v243, v243, v204\n"
    "v_mfma_f32_16x16x32_bf16 v[80:83], v[196:199], a[176:179], v[80:83]\n"
    "buffer_store_dwordx4 v[32:35], v249, s[56:59], s67 offen offset:256 sc1\n"
    "v_exp_f32 v205, v205\n"
    "v_mfma_f32_16x16x32_bf16 v[84:87], v[196:199], a[180:183], v[84:87]\n"
    "ds_read_b64_tr_b16 a[202:203], v251 offset:24768\n"
    "v_add_f32 v243, v243, v205\n"
    "v_and_b32 v248, s76, v242\n"
    "v_mfma_f32_16x16x32_bf16 v[84:87], v[192:195], a[212:215], v[84:87]\n"
    "v_exp_f32 v206, v206\n"
    "v_mfma_f32_16x16x32_bf16 v[88:91], v[192:195], a[240:243], v[88:91]\n"
    "ds_read_b64_tr_b16 a[208:209], v252 offset:16512\n"
    "v_add_f32 v243, v243, v206\n"
    "v_sub_f32 v242, v242, v248\n"
    "v_mfma_f32_16x16x32_bf16 v[88:91], v[196:199], a[184:187], v[88:91]\n"
    "v_exp_f32 v207, v207\n"
    "v_mfma_f32_16x16x32_bf16 v[92:95], v[196:199], a[188:191], v[92:95]\n"
    "ds_read_b64_tr_b16 a[210:211], v252 offset:24768\n"
    "v_add_f32 v243, v243, v207\n"
    "v_cvt_pk_bf16_f32 v248, v248, v242\n"
    "v_mfma_f32_16x16x32_bf16 v[92:95], v[192:195], a[244:247], v[92:95]\n"
    "v_exp_f32 v212, v212\n"
    "v_mfma_f32_16x16x16_bf16 v[224:227], v[248:249], a[252:253], 0\n"
    "v_mfma_f32_16x16x32_bf16 v[192:195], a[96:99], a[48:51], v[228:231]\n"
    "v_add_f32 v243, v243, v212\n"
    "v_cvt_pk_bf16_f32 v204, v204, v205\n"
    "v_mfma_f32_16x16x32_bf16 v[192:195], a[100:103], a[52:55], v[192:195]\n"
    "v_exp_f32 v213, v213\n"
    "v_mfma_f32_16x16x32_bf16 v[192:195], a[104:107], a[56:59], v[192:195]\n"
    "ds_read_b64_tr_b16 a[212:213], v252 offset:16520\n"
    "v_add_f32 v243, v243, v213\n"
    "v_cvt_pk_bf16_f32 v205, v206, v207\n"
    "v_mfma_f32_16x16x32_bf16 v[192:195], a[108:111], a[60:63], v[192:195]\n"
    "ds_read_b64_tr_b16 a[214:215], v252 offset:24776\n"
    "v_exp_f32 v214, v214\n"
    "v_mfma_f32_16x16x32_bf16 v[208:211], a[124:127], a[60:63], v[228:231]\n"
    "ds_read_b128 a[148:151], v246 offset:58048\n"
    "v_add_f32 v243, v243, v214\n"
    "v_cvt_pk_bf16_f32 v206, v212, v213\n"
    "v_mfma_f32_16x16x32_bf16 v[208:211], a[120:123], a[56:59], v[208:211]\n"
    "buffer_store_dwordx4 v[52:55], v249, s[56:59], s67 offen offset:768 sc1\n"
    "v_exp_f32 v215, v215\n"
    "v_mfma_f32_16x16x32_bf16 v[208:211], a[116:119], a[52:55], v[208:211]\n"
    "v_add_f32 v243, v243, v215\n"
    "v_cvt_pk_bf16_f32 v207, v214, v215\n"
    "v_mfma_f32_16x16x32_bf16 v[208:211], a[112:115], a[48:51], v[208:211]\n"
    "s_waitcnt lgkmcnt(3)\n"
    "v_exp_f32 v192, v192\n"
    "v_mfma_f32_16x16x32_bf16 v[64:67], v[200:203], a[192:195], v[64:67]\n"
    "ds_read_b128 a[128:131], v246 offset:49536\n"
    "v_add_f32 v243, v243, v192\n"
    "v_maximum3_f32 v253, v224, v225, v253\n"
    "v_mfma_f32_16x16x32_bf16 v[68:71], v[200:203], a[196:199], v[68:71]\n"
    "v_exp_f32 v193, v193\n"
    "v_mfma_f32_16x16x32_bf16 v[72:75], v[200:203], a[200:203], v[72:75]\n"
    "ds_read_b128 a[132:135], v246 offset:49792\n"
    "v_add_f32 v243, v243, v193\n"
    "v_maximum3_f32 v253, v226, v227, v253\n"
    "v_mfma_f32_16x16x32_bf16 v[76:79], v[200:203], a[204:207], v[76:79]\n"
    "v_exp_f32 v194, v194\n"
    "v_mfma_f32_16x16x32_bf16 v[80:83], v[200:203], a[208:211], v[80:83]\n"
    "s_waitcnt lgkmcnt(2)\n"
    "ds_read_b128 a[136:139], v246 offset:50048\n"
    "v_add_f32 v243, v243, v194\n"
    "v_mfma_f32_16x16x32_bf16 v[84:87], v[200:203], a[212:215], v[84:87]\n"
    "v_exp_f32 v195, v195\n"
    "v_mfma_f32_16x16x32_bf16 v[88:91], v[200:203], a[216:219], v[88:91]\n"
    "ds_read_b128 a[156:159], v246 offset:58560\n"
    "v_add_f32 v243, v243, v195\n"
    "v_mfma_f32_16x16x32_bf16 v[92:95], v[200:203], a[220:223], v[92:95]\n"
    "ds_read_b128 a[144:147], v246 offset:57792\n"
    "v_exp_f32 v208, v208\n"
    "v_rcp_f32 v224, v224\n"
    "v_rcp_f32 v225, v225\n"
    "v_rcp_f32 v226, v226\n"
    "v_rcp_f32 v227, v227\n"
    "v_maximum3_f32 v253, v224, v225, v253\n"
    "v_maximum3_f32 v253, v226, v227, v253\n"
    "v_pk_mul_f32 v[64:65], v[64:65], v[224:225]\n"
    "v_pk_mul_f32 v[66:67], v[66:67], v[226:227]\n"
    "v_pk_mul_f32 v[72:73], v[72:73], v[224:225]\n"
    "v_pk_mul_f32 v[74:75], v[74:75], v[226:227]\n"
    "v_cvt_pk_bf16_f32 v64, v64, v72\n"
    "v_cvt_pk_bf16_f32 v66, v66, v74\n"
    "v_cvt_pk_bf16_f32 v72, v65, v73\n"
    "v_cvt_pk_bf16_f32 v74, v67, v75\n"
    "v_pk_mul_f32 v[80:81], v[80:81], v[224:225]\n"
    "v_pk_mul_f32 v[82:83], v[82:83], v[226:227]\n"
    "v_pk_mul_f32 v[88:89], v[88:89], v[224:225]\n"
    "v_pk_mul_f32 v[90:91], v[90:91], v[226:227]\n"
    "v_cvt_pk_bf16_f32 v65, v80, v88\n"
    "v_cvt_pk_bf16_f32 v67, v82, v90\n"
    "v_cvt_pk_bf16_f32 v73, v81, v89\n"
    "v_cvt_pk_bf16_f32 v75, v83, v91\n"
    "v_pk_mul_f32 v[68:69], v[68:69], v[224:225]\n"
    "v_pk_mul_f32 v[70:71], v[70:71], v[226:227]\n"
    "v_pk_mul_f32 v[76:77], v[76:77], v[224:225]\n"
    "v_pk_mul_f32 v[78:79], v[78:79], v[226:227]\n"
    "v_cvt_pk_bf16_f32 v68, v68, v76\n"
    "v_cvt_pk_bf16_f32 v70, v70, v78\n"
    "v_cvt_pk_bf16_f32 v76, v69, v77\n"
    "v_cvt_pk_bf16_f32 v78, v71, v79\n"
    "v_pk_mul_f32 v[84:85], v[84:85], v[224:225]\n"
    "v_pk_mul_f32 v[86:87], v[86:87], v[226:227]\n"
    "v_pk_mul_f32 v[92:93], v[92:93], v[224:225]\n"
    "v_pk_mul_f32 v[94:95], v[94:95], v[226:227]\n"
    "v_mfma_f32_4x4x4_16b_bf16 v[80:83], v[64:65], a[254:255], 0\n"
    "v_cvt_pk_bf16_f32 v69, v84, v92\n"
    "v_cvt_pk_bf16_f32 v71, v86, v94\n"
    "v_mfma_f32_4x4x4_16b_bf16 v[88:91], v[66:67], a[254:255], 0\n"
    "v_cvt_pk_bf16_f32 v77, v85, v93\n"
    "v_cvt_pk_bf16_f32 v79, v87, v95\n"
    "v_mfma_f32_4x4x4_16b_bf16 v[84:87], v[68:69], a[254:255], 0\n"
    "v_cvt_pk_bf16_f32 v80, v80, v81\n"
    "v_cvt_pk_bf16_f32 v81, v82, v83\n"
    "v_mfma_f32_4x4x4_16b_bf16 v[92:95], v[70:71], a[254:255], 0\n"
    "v_cvt_pk_bf16_f32 v88, v88, v89\n"
    "v_cvt_pk_bf16_f32 v89, v90, v91\n"
    "v_mfma_f32_4x4x4_16b_bf16 v[64:67], v[72:73], a[254:255], 0\n"
    "v_cvt_pk_bf16_f32 v82, v84, v85\n"
    "v_cvt_pk_bf16_f32 v83, v86, v87\n"
    "v_mfma_f32_4x4x4_16b_bf16 v[68:71], v[76:77], a[254:255], 0\n"
    "v_cvt_pk_bf16_f32 v90, v92, v93\n"
    "v_cvt_pk_bf16_f32 v91, v94, v95\n"
    "v_mfma_f32_4x4x4_16b_bf16 v[84:87], v[74:75], a[254:255], 0\n"
    "v_cvt_pk_bf16_f32 v64, v64, v65\n"
    "v_cvt_pk_bf16_f32 v65, v66, v67\n"
    "v_mfma_f32_4x4x4_16b_bf16 v[92:95], v[78:79], a[254:255], 0\n"
    "v_cvt_pk_bf16_f32 v66, v68, v69\n"
    "v_cvt_pk_bf16_f32 v67, v70, v71\n"
    "v_cvt_pk_bf16_f32 v84, v84, v85\n"
    "v_cvt_pk_bf16_f32 v85, v86, v87\n"
    "s_nop 0\n"
    "v_cvt_pk_bf16_f32 v86, v92, v93\n"
    "v_cvt_pk_bf16_f32 v87, v94, v95\n"
    "s_waitcnt lgkmcnt(0)\n"
    "v_mfma_f32_16x16x32_bf16 v[196:199], a[128:131], a[48:51], v[228:231]\n"
    "v_add_f32 v243, v243, v208\n"
    "v_cvt_pk_bf16_f32 v192, v192, v193\n"
    "v_mfma_f32_16x16x32_bf16 v[196:199], a[132:135], a[52:55], v[196:199]\n"
    "v_exp_f32 v209, v209\n"
    "v_mfma_f32_16x16x32_bf16 v[196:199], a[136:139], a[56:59], v[196:199]\n"
    "s_add_u32 s67, s66, 24125440\n"
    "v_add_f32 v243, v243, v209\n"
    "v_cvt_pk_bf16_f32 v193, v194, v195\n"
    "v_mfma_f32_16x16x32_bf16 v[196:199], a[140:143], a[60:63], v[196:199]\n"
    "buffer_store_dwordx4 v[80:83], v249, s[56:59], s67 offen offset:0 sc1\n"
    "v_exp_f32 v210, v210\n"
    "v_mfma_f32_16x16x32_bf16 v[212:215], a[156:159], a[60:63], v[228:231]\n"
    "v_add_f32 v243, v243, v210\n"
    "v_cvt_pk_bf16_f32 v194, v208, v209\n"
    "v_mfma_f32_16x16x32_bf16 v[212:215], a[152:155], a[56:59], v[212:215]\n"
    "v_exp_f32 v211, v211\n"
    "v_mfma_f32_16x16x32_bf16 v[212:215], a[148:151], a[52:55], v[212:215]\n"
    "v_add_f32 v243, v243, v211\n"
    "v_cvt_pk_bf16_f32 v195, v210, v211\n"
    "v_mfma_f32_16x16x32_bf16 v[212:215], a[144:147], a[48:51], v[212:215]\n"
    "v_exp_f32 v196, v196\n"
    "v_mfma_f32_16x16x32_bf16 v[96:99], v[204:207], a[192:195], v[96:99]\n"
    "ds_read_b64_tr_b16 a[192:193], v247 offset:49536\n"
    "v_add_f32 v243, v243, v196\n"
    "v_mfma_f32_16x16x32_bf16 v[96:99], v[192:195], a[160:163], v[96:99]\n"
    "v_exp_f32 v197, v197\n"
    "v_mfma_f32_16x16x32_bf16 v[100:103], v[192:195], a[164:167], v[100:103]\n"
    "ds_read_b64_tr_b16 a[194:195], v247 offset:57792\n"
    "v_add_f32 v243, v243, v197\n"
    "v_mfma_f32_16x16x32_bf16 v[100:103], v[204:207], a[196:199], v[100:103]\n"
    "v_exp_f32 v198, v198\n"
    "v_mfma_f32_16x16x32_bf16 v[104:107], v[204:207], a[200:203], v[104:107]\n"
    "ds_read_b64_tr_b16 a[196:197], v247 offset:49544\n"
    "v_add_f32 v243, v243, v198\n"
    "v_mfma_f32_16x16x32_bf16 v[104:107], v[192:195], a[168:171], v[104:107]\n"
    "buffer_store_dwordx4 v[88:91], v249, s[56:59], s67 offen offset:512 sc1\n"
    "v_exp_f32 v199, v199\n"
    "v_mfma_f32_16x16x32_bf16 v[108:111], v[192:195], a[172:175], v[108:111]\n"
    "ds_read_b64_tr_b16 a[198:199], v247 offset:57800\n"
    "v_add_f32 v243, v243, v199\n"
    "v_mfma_f32_16x16x32_bf16 v[108:111], v[204:207], a[204:207], v[108:111]\n"
    "v_exp_f32 v212, v212\n"
    "v_mfma_f32_16x16x32_bf16 v[200:203], a[128:131], a[64:67], v[232:235]\n"
    "v_add_f32 v243, v243, v212\n"
    "v_cvt_pk_bf16_f32 v196, v196, v197\n"
    "v_mfma_f32_16x16x32_bf16 v[200:203], a[132:135], a[68:71], v[200:203]\n"
    "v_exp_f32 v213, v213\n"
    "v_mfma_f32_16x16x32_bf16 v[200:203], a[136:139], a[72:75], v[200:203]\n"
    "v_add_f32 v243, v243, v213\n"
    "v_cvt_pk_bf16_f32 v197, v198, v199\n"
    "v_mfma_f32_16x16x32_bf16 v[200:203], a[140:143], a[76:79], v[200:203]\n"
    "ds_read_b128 a[140:143], v246 offset:17280\n"
    "v_exp_f32 v214, v214\n"
    "v_mfma_f32_16x16x32_bf16 v[208:211], a[156:159], a[76:79], v[232:235]\n"
    "v_add_f32 v243, v243, v214\n"
    "v_cvt_pk_bf16_f32 v198, v212, v213\n"
    "v_mfma_f32_16x16x32_bf16 v[208:211], a[152:155], a[72:75], v[208:211]\n"
    "ds_read_b128 a[152:155], v246 offset:25280\n"
    "v_exp_f32 v215, v215\n"
    "v_mfma_f32_16x16x32_bf16 v[208:211], a[148:151], a[68:71], v[208:211]\n"
    "v_add_f32 v243, v243, v215\n"
    "v_cvt_pk_bf16_f32 v199, v214, v215\n"
    "v_mfma_f32_16x16x32_bf16 v[208:211], a[144:147], a[64:67], v[208:211]\n"
    "v_exp_f32 v200, v200\n"
    "v_mfma_f32_16x16x32_bf16 v[112:115], v[204:207], a[208:211], v[112:115]\n"
    "ds_read_b64_tr_b16 a[200:201], v251 offset:49536\n"
    "v_add_f32 v244, v244, v200\n"
    "v_mfma_f32_16x16x32_bf16 v[112:115], v[192:195], a[176:179], v[112:115]\n"
    "buffer_store_dwordx4 v[64:67], v249, s[56:59], s67 offen offset:256 sc1\n"
    "v_exp_f32 v201, v201\n"
    "v_mfma_f32_16x16x32_bf16 v[116:119], v[192:195], a[180:183], v[116:119]\n"
    "ds_read_b64_tr_b16 a[202:203], v251 offset:57792\n"
    "v_add_f32 v244, v244, v201\n"
    "v_and_b32 v248, s76, v243\n"
    "v_mfma_f32_16x16x32_bf16 v[116:119], v[204:207], a[212:215], v[116:119]\n"
    "v_exp_f32 v202, v202\n"
    "v_mfma_f32_16x16x32_bf16 v[120:123], v[204:207], a[216:219], v[120:123]\n"
    "ds_read_b64_tr_b16 a[208:209], v252 offset:49536\n"
    "v_add_f32 v244, v244, v202\n"
    "v_sub_f32 v243, v243, v248\n"
    "v_mfma_f32_16x16x32_bf16 v[120:123], v[192:195], a[184:187], v[120:123]\n"
    "v_exp_f32 v203, v203\n"
    "v_mfma_f32_16x16x32_bf16 v[124:127], v[192:195], a[188:191], v[124:127]\n"
    "ds_read_b64_tr_b16 a[210:211], v252 offset:57792\n"
    "v_add_f32 v244, v244, v203\n"
    "v_cvt_pk_bf16_f32 v248, v248, v243\n"
    "v_mfma_f32_16x16x32_bf16 v[124:127], v[204:207], a[220:223], v[124:127]\n"
    "v_exp_f32 v208, v208\n"
    "v_mfma_f32_16x16x16_bf16 v[228:231], v[248:249], a[252:253], 0\n"
    "v_mfma_f32_16x16x32_bf16 v[204:207], a[96:99], a[64:67], v[232:235]\n"
    "v_add_f32 v244, v244, v208\n"
    "v_cvt_pk_bf16_f32 v200, v200, v201\n"
    "v_mfma_f32_16x16x32_bf16 v[204:207], a[100:103], a[68:71], v[204:207]\n"
    "v_exp_f32 v209, v209\n"
    "v_mfma_f32_16x16x32_bf16 v[204:207], a[104:107], a[72:75], v[204:207]\n"
    "ds_read_b64_tr_b16 a[212:213], v252 offset:49544\n"
    "v_add_f32 v244, v244, v209\n"
    "v_cvt_pk_bf16_f32 v201, v202, v203\n"
    "v_mfma_f32_16x16x32_bf16 v[204:207], a[108:111], a[76:79], v[204:207]\n"
    "ds_read_b64_tr_b16 a[214:215], v252 offset:57800\n"
    "v_exp_f32 v210, v210\n"
    "v_mfma_f32_16x16x32_bf16 v[212:215], a[124:127], a[76:79], v[232:235]\n"
    "ds_read_b128 a[148:151], v246 offset:25024\n"
    "v_add_f32 v244, v244, v210\n"
    "v_cvt_pk_bf16_f32 v202, v208, v209\n"
    "v_mfma_f32_16x16x32_bf16 v[212:215], a[120:123], a[72:75], v[212:215]\n"
    "buffer_store_dwordx4 v[84:87], v249, s[56:59], s67 offen offset:768 sc1\n"
    "v_exp_f32 v211, v211\n"
    "v_mfma_f32_16x16x32_bf16 v[212:215], a[116:119], a[68:71], v[212:215]\n"
    "v_add_f32 v244, v244, v211\n"
    "v_cvt_pk_bf16_f32 v203, v210, v211\n"
    "v_mfma_f32_16x16x32_bf16 v[212:215], a[112:115], a[64:67], v[212:215]\n"
    "s_waitcnt lgkmcnt(3)\n"
    "v_exp_f32 v204, v204\n"
    "v_mfma_f32_16x16x32_bf16 v[96:99], v[196:199], a[192:195], v[96:99]\n"
    "ds_read_b128 a[128:131], v246 offset:16512\n"
    "v_add_f32 v244, v244, v204\n"
    "v_maximum3_f32 v253, v228, v229, v253\n"
    "v_mfma_f32_16x16x32_bf16 v[100:103], v[196:199], a[196:199], v[100:103]\n"
    "v_exp_f32 v205, v205\n"
    "v_mfma_f32_16x16x32_bf16 v[104:107], v[196:199], a[200:203], v[104:107]\n"
    "ds_read_b128 a[132:135], v246 offset:16768\n"
    "v_add_f32 v244, v244, v205\n"
    "v_maximum3_f32 v253, v230, v231, v253\n"
    "v_mfma_f32_16x16x32_bf16 v[108:111], v[196:199], a[236:239], v[108:111]\n"
    "v_exp_f32 v206, v206\n"
    "v_mfma_f32_16x16x32_bf16 v[112:115], v[196:199], a[208:211], v[112:115]\n"
    "s_waitcnt lgkmcnt(2)\n"
    "ds_read_b128 a[136:139], v246 offset:17024\n"
    "v_add_f32 v244, v244, v206\n"
    "v_mfma_f32_16x16x32_bf16 v[116:119], v[196:199], a[212:215], v[116:119]\n"
    "v_exp_f32 v207, v207\n"
    "v_mfma_f32_16x16x32_bf16 v[120:123], v[196:199], a[240:243], v[120:123]\n"
    "ds_read_b128 a[156:159], v246 offset:25536\n"
    "v_add_f32 v244, v244, v207\n"
    "v_mfma_f32_16x16x32_bf16 v[124:127], v[196:199], a[244:247], v[124:127]\n"
    "ds_read_b128 a[144:147], v246 offset:24768\n"
    "v_exp_f32 v212, v212\n"
    "v_rcp_f32 v228, v228\n"
    "v_rcp_f32 v229, v229\n"
    "v_rcp_f32 v230, v230\n"
    "v_rcp_f32 v231, v231\n"
    "v_maximum3_f32 v253, v228, v229, v253\n"
    "v_maximum3_f32 v253, v230, v231, v253\n"
    "v_pk_mul_f32 v[96:97], v[96:97], v[228:229]\n"
    "v_pk_mul_f32 v[98:99], v[98:99], v[230:231]\n"
    "v_pk_mul_f32 v[104:105], v[104:105], v[228:229]\n"
    "v_pk_mul_f32 v[106:107], v[106:107], v[230:231]\n"
    "v_cvt_pk_bf16_f32 v96, v96, v104\n"
    "v_cvt_pk_bf16_f32 v98, v98, v106\n"
    "v_cvt_pk_bf16_f32 v104, v97, v105\n"
    "v_cvt_pk_bf16_f32 v106, v99, v107\n"
    "v_pk_mul_f32 v[112:113], v[112:113], v[228:229]\n"
    "v_pk_mul_f32 v[114:115], v[114:115], v[230:231]\n"
    "v_pk_mul_f32 v[120:121], v[120:121], v[228:229]\n"
    "v_pk_mul_f32 v[122:123], v[122:123], v[230:231]\n"
    "v_cvt_pk_bf16_f32 v97, v112, v120\n"
    "v_cvt_pk_bf16_f32 v99, v114, v122\n"
    "v_cvt_pk_bf16_f32 v105, v113, v121\n"
    "v_cvt_pk_bf16_f32 v107, v115, v123\n"
    "v_pk_mul_f32 v[100:101], v[100:101], v[228:229]\n"
    "v_pk_mul_f32 v[102:103], v[102:103], v[230:231]\n"
    "v_pk_mul_f32 v[108:109], v[108:109], v[228:229]\n"
    "v_pk_mul_f32 v[110:111], v[110:111], v[230:231]\n"
    "v_cvt_pk_bf16_f32 v100, v100, v108\n"
    "v_cvt_pk_bf16_f32 v102, v102, v110\n"
    "v_cvt_pk_bf16_f32 v108, v101, v109\n"
    "v_cvt_pk_bf16_f32 v110, v103, v111\n"
    "v_pk_mul_f32 v[116:117], v[116:117], v[228:229]\n"
    "v_pk_mul_f32 v[118:119], v[118:119], v[230:231]\n"
    "v_pk_mul_f32 v[124:125], v[124:125], v[228:229]\n"
    "v_pk_mul_f32 v[126:127], v[126:127], v[230:231]\n"
    "v_mfma_f32_4x4x4_16b_bf16 v[112:115], v[96:97], a[254:255], 0\n"
    "v_cvt_pk_bf16_f32 v101, v116, v124\n"
    "v_cvt_pk_bf16_f32 v103, v118, v126\n"
    "v_mfma_f32_4x4x4_16b_bf16 v[120:123], v[98:99], a[254:255], 0\n"
    "v_cvt_pk_bf16_f32 v109, v117, v125\n"
    "v_cvt_pk_bf16_f32 v111, v119, v127\n"
    "v_mfma_f32_4x4x4_16b_bf16 v[116:119], v[100:101], a[254:255], 0\n"
    "v_cvt_pk_bf16_f32 v112, v112, v113\n"
    "v_cvt_pk_bf16_f32 v113, v114, v115\n"
    "v_mfma_f32_4x4x4_16b_bf16 v[124:127], v[102:103], a[254:255], 0\n"
    "v_cvt_pk_bf16_f32 v120, v120, v121\n"
    "v_cvt_pk_bf16_f32 v121, v122, v123\n"
    "v_mfma_f32_4x4x4_16b_bf16 v[96:99], v[104:105], a[254:255], 0\n"
    "v_cvt_pk_bf16_f32 v114, v116, v117\n"
    "v_cvt_pk_bf16_f32 v115, v118, v119\n"
    "v_mfma_f32_4x4x4_16b_bf16 v[100:103], v[108:109], a[254:255], 0\n"
    "v_cvt_pk_bf16_f32 v122, v124, v125\n"
    "v_cvt_pk_bf16_f32 v123, v126, v127\n"
    "v_mfma_f32_4x4x4_16b_bf16 v[116:119], v[106:107], a[254:255], 0\n"
    "v_cvt_pk_bf16_f32 v96, v96, v97\n"
    "v_cvt_pk_bf16_f32 v97, v98, v99\n"
    "v_mfma_f32_4x4x4_16b_bf16 v[124:127], v[110:111], a[254:255], 0\n"
    "v_cvt_pk_bf16_f32 v98, v100, v101\n"
    "v_cvt_pk_bf16_f32 v99, v102, v103\n"
    "v_cvt_pk_bf16_f32 v116, v116, v117\n"
    "v_cvt_pk_bf16_f32 v117, v118, v119\n"
    "s_nop 0\n"
    "v_cvt_pk_bf16_f32 v118, v124, v125\n"
    "v_cvt_pk_bf16_f32 v119, v126, v127\n"
    "s_waitcnt lgkmcnt(0)\n"
    "v_mfma_f32_16x16x32_bf16 v[192:195], a[128:131], a[64:67], v[232:235]\n"
    "v_add_f32 v244, v244, v212\n"
    "v_cvt_pk_bf16_f32 v204, v204, v205\n"
    "v_mfma_f32_16x16x32_bf16 v[192:195], a[132:135], a[68:71], v[192:195]\n"
    "v_exp_f32 v213, v213\n"
    "v_mfma_f32_16x16x32_bf16 v[192:195], a[136:139], a[72:75], v[192:195]\n"
    "s_add_u32 s67, s66, 24129536\n"
    "v_add_f32 v244, v244, v213\n"
    "v_cvt_pk_bf16_f32 v205, v206, v207\n"
    "v_mfma_f32_16x16x32_bf16 v[192:195], a[140:143], a[76:79], v[192:195]\n"
    "buffer_store_dwordx4 v[112:115], v249, s[56:59], s67 offen offset:0 sc1\n"
    "v_exp_f32 v214, v214\n"
    "v_mfma_f32_16x16x32_bf16 v[208:211], a[156:159], a[76:79], v[232:235]\n"
    "v_add_f32 v244, v244, v214\n"
    "v_cvt_pk_bf16_f32 v206, v212, v213\n"
    "v_mfma_f32_16x16x32_bf16 v[208:211], a[152:155], a[72:75], v[208:211]\n"
    "v_exp_f32 v215, v215\n"
    "v_mfma_f32_16x16x32_bf16 v[208:211], a[148:151], a[68:71], v[208:211]\n"
    "v_add_f32 v244, v244, v215\n"
    "v_cvt_pk_bf16_f32 v207, v214, v215\n"
    "v_mfma_f32_16x16x32_bf16 v[208:211], a[144:147], a[64:67], v[208:211]\n"
    "v_exp_f32 v192, v192\n"
    "v_mfma_f32_16x16x32_bf16 v[128:131], v[200:203], a[192:195], v[128:131]\n"
    "ds_read_b64_tr_b16 a[192:193], v247 offset:16512\n"
    "v_add_f32 v244, v244, v192\n"
    "v_mfma_f32_16x16x32_bf16 v[128:131], v[204:207], a[160:163], v[128:131]\n"
    "v_exp_f32 v193, v193\n"
    "v_mfma_f32_16x16x32_bf16 v[132:135], v[204:207], a[164:167], v[132:135]\n"
    "ds_read_b64_tr_b16 a[194:195], v247 offset:24768\n"
    "v_add_f32 v244, v244, v193\n"
    "v_mfma_f32_16x16x32_bf16 v[132:135], v[200:203], a[196:199], v[132:135]\n"
    "v_exp_f32 v194, v194\n"
    "v_mfma_f32_16x16x32_bf16 v[136:139], v[200:203], a[200:203], v[136:139]\n"
    "ds_read_b64_tr_b16 a[196:197], v247 offset:16520\n"
    "v_add_f32 v244, v244, v194\n"
    "v_mfma_f32_16x16x32_bf16 v[136:139], v[204:207], a[168:171], v[136:139]\n"
    "buffer_store_dwordx4 v[120:123], v249, s[56:59], s67 offen offset:512 sc1\n"
    "v_exp_f32 v195, v195\n"
    "v_mfma_f32_16x16x32_bf16 v[140:143], v[204:207], a[172:175], v[140:143]\n"
    "ds_read_b64_tr_b16 a[198:199], v247 offset:24776\n"
    "v_add_f32 v244, v244, v195\n"
    "v_mfma_f32_16x16x32_bf16 v[140:143], v[200:203], a[236:239], v[140:143]\n"
    "v_exp_f32 v208, v208\n"
    "v_mfma_f32_16x16x32_bf16 v[196:199], a[128:131], a[80:83], v[236:239]\n"
    "v_add_f32 v244, v244, v208\n"
    "v_cvt_pk_bf16_f32 v192, v192, v193\n"
    "v_mfma_f32_16x16x32_bf16 v[196:199], a[132:135], a[84:87], v[196:199]\n"
    "v_exp_f32 v209, v209\n"
    "v_mfma_f32_16x16x32_bf16 v[196:199], a[136:139], a[88:91], v[196:199]\n"
    "v_add_f32 v244, v244, v209\n"
    "v_cvt_pk_bf16_f32 v193, v194, v195\n"
    "v_mfma_f32_16x16x32_bf16 v[196:199], a[140:143], a[92:95], v[196:199]\n"
    "ds_read_b128 a[140:143], v246 offset:50304\n"
    "v_exp_f32 v210, v210\n"
    "v_mfma_f32_16x16x32_bf16 v[212:215], a[156:159], a[92:95], v[236:239]\n"
    "v_add_f32 v244, v244, v210\n"
    "v_cvt_pk_bf16_f32 v194, v208, v209\n"
    "v_mfma_f32_16x16x32_bf16 v[212:215], a[152:155], a[88:91], v[212:215]\n"
    "ds_read_b128 a[152:155], v246 offset:58304\n"
    "v_exp_f32 v211, v211\n"
    "v_mfma_f32_16x16x32_bf16 v[212:215], a[148:151], a[84:87], v[212:215]\n"
    "v_add_f32 v244, v244, v211\n"
    "v_cvt_pk_bf16_f32 v195, v210, v211\n"
    "v_mfma_f32_16x16x32_bf16 v[212:215], a[144:147], a[80:83], v[212:215]\n"
    "v_exp_f32 v196, v196\n"
    "v_mfma_f32_16x16x32_bf16 v[144:147], v[200:203], a[208:211], v[144:147]\n"
    "ds_read_b64_tr_b16 a[200:201], v251 offset:16512\n"
    "v_add_f32 v245, v245, v196\n"
    "v_mfma_f32_16x16x32_bf16 v[144:147], v[204:207], a[176:179], v[144:147]\n"
    "buffer_store_dwordx4 v[96:99], v249, s[56:59], s67 offen offset:256 sc1\n"
    "v_exp_f32 v197, v197\n"
    "v_mfma_f32_16x16x32_bf16 v[148:151], v[204:207], a[180:183], v[148:151]\n"
    "ds_read_b64_tr_b16 a[202:203], v251 offset:24768\n"
    "v_add_f32 v245, v245, v197\n"
    "v_and_b32 v248, s76, v244\n"
    "v_mfma_f32_16x16x32_bf16 v[148:151], v[200:203], a[212:215], v[148:151]\n"
    "v_exp_f32 v198, v198\n"
    "v_mfma_f32_16x16x32_bf16 v[152:155], v[200:203], a[240:243], v[152:155]\n"
    "ds_read_b64_tr_b16 a[208:209], v252 offset:16512\n"
    "v_add_f32 v245, v245, v198\n"
    "v_sub_f32 v244, v244, v248\n"
    "v_mfma_f32_16x16x32_bf16 v[152:155], v[204:207], a[184:187], v[152:155]\n"
    "v_exp_f32 v199, v199\n"
    "v_mfma_f32_16x16x32_bf16 v[156:159], v[204:207], a[188:191], v[156:159]\n"
    "ds_read_b64_tr_b16 a[210:211], v252 offset:24768\n"
    "v_add_f32 v245, v245, v199\n"
    "v_cvt_pk_bf16_f32 v248, v248, v244\n"
    "v_mfma_f32_16x16x32_bf16 v[156:159], v[200:203], a[244:247], v[156:159]\n"
    "v_exp_f32 v212, v212\n"
    "v_mfma_f32_16x16x16_bf16 v[232:235], v[248:249], a[252:253], 0\n"
    "v_mfma_f32_16x16x32_bf16 v[200:203], a[96:99], a[80:83], v[236:239]\n"
    "v_add_f32 v245, v245, v212\n"
    "v_cvt_pk_bf16_f32 v196, v196, v197\n"
    "v_mfma_f32_16x16x32_bf16 v[200:203], a[100:103], a[84:87], v[200:203]\n"
    "v_exp_f32 v213, v213\n"
    "v_mfma_f32_16x16x32_bf16 v[200:203], a[104:107], a[88:91], v[200:203]\n"
    "ds_read_b64_tr_b16 a[212:213], v252 offset:16520\n"
    "v_add_f32 v245, v245, v213\n"
    "v_cvt_pk_bf16_f32 v197, v198, v199\n"
    "v_mfma_f32_16x16x32_bf16 v[200:203], a[108:111], a[92:95], v[200:203]\n"
    "ds_read_b64_tr_b16 a[214:215], v252 offset:24776\n"
    "v_exp_f32 v214, v214\n"
    "v_mfma_f32_16x16x32_bf16 v[208:211], a[124:127], a[92:95], v[236:239]\n"
    "ds_read_b128 a[148:151], v246 offset:58048\n"
    "v_add_f32 v245, v245, v214\n"
    "v_cvt_pk_bf16_f32 v198, v212, v213\n"
    "v_mfma_f32_16x16x32_bf16 v[208:211], a[120:123], a[88:91], v[208:211]\n"
    "buffer_store_dwordx4 v[116:119], v249, s[56:59], s67 offen offset:768 sc1\n"
    "v_exp_f32 v215, v215\n"
    "v_mfma_f32_16x16x32_bf16 v[208:211], a[116:119], a[84:87], v[208:211]\n"
    "v_add_f32 v245, v245, v215\n"
    "v_cvt_pk_bf16_f32 v199, v214, v215\n"
    "v_mfma_f32_16x16x32_bf16 v[208:211], a[112:115], a[80:83], v[208:211]\n"
    "s_waitcnt lgkmcnt(3)\n"
    "v_exp_f32 v200, v200\n"
    "v_mfma_f32_16x16x32_bf16 v[128:131], v[192:195], a[192:195], v[128:131]\n"
    "ds_read_b128 a[128:131], v246 offset:49536\n"
    "v_add_f32 v245, v245, v200\n"
    "v_maximum3_f32 v253, v232, v233, v253\n"
    "v_mfma_f32_16x16x32_bf16 v[132:135], v[192:195], a[196:199], v[132:135]\n"
    "v_exp_f32 v201, v201\n"
    "v_mfma_f32_16x16x32_bf16 v[136:139], v[192:195], a[200:203], v[136:139]\n"
    "ds_read_b128 a[132:135], v246 offset:49792\n"
    "v_add_f32 v245, v245, v201\n"
    "v_maximum3_f32 v253, v234, v235, v253\n"
    "v_mfma_f32_16x16x32_bf16 v[140:143], v[192:195], a[204:207], v[140:143]\n"
    "v_exp_f32 v202, v202\n"
    "v_mfma_f32_16x16x32_bf16 v[144:147], v[192:195], a[208:211], v[144:147]\n"
    "s_waitcnt lgkmcnt(2)\n"
    "ds_read_b128 a[136:139], v246 offset:50048\n"
    "v_add_f32 v245, v245, v202\n"
    "v_mfma_f32_16x16x32_bf16 v[148:151], v[192:195], a[212:215], v[148:151]\n"
    "v_exp_f32 v203, v203\n"
    "v_mfma_f32_16x16x32_bf16 v[152:155], v[192:195], a[216:219], v[152:155]\n"
    "ds_read_b128 a[156:159], v246 offset:58560\n"
    "v_add_f32 v245, v245, v203\n"
    "v_mfma_f32_16x16x32_bf16 v[156:159], v[192:195], a[220:223], v[156:159]\n"
    "ds_read_b128 a[144:147], v246 offset:57792\n"
    "v_exp_f32 v208, v208\n"
    "v_rcp_f32 v232, v232\n"
    "v_rcp_f32 v233, v233\n"
    "v_rcp_f32 v234, v234\n"
    "v_rcp_f32 v235, v235\n"
    "v_maximum3_f32 v253, v232, v233, v253\n"
    "v_maximum3_f32 v253, v234, v235, v253\n"
    "v_pk_mul_f32 v[128:129], v[128:129], v[232:233]\n"
    "v_pk_mul_f32 v[130:131], v[130:131], v[234:235]\n"
    "LPG8:\n"
    "s_cbranch_execz LPG14\n"
    "s_nop 0\n"
    "v_pk_mul_f32 v[136:137], v[136:137], v[232:233]\n"
    "v_pk_mul_f32 v[138:139], v[138:139], v[234:235]\n"
    "v_cvt_pk_bf16_f32 v128, v128, v136\n"
    "v_cvt_pk_bf16_f32 v130, v130, v138\n"
    "v_cvt_pk_bf16_f32 v136, v129, v137\n"
    "v_cvt_pk_bf16_f32 v138, v131, v139\n"
    "v_pk_mul_f32 v[144:145], v[144:145], v[232:233]\n"
    "v_pk_mul_f32 v[146:147], v[146:147], v[234:235]\n"
    "v_pk_mul_f32 v[152:153], v[152:153], v[232:233]\n"
    "v_pk_mul_f32 v[154:155], v[154:155], v[234:235]\n"
    "v_cvt_pk_bf16_f32 v129, v144, v152\n"
    "v_cvt_pk_bf16_f32 v131, v146, v154\n"
    "v_cvt_pk_bf16_f32 v137, v145, v153\n"
    "v_cvt_pk_bf16_f32 v139, v147, v155\n"
    "v_pk_mul_f32 v[132:133], v[132:133], v[232:233]\n"
    "v_pk_mul_f32 v[134:135], v[134:135], v[234:235]\n"
    "v_pk_mul_f32 v[140:141], v[140:141], v[232:233]\n"
    "v_pk_mul_f32 v[142:143], v[142:143], v[234:235]\n"
    "v_cvt_pk_bf16_f32 v132, v132, v140\n"
    "v_cvt_pk_bf16_f32 v134, v134, v142\n"
    "v_cvt_pk_bf16_f32 v140, v133, v141\n"
    "v_cvt_pk_bf16_f32 v142, v135, v143\n"
    "v_pk_mul_f32 v[148:149], v[148:149], v[232:233]\n"
    "v_pk_mul_f32 v[150:151], v[150:151], v[234:235]\n"
    "v_pk_mul_f32 v[156:157], v[156:157], v[232:233]\n"
    "v_pk_mul_f32 v[158:159], v[158:159], v[234:235]\n"
    "v_mfma_f32_4x4x4_16b_bf16 v[144:147], v[128:129], a[254:255], 0\n"
    "v_cvt_pk_bf16_f32 v133, v148, v156\n"
    "v_cvt_pk_bf16_f32 v135, v150, v158\n"
    "v_mfma_f32_4x4x4_16b_bf16 v[152:155], v[130:131], a[254:255], 0\n"
    "v_cvt_pk_bf16_f32 v141, v149, v157\n"
    "v_cvt_pk_bf16_f32 v143, v151, v159\n"
    "v_mfma_f32_4x4x4_16b_bf16 v[148:151], v[132:133], a[254:255], 0\n"
    "v_cvt_pk_bf16_f32 v144, v144, v145\n"
    "v_cvt_pk_bf16_f32 v145, v146, v147\n"
    "v_mfma_f32_4x4x4_16b_bf16 v[156:159], v[134:135], a[254:255], 0\n"
    "v_cvt_pk_bf16_f32 v152, v152, v153\n"
    "v_cvt_pk_bf16_f32 v153, v154, v155\n"
    "v_mfma_f32_4x4x4_16b_bf16 v[128:131], v[136:137], a[254:255], 0\n"
    "v_cvt_pk_bf16_f32 v146, v148, v149\n"
    "v_cvt_pk_bf16_f32 v147, v150, v151\n"
    "v_mfma_f32_4x4x4_16b_bf16 v[132:135], v[140:141], a[254:255], 0\n"
    "v_cvt_pk_bf16_f32 v154, v156, v157\n"
    "v_cvt_pk_bf16_f32 v155, v158, v159\n"
    "v_mfma_f32_4x4x4_16b_bf16 v[148:151], v[138:139], a[254:255], 0\n"
    "v_cvt_pk_bf16_f32 v128, v128, v129\n"
    "v_cvt_pk_bf16_f32 v129, v130, v131\n"
    "v_mfma_f32_4x4x4_16b_bf16 v[156:159], v[142:143], a[254:255], 0\n"
    "v_cvt_pk_bf16_f32 v130, v132, v133\n"
    "v_cvt_pk_bf16_f32 v131, v134, v135\n"
    "v_cvt_pk_bf16_f32 v148, v148, v149\n"
    "v_cvt_pk_bf16_f32 v149, v150, v151\n"
    "s_nop 0\n"
    "v_cvt_pk_bf16_f32 v150, v156, v157\n"
    "v_cvt_pk_bf16_f32 v151, v158, v159\n"
    "s_waitcnt lgkmcnt(0)\n"
    "v_mfma_f32_16x16x32_bf16 v[204:207], a[128:131], a[80:83], v[236:239]\n"
    "v_add_f32 v245, v245, v208\n"
    "v_cvt_pk_bf16_f32 v200, v200, v201\n"
    "v_mfma_f32_16x16x32_bf16 v[204:207], a[132:135], a[84:87], v[204:207]\n"
    "v_exp_f32 v209, v209\n"
    "v_mfma_f32_16x16x32_bf16 v[204:207], a[136:139], a[88:91], v[204:207]\n"
    "s_add_u32 s67, s66, 24133632\n"
    "v_add_f32 v245, v245, v209\n"
    "v_cvt_pk_bf16_f32 v201, v202, v203\n"
    "v_mfma_f32_16x16x32_bf16 v[204:207], a[140:143], a[92:95], v[204:207]\n"
    "buffer_store_dwordx4 v[144:147], v249, s[56:59], s67 offen offset:0 sc1\n"
    "v_exp_f32 v210, v210\n"
    "v_mfma_f32_16x16x32_bf16 v[212:215], a[156:159], a[92:95], v[236:239]\n"
    "v_add_f32 v245, v245, v210\n"
    "v_cvt_pk_bf16_f32 v202, v208, v209\n"
    "v_mfma_f32_16x16x32_bf16 v[212:215], a[152:155], a[88:91], v[212:215]\n"
    "v_exp_f32 v211, v211\n"
    "v_mfma_f32_16x16x32_bf16 v[212:215], a[148:151], a[84:87], v[212:215]\n"
    "v_add_f32 v245, v245, v211\n"
    "v_cvt_pk_bf16_f32 v203, v210, v211\n"
    "v_mfma_f32_16x16x32_bf16 v[212:215], a[144:147], a[80:83], v[212:215]\n"
    "v_exp_f32 v204, v204\n"
    "v_mfma_f32_16x16x32_bf16 v[160:163], v[196:199], a[192:195], v[160:163]\n"
    "ds_read_b64_tr_b16 a[192:193], v247 offset:49536\n"
    "v_add_f32 v245, v245, v204\n"
    "v_mfma_f32_16x16x32_bf16 v[160:163], v[200:203], a[160:163], v[160:163]\n"
    "buffer_store_dwordx4 v[152:155], v249, s[56:59], s67 offen offset:512 sc1\n"
    "v_exp_f32 v205, v205\n"
    "v_mfma_f32_16x16x32_bf16 v[164:167], v[200:203], a[164:167], v[164:167]\n"
    "ds_read_b64_tr_b16 a[194:195], v247 offset:57792\n"
    "v_add_f32 v245, v245, v205\n"
    "v_mfma_f32_16x16x32_bf16 v[164:167], v[196:199], a[196:199], v[164:167]\n"
    "v_exp_f32 v206, v206\n"
    "v_mfma_f32_16x16x32_bf16 v[168:171], v[196:199], a[200:203], v[168:171]\n"
    "ds_read_b64_tr_b16 a[196:197], v247 offset:49544\n"
    "v_add_f32 v245, v245, v206\n"
    "v_mfma_f32_16x16x32_bf16 v[168:171], v[200:203], a[168:171], v[168:171]\n"
    "v_exp_f32 v207, v207\n"
    "v_mfma_f32_16x16x32_bf16 v[172:175], v[200:203], a[172:175], v[172:175]\n"
    "ds_read_b64_tr_b16 a[198:199], v247 offset:57800\n"
    "v_add_f32 v245, v245, v207\n"
    "v_mfma_f32_16x16x32_bf16 v[172:175], v[196:199], a[204:207], v[172:175]\n"
    "v_exp_f32 v212, v212\n"
    "v_cvt_pk_bf16_f32 v204, v204, v205\n"
    "v_add_f32 v245, v245, v212\n"
    "v_exp_f32 v213, v213\n"
    "v_cvt_pk_bf16_f32 v205, v206, v207\n"
    "v_add_f32 v245, v245, v213\n"
    "v_exp_f32 v214, v214\n"
    "v_cvt_pk_bf16_f32 v206, v212, v213\n"
    "v_add_f32 v245, v245, v214\n"
    "v_exp_f32 v215, v215\n"
    "s_nop 0\n"
    "v_add_f32 v245, v245, v215\n"
    "v_cvt_pk_bf16_f32 v207, v214, v215\n"
    "v_mfma_f32_16x16x32_bf16 v[176:179], v[196:199], a[208:211], v[176:179]\n"
    "ds_read_b64_tr_b16 a[200:201], v251 offset:49536\n"
    "v_mfma_f32_16x16x32_bf16 v[176:179], v[200:203], a[176:179], v[176:179]\n"
    "buffer_store_dwordx4 v[128:131], v249, s[56:59], s67 offen offset:256 sc1\n"
    "v_mfma_f32_16x16x32_bf16 v[180:183], v[200:203], a[180:183], v[180:183]\n"
    "ds_read_b64_tr_b16 a[202:203], v251 offset:57792\n"
    "v_and_b32 v248, s76, v245\n"
    "v_mfma_f32_16x16x32_bf16 v[180:183], v[196:199], a[212:215], v[180:183]\n"
    "ds_read_b64_tr_b16 a[214:215], v252 offset:57800\n"
    "v_mfma_f32_16x16x32_bf16 v[184:187], v[196:199], a[216:219], v[184:187]\n"
    "ds_read_b64_tr_b16 a[208:209], v252 offset:49536\n"
    "v_sub_f32 v245, v245, v248\n"
    "v_mfma_f32_16x16x32_bf16 v[184:187], v[200:203], a[184:187], v[184:187]\n"
    "v_mfma_f32_16x16x32_bf16 v[188:191], v[200:203], a[188:191], v[188:191]\n"
    "ds_read_b64_tr_b16 a[210:211], v252 offset:57792\n"
    "v_cvt_pk_bf16_f32 v248, v248, v245\n"
    "v_mfma_f32_16x16x32_bf16 v[188:191], v[196:199], a[220:223], v[188:191]\n"
    "s_waitcnt lgkmcnt(5)\n"
    "ds_read_b64_tr_b16 a[212:213], v252 offset:49544\n"
    "v_mfma_f32_16x16x16_bf16 v[236:239], v[248:249], a[252:253], 0\n"
    "v_mfma_f32_16x16x32_bf16 v[160:163], v[204:207], a[192:195], v[160:163]\n"
    "v_mfma_f32_16x16x32_bf16 v[164:167], v[204:207], a[196:199], v[164:167]\n"
    "s_waitcnt lgkmcnt(4)\n"
    "buffer_store_dwordx4 v[148:151], v249, s[56:59], s67 offen offset:768 sc1\n"
    "v_mfma_f32_16x16x32_bf16 v[168:171], v[204:207], a[200:203], v[168:171]\n"
    "s_nop 2\n"
    "v_maximum3_f32 v253, v236, v237, v253\n"
    "v_maximum3_f32 v253, v238, v239, v253\n"
    "v_mfma_f32_16x16x32_bf16 v[172:175], v[204:207], a[236:239], v[172:175]\n"
    "s_waitcnt lgkmcnt(1)\n"
    "v_rcp_f32 v236, v236\n"
    "v_mfma_f32_16x16x32_bf16 v[176:179], v[204:207], a[208:211], v[176:179]\n"
    "s_waitcnt lgkmcnt(0)\n"
    "v_rcp_f32 v237, v237\n"
    "v_mfma_f32_16x16x32_bf16 v[180:183], v[204:207], a[212:215], v[180:183]\n"
    "v_rcp_f32 v238, v238\n"
    "v_mfma_f32_16x16x32_bf16 v[184:187], v[204:207], a[240:243], v[184:187]\n"
    "v_rcp_f32_e64 v239, v239\n"
    "v_mfma_f32_16x16x32_bf16 v[188:191], v[204:207], a[244:247], v[188:191]\n"
    "v_maximum3_f32 v253, v236, v237, v253\n"
    "v_maximum3_f32 v253, v238, v239, v253\n"
    "v_cmp_ngt_f32 vcc, s72, v253\n"
    "s_cmp_lg_u64 vcc, 0\n"
    "s_cselect_b32 s69, 2, 0\n"
    "s_or_b32 s65, s65, s69\n"
    "s_add_u32 s67, s66, 24137728\n"
    "v_pk_mul_f32 v[160:161], v[160:161], v[236:237]\n"
    "v_pk_mul_f32 v[162:163], v[162:163], v[238:239]\n"
    "v_pk_mul_f32 v[168:169], v[168:169], v[236:237]\n"
    "v_pk_mul_f32 v[170:171], v[170:171], v[238:239]\n"
    "v_cvt_pk_bf16_f32 v160, v160, v168\n"
    "v_cvt_pk_bf16_f32 v162, v162, v170\n"
    "v_cvt_pk_bf16_f32 v168, v161, v169\n"
    "v_cvt_pk_bf16_f32 v170, v163, v171\n"
    "v_pk_mul_f32 v[176:177], v[176:177], v[236:237]\n"
    "v_pk_mul_f32 v[178:179], v[178:179], v[238:239]\n"
    "v_pk_mul_f32 v[184:185], v[184:185], v[236:237]\n"
    "v_pk_mul_f32 v[186:187], v[186:187], v[238:239]\n"
    "v_cvt_pk_bf16_f32 v161, v176, v184\n"
    "v_cvt_pk_bf16_f32 v163, v178, v186\n"
    "v_cvt_pk_bf16_f32 v169, v177, v185\n"
    "v_cvt_pk_bf16_f32 v171, v179, v187\n"
    "v_pk_mul_f32 v[164:165], v[164:165], v[236:237]\n"
    "v_pk_mul_f32 v[166:167], v[166:167], v[238:239]\n"
    "v_pk_mul_f32 v[172:173], v[172:173], v[236:237]\n"
    "v_pk_mul_f32 v[174:175], v[174:175], v[238:239]\n"
    "v_cvt_pk_bf16_f32 v164, v164, v172\n"
    "v_cvt_pk_bf16_f32 v166, v166, v174\n"
    "v_cvt_pk_bf16_f32 v172, v165, v173\n"
    "v_cvt_pk_bf16_f32 v174, v167, v175\n"
    "v_pk_mul_f32 v[180:181], v[180:181], v[236:237]\n"
    "v_pk_mul_f32 v[182:183], v[182:183], v[238:239]\n"
    "v_pk_mul_f32 v[188:189], v[188:189], v[236:237]\n"
    "v_pk_mul_f32 v[190:191], v[190:191], v[238:239]\n"
    "v_mfma_f32_4x4x4_16b_bf16 v[176:179], v[160:161], a[254:255], 0\n"
    "v_cvt_pk_bf16_f32 v165, v180, v188\n"
    "v_cvt_pk_bf16_f32 v167, v182, v190\n"
    "v_mfma_f32_4x4x4_16b_bf16 v[184:187], v[162:163], a[254:255], 0\n"
    "v_cvt_pk_bf16_f32 v173, v181, v189\n"
    "v_cvt_pk_bf16_f32 v175, v183, v191\n"
    "v_mfma_f32_4x4x4_16b_bf16 v[180:183], v[164:165], a[254:255], 0\n"
    "v_cvt_pk_bf16_f32 v176, v176, v177\n"
    "v_cvt_pk_bf16_f32 v177, v178, v179\n"
    "v_mfma_f32_4x4x4_16b_bf16 v[188:191], v[166:167], a[254:255], 0\n"
    "v_cvt_pk_bf16_f32 v184, v184, v185\n"
    "v_cvt_pk_bf16_f32 v185, v186, v187\n"
    "v_mfma_f32_4x4x4_16b_bf16 v[160:163], v[168:169], a[254:255], 0\n"
    "v_cvt_pk_bf16_f32 v178, v180, v181\n"
    "v_cvt_pk_bf16_f32 v179, v182, v183\n"
    "v_mfma_f32_4x4x4_16b_bf16 v[164:167], v[172:173], a[254:255], 0\n"
    "buffer_store_dwordx4 v[176:179], v249, s[56:59], s67 offen offset:0 sc1\n"
    "v_cvt_pk_bf16_f32 v186, v188, v189\n"
    "v_cvt_pk_bf16_f32 v187, v190, v191\n"
    "v_mfma_f32_4x4x4_16b_bf16 v[180:183], v[170:171], a[254:255], 0\n"
    "buffer_store_dwordx4 v[184:187], v249, s[56:59], s67 offen offset:512 sc1\n"
    "v_cvt_pk_bf16_f32 v160, v160, v161\n"
    "v_cvt_pk_bf16_f32 v161, v162, v163\n"
    "v_mfma_f32_4x4x4_16b_bf16 v[188:191], v[174:175], a[254:255], 0\n"
    "v_cvt_pk_bf16_f32 v162, v164, v165\n"
    "v_cvt_pk_bf16_f32 v163, v166, v167\n"
    "buffer_store_dwordx4 v[160:163], v249, s[56:59], s67 offen offset:256 sc1\n"
    "v_cvt_pk_bf16_f32 v180, v180, v181\n"
    "v_cvt_pk_bf16_f32 v181, v182, v183\n"
    "v_cvt_pk_bf16_f32 v182, v188, v189\n"
    "v_cvt_pk_bf16_f32 v183, v190, v191\n"
    "buffer_store_dwordx4 v[180:183], v249, s[56:59], s67 offen offset:768 sc1\n"
    "s_cmp_lg_u32 s65, 0\n"
    "s_cbranch_scc1 LEND\n"
    "s_endpgm\n"
    "LPROG_B:\n"
    "s_nop 0\n"
    "s_add_u32 m0, s73, 33024\n"
    "s_add_u32 s69, s70, 16384\n"
    "buffer_load_dwordx4 v253, s[36:39], s69 offen offset:0 lds\n"
    "buffer_load_dwordx4 v253, s[36:39], s69 offen offset:1024 lds\n"
    "buffer_load_dwordx4 v253, s[40:43], s69 offen offset:2048 lds\n"
    "buffer_load_dwordx4 v253, s[44:47], s69 offen offset:3080 lds\n"
    "s_waitcnt lgkmcnt(3)\n"
    "v_mfma_f32_4x4x4_16b_bf16 v[104:107], a[248:249], a[48:49], 0\n"
    "s_nop 1\n"
    "v_mfma_f32_4x4x4_16b_bf16 v[104:107], a[250:251], a[48:49], v[104:107]\n"
    "v_mfma_f32_4x4x4_16b_bf16 v[108:111], a[248:249], a[50:51], 0\n"
    "s_nop 1\n"
    "v_mfma_f32_4x4x4_16b_bf16 v[108:111], a[250:251], a[50:51], v[108:111]\n"
    "v_nop_e64\n"
    "v_cvt_pk_bf16_f32 v104, v104, v105\n"
    "s_waitcnt lgkmcnt(2)\n"
    "v_mfma_f32_4x4x4_16b_bf16 v[100:103], a[248:249], a[52:53], 0\n"
    "v_cvt_pk_bf16_f32 v105, v106, v107\n"
    "v_accvgpr_write_b32 a48, v104\n"
    "v_mfma_f32_4x4x4_16b_bf16 v[100:103], a[250:251], a[52:53], v[100:103]\n"
    "v_cvt_pk_bf16_f32 v108, v108, v109\n"
    "v_accvgpr_write_b32 a49, v105\n"
    "v_mfma_f32_4x4x4_16b_bf16 v[104:107], a[248:249], a[54:55], 0\n"
    "v_cvt_pk_bf16_f32 v109, v110, v111\n"
    "v_accvgpr_write_b32 a50, v108\n"
    "v_mfma_f32_4x4x4_16b_bf16 v[104:107], a[250:251], a[54:55], v[104:107]\n"
    "v_cvt_pk_bf16_f32 v100, v100, v101\n"
    "v_accvgpr_write_b32 a51, v109\n"
    "s_waitcnt lgkmcnt(0)\n"
    "s_nop 0\n"
    "v_mfma_f32_4x4x4_16b_bf16 v[108:111], a[248:249], a[56:57], 0\n"
    "v_cvt_pk_bf16_f32 v101, v102, v103\n"
    "v_accvgpr_write_b32 a52, v100\n"
    "v_mfma_f32_4x4x4_16b_bf16 v[108:111], a[250:251], a[56:57], v[108:111]\n"
    "v_cvt_pk_bf16_f32 v104, v104, v105\n"
    "v_accvgpr_write_b32 a53, v101\n"
    "v_mfma_f32_4x4x4_16b_bf16 v[100:103], a[248:249], a[58:59], 0\n"
    "v_cvt_pk_bf16_f32 v105, v106, v107\n"
    "v_accvgpr_write_b32 a54, v104\n"
    "v_mfma_f32_4x4x4_16b_bf16 v[100:103], a[250:251], a[58:59], v[100:103]\n"
    "v_cvt_pk_bf16_f32 v108, v108, v109\n"
    "v_accvgpr_write_b32 a55, v105\n"
    "v_mfma_f32_4x4x4_16b_bf16 v[104:107], a[248:249], a[60:61], 0\n"
    "v_cvt_pk_bf16_f32 v109, v110, v111\n"
    "v_accvgpr_write_b32 a56, v108\n"
    "v_mfma_f32_4x4x4_16b_bf16 v[104:107], a[250:251], a[60:61], v[104:107]\n"
    "v_cvt_pk_bf16_f32 v100, v100, v101\n"
    "v_accvgpr_write_b32 a57, v109\n"
    "v_mfma_f32_4x4x4_16b_bf16 v[108:111], a[248:249], a[62:63], 0\n"
    "v_cvt_pk_bf16_f32 v101, v102, v103\n"
    "v_accvgpr_write_b32 a58, v100\n"
    "v_mfma_f32_4x4x4_16b_bf16 v[108:111], a[250:251], a[62:63], v[108:111]\n"
    "v_cvt_pk_bf16_f32 v104, v104, v105\n"
    "v_accvgpr_write_b32 a59, v101\n"
    "v_mfma_f32_16x16x32_bf16 v[124:127], a[96:99], a[48:51], 0\n"
    "v_cvt_pk_bf16_f32 v105, v106, v107\n"
    "v_accvgpr_write_b32 a60, v104\n"
    "v_mfma_f32_16x16x32_bf16 v[124:127], a[100:103], a[52:55], v[124:127]\n"
    "v_cvt_pk_bf16_f32 v108, v108, v109\n"
    "v_accvgpr_write_b32 a61, v105\n"
    "v_mfma_f32_16x16x32_bf16 v[124:127], a[104:107], a[56:59], v[124:127]\n"
    "v_cvt_pk_bf16_f32 v109, v110, v111\n"
    "v_accvgpr_write_b32 a62, v108\n"
    "v_accvgpr_write_b32 a63, v109\n"
    "v_add_f32 v242, v242, v212\n"
    "v_cvt_pk_bf16_f32 v196, v196, v197\n"
    "v_mfma_f32_16x16x32_bf16 v[124:127], a[108:111], a[60:63], v[124:127]\n"
    "v_exp_f32 v213, v213\n"
    "v_cvt_pk_bf16_f32 v197, v198, v199\n"
    "v_add_f32 v242, v242, v213\n"
    "v_exp_f32 v214, v214\n"
    "v_cvt_pk_bf16_f32 v198, v212, v213\n"
    "v_add_f32 v242, v242, v214\n"
    "v_exp_f32 v215, v215\n"
    "s_nop 0\n"
    "v_max3_f32 v248, v124, v125, v126\n"
    "v_max_f32 v248, v248, v127\n"
    "v_mul_f32 v248, 0xbe800000, v248\n"
    "v_cvt_pk_bf16_f32 v248, v248, s80\n"
    "v_add_f32 v242, v242, v215\n"
    "v_cvt_pk_bf16_f32 v199, v214, v215\n"
    "v_mfma_f32_16x16x16_bf16 v[228:231], a[252:253], v[248:249], 0\n"
    "s_nop 7\n"
    "v_add_f32 v200, v124, v228\n"
    "v_add_f32 v201, v125, v229\n"
    "v_add_f32 v202, v126, v230\n"
    "v_add_f32 v203, v127, v231\n"
    "v_mfma_f32_16x16x32_bf16 v[208:211], a[124:127], a[60:63], v[228:231]\n"
    "v_mfma_f32_16x16x32_bf16 v[208:211], a[120:123], a[56:59], v[208:211]\n"
    "v_mfma_f32_16x16x32_bf16 v[208:211], a[116:119], a[52:55], v[208:211]\n"
    "v_mfma_f32_16x16x32_bf16 v[208:211], a[112:115], a[48:51], v[208:211]\n"
    "v_exp_f32 v200, v200\n"
    "v_mfma_f32_16x16x32_bf16 v[64:67], v[192:195], a[160:163], 0\n"
    "v_mov_b32 v243, v200\n"
    "v_mfma_f32_16x16x32_bf16 v[64:67], v[196:199], a[192:195], v[64:67]\n"
    "v_exp_f32 v201, v201\n"
    "v_mfma_f32_16x16x32_bf16 v[68:71], v[196:199], a[196:199], 0\n"
    "v_add_f32 v243, v243, v201\n"
    "v_mfma_f32_16x16x32_bf16 v[68:71], v[192:195], a[164:167], v[68:71]\n"
    "v_exp_f32 v202, v202\n"
    "v_mfma_f32_16x16x32_bf16 v[72:75], v[192:195], a[168:171], 0\n"
    "v_add_f32 v243, v243, v202\n"
    "v_mfma_f32_16x16x32_bf16 v[72:75], v[196:199], a[200:203], v[72:75]\n"
    "v_exp_f32 v203, v203\n"
    "v_mfma_f32_16x16x32_bf16 v[76:79], v[196:199], a[204:207], 0\n"
    "v_add_f32 v243, v243, v203\n"
    "v_mfma_f32_16x16x32_bf16 v[76:79], v[192:195], a[172:175], v[76:79]\n"
    "v_exp_f32 v208, v208\n"
    "v_mfma_f32_16x16x32_bf16 v[204:207], a[128:131], a[48:51], v[228:231]\n"
    "v_add_f32 v243, v243, v208\n"
    "v_cvt_pk_bf16_f32 v200, v200, v201\n"
    "v_mfma_f32_16x16x32_bf16 v[204:207], a[132:135], a[52:55], v[204:207]\n"
    "v_exp_f32 v209, v209\n"
    "v_mfma_f32_16x16x32_bf16 v[204:207], a[136:139], a[56:59], v[204:207]\n"
    "v_add_f32 v243, v243, v209\n"
    "v_cvt_pk_bf16_f32 v201, v202, v203\n"
    "v_mfma_f32_16x16x32_bf16 v[204:207], a[140:143], a[60:63], v[204:207]\n"
    "v_exp_f32 v210, v210\n"
    "v_mfma_f32_16x16x32_bf16 v[212:215], a[156:159], a[60:63], v[228:231]\n"
    "v_add_f32 v243, v243, v210\n"
    "v_cvt_pk_bf16_f32 v202, v208, v209\n"
    "v_mfma_f32_16x16x32_bf16 v[212:215], a[152:155], a[56:59], v[212:215]\n"
    "v_exp_f32 v211, v211\n"
    "v_mfma_f32_16x16x32_bf16 v[212:215], a[148:151], a[52:55], v[212:215]\n"
    "v_add_f32 v243, v243, v211\n"
    "v_cvt_pk_bf16_f32 v203, v210, v211\n"
    "v_mfma_f32_16x16x32_bf16 v[212:215], a[144:147], a[48:51], v[212:215]\n"
    "v_exp_f32 v204, v204\n"
    "v_mfma_f32_16x16x32_bf16 v[80:83], v[192:195], a[176:179], 0\n"
    "v_add_f32 v243, v243, v204\n"
    "v_mfma_f32_16x16x32_bf16 v[80:83], v[196:199], a[208:211], v[80:83]\n"
    "v_exp_f32 v205, v205\n"
    "v_mfma_f32_16x16x32_bf16 v[84:87], v[196:199], a[212:215], 0\n"
    "v_add_f32 v243, v243, v205\n"
    "v_mfma_f32_16x16x32_bf16 v[84:87], v[192:195], a[180:183], v[84:87]\n"
    "v_exp_f32 v206, v206\n"
    "v_mfma_f32_16x16x32_bf16 v[88:91], v[192:195], a[184:187], 0\n"
    "v_add_f32 v243, v243, v206\n"
    "v_mfma_f32_16x16x32_bf16 v[88:91], v[196:199], a[216:219], v[88:91]\n"
    "v_exp_f32 v207, v207\n"
    "v_mfma_f32_16x16x32_bf16 v[92:95], v[196:199], a[220:223], 0\n"
    "v_add_f32 v243, v243, v207\n"
    "v_mfma_f32_16x16x32_bf16 v[92:95], v[192:195], a[188:191], v[92:95]\n"
    "v_exp_f32 v212, v212\n"
    "s_waitcnt vmcnt(4)\n"
    "ds_read_b128 a[64:67], v250 offset:16512\n"
    "ds_read_b128 a[68:71], v250 offset:16768\n"
    "ds_read_b128 a[72:75], v250 offset:17024\n"
    "ds_read_b128 a[76:79], v250 offset:17280\n"
    "s_add_u32 m0, s73, 49536\n"
    "s_add_u32 s69, s70, 24576\n"
    "buffer_load_dwordx4 v253, s[36:39], s69 offen offset:0 lds\n"
    "buffer_load_dwordx4 v253, s[36:39], s69 offen offset:1024 lds\n"
    "buffer_load_dwordx4 v253, s[40:43], s69 offen offset:2048 lds\n"
    "buffer_load_dwordx4 v253, s[44:47], s69 offen offset:3080 lds\n"
    "s_mov_b32 m0, s73\n"
    "s_add_u32 s69, s70, 32768\n"
    "buffer_load_dwordx4 v253, s[36:39], s69 offen offset:0 lds\n"
    "buffer_load_dwordx4 v253, s[36:39], s69 offen offset:1024 lds\n"
    "buffer_load_dwordx4 v253, s[40:43], s69 offen offset:2048 lds\n"
    "buffer_load_dwordx4 v253, s[44:47], s69 offen offset:3080 lds\n"
    "s_waitcnt lgkmcnt(3)\n"
    "v_mfma_f32_4x4x4_16b_bf16 v[136:139], a[248:249], a[64:65], 0\n"
    "s_nop 1\n"
    "v_mfma_f32_4x4x4_16b_bf16 v[136:139], a[250:251], a[64:65], v[136:139]\n"
    "v_mfma_f32_4x4x4_16b_bf16 v[140:143], a[248:249], a[66:67], 0\n"
    "s_nop 1\n"
    "v_mfma_f32_4x4x4_16b_bf16 v[140:143], a[250:251], a[66:67], v[140:143]\n"
    "s_nop 0\n"
    "v_cvt_pk_bf16_f32 v136, v136, v137\n"
    "s_waitcnt lgkmcnt(0)\n"
    "v_mfma_f32_4x4x4_16b_bf16 v[132:135], a[248:249], a[68:69], 0\n"
    "v_cvt_pk_bf16_f32 v137, v138, v139\n"
    "v_accvgpr_write_b32 a64, v136\n"
    "v_mfma_f32_4x4x4_16b_bf16 v[132:135], a[250:251], a[68:69], v[132:135]\n"
    "v_cvt_pk_bf16_f32 v140, v140, v141\n"
    "v_accvgpr_write_b32 a65, v137\n"
    "v_mfma_f32_4x4x4_16b_bf16 v[136:139], a[248:249], a[70:71], 0\n"
    "v_cvt_pk_bf16_f32 v141, v142, v143\n"
    "v_accvgpr_write_b32 a66, v140\n"
    "v_mfma_f32_4x4x4_16b_bf16 v[136:139], a[250:251], a[70:71], v[136:139]\n"
    "v_cvt_pk_bf16_f32 v132, v132, v133\n"
    "v_accvgpr_write_b32 a67, v141\n"
    "v_mfma_f32_4x4x4_16b_bf16 v[140:143], a[248:249], a[72:73], 0\n"
    "v_cvt_pk_bf16_f32 v133, v134, v135\n"
    "v_accvgpr_write_b32 a68, v132\n"
    "v_mfma_f32_4x4x4_16b_bf16 v[140:143], a[250:251], a[72:73], v[140:143]\n"
    "v_cvt_pk_bf16_f32 v136, v136, v137\n"
    "v_accvgpr_write_b32 a69, v133\n"
    "v_mfma_f32_4x4x4_16b_bf16 v[132:135], a[248:249], a[74:75], 0\n"
    "v_cvt_pk_bf16_f32 v137, v138, v139\n"
    "v_accvgpr_write_b32 a70, v136\n"
    "v_mfma_f32_4x4x4_16b_bf16 v[132:135], a[250:251], a[74:75], v[132:135]\n"
    "v_cvt_pk_bf16_f32 v140, v140, v141\n"
    "v_accvgpr_write_b32 a71, v137\n"
    "v_mfma_f32_4x4x4_16b_bf16 v[136:139], a[248:249], a[76:77], 0\n"
    "v_cvt_pk_bf16_f32 v141, v142, v143\n"
    "v_accvgpr_write_b32 a72, v140\n"
    "v_mfma_f32_4x4x4_16b_bf16 v[136:139], a[250:251], a[76:77], v[136:139]\n"
    "v_cvt_pk_bf16_f32 v132, v132, v133\n"
    "v_accvgpr_write_b32 a73, v141\n"
    "v_mfma_f32_4x4x4_16b_bf16 v[140:143], a[248:249], a[78:79], 0\n"
    "v_cvt_pk_bf16_f32 v133, v134, v135\n"
    "v_accvgpr_write_b32 a74, v132\n"
    "v_mfma_f32_4x4x4_16b_bf16 v[140:143], a[250:251], a[78:79], v[140:143]\n"
    "v_cvt_pk_bf16_f32 v136, v136, v137\n"
    "v_accvgpr_write_b32 a75, v133\n"
    "v_mfma_f32_16x16x32_bf16 v[156:159], a[96:99], a[64:67], 0\n"
    "v_cvt_pk_bf16_f32 v137, v138, v139\n"
    "v_accvgpr_write_b32 a76, v136\n"
    "v_mfma_f32_16x16x32_bf16 v[156:159], a[100:103], a[68:71], v[156:159]\n"
    "v_cvt_pk_bf16_f32 v140, v140, v141\n"
    "v_accvgpr_write_b32 a77, v137\n"
    "v_mfma_f32_16x16x32_bf16 v[156:159], a[104:107], a[72:75], v[156:159]\n"
    "v_cvt_pk_bf16_f32 v141, v142, v143\n"
    "v_accvgpr_write_b32 a78, v140\n"
    "v_accvgpr_write_b32 a79, v141\n"
    "s_nop 1\n"
    "v_mfma_f32_16x16x32_bf16 v[156:159], a[108:111], a[76:79], v[156:159]\n"
    "s_nop 7\n"
    "v_max3_f32 v248, v156, v157, v158\n"
    "v_max_f32 v248, v248, v159\n"
    "v_mul_f32 v248, 0xbe800000, v248\n"
    "v_cvt_pk_bf16_f32 v248, v248, s80\n"
    "s_nop 1\n"
    "v_mfma_f32_16x16x16_bf16 v[232:235], a[252:253], v[248:249], 0\n"
    "s_mov_b64 exec, 0\n"
    "s_branch LPG2\n"
    "LWRET3:\n"
    "s_mov_b64 exec, -1\n"
    "v_add_f32 v243, v243, v212\n"
    "v_cvt_pk_bf16_f32 v204, v204, v205\n"
    "v_exp_f32 v213, v213\n"
    "v_cvt_pk_bf16_f32 v205, v206, v207\n"
    "v_add_f32 v243, v243, v213\n"
    "v_exp_f32 v214, v214\n"
    "v_add_f32 v192, v156, v232\n"
    "v_add_f32 v193, v157, v233\n"
    "v_add_f32 v194, v158, v234\n"
    "v_add_f32 v195, v159, v235\n"
    "v_mfma_f32_16x16x32_bf16 v[208:211], a[124:127], a[76:79], v[232:235]\n"
    "v_add_f32 v243, v243, v214\n"
    "v_cvt_pk_bf16_f32 v206, v212, v213\n"
    "v_mfma_f32_16x16x32_bf16 v[208:211], a[120:123], a[72:75], v[208:211]\n"
    "v_exp_f32 v215, v215\n"
    "v_mfma_f32_16x16x32_bf16 v[208:211], a[116:119], a[68:71], v[208:211]\n"
    "s_waitcnt vmcnt(8) lgkmcnt(0)\n"
    "v_add_f32 v243, v243, v215\n"
    "v_cvt_pk_bf16_f32 v207, v214, v215\n"
    "v_mfma_f32_16x16x32_bf16 v[208:211], a[112:115], a[64:67], v[208:211]\n"
    "s_barrier\n"
    "v_exp_f32 v192, v192\n"
    "v_mfma_f32_16x16x32_bf16 v[96:99], v[200:203], a[160:163], 0\n"
    "s_cmp_eq_u32 s62, 23\n"
    "ds_read_b128 a[96:99], v246 offset:33024\n"
    "v_mov_b32 v244, v192\n"
    "v_mfma_f32_16x16x32_bf16 v[96:99], v[204:207], a[192:195], v[96:99]\n"
    "s_cselect_b32 s60, s61, s60\n"
    "v_exp_f32 v193, v193\n"
    "v_mfma_f32_16x16x32_bf16 v[100:103], v[204:207], a[196:199], 0\n"
    "s_cselect_b32 s61, s79, s61\n"
    "ds_read_b128 a[100:103], v246 offset:33280\n"
    "v_add_f32 v244, v244, v193\n"
    "v_mfma_f32_16x16x32_bf16 v[100:103], v[200:203], a[164:167], v[100:103]\n"
    "s_cselect_b32 s62, 0, s62\n"
    "v_exp_f32 v194, v194\n"
    "v_mfma_f32_16x16x32_bf16 v[104:107], v[200:203], a[168:171], 0\n"
    "s_mov_b32 m0, 16512\n"
    "ds_read_b128 a[104:107], v246 offset:33536\n"
    "v_add_f32 v244, v244, v194\n"
    "v_mfma_f32_16x16x32_bf16 v[104:107], v[204:207], a[200:203], v[104:107]\n"
    "buffer_load_dwordx4 v253, s[36:39], s60 offen offset:0 lds\n"
    "v_exp_f32 v195, v195\n"
    "v_mfma_f32_16x16x32_bf16 v[108:111], v[204:207], a[204:207], 0\n"
    "s_add_u32 s62, s62, 1\n"
    "ds_read_b128 a[108:111], v246 offset:33792\n"
    "v_add_f32 v244, v244, v195\n"
    "v_mfma_f32_16x16x32_bf16 v[108:111], v[200:203], a[172:175], v[108:111]\n"
    "v_exp_f32 v208, v208\n"
    "v_mfma_f32_16x16x32_bf16 v[196:199], a[128:131], a[64:67], v[232:235]\n"
    "v_add_f32 v244, v244, v208\n"
    "v_cvt_pk_bf16_f32 v192, v192, v193\n"
    "v_mfma_f32_16x16x32_bf16 v[196:199], a[132:135], a[68:71], v[196:199]\n"
    "v_exp_f32 v209, v209\n"
    "v_mfma_f32_16x16x32_bf16 v[196:199], a[136:139], a[72:75], v[196:199]\n"
    "ds_read_b64_tr_b16 a[240:241], v254 offset:33032\n"
    "v_add_f32 v244, v244, v209\n"
    "v_cvt_pk_bf16_f32 v193, v194, v195\n"
    "v_mfma_f32_16x16x32_bf16 v[196:199], a[140:143], a[76:79], v[196:199]\n"
    "v_exp_f32 v210, v210\n"
    "v_mfma_f32_16x16x32_bf16 v[212:215], a[156:159], a[76:79], v[232:235]\n"
    "buffer_load_dwordx4 v253, s[36:39], s60 offen offset:1024 lds\n"
    "v_add_f32 v244, v244, v210\n"
    "v_cvt_pk_bf16_f32 v194, v208, v209\n"
    "v_mfma_f32_16x16x32_bf16 v[212:215], a[152:155], a[72:75], v[212:215]\n"
    "v_exp_f32 v211, v211\n"
    "v_mfma_f32_16x16x32_bf16 v[212:215], a[148:151], a[68:71], v[212:215]\n"
    "v_add_f32 v244, v244, v211\n"
    "v_cvt_pk_bf16_f32 v195, v210, v211\n"
    "v_mfma_f32_16x16x32_bf16 v[212:215], a[144:147], a[64:67], v[212:215]\n"
    "buffer_load_dwordx4 v253, s[40:43], s60 offen offset:2048 lds\n"
    "v_exp_f32 v196, v196\n"
    "v_mfma_f32_16x16x32_bf16 v[112:115], v[200:203], a[176:179], 0\n"
    "ds_read_b128 a[124:127], v246 offset:42048\n"
    "v_add_f32 v244, v244, v196\n"
    "v_mfma_f32_16x16x32_bf16 v[112:115], v[204:207], a[208:211], v[112:115]\n"
    "v_exp_f32 v197, v197\n"
    "v_mfma_f32_16x16x32_bf16 v[116:119], v[204:207], a[212:215], 0\n"
    "ds_read_b128 a[120:123], v246 offset:41792\n"
    "v_add_f32 v244, v244, v197\n"
    "v_mfma_f32_16x16x32_bf16 v[116:119], v[200:203], a[180:183], v[116:119]\n"
    "v_exp_f32 v198, v198\n"
    "v_mfma_f32_16x16x32_bf16 v[120:123], v[200:203], a[184:187], 0\n"
    "ds_read_b128 a[116:119], v246 offset:41536\n"
    "v_add_f32 v244, v244, v198\n"
    "v_mfma_f32_16x16x32_bf16 v[120:123], v[204:207], a[216:219], v[120:123]\n"
    "v_exp_f32 v199, v199\n"
    "v_mfma_f32_16x16x32_bf16 v[124:127], v[204:207], a[220:223], 0\n"
    "ds_read_b128 a[112:115], v246 offset:41280\n"
    "v_add_f32 v244, v244, v199\n"
    "v_mfma_f32_16x16x32_bf16 v[124:127], v[200:203], a[188:191], v[124:127]\n"
    "s_waitcnt lgkmcnt(3)\n"
    "buffer_load_dwordx4 v253, s[44:47], s60 offen offset:3080 lds\n"
    "v_exp_f32 v212, v212\n"
    "v_mfma_f32_16x16x32_bf16 v[200:203], a[96:99], a[0:3], v[216:219]\n"
    "v_add_f32 v244, v244, v212\n"
    "v_cvt_pk_bf16_f32 v196, v196, v197\n"
    "v_mfma_f32_16x16x32_bf16 v[200:203], a[100:103], a[4:7], v[200:203]\n"
    "v_exp_f32 v213, v213\n"
    "v_mfma_f32_16x16x32_bf16 v[200:203], a[104:107], a[8:11], v[200:203]\n"
    "s_add_u32 s60, s60, 2048\n"
    "ds_read_b64_tr_b16 a[242:243], v254 offset:41288\n"
    "v_add_f32 v244, v244, v213\n"
    "v_cvt_pk_bf16_f32 v197, v198, v199\n"
    "v_mfma_f32_16x16x32_bf16 v[200:203], a[108:111], a[12:15], v[200:203]\n"
    "s_mov_b32 m0, 20640\n"
    "v_exp_f32 v214, v214\n"
    "v_mfma_f32_16x16x32_bf16 v[208:211], a[124:127], a[12:15], v[216:219]\n"
    "s_waitcnt lgkmcnt(1)\n"
    "v_add_f32 v244, v244, v214\n"
    "v_cvt_pk_bf16_f32 v198, v212, v213\n"
    "v_mfma_f32_16x16x32_bf16 v[208:211], a[120:123], a[8:11], v[208:211]\n"
    "buffer_load_dwordx4 v253, s[36:39], s60 offen offset:0 lds\n"
    "v_exp_f32 v215, v215\n"
    "v_mfma_f32_16x16x32_bf16 v[208:211], a[116:119], a[4:7], v[208:211]\n"
    "v_add_f32 v244, v244, v215\n"
    "v_cvt_pk_bf16_f32 v199, v214, v215\n"
    "v_mfma_f32_16x16x32_bf16 v[208:211], a[112:115], a[0:3], v[208:211]\n"
    "v_exp_f32 v200, v200\n"
    "v_mfma_f32_16x16x32_bf16 v[128:131], v[192:195], a[160:163], 0\n"
    "ds_read_b64_tr_b16 a[224:225], v251 offset:33032\n"
    "v_add_f32 v240, v240, v200\n"
    "v_mfma_f32_16x16x32_bf16 v[128:131], v[196:199], a[192:195], v[128:131]\n"
    "buffer_load_dwordx4 v253, s[36:39], s60 offen offset:1024 lds\n"
    "v_exp_f32 v201, v201\n"
    "v_mfma_f32_16x16x32_bf16 v[132:135], v[196:199], a[196:199], 0\n"
    "ds_read_b64_tr_b16 a[226:227], v251 offset:41288\n"
    "v_add_f32 v240, v240, v201\n"
    "v_mfma_f32_16x16x32_bf16 v[132:135], v[192:195], a[164:167], v[132:135]\n"
    "v_exp_f32 v202, v202\n"
    "v_mfma_f32_16x16x32_bf16 v[136:139], v[192:195], a[168:171], 0\n"
    "ds_read_b64_tr_b16 a[228:229], v252 offset:33024\n"
    "v_add_f32 v240, v240, v202\n"
    "v_mfma_f32_16x16x32_bf16 v[136:139], v[196:199], a[200:203], v[136:139]\n"
    "ds_read_b64_tr_b16 a[166:167], v247 offset:41288\n"
    "v_exp_f32 v203, v203\n"
    "v_mfma_f32_16x16x32_bf16 v[140:143], v[196:199], a[204:207], 0\n"
    "ds_read_b64_tr_b16 a[230:231], v252 offset:41280\n"
    "v_add_f32 v240, v240, v203\n"
    "v_mfma_f32_16x16x32_bf16 v[140:143], v[192:195], a[172:175], v[140:143]\n"
    "v_exp_f32 v208, v208\n"
    "v_mfma_f32_16x16x32_bf16 v[204:207], a[96:99], a[16:19], v[220:223]\n"
    "v_add_f32 v240, v240, v208\n"
    "v_cvt_pk_bf16_f32 v200, v200, v201\n"
    "v_mfma_f32_16x16x32_bf16 v[204:207], a[100:103], a[20:23], v[204:207]\n"
    "v_exp_f32 v209, v209\n"
    "v_mfma_f32_16x16x32_bf16 v[204:207], a[104:107], a[24:27], v[204:207]\n"
    "ds_read_b64_tr_b16 a[160:161], v247 offset:33024\n"
    "v_add_f32 v240, v240, v209\n"
    "v_cvt_pk_bf16_f32 v201, v202, v203\n"
    "v_mfma_f32_16x16x32_bf16 v[204:207], a[108:111], a[28:31], v[204:207]\n"
    "ds_read_b64_tr_b16 a[162:163], v247 offset:41280\n"
    "v_exp_f32 v210, v210\n"
    "v_mfma_f32_16x16x32_bf16 v[212:215], a[124:127], a[28:31], v[220:223]\n"
    "ds_read_b64_tr_b16 a[164:165], v247 offset:33032\n"
    "v_add_f32 v240, v240, v210\n"
    "v_cvt_pk_bf16_f32 v202, v208, v209\n"
    "v_mfma_f32_16x16x32_bf16 v[212:215], a[120:123], a[24:27], v[212:215]\n"
    "v_exp_f32 v211, v211\n"
    "v_mfma_f32_16x16x32_bf16 v[212:215], a[116:119], a[20:23], v[212:215]\n"
    "ds_read_b64_tr_b16 a[168:169], v251 offset:33024\n"
    "v_add_f32 v240, v240, v211\n"
    "v_cvt_pk_bf16_f32 v203, v210, v211\n"
    "v_mfma_f32_16x16x32_bf16 v[212:215], a[112:115], a[16:19], v[212:215]\n"
    "buffer_load_dwordx4 v253, s[40:43], s60 offen offset:2048 lds\n"
    "v_exp_f32 v204, v204\n"
    "v_mfma_f32_16x16x32_bf16 v[144:147], v[192:195], a[176:179], 0\n"
    "ds_read_b64_tr_b16 a[232:233], v252 offset:33032\n"
    "v_add_f32 v241, v241, v204\n"
    "v_mfma_f32_16x16x32_bf16 v[144:147], v[196:199], a[208:211], v[144:147]\n"
    "v_exp_f32 v205, v205\n"
    "v_mfma_f32_16x16x32_bf16 v[148:151], v[196:199], a[212:215], 0\n"
    "ds_read_b64_tr_b16 a[234:235], v252 offset:41288\n"
    "v_add_f32 v241, v241, v205\n"
    "v_mfma_f32_16x16x32_bf16 v[148:151], v[192:195], a[180:183], v[148:151]\n"
    "v_exp_f32 v206, v206\n"
    "v_mfma_f32_16x16x32_bf16 v[152:155], v[192:195], a[184:187], 0\n"
    "ds_read_b64_tr_b16 a[236:237], v254 offset:33024\n"
    "v_add_f32 v241, v241, v206\n"
    "v_mfma_f32_16x16x32_bf16 v[152:155], v[196:199], a[216:219], v[152:155]\n"
    "ds_read_b64_tr_b16 a[170:171], v251 offset:41280\n"
    "v_exp_f32 v207, v207\n"
    "v_mfma_f32_16x16x32_bf16 v[156:159], v[196:199], a[220:223], 0\n"
    "ds_read_b64_tr_b16 a[238:239], v254 offset:41280\n"
    "v_add_f32 v241, v241, v207\n"
    "v_mfma_f32_16x16x32_bf16 v[156:159], v[192:195], a[188:191], v[156:159]\n"
    "buffer_load_dwordx4 v253, s[44:47], s60 offen offset:3080 lds\n"
    "s_add_u32 s60, s60, 2048\n"
    "v_exp_f32 v212, v212\n"
    "v_mfma_f32_16x16x32_bf16 v[192:195], a[96:99], a[32:35], v[224:227]\n"
    "v_add_f32 v241, v241, v212\n"
    "v_cvt_pk_bf16_f32 v204, v204, v205\n"
    "v_mfma_f32_16x16x32_bf16 v[192:195], a[100:103], a[36:39], v[192:195]\n"
    "v_exp_f32 v213, v213\n"
    "v_mfma_f32_16x16x32_bf16 v[192:195], a[104:107], a[40:43], v[192:195]\n"
    "v_add_f32 v241, v241, v213\n"
    "v_cvt_pk_bf16_f32 v205, v206, v207\n"
    "v_mfma_f32_16x16x32_bf16 v[192:195], a[108:111], a[44:47], v[192:195]\n"
    "v_exp_f32 v214, v214\n"
    "v_mfma_f32_16x16x32_bf16 v[208:211], a[124:127], a[44:47], v[224:227]\n"
    "v_add_f32 v241, v241, v214\n"
    "v_cvt_pk_bf16_f32 v206, v212, v213\n"
    "v_mfma_f32_16x16x32_bf16 v[208:211], a[120:123], a[40:43], v[208:211]\n"
    "v_exp_f32 v215, v215\n"
    "v_mfma_f32_16x16x32_bf16 v[208:211], a[116:119], a[36:39], v[208:211]\n"
    "s_waitcnt vmcnt(12) lgkmcnt(0)\n"
    "v_add_f32 v241, v241, v215\n"
    "v_cvt_pk_bf16_f32 v207, v214, v215\n"
    "v_mfma_f32_16x16x32_bf16 v[208:211], a[112:115], a[32:35], v[208:211]\n"
    "s_barrier\n"
    "v_exp_f32 v192, v192\n"
    "v_mfma_f32_16x16x32_bf16 v[0:3], v[200:203], a[160:163], v[0:3]\n"
    "s_cmp_eq_u32 s62, 23\n"
    "ds_read_b128 a[128:131], v246 offset:49536\n"
    "v_add_f32 v242, v242, v192\n"
    "v_mfma_f32_16x16x32_bf16 v[4:7], v[200:203], a[164:167], v[4:7]\n"
    "s_cselect_b32 s60, s61, s60\n"
    "ds_read_b64_tr_b16 a[84:85], v252 offset:49544\n"
    "v_exp_f32 v193, v193\n"
    "v_mfma_f32_16x16x32_bf16 v[8:11], v[200:203], a[168:171], v[8:11]\n"
    "s_cselect_b32 s61, s79, s61\n"
    "ds_read_b128 a[132:135], v246 offset:49792\n"
    "v_add_f32 v242, v242, v193\n"
    "v_mfma_f32_16x16x32_bf16 v[12:15], v[200:203], a[224:227], v[12:15]\n"
    "s_cselect_b32 s62, 0, s62\n"
    "v_exp_f32 v194, v194\n"
    "v_mfma_f32_16x16x32_bf16 v[16:19], v[200:203], a[228:231], v[16:19]\n"
    "s_mov_b32 m0, 24768\n"
    "ds_read_b128 a[136:139], v246 offset:50048\n"
    "v_add_f32 v242, v242, v194\n"
    "v_mfma_f32_16x16x32_bf16 v[20:23], v[200:203], a[232:235], v[20:23]\n"
    "ds_read_b64_tr_b16 a[86:87], v252 offset:57800\n"
    "buffer_load_dwordx4 v253, s[36:39], s60 offen offset:0 lds\n"
    "v_exp_f32 v195, v195\n"
    "v_mfma_f32_16x16x32_bf16 v[24:27], v[200:203], a[236:239], v[24:27]\n"
    "ds_read_b128 a[140:143], v246 offset:50304\n"
    "buffer_load_dwordx4 v253, s[36:39], s60 offen offset:1024 lds\n"
    "v_add_f32 v242, v242, v195\n"
    "v_mfma_f32_16x16x32_bf16 v[28:31], v[200:203], a[240:243], v[28:31]\n"
    "buffer_load_dwordx4 v253, s[40:43], s60 offen offset:2048 lds\n"
    "v_exp_f32 v208, v208\n"
    "v_mfma_f32_16x16x32_bf16 v[196:199], a[96:99], a[48:51], v[228:231]\n"
    "v_add_f32 v242, v242, v208\n"
    "v_cvt_pk_bf16_f32 v192, v192, v193\n"
    "v_mfma_f32_16x16x32_bf16 v[196:199], a[100:103], a[52:55], v[196:199]\n"
    "v_exp_f32 v209, v209\n"
    "v_mfma_f32_16x16x32_bf16 v[196:199], a[104:107], a[56:59], v[196:199]\n"
    "ds_read_b64_tr_b16 a[200:201], v251 offset:49536\n"
    "buffer_load_dwordx4 v253, s[44:47], s60 offen offset:3080 lds\n"
    "s_add_u32 s60, s60, 2048\n"
    "s_mov_b32 m0, 28896\n"
    "v_add_f32_e64 v242, v242, v209\n"
    "v_cvt_pk_bf16_f32 v193, v194, v195\n"
    "v_mfma_f32_16x16x32_bf16 v[196:199], a[108:111], a[60:63], v[196:199]\n"
    "ds_read_b64_tr_b16 a[244:245], v251 offset:49544\n"
    "buffer_load_dwordx4 v253, s[36:39], s60 offen offset:0 lds\n"
    "v_exp_f32 v210, v210\n"
    "v_mfma_f32_16x16x32_bf16 v[212:215], a[124:127], a[60:63], v[228:231]\n"
    "ds_read_b64_tr_b16 a[80:81], v252 offset:49536\n"
    "buffer_load_dwordx4 v253, s[36:39], s60 offen offset:1024 lds\n"
    "v_add_f32 v242, v242, v210\n"
    "v_cvt_pk_bf16_f32 v194, v208, v209\n"
    "v_mfma_f32_16x16x32_bf16 v[212:215], a[120:123], a[56:59], v[212:215]\n"
    "buffer_load_dwordx4 v253, s[40:43], s60 offen offset:2048 lds\n"
    "v_exp_f32_e64 v211, v211\n"
    "v_mfma_f32_16x16x32_bf16 v[212:215], a[116:119], a[52:55], v[212:215]\n"
    "ds_read_b64_tr_b16 a[88:89], v254 offset:49536\n"
    "buffer_load_dwordx4 v253, s[44:47], s60 offen offset:3080 lds\n"
    "s_add_u32 s60, s60, 2048\n"
    "s_mov_b32 m0, s78\n"
    "v_add_f32 v242, v242, v211\n"
    "v_cvt_pk_bf16_f32 v195, v210, v211\n"
    "v_mfma_f32_16x16x32_bf16 v[212:215], a[112:115], a[48:51], v[212:215]\n"
    "buffer_load_dwordx4 v253, s[48:51], s77 offen offset:0 lds\n"
    "s_add_u32 s78, s78, 4128\n"
    "v_exp_f32 v196, v196\n"
    "v_mfma_f32_16x16x32_bf16 v[32:35], v[204:207], a[160:163], v[32:35]\n"
    "ds_read_b128 a[156:159], v246 offset:58560\n"
    "buffer_load_dwordx4 v253, s[48:51], s77 offen offset:1024 lds\n"
    "v_add_f32 v243, v243, v196\n"
    "v_mfma_f32_16x16x32_bf16 v[36:39], v[204:207], a[164:167], v[36:39]\n"
    "buffer_load_dwordx4 v253, s[52:55], s77 offen offset:2080 lds\n"
    "v_exp_f32_e64 v197, v197\n"
    "v_mfma_f32_16x16x32_bf16 v[40:43], v[204:207], a[168:171], v[40:43]\n"
    "ds_read_b128 a[152:155], v246 offset:58304\n"
    "buffer_load_dwordx4 v253, s[52:55], s77 offen offset:3104 lds\n"
    "s_add_u32 s77, s77, 4096\n"
    "s_mov_b32 m0, 33024\n"
    "v_add_f32 v243, v243, v197\n"
    "v_mfma_f32_16x16x32_bf16 v[44:47], v[204:207], a[224:227], v[44:47]\n"
    "buffer_load_dwordx4 v253, s[36:39], s60 offen offset:0 lds\n"
    "s_add_u32 s62, s62, 1\n"
    "v_exp_f32 v198, v198\n"
    "v_mfma_f32_16x16x32_bf16 v[48:51], v[204:207], a[228:231], v[48:51]\n"
    "ds_read_b128 a[148:151], v246 offset:58048\n"
    "buffer_load_dwordx4 v253, s[36:39], s60 offen offset:1024 lds\n"
    "v_add_f32 v243, v243, v198\n"
    "v_mfma_f32_16x16x32_bf16 v[52:55], v[204:207], a[232:235], v[52:55]\n"
    "ds_read_b64_tr_b16 a[90:91], v254 offset:57792\n"
    "buffer_load_dwordx4 v253, s[40:43], s60 offen offset:2048 lds\n"
    "v_exp_f32 v199, v199\n"
    "v_mfma_f32_16x16x32_bf16 v[56:59], v[204:207], a[236:239], v[56:59]\n"
    "s_waitcnt lgkmcnt(2)\n"
    "ds_read_b128 a[144:147], v246 offset:57792\n"
    "buffer_load_dwordx4 v253, s[44:47], s60 offen offset:3080 lds\n"
    "v_add_f32 v243, v243, v199\n"
    "v_mfma_f32_16x16x32_bf16 v[60:63], v[204:207], a[240:243], v[60:63]\n"
    "s_add_u32 s60, s60, 2048\n"
    "s_mov_b32 m0, 37152\n"
    "v_exp_f32 v212, v212\n"
    "v_mfma_f32_16x16x32_bf16 v[200:203], a[128:131], a[48:51], v[228:231]\n"
    "v_add_f32 v243, v243, v212\n"
    "v_cvt_pk_bf16_f32 v196, v196, v197\n"
    "v_mfma_f32_16x16x32_bf16 v[200:203], a[132:135], a[52:55], v[200:203]\n"
    "v_exp_f32 v213, v213\n"
    "v_mfma_f32_16x16x32_bf16 v[200:203], a[136:139], a[56:59], v[200:203]\n"
    "ds_read_b64_tr_b16 a[202:203], v251 offset:57792\n"
    "buffer_load_dwordx4 v253, s[36:39], s60 offen offset:0 lds\n"
    "v_add_f32_e64 v243, v243, v213\n"
    "v_cvt_pk_bf16_f32 v197, v198, v199\n"
    "v_mfma_f32_16x16x32_bf16 v[200:203], a[140:143], a[60:63], v[200:203]\n"
    "ds_read_b64_tr_b16 a[246:247], v251 offset:57800\n"
    "buffer_load_dwordx4 v253, s[36:39], s60 offen offset:1024 lds\n"
    "v_exp_f32 v214, v214\n"
    "v_mfma_f32_16x16x32_bf16 v[208:211], a[156:159], a[60:63], v[228:231]\n"
    "ds_read_b64_tr_b16 a[82:83], v252 offset:57792\n"
    "buffer_load_dwordx4 v253, s[40:43], s60 offen offset:2048 lds\n"
    "v_add_f32 v243, v243, v214\n"
    "v_cvt_pk_bf16_f32 v198, v212, v213\n"
    "v_mfma_f32_16x16x32_bf16 v[208:211], a[152:155], a[56:59], v[208:211]\n"
    "s_waitcnt lgkmcnt(3)\n"
    "buffer_load_dwordx4 v253, s[44:47], s60 offen offset:3080 lds\n"
    "s_add_u32 s60, s60, 2048\n"
    "s_mov_b32 m0, 41280\n"
    "v_exp_f32 v215, v215\n"
    "v_mfma_f32_16x16x32_bf16 v[208:211], a[148:151], a[52:55], v[208:211]\n"
    "ds_read_b64_tr_b16 a[92:93], v254 offset:49544\n"
    "buffer_load_dwordx4 v253, s[36:39], s60 offen offset:0 lds\n"
    "v_add_f32 v243, v243, v215\n"
    "v_cvt_pk_bf16_f32 v199, v214, v215\n"
    "v_mfma_f32_16x16x32_bf16 v[208:211], a[144:147], a[48:51], v[208:211]\n"
    "buffer_load_dwordx4 v253, s[36:39], s60 offen offset:1024 lds\n"
    "v_exp_f32 v200, v200\n"
    "v_mfma_f32_16x16x32_bf16 v[64:67], v[192:195], a[160:163], v[64:67]\n"
    "ds_read_b64_tr_b16 a[192:193], v247 offset:49536\n"
    "buffer_load_dwordx4 v253, s[40:43], s60 offen offset:2048 lds\n"
    "v_add_f32_e64 v243, v243, v200\n"
    "v_mfma_f32_16x16x32_bf16 v[68:71], v[192:195], a[164:167], v[68:71]\n"
    "buffer_load_dwordx4 v253, s[44:47], s60 offen offset:3080 lds\n"
    "s_add_u32 s60, s60, 2048\n"
    "s_mov_b32 m0, 45408\n"
    "v_exp_f32 v201, v201\n"
    "v_mfma_f32_16x16x32_bf16 v[72:75], v[192:195], a[168:171], v[72:75]\n"
    "ds_read_b64_tr_b16 a[194:195], v247 offset:57792\n"
    "buffer_load_dwordx4 v253, s[36:39], s60 offen offset:0 lds\n"
    "v_add_f32 v243, v243, v201\n"
    "v_mfma_f32_16x16x32_bf16 v[76:79], v[192:195], a[224:227], v[76:79]\n"
    "buffer_load_dwordx4 v253, s[36:39], s60 offen offset:1024 lds\n"
    "v_exp_f32 v202, v202\n"
    "v_mfma_f32_16x16x32_bf16 v[80:83], v[192:195], a[228:231], v[80:83]\n"
    "ds_read_b64_tr_b16 a[196:197], v247 offset:49544\n"
    "buffer_load_dwordx4 v253, s[40:43], s60 offen offset:2048 lds\n"
    "v_add_f32 v243, v243, v202\n"
    "v_mfma_f32_16x16x32_bf16 v[84:87], v[192:195], a[232:235], v[84:87]\n"
    "ds_read_b64_tr_b16 a[94:95], v254 offset:57800\n"
    "buffer_load_dwordx4 v253, s[44:47], s60 offen offset:3080 lds\n"
    "s_add_u32 s60, s60, 2048\n"
    "s_mov_b32 m0, s78\n"
    "v_exp_f32 v203, v203\n"
    "v_mfma_f32_16x16x32_bf16 v[88:91], v[192:195], a[236:239], v[88:91]\n"
    "ds_read_b64_tr_b16 a[198:199], v247 offset:57800\n"
    "buffer_load_dwordx4 v253, s[48:51], s77 offen offset:0 lds\n"
    "s_add_u32 s78, s78, 4128\n"
    "v_add_f32 v243, v243, v203\n"
    "v_mfma_f32_16x16x32_bf16 v[92:95], v[192:195], a[240:243], v[92:95]\n"
    "buffer_load_dwordx4 v253, s[48:51], s77 offen offset:1024 lds\n"
    "v_exp_f32 v208, v208\n"
    "v_mfma_f32_16x16x32_bf16 v[204:207], a[96:99], a[64:67], v[232:235]\n"
    "v_add_f32 v243, v243, v208\n"
    "v_cvt_pk_bf16_f32 v200, v200, v201\n"
    "v_mfma_f32_16x16x32_bf16 v[204:207], a[100:103], a[68:71], v[204:207]\n"
    "v_exp_f32 v209, v209\n"
    "v_mfma_f32_16x16x32_bf16 v[204:207], a[104:107], a[72:75], v[204:207]\n"
    "buffer_load_dwordx4 v253, s[52:55], s77 offen offset:2080 lds\n"
    "v_add_f32_e64 v243, v243, v209\n"
    "v_cvt_pk_bf16_f32 v201, v202, v203\n"
    "v_mfma_f32_16x16x32_bf16 v[204:207], a[108:111], a[76:79], v[204:207]\n"
    "buffer_load_dwordx4 v253, s[52:55], s77 offen offset:3104 lds\n"
    "s_add_u32 s77, s77, 4096\n"
    "v_exp_f32 v210, v210\n"
    "v_mfma_f32_16x16x32_bf16 v[212:215], a[124:127], a[76:79], v[232:235]\n"
    "v_add_f32 v243, v243, v210\n"
    "v_cvt_pk_bf16_f32 v202, v208, v209\n"
    "v_mfma_f32_16x16x32_bf16 v[212:215], a[120:123], a[72:75], v[212:215]\n"
    "v_exp_f32 v211, v211\n"
    "v_mfma_f32_16x16x32_bf16 v[212:215], a[116:119], a[68:71], v[212:215]\n"
    "s_waitcnt vmcnt(40) lgkmcnt(0)\n"
    "v_add_f32 v243, v243, v211\n"
    "v_cvt_pk_bf16_f32 v203, v210, v211\n"
    "v_mfma_f32_16x16x32_bf16 v[212:215], a[112:115], a[64:67], v[212:215]\n"
    "s_barrier\n"
    "v_exp_f32 v204, v204\n"
    "v_mfma_f32_16x16x32_bf16 v[96:99], v[196:199], a[160:163], v[96:99]\n"
    "s_cmp_eq_u32 s62, 23\n"
    "ds_read_b128 a[96:99], v246 offset:0\n"
    "v_add_f32 v244, v244, v204\n"
    "v_mfma_f32_16x16x32_bf16 v[96:99], v[200:203], a[192:195], v[96:99]\n"
    "s_cselect_b32 s60, s61, s60\n"
    "v_exp_f32 v205, v205\n"
    "v_mfma_f32_16x16x32_bf16 v[100:103], v[200:203], a[196:199], v[100:103]\n"
    "s_cselect_b32 s61, s79, s61\n"
    "ds_read_b128 a[100:103], v246 offset:256\n"
    "v_add_f32 v244, v244, v205\n"
    "v_mfma_f32_16x16x32_bf16 v[100:103], v[196:199], a[164:167], v[100:103]\n"
    "s_cselect_b32 s62, 0, s62\n"
    "v_exp_f32 v206, v206\n"
    "v_mfma_f32_16x16x32_bf16 v[104:107], v[196:199], a[168:171], v[104:107]\n"
    "s_mov_b32 m0, 49536\n"
    "ds_read_b128 a[104:107], v246 offset:512\n"
    "v_add_f32 v244, v244, v206\n"
    "v_mfma_f32_16x16x32_bf16 v[104:107], v[200:203], a[200:203], v[104:107]\n"
    "buffer_load_dwordx4 v253, s[36:39], s60 offen offset:0 lds\n"
    "v_exp_f32 v207, v207\n"
    "v_mfma_f32_16x16x32_bf16 v[108:111], v[200:203], a[244:247], v[108:111]\n"
    "s_add_u32 s62, s62, 1\n"
    "ds_read_b128 a[108:111], v246 offset:768\n"
    "v_add_f32 v244, v244, v207\n"
    "v_mfma_f32_16x16x32_bf16 v[108:111], v[196:199], a[224:227], v[108:111]\n"
    "buffer_load_dwordx4 v253, s[36:39], s60 offen offset:1024 lds\n"
    "v_exp_f32 v212, v212\n"
    "v_mfma_f32_16x16x32_bf16 v[192:195], a[128:131], a[64:67], v[232:235]\n"
    "v_add_f32 v244, v244, v212\n"
    "v_cvt_pk_bf16_f32 v204, v204, v205\n"
    "v_mfma_f32_16x16x32_bf16 v[192:195], a[132:135], a[68:71], v[192:195]\n"
    "s_mov_b32 s63, 4\n"
    "LOOP1_B:\n"
    "v_exp_f32 v213, v213\n"
    "v_mfma_f32_16x16x32_bf16 v[192:195], a[136:139], a[72:75], v[192:195]\n"
    "ds_read_b64_tr_b16 a[184:185], v254 offset:0\n"
    "v_add_f32 v244, v244, v213\n"
    "v_cvt_pk_bf16_f32 v205, v206, v207\n"
    "v_mfma_f32_16x16x32_bf16 v[192:195], a[140:143], a[76:79], v[192:195]\n"
    "v_exp_f32 v214, v214\n"
    "v_mfma_f32_16x16x32_bf16 v[208:211], a[156:159], a[76:79], v[232:235]\n"
    "buffer_load_dwordx4 v253, s[40:43], s60 offen offset:2048 lds\n"
    "v_add_f32 v244, v244, v214\n"
    "v_cvt_pk_bf16_f32 v206, v212, v213\n"
    "v_mfma_f32_16x16x32_bf16 v[208:211], a[152:155], a[72:75], v[208:211]\n"
    "buffer_load_dwordx4 v253, s[44:47], s60 offen offset:3080 lds\n"
    "v_exp_f32 v215, v215\n"
    "v_mfma_f32_16x16x32_bf16 v[208:211], a[148:151], a[68:71], v[208:211]\n"
    "s_add_u32 s60, s60, 2048\n"
    "s_mov_b32 m0, 53664\n"
    "v_add_f32 v244, v244, v215\n"
    "v_cvt_pk_bf16_f32 v207, v214, v215\n"
    "v_mfma_f32_16x16x32_bf16 v[208:211], a[144:147], a[64:67], v[208:211]\n"
    "buffer_load_dwordx4 v253, s[36:39], s60 offen offset:0 lds\n"
    "v_exp_f32 v192, v192\n"
    "v_mfma_f32_16x16x32_bf16 v[112:115], v[196:199], a[228:231], v[112:115]\n"
    "ds_read_b128 a[124:127], v246 offset:9024\n"
    "v_add_f32 v244, v244, v192\n"
    "v_mfma_f32_16x16x32_bf16 v[112:115], v[200:203], a[80:83], v[112:115]\n"
    "buffer_load_dwordx4 v253, s[36:39], s60 offen offset:1024 lds\n"
    "v_exp_f32 v193, v193\n"
    "v_mfma_f32_16x16x32_bf16 v[116:119], v[200:203], a[84:87], v[116:119]\n"
    "ds_read_b128 a[120:123], v246 offset:8768\n"
    "v_add_f32 v244, v244, v193\n"
    "v_mfma_f32_16x16x32_bf16 v[116:119], v[196:199], a[232:235], v[116:119]\n"
    "v_exp_f32 v194, v194\n"
    "v_mfma_f32_16x16x32_bf16 v[120:123], v[196:199], a[236:239], v[120:123]\n"
    "ds_read_b128 a[116:119], v246 offset:8512\n"
    "v_add_f32 v244, v244, v194\n"
    "v_mfma_f32_16x16x32_bf16 v[120:123], v[200:203], a[88:91], v[120:123]\n"
    "buffer_load_dwordx4 v253, s[40:43], s60 offen offset:2048 lds\n"
    "v_exp_f32 v195, v195\n"
    "v_mfma_f32_16x16x32_bf16 v[124:127], v[200:203], a[92:95], v[124:127]\n"
    "ds_read_b128 a[112:115], v246 offset:8256\n"
    "v_add_f32 v244, v244, v195\n"
    "v_mfma_f32_16x16x32_bf16 v[124:127], v[196:199], a[240:243], v[124:127]\n"
    "buffer_load_dwordx4 v253, s[44:47], s60 offen offset:3080 lds\n"
    "v_exp_f32 v208, v208\n"
    "v_mfma_f32_16x16x32_bf16 v[196:199], a[128:131], a[0:3], v[216:219]\n"
    "v_add_f32 v244, v244, v208\n"
    "v_cvt_pk_bf16_f32 v192, v192, v193\n"
    "v_mfma_f32_16x16x32_bf16 v[196:199], a[132:135], a[4:7], v[196:199]\n"
    "v_exp_f32 v209, v209\n"
    "v_mfma_f32_16x16x32_bf16 v[196:199], a[136:139], a[8:11], v[196:199]\n"
    "s_add_u32 s60, s60, 2048\n"
    "ds_read_b64_tr_b16 a[186:187], v254 offset:8256\n"
    "v_add_f32 v244, v244, v209\n"
    "v_cvt_pk_bf16_f32 v193, v194, v195\n"
    "v_mfma_f32_16x16x32_bf16 v[196:199], a[140:143], a[12:15], v[196:199]\n"
    "s_mov_b32 m0, 57792\n"
    "v_exp_f32 v210, v210\n"
    "v_mfma_f32_16x16x32_bf16 v[212:215], a[156:159], a[12:15], v[216:219]\n"
    "buffer_load_dwordx4 v253, s[36:39], s60 offen offset:0 lds\n"
    "v_add_f32 v244, v244, v210\n"
    "v_cvt_pk_bf16_f32 v194, v208, v209\n"
    "v_mfma_f32_16x16x32_bf16 v[212:215], a[152:155], a[8:11], v[212:215]\n"
    "buffer_load_dwordx4 v253, s[36:39], s60 offen offset:1024 lds\n"
    "v_exp_f32 v211, v211\n"
    "v_mfma_f32_16x16x32_bf16 v[212:215], a[148:151], a[4:7], v[212:215]\n"
    "v_add_f32 v244, v244, v211\n"
    "v_cvt_pk_bf16_f32 v195, v210, v211\n"
    "v_mfma_f32_16x16x32_bf16 v[212:215], a[144:147], a[0:3], v[212:215]\n"
    "buffer_load_dwordx4 v253, s[40:43], s60 offen offset:2048 lds\n"
    "v_exp_f32 v196, v196\n"
    "v_mfma_f32_16x16x32_bf16 v[128:131], v[204:207], a[160:163], v[128:131]\n"
    "ds_read_b64_tr_b16 a[160:161], v247 offset:0\n"
    "v_add_f32 v240, v240, v196\n"
    "v_mfma_f32_16x16x32_bf16 v[128:131], v[192:195], a[192:195], v[128:131]\n"
    "buffer_load_dwordx4 v253, s[44:47], s60 offen offset:3080 lds\n"
    "v_exp_f32 v197, v197\n"
    "v_mfma_f32_16x16x32_bf16 v[132:135], v[192:195], a[196:199], v[132:135]\n"
    "s_add_u32 s60, s60, 2048\n"
    "ds_read_b64_tr_b16 a[162:163], v247 offset:8256\n"
    "v_add_f32 v240, v240, v197\n"
    "v_mfma_f32_16x16x32_bf16 v[132:135], v[204:207], a[164:167], v[132:135]\n"
    "s_mov_b32 m0, 61920\n"
    "v_exp_f32 v198, v198\n"
    "v_mfma_f32_16x16x32_bf16 v[136:139], v[204:207], a[168:171], v[136:139]\n"
    "ds_read_b64_tr_b16 a[164:165], v247 offset:8\n"
    "v_add_f32 v240, v240, v198\n"
    "v_mfma_f32_16x16x32_bf16 v[136:139], v[192:195], a[200:203], v[136:139]\n"
    "buffer_load_dwordx4 v253, s[36:39], s60 offen offset:0 lds\n"
    "v_exp_f32 v199, v199\n"
    "v_mfma_f32_16x16x32_bf16 v[140:143], v[192:195], a[244:247], v[140:143]\n"
    "ds_read_b64_tr_b16 a[166:167], v247 offset:8264\n"
    "v_add_f32 v240, v240, v199\n"
    "v_mfma_f32_16x16x32_bf16 v[140:143], v[204:207], a[224:227], v[140:143]\n"
    "s_waitcnt lgkmcnt(3)\n"
    "buffer_load_dwordx4 v253, s[36:39], s60 offen offset:1024 lds\n"
    "v_exp_f32 v212, v212\n"
    "v_mfma_f32_16x16x32_bf16 v[200:203], a[96:99], a[0:3], v[216:219]\n"
    "v_add_f32 v240, v240, v212\n"
    "v_cvt_pk_bf16_f32 v196, v196, v197\n"
    "v_mfma_f32_16x16x32_bf16 v[200:203], a[100:103], a[4:7], v[200:203]\n"
    "v_exp_f32 v213, v213\n"
    "v_mfma_f32_16x16x32_bf16 v[200:203], a[104:107], a[8:11], v[200:203]\n"
    "ds_read_b64_tr_b16 a[188:189], v254 offset:8\n"
    "v_add_f32 v240, v240, v213\n"
    "v_cvt_pk_bf16_f32 v197, v198, v199\n"
    "v_mfma_f32_16x16x32_bf16 v[200:203], a[108:111], a[12:15], v[200:203]\n"
    "v_exp_f32 v214, v214\n"
    "v_mfma_f32_16x16x32_bf16 v[208:211], a[124:127], a[12:15], v[216:219]\n"
    "buffer_load_dwordx4 v253, s[40:43], s60 offen offset:2048 lds\n"
    "v_add_f32 v240, v240, v214\n"
    "v_cvt_pk_bf16_f32 v198, v212, v213\n"
    "v_mfma_f32_16x16x32_bf16 v[208:211], a[120:123], a[8:11], v[208:211]\n"
    "buffer_load_dwordx4 v253, s[44:47], s60 offen offset:3080 lds\n"
    "v_exp_f32 v215, v215\n"
    "v_mfma_f32_16x16x32_bf16 v[208:211], a[116:119], a[4:7], v[208:211]\n"
    "s_add_u32 s60, s60, 2048\n"
    "v_add_f32 v240, v240, v215\n"
    "v_cvt_pk_bf16_f32 v199, v214, v215\n"
    "v_mfma_f32_16x16x32_bf16 v[208:211], a[112:115], a[0:3], v[208:211]\n"
    "s_mov_b32 m0, s78\n"
    "v_exp_f32 v200, v200\n"
    "v_mfma_f32_16x16x32_bf16 v[144:147], v[204:207], a[228:231], v[144:147]\n"
    "ds_read_b64_tr_b16 a[168:169], v251 offset:0\n"
    "v_add_f32 v240, v240, v200\n"
    "v_mfma_f32_16x16x32_bf16 v[144:147], v[192:195], a[80:83], v[144:147]\n"
    "buffer_load_dwordx4 v253, s[48:51], s77 offen offset:0 lds\n"
    "v_exp_f32 v201, v201\n"
    "v_mfma_f32_16x16x32_bf16 v[148:151], v[192:195], a[84:87], v[148:151]\n"
    "s_add_u32 s78, s78, 4128\n"
    "ds_read_b64_tr_b16 a[170:171], v251 offset:8256\n"
    "v_add_f32 v240, v240, v201\n"
    "v_mfma_f32_16x16x32_bf16 v[148:151], v[204:207], a[232:235], v[148:151]\n"
    "v_exp_f32 v202, v202\n"
    "v_mfma_f32_16x16x32_bf16 v[152:155], v[204:207], a[236:239], v[152:155]\n"
    "ds_read_b64_tr_b16 a[172:173], v251 offset:8\n"
    "v_add_f32 v240, v240, v202\n"
    "v_mfma_f32_16x16x32_bf16 v[152:155], v[192:195], a[88:91], v[152:155]\n"
    "v_exp_f32 v203, v203\n"
    "v_mfma_f32_16x16x32_bf16 v[156:159], v[192:195], a[92:95], v[156:159]\n"
    "ds_read_b64_tr_b16 a[174:175], v251 offset:8264\n"
    "v_add_f32 v240, v240, v203\n"
    "v_mfma_f32_16x16x32_bf16 v[156:159], v[204:207], a[240:243], v[156:159]\n"
    "buffer_load_dwordx4 v253, s[48:51], s77 offen offset:1024 lds\n"
    "v_exp_f32 v208, v208\n"
    "v_mfma_f32_16x16x32_bf16 v[204:207], a[128:131], a[16:19], v[220:223]\n"
    "v_add_f32 v240, v240, v208\n"
    "v_cvt_pk_bf16_f32 v200, v200, v201\n"
    "v_mfma_f32_16x16x32_bf16 v[204:207], a[132:135], a[20:23], v[204:207]\n"
    "v_exp_f32 v209, v209\n"
    "v_mfma_f32_16x16x32_bf16 v[204:207], a[136:139], a[24:27], v[204:207]\n"
    "ds_read_b64_tr_b16 a[190:191], v254 offset:8264\n"
    "v_add_f32 v240, v240, v209\n"
    "v_cvt_pk_bf16_f32 v201, v202, v203\n"
    "v_mfma_f32_16x16x32_bf16 v[204:207], a[140:143], a[28:31], v[204:207]\n"
    "v_exp_f32 v210, v210\n"
    "v_mfma_f32_16x16x32_bf16 v[212:215], a[156:159], a[28:31], v[220:223]\n"
    "v_add_f32 v240, v240, v210\n"
    "v_cvt_pk_bf16_f32 v202, v208, v209\n"
    "v_mfma_f32_16x16x32_bf16 v[212:215], a[152:155], a[24:27], v[212:215]\n"
    "buffer_load_dwordx4 v253, s[52:55], s77 offen offset:2080 lds\n"
    "v_exp_f32 v211, v211\n"
    "v_mfma_f32_16x16x32_bf16 v[212:215], a[148:151], a[20:23], v[212:215]\n"
    "v_add_f32 v240, v240, v211\n"
    "v_cvt_pk_bf16_f32 v203, v210, v211\n"
    "v_mfma_f32_16x16x32_bf16 v[212:215], a[144:147], a[16:19], v[212:215]\n"
    "v_exp_f32 v204, v204\n"
    "v_mfma_f32_16x16x32_bf16 v[0:3], v[196:199], a[192:195], v[0:3]\n"
    "s_waitcnt lgkmcnt(1)\n"
    "ds_read_b64_tr_b16 a[176:177], v252 offset:0\n"
    "v_add_f32 v241, v241, v204\n"
    "v_mfma_f32_16x16x32_bf16 v[0:3], v[200:203], a[160:163], v[0:3]\n"
    "buffer_load_dwordx4 v253, s[52:55], s77 offen offset:3104 lds\n"
    "v_exp_f32 v205, v205\n"
    "v_mfma_f32_16x16x32_bf16 v[4:7], v[200:203], a[164:167], v[4:7]\n"
    "s_add_u32 s77, s77, 4096\n"
    "ds_read_b64_tr_b16 a[178:179], v252 offset:8256\n"
    "v_add_f32 v241, v241, v205\n"
    "v_mfma_f32_16x16x32_bf16 v[4:7], v[196:199], a[196:199], v[4:7]\n"
    "v_exp_f32 v206, v206\n"
    "v_mfma_f32_16x16x32_bf16 v[8:11], v[196:199], a[200:203], v[8:11]\n"
    "ds_read_b64_tr_b16 a[180:181], v252 offset:8\n"
    "v_add_f32 v241, v241, v206\n"
    "v_mfma_f32_16x16x32_bf16 v[8:11], v[200:203], a[168:171], v[8:11]\n"
    "v_exp_f32 v207, v207\n"
    "v_mfma_f32_16x16x32_bf16 v[12:15], v[200:203], a[172:175], v[12:15]\n"
    "ds_read_b64_tr_b16 a[182:183], v252 offset:8264\n"
    "v_add_f32 v241, v241, v207\n"
    "v_mfma_f32_16x16x32_bf16 v[12:15], v[196:199], a[244:247], v[12:15]\n"
    "global_load_dword v161, v160, s[82:83]\n"
    "v_exp_f32 v212, v212\n"
    "v_mfma_f32_16x16x32_bf16 v[192:195], a[96:99], a[16:19], v[220:223]\n"
    "v_add_f32 v241, v241, v212\n"
    "v_cvt_pk_bf16_f32 v204, v204, v205\n"
    "v_mfma_f32_16x16x32_bf16 v[192:195], a[100:103], a[20:23], v[192:195]\n"
    "v_exp_f32 v213, v213\n"
    "v_mfma_f32_16x16x32_bf16 v[192:195], a[104:107], a[24:27], v[192:195]\n"
    "v_add_f32 v241, v241, v213\n"
    "v_cvt_pk_bf16_f32 v205, v206, v207\n"
    "v_mfma_f32_16x16x32_bf16 v[192:195], a[108:111], a[28:31], v[192:195]\n"
    "v_exp_f32 v214, v214\n"
    "v_mfma_f32_16x16x32_bf16 v[208:211], a[124:127], a[28:31], v[220:223]\n"
    "v_add_f32 v241, v241, v214\n"
    "v_cvt_pk_bf16_f32 v206, v212, v213\n"
    "v_mfma_f32_16x16x32_bf16 v[208:211], a[120:123], a[24:27], v[208:211]\n"
    "v_exp_f32 v215, v215\n"
    "v_mfma_f32_16x16x32_bf16 v[208:211], a[116:119], a[20:23], v[208:211]\n"
    "s_waitcnt vmcnt(45) lgkmcnt(0)\n"
    "v_add_f32 v241, v241, v215\n"
    "v_cvt_pk_bf16_f32 v207, v214, v215\n"
    "v_mfma_f32_16x16x32_bf16 v[208:211], a[112:115], a[16:19], v[208:211]\n"
    "s_barrier\n"
    "v_exp_f32 v192, v192\n"
    "v_mfma_f32_16x16x32_bf16 v[16:19], v[196:199], a[80:83], v[16:19]\n"
    "s_cmp_eq_u32 s62, 23\n"
    "ds_read_b64_tr_b16 a[204:205], v251 offset:16520\n"
    "v_add_f32 v241, v241, v192\n"
    "v_mfma_f32_16x16x32_bf16 v[16:19], v[200:203], a[176:179], v[16:19]\n"
    "s_cselect_b32 s60, s61, s60\n"
    "v_exp_f32 v193, v193\n"
    "v_mfma_f32_16x16x32_bf16 v[20:23], v[200:203], a[180:183], v[20:23]\n"
    "s_cselect_b32 s61, s79, s61\n"
    "ds_read_b64_tr_b16 a[206:207], v251 offset:24776\n"
    "v_add_f32 v241, v241, v193\n"
    "v_mfma_f32_16x16x32_bf16 v[20:23], v[196:199], a[84:87], v[20:23]\n"
    "s_cselect_b32 s62, 0, s62\n"
    "v_exp_f32 v194, v194\n"
    "v_mfma_f32_16x16x32_bf16 v[24:27], v[196:199], a[88:91], v[24:27]\n"
    "s_mov_b32 m0, 0\n"
    "ds_read_b64_tr_b16 a[208:209], v252 offset:16512\n"
    "v_add_f32 v241, v241, v194\n"
    "v_mfma_f32_16x16x32_bf16 v[24:27], v[200:203], a[184:187], v[24:27]\n"
    "buffer_load_dwordx4 v253, s[36:39], s60 offen offset:0 lds\n"
    "v_exp_f32 v195, v195\n"
    "v_mfma_f32_16x16x32_bf16 v[28:31], v[200:203], a[188:191], v[28:31]\n"
    "s_add_u32 s62, s62, 1\n"
    "ds_read_b64_tr_b16 a[210:211], v252 offset:24768\n"
    "v_add_f32 v241, v241, v195\n"
    "v_mfma_f32_16x16x32_bf16 v[28:31], v[196:199], a[92:95], v[28:31]\n"
    "buffer_load_dwordx4 v253, s[36:39], s60 offen offset:1024 lds\n"
    "v_exp_f32 v208, v208\n"
    "v_mfma_f32_16x16x32_bf16 v[196:199], a[128:131], a[32:35], v[224:227]\n"
    "v_add_f32 v241, v241, v208\n"
    "v_cvt_pk_bf16_f32 v192, v192, v193\n"
    "v_mfma_f32_16x16x32_bf16 v[196:199], a[132:135], a[36:39], v[196:199]\n"
    "v_exp_f32 v209, v209\n"
    "v_mfma_f32_16x16x32_bf16 v[196:199], a[136:139], a[40:43], v[196:199]\n"
    "v_add_f32 v241, v241, v209\n"
    "v_cvt_pk_bf16_f32 v193, v194, v195\n"
    "v_mfma_f32_16x16x32_bf16 v[196:199], a[140:143], a[44:47], v[196:199]\n"
    "buffer_load_dwordx4 v253, s[40:43], s60 offen offset:2048 lds\n"
    "v_exp_f32 v210, v210\n"
    "v_mfma_f32_16x16x32_bf16 v[212:215], a[156:159], a[44:47], v[224:227]\n"
    "v_add_f32_e64 v241, v241, v210\n"
    "v_cvt_pk_bf16_f32 v194, v208, v209\n"
    "v_mfma_f32_16x16x32_bf16 v[212:215], a[152:155], a[40:43], v[212:215]\n"
    "buffer_load_dwordx4 v253, s[44:47], s60 offen offset:3080 lds\n"
    "s_add_u32 s60, s60, 2048\n"
    "s_mov_b32 m0, 4128\n"
    "v_exp_f32 v211, v211\n"
    "v_mfma_f32_16x16x32_bf16 v[212:215], a[148:151], a[36:39], v[212:215]\n"
    "buffer_load_dwordx4 v253, s[36:39], s60 offen offset:0 lds\n"
    "v_add_f32 v241, v241, v211\n"
    "v_cvt_pk_bf16_f32 v195, v210, v211\n"
    "v_mfma_f32_16x16x32_bf16 v[212:215], a[144:147], a[32:35], v[212:215]\n"
    "v_exp_f32 v196, v196\n"
    "v_mfma_f32_16x16x32_bf16 v[32:35], v[204:207], a[192:195], v[32:35]\n"
    "ds_read_b128 a[128:131], v246 offset:16512\n"
    "v_add_f32 v242, v242, v196\n"
    "v_mfma_f32_16x16x32_bf16 v[32:35], v[192:195], a[160:163], v[32:35]\n"
    "buffer_load_dwordx4 v253, s[36:39], s60 offen offset:1024 lds\n"
    "v_exp_f32 v197, v197\n"
    "v_mfma_f32_16x16x32_bf16 v[36:39], v[192:195], a[164:167], v[36:39]\n"
    "ds_read_b128 a[132:135], v246 offset:16768\n"
    "v_add_f32 v242, v242, v197\n"
    "v_mfma_f32_16x16x32_bf16 v[36:39], v[204:207], a[196:199], v[36:39]\n"
    "v_exp_f32 v198, v198\n"
    "v_mfma_f32_16x16x32_bf16 v[40:43], v[204:207], a[200:203], v[40:43]\n"
    "ds_read_b128 a[136:139], v246 offset:17024\n"
    "v_add_f32 v242, v242, v198\n"
    "v_mfma_f32_16x16x32_bf16 v[40:43], v[192:195], a[168:171], v[40:43]\n"
    "buffer_load_dwordx4 v253, s[40:43], s60 offen offset:2048 lds\n"
    "v_exp_f32 v199, v199\n"
    "v_mfma_f32_16x16x32_bf16 v[44:47], v[192:195], a[172:175], v[44:47]\n"
    "ds_read_b128 a[140:143], v246 offset:17280\n"
    "v_add_f32 v242, v242, v199\n"
    "v_mfma_f32_16x16x32_bf16 v[44:47], v[204:207], a[244:247], v[44:47]\n"
    "buffer_load_dwordx4 v253, s[44:47], s60 offen offset:3080 lds\n"
    "v_exp_f32 v212, v212\n"
    "v_mfma_f32_16x16x32_bf16 v[200:203], a[96:99], a[32:35], v[224:227]\n"
    "v_add_f32 v242, v242, v212\n"
    "v_cvt_pk_bf16_f32 v196, v196, v197\n"
    "v_mfma_f32_16x16x32_bf16 v[200:203], a[100:103], a[36:39], v[200:203]\n"
    "v_exp_f32 v213, v213\n"
    "v_mfma_f32_16x16x32_bf16 v[200:203], a[104:107], a[40:43], v[200:203]\n"
    "s_add_u32 s60, s60, 2048\n"
    "s_mov_b32 m0, 8256\n"
    "v_add_f32 v242, v242, v213\n"
    "v_cvt_pk_bf16_f32 v197, v198, v199\n"
    "v_mfma_f32_16x16x32_bf16 v[200:203], a[108:111], a[44:47], v[200:203]\n"
    "buffer_load_dwordx4 v253, s[36:39], s60 offen offset:0 lds\n"
    "v_exp_f32 v214, v214\n"
    "v_mfma_f32_16x16x32_bf16 v[208:211], a[124:127], a[44:47], v[224:227]\n"
    "v_add_f32 v242, v242, v214\n"
    "v_cvt_pk_bf16_f32 v198, v212, v213\n"
    "v_mfma_f32_16x16x32_bf16 v[208:211], a[120:123], a[40:43], v[208:211]\n"
    "buffer_load_dwordx4 v253, s[36:39], s60 offen offset:1024 lds\n"
    "v_exp_f32 v215, v215\n"
    "v_mfma_f32_16x16x32_bf16 v[208:211], a[116:119], a[36:39], v[208:211]\n"
    "buffer_load_dwordx4 v253, s[40:43], s60 offen offset:2048 lds\n"
    "v_add_f32 v242, v242, v215\n"
    "v_cvt_pk_bf16_f32 v199, v214, v215\n"
    "v_mfma_f32_16x16x32_bf16 v[208:211], a[112:115], a[32:35], v[208:211]\n"
    "v_exp_f32 v200, v200\n"
    "v_mfma_f32_16x16x32_bf16 v[48:51], v[204:207], a[80:83], v[48:51]\n"
    "ds_read_b128 a[156:159], v246 offset:25536\n"
    "v_add_f32 v242, v242, v200\n"
    "v_mfma_f32_16x16x32_bf16 v[48:51], v[192:195], a[176:179], v[48:51]\n"
    "buffer_load_dwordx4 v253, s[44:47], s60 offen offset:3080 lds\n"
    "v_exp_f32 v201, v201\n"
    "v_mfma_f32_16x16x32_bf16 v[52:55], v[192:195], a[180:183], v[52:55]\n"
    "s_add_u32 s60, s60, 2048\n"
    "ds_read_b128 a[152:155], v246 offset:25280\n"
    "v_add_f32 v242, v242, v201\n"
    "v_mfma_f32_16x16x32_bf16 v[52:55], v[204:207], a[84:87], v[52:55]\n"
    "s_mov_b32 m0, 12384\n"
    "v_exp_f32 v202, v202\n"
    "v_mfma_f32_16x16x32_bf16 v[56:59], v[204:207], a[88:91], v[56:59]\n"
    "ds_read_b128 a[148:151], v246 offset:25024\n"
    "v_add_f32 v242, v242, v202\n"
    "v_mfma_f32_16x16x32_bf16 v[56:59], v[192:195], a[184:187], v[56:59]\n"
    "buffer_load_dwordx4 v253, s[36:39], s60 offen offset:0 lds\n"
    "v_exp_f32 v203, v203\n"
    "v_mfma_f32_16x16x32_bf16 v[60:63], v[192:195], a[188:191], v[60:63]\n"
    "ds_read_b128 a[144:147], v246 offset:24768\n"
    "v_add_f32 v242, v242, v203\n"
    "v_mfma_f32_16x16x32_bf16 v[60:63], v[204:207], a[92:95], v[60:63]\n"
    "buffer_load_dwordx4 v253, s[36:39], s60 offen offset:1024 lds\n"
    "v_exp_f32 v208, v208\n"
    "v_mfma_f32_16x16x32_bf16 v[204:207], a[96:99], a[48:51], v[228:231]\n"
    "v_add_f32 v242, v242, v208\n"
    "v_cvt_pk_bf16_f32 v200, v200, v201\n"
    "v_mfma_f32_16x16x32_bf16 v[204:207], a[100:103], a[52:55], v[204:207]\n"
    "v_exp_f32 v209, v209\n"
    "v_mfma_f32_16x16x32_bf16 v[204:207], a[104:107], a[56:59], v[204:207]\n"
    "v_add_f32 v242, v242, v209\n"
    "v_cvt_pk_bf16_f32 v201, v202, v203\n"
    "v_mfma_f32_16x16x32_bf16 v[204:207], a[108:111], a[60:63], v[204:207]\n"
    "buffer_load_dwordx4 v253, s[40:43], s60 offen offset:2048 lds\n"
    "v_exp_f32 v210, v210\n"
    "v_mfma_f32_16x16x32_bf16 v[212:215], a[124:127], a[60:63], v[228:231]\n"
    "v_add_f32 v242, v242, v210\n"
    "v_cvt_pk_bf16_f32 v202, v208, v209\n"
    "v_mfma_f32_16x16x32_bf16 v[212:215], a[120:123], a[56:59], v[212:215]\n"
    "buffer_load_dwordx4 v253, s[44:47], s60 offen offset:3080 lds\n"
    "v_exp_f32 v211, v211\n"
    "v_mfma_f32_16x16x32_bf16 v[212:215], a[116:119], a[52:55], v[212:215]\n"
    "s_add_u32 s60, s60, 2048\n"
    "v_add_f32 v242, v242, v211\n"
    "v_cvt_pk_bf16_f32 v203, v210, v211\n"
    "v_mfma_f32_16x16x32_bf16 v[212:215], a[112:115], a[48:51], v[212:215]\n"
    "s_mov_b32 m0, s78\n"
    "v_exp_f32 v204, v204\n"
    "v_mfma_f32_16x16x32_bf16 v[64:67], v[196:199], a[192:195], v[64:67]\n"
    "ds_read_b64_tr_b16 a[212:213], v252 offset:16520\n"
    "v_add_f32 v243, v243, v204\n"
    "v_mfma_f32_16x16x32_bf16 v[64:67], v[200:203], a[160:163], v[64:67]\n"
    "buffer_load_dwordx4 v253, s[48:51], s77 offen offset:0 lds\n"
    "v_exp_f32 v205, v205\n"
    "v_mfma_f32_16x16x32_bf16 v[68:71], v[200:203], a[164:167], v[68:71]\n"
    "s_add_u32 s78, s78, 4128\n"
    "ds_read_b64_tr_b16 a[214:215], v252 offset:24776\n"
    "v_add_f32 v243, v243, v205\n"
    "v_mfma_f32_16x16x32_bf16 v[68:71], v[196:199], a[196:199], v[68:71]\n"
    "v_exp_f32 v206, v206\n"
    "v_mfma_f32_16x16x32_bf16 v[72:75], v[196:199], a[200:203], v[72:75]\n"
    "ds_read_b64_tr_b16 a[216:217], v254 offset:16512\n"
    "v_add_f32 v243, v243, v206\n"
    "v_mfma_f32_16x16x32_bf16 v[72:75], v[200:203], a[168:171], v[72:75]\n"
    "ds_read_b64_tr_b16 a[202:203], v251 offset:24768\n"
    "v_exp_f32 v207, v207\n"
    "v_mfma_f32_16x16x32_bf16 v[76:79], v[200:203], a[172:175], v[76:79]\n"
    "ds_read_b64_tr_b16 a[218:219], v254 offset:24768\n"
    "v_add_f32 v243, v243, v207\n"
    "v_mfma_f32_16x16x32_bf16 v[76:79], v[196:199], a[244:247], v[76:79]\n"
    "s_waitcnt lgkmcnt(4)\n"
    "v_exp_f32 v212, v212\n"
    "v_mfma_f32_16x16x32_bf16 v[192:195], a[128:131], a[48:51], v[228:231]\n"
    "v_add_f32 v243, v243, v212\n"
    "v_cvt_pk_bf16_f32 v204, v204, v205\n"
    "v_mfma_f32_16x16x32_bf16 v[192:195], a[132:135], a[52:55], v[192:195]\n"
    "v_exp_f32 v213, v213\n"
    "v_mfma_f32_16x16x32_bf16 v[192:195], a[136:139], a[56:59], v[192:195]\n"
    "ds_read_b64_tr_b16 a[196:197], v247 offset:16520\n"
    "v_add_f32 v243, v243, v213\n"
    "v_cvt_pk_bf16_f32 v205, v206, v207\n"
    "v_mfma_f32_16x16x32_bf16 v[192:195], a[140:143], a[60:63], v[192:195]\n"
    "ds_read_b64_tr_b16 a[198:199], v247 offset:24776\n"
    "v_exp_f32 v214, v214\n"
    "v_mfma_f32_16x16x32_bf16 v[208:211], a[156:159], a[60:63], v[228:231]\n"
    "ds_read_b64_tr_b16 a[200:201], v251 offset:16512\n"
    "v_add_f32 v243, v243, v214\n"
    "v_cvt_pk_bf16_f32 v206, v212, v213\n"
    "v_mfma_f32_16x16x32_bf16 v[208:211], a[152:155], a[56:59], v[208:211]\n"
    "buffer_load_dwordx4 v253, s[48:51], s77 offen offset:1024 lds\n"
    "v_exp_f32 v215, v215\n"
    "v_mfma_f32_16x16x32_bf16 v[208:211], a[148:151], a[52:55], v[208:211]\n"
    "v_add_f32 v243, v243, v215\n"
    "v_cvt_pk_bf16_f32 v207, v214, v215\n"
    "v_mfma_f32_16x16x32_bf16 v[208:211], a[144:147], a[48:51], v[208:211]\n"
    "v_exp_f32 v192, v192\n"
    "v_mfma_f32_16x16x32_bf16 v[80:83], v[196:199], a[80:83], v[80:83]\n"
    "ds_read_b64_tr_b16 a[220:221], v254 offset:16520\n"
    "v_add_f32 v243, v243, v192\n"
    "v_mfma_f32_16x16x32_bf16 v[80:83], v[200:203], a[176:179], v[80:83]\n"
    "buffer_load_dwordx4 v253, s[52:55], s77 offen offset:2080 lds\n"
    "v_exp_f32 v193, v193\n"
    "v_mfma_f32_16x16x32_bf16 v[84:87], v[200:203], a[180:183], v[84:87]\n"
    "ds_read_b64_tr_b16 a[222:223], v254 offset:24776\n"
    "v_add_f32 v243, v243, v193\n"
    "v_mfma_f32_16x16x32_bf16 v[84:87], v[196:199], a[84:87], v[84:87]\n"
    "v_exp_f32 v194, v194\n"
    "v_mfma_f32_16x16x32_bf16 v[88:91], v[196:199], a[88:91], v[88:91]\n"
    "ds_read_b64_tr_b16 a[192:193], v247 offset:16512\n"
    "v_add_f32 v243, v243, v194\n"
    "v_mfma_f32_16x16x32_bf16 v[88:91], v[200:203], a[184:187], v[88:91]\n"
    "v_exp_f32 v195, v195\n"
    "v_mfma_f32_16x16x32_bf16 v[92:95], v[200:203], a[188:191], v[92:95]\n"
    "ds_read_b64_tr_b16 a[194:195], v247 offset:24768\n"
    "v_add_f32 v243, v243, v195\n"
    "v_mfma_f32_16x16x32_bf16 v[92:95], v[196:199], a[92:95], v[92:95]\n"
    "buffer_load_dwordx4 v253, s[52:55], s77 offen offset:3104 lds\n"
    "s_add_u32 s77, s77, 4096\n"
    "v_exp_f32 v208, v208\n"
    "v_mfma_f32_16x16x32_bf16 v[196:199], a[96:99], a[64:67], v[232:235]\n"
    "v_add_f32 v243, v243, v208\n"
    "v_cvt_pk_bf16_f32 v192, v192, v193\n"
    "v_mfma_f32_16x16x32_bf16 v[196:199], a[100:103], a[68:71], v[196:199]\n"
    "v_exp_f32 v209, v209\n"
    "v_mfma_f32_16x16x32_bf16 v[196:199], a[104:107], a[72:75], v[196:199]\n"
    "v_add_f32 v243, v243, v209\n"
    "v_cvt_pk_bf16_f32 v193, v194, v195\n"
    "v_mfma_f32_16x16x32_bf16 v[196:199], a[108:111], a[76:79], v[196:199]\n"
    "v_exp_f32 v210, v210\n"
    "v_mfma_f32_16x16x32_bf16 v[212:215], a[124:127], a[76:79], v[232:235]\n"
    "v_add_f32 v243, v243, v210\n"
    "v_cvt_pk_bf16_f32 v194, v208, v209\n"
    "v_mfma_f32_16x16x32_bf16 v[212:215], a[120:123], a[72:75], v[212:215]\n"
    "v_exp_f32 v211, v211\n"
    "v_mfma_f32_16x16x32_bf16 v[212:215], a[116:119], a[68:71], v[212:215]\n"
    "s_waitcnt vmcnt(45) lgkmcnt(0)\n"
    "v_add_f32 v243, v243, v211\n"
    "v_cvt_pk_bf16_f32 v195, v210, v211\n"
    "v_mfma_f32_16x16x32_bf16 v[212:215], a[112:115], a[64:67], v[212:215]\n"
    "s_barrier\n"
    "v_exp_f32 v196, v196\n"
    "v_mfma_f32_16x16x32_bf16 v[96:99], v[204:207], a[160:163], v[96:99]\n"
    "s_cmp_eq_u32 s62, 23\n"
    "ds_read_b128 a[96:99], v246 offset:33024\n"
    "v_add_f32 v244, v244, v196\n"
    "v_mfma_f32_16x16x32_bf16 v[96:99], v[192:195], a[192:195], v[96:99]\n"
    "s_cselect_b32 s60, s61, s60\n"
    "v_exp_f32 v197, v197\n"
    "v_mfma_f32_16x16x32_bf16 v[100:103], v[192:195], a[196:199], v[100:103]\n"
    "s_cselect_b32 s61, s79, s61\n"
    "ds_read_b128 a[100:103], v246 offset:33280\n"
    "v_add_f32 v244, v244, v197\n"
    "v_mfma_f32_16x16x32_bf16 v[100:103], v[204:207], a[164:167], v[100:103]\n"
    "s_cselect_b32 s62, 0, s62\n"
    "v_exp_f32 v198, v198\n"
    "v_mfma_f32_16x16x32_bf16 v[104:107], v[204:207], a[168:171], v[104:107]\n"
    "s_mov_b32 m0, 16512\n"
    "ds_read_b128 a[104:107], v246 offset:33536\n"
    "v_add_f32 v244, v244, v198\n"
    "v_mfma_f32_16x16x32_bf16 v[104:107], v[192:195], a[200:203], v[104:107]\n"
    "buffer_load_dwordx4 v253, s[36:39], s60 offen offset:0 lds\n"
    "v_exp_f32 v199, v199\n"
    "v_mfma_f32_16x16x32_bf16 v[108:111], v[192:195], a[204:207], v[108:111]\n"
    "s_add_u32 s62, s62, 1\n"
    "ds_read_b128 a[108:111], v246 offset:33792\n"
    "v_add_f32 v244, v244, v199\n"
    "v_mfma_f32_16x16x32_bf16 v[108:111], v[204:207], a[172:175], v[108:111]\n"
    "buffer_load_dwordx4 v253, s[36:39], s60 offen offset:1024 lds\n"
    "v_exp_f32 v212, v212\n"
    "v_mfma_f32_16x16x32_bf16 v[200:203], a[128:131], a[64:67], v[232:235]\n"
    "v_add_f32 v244, v244, v212\n"
    "v_cvt_pk_bf16_f32 v196, v196, v197\n"
    "v_mfma_f32_16x16x32_bf16 v[200:203], a[132:135], a[68:71], v[200:203]\n"
    "v_exp_f32 v213, v213\n"
    "v_mfma_f32_16x16x32_bf16 v[200:203], a[136:139], a[72:75], v[200:203]\n"
    "ds_read_b64_tr_b16 a[236:237], v254 offset:33024\n"
    "v_add_f32 v244, v244, v213\n"
    "v_cvt_pk_bf16_f32 v197, v198, v199\n"
    "v_mfma_f32_16x16x32_bf16 v[200:203], a[140:143], a[76:79], v[200:203]\n"
    "v_exp_f32 v214, v214\n"
    "v_mfma_f32_16x16x32_bf16 v[208:211], a[156:159], a[76:79], v[232:235]\n"
    "buffer_load_dwordx4 v253, s[40:43], s60 offen offset:2048 lds\n"
    "v_add_f32 v244, v244, v214\n"
    "v_cvt_pk_bf16_f32 v198, v212, v213\n"
    "v_mfma_f32_16x16x32_bf16 v[208:211], a[152:155], a[72:75], v[208:211]\n"
    "buffer_load_dwordx4 v253, s[44:47], s60 offen offset:3080 lds\n"
    "v_exp_f32 v215, v215\n"
    "v_mfma_f32_16x16x32_bf16 v[208:211], a[148:151], a[68:71], v[208:211]\n"
    "s_add_u32 s60, s60, 2048\n"
    "s_mov_b32 m0, 20640\n"
    "v_add_f32 v244, v244, v215\n"
    "v_cvt_pk_bf16_f32 v199, v214, v215\n"
    "v_mfma_f32_16x16x32_bf16 v[208:211], a[144:147], a[64:67], v[208:211]\n"
    "buffer_load_dwordx4 v253, s[36:39], s60 offen offset:0 lds\n"
    "v_exp_f32 v200, v200\n"
    "v_mfma_f32_16x16x32_bf16 v[112:115], v[204:207], a[176:179], v[112:115]\n"
    "ds_read_b128 a[124:127], v246 offset:42048\n"
    "v_add_f32 v244, v244, v200\n"
    "v_mfma_f32_16x16x32_bf16 v[112:115], v[192:195], a[208:211], v[112:115]\n"
    "buffer_load_dwordx4 v253, s[36:39], s60 offen offset:1024 lds\n"
    "v_exp_f32 v201, v201\n"
    "v_mfma_f32_16x16x32_bf16 v[116:119], v[192:195], a[212:215], v[116:119]\n"
    "ds_read_b128 a[120:123], v246 offset:41792\n"
    "v_add_f32 v244, v244, v201\n"
    "v_mfma_f32_16x16x32_bf16 v[116:119], v[204:207], a[180:183], v[116:119]\n"
    "v_exp_f32 v202, v202\n"
    "v_mfma_f32_16x16x32_bf16 v[120:123], v[204:207], a[184:187], v[120:123]\n"
    "ds_read_b128 a[116:119], v246 offset:41536\n"
    "v_add_f32 v244, v244, v202\n"
    "v_mfma_f32_16x16x32_bf16 v[120:123], v[192:195], a[216:219], v[120:123]\n"
    "buffer_load_dwordx4 v253, s[40:43], s60 offen offset:2048 lds\n"
    "v_exp_f32 v203, v203\n"
    "v_mfma_f32_16x16x32_bf16 v[124:127], v[192:195], a[220:223], v[124:127]\n"
    "ds_read_b128 a[112:115], v246 offset:41280\n"
    "v_add_f32 v244, v244, v203\n"
    "v_mfma_f32_16x16x32_bf16 v[124:127], v[204:207], a[188:191], v[124:127]\n"
    "buffer_load_dwordx4 v253, s[44:47], s60 offen offset:3080 lds\n"
    "v_exp_f32 v208, v208\n"
    "v_mfma_f32_16x16x32_bf16 v[204:207], a[128:131], a[0:3], v[216:219]\n"
    "v_add_f32 v244, v244, v208\n"
    "v_cvt_pk_bf16_f32 v200, v200, v201\n"
    "v_mfma_f32_16x16x32_bf16 v[204:207], a[132:135], a[4:7], v[204:207]\n"
    "v_exp_f32 v209, v209\n"
    "v_mfma_f32_16x16x32_bf16 v[204:207], a[136:139], a[8:11], v[204:207]\n"
    "s_add_u32 s60, s60, 2048\n"
    "ds_read_b64_tr_b16 a[238:239], v254 offset:41280\n"
    "v_add_f32 v244, v244, v209\n"
    "v_cvt_pk_bf16_f32 v201, v202, v203\n"
    "v_mfma_f32_16x16x32_bf16 v[204:207], a[140:143], a[12:15], v[204:207]\n"
    "s_mov_b32 m0, 24768\n"
    "v_exp_f32 v210, v210\n"
    "v_mfma_f32_16x16x32_bf16 v[212:215], a[156:159], a[12:15], v[216:219]\n"
    "buffer_load_dwordx4 v253, s[36:39], s60 offen offset:0 lds\n"
    "v_add_f32 v244, v244, v210\n"
    "v_cvt_pk_bf16_f32 v202, v208, v209\n"
    "v_mfma_f32_16x16x32_bf16 v[212:215], a[152:155], a[8:11], v[212:215]\n"
    "buffer_load_dwordx4 v253, s[36:39], s60 offen offset:1024 lds\n"
    "v_exp_f32 v211, v211\n"
    "v_mfma_f32_16x16x32_bf16 v[212:215], a[148:151], a[4:7], v[212:215]\n"
    "v_add_f32 v244, v244, v211\n"
    "v_cvt_pk_bf16_f32 v203, v210, v211\n"
    "v_mfma_f32_16x16x32_bf16 v[212:215], a[144:147], a[0:3], v[212:215]\n"
    "buffer_load_dwordx4 v253, s[40:43], s60 offen offset:2048 lds\n"
    "v_exp_f32 v204, v204\n"
    "v_mfma_f32_16x16x32_bf16 v[128:131], v[196:199], a[160:163], v[128:131]\n"
    "ds_read_b64_tr_b16 a[160:161], v247 offset:33024\n"
    "v_add_f32 v240, v240, v204\n"
    "v_mfma_f32_16x16x32_bf16 v[128:131], v[200:203], a[192:195], v[128:131]\n"
    "buffer_load_dwordx4 v253, s[44:47], s60 offen offset:3080 lds\n"
    "v_exp_f32 v205, v205\n"
    "v_mfma_f32_16x16x32_bf16 v[132:135], v[200:203], a[196:199], v[132:135]\n"
    "s_add_u32 s60, s60, 2048\n"
    "ds_read_b64_tr_b16 a[162:163], v247 offset:41280\n"
    "v_add_f32 v240, v240, v205\n"
    "v_mfma_f32_16x16x32_bf16 v[132:135], v[196:199], a[164:167], v[132:135]\n"
    "s_mov_b32 m0, 28896\n"
    "v_exp_f32 v206, v206\n"
    "v_mfma_f32_16x16x32_bf16 v[136:139], v[196:199], a[168:171], v[136:139]\n"
    "ds_read_b64_tr_b16 a[164:165], v247 offset:33032\n"
    "v_add_f32 v240, v240, v206\n"
    "v_mfma_f32_16x16x32_bf16 v[136:139], v[200:203], a[200:203], v[136:139]\n"
    "buffer_load_dwordx4 v253, s[36:39], s60 offen offset:0 lds\n"
    "v_exp_f32 v207, v207\n"
    "v_mfma_f32_16x16x32_bf16 v[140:143], v[200:203], a[204:207], v[140:143]\n"
    "ds_read_b64_tr_b16 a[166:167], v247 offset:41288\n"
    "v_add_f32 v240, v240, v207\n"
    "v_mfma_f32_16x16x32_bf16 v[140:143], v[196:199], a[172:175], v[140:143]\n"
    "s_waitcnt lgkmcnt(3)\n"
    "buffer_load_dwordx4 v253, s[36:39], s60 offen offset:1024 lds\n"
    "v_exp_f32 v212, v212\n"
    "v_mfma_f32_16x16x32_bf16 v[192:195], a[96:99], a[0:3], v[216:219]\n"
    "v_add_f32 v240, v240, v212\n"
    "v_cvt_pk_bf16_f32 v204, v204, v205\n"
    "v_mfma_f32_16x16x32_bf16 v[192:195], a[100:103], a[4:7], v[192:195]\n"
    "v_exp_f32 v213, v213\n"
    "v_mfma_f32_16x16x32_bf16 v[192:195], a[104:107], a[8:11], v[192:195]\n"
    "ds_read_b64_tr_b16 a[240:241], v254 offset:33032\n"
    "v_add_f32 v240, v240, v213\n"
    "v_cvt_pk_bf16_f32 v205, v206, v207\n"
    "v_mfma_f32_16x16x32_bf16 v[192:195], a[108:111], a[12:15], v[192:195]\n"
    "v_exp_f32 v214, v214\n"
    "v_mfma_f32_16x16x32_bf16 v[208:211], a[124:127], a[12:15], v[216:219]\n"
    "buffer_load_dwordx4 v253, s[40:43], s60 offen offset:2048 lds\n"
    "v_add_f32 v240, v240, v214\n"
    "v_cvt_pk_bf16_f32 v206, v212, v213\n"
    "v_mfma_f32_16x16x32_bf16 v[208:211], a[120:123], a[8:11], v[208:211]\n"
    "buffer_load_dwordx4 v253, s[44:47], s60 offen offset:3080 lds\n"
    "v_exp_f32 v215, v215\n"
    "v_mfma_f32_16x16x32_bf16 v[208:211], a[116:119], a[4:7], v[208:211]\n"
    "s_add_u32 s60, s60, 2048\n"
    "v_add_f32 v240, v240, v215\n"
    "v_cvt_pk_bf16_f32 v207, v214, v215\n"
    "v_mfma_f32_16x16x32_bf16 v[208:211], a[112:115], a[0:3], v[208:211]\n"
    "s_mov_b32 m0, s78\n"
    "v_exp_f32 v192, v192\n"
    "v_mfma_f32_16x16x32_bf16 v[144:147], v[196:199], a[176:179], v[144:147]\n"
    "ds_read_b64_tr_b16 a[168:169], v251 offset:33024\n"
    "v_add_f32 v240, v240, v192\n"
    "v_mfma_f32_16x16x32_bf16 v[144:147], v[200:203], a[208:211], v[144:147]\n"
    "buffer_load_dwordx4 v253, s[48:51], s77 offen offset:0 lds\n"
    "v_exp_f32 v193, v193\n"
    "v_mfma_f32_16x16x32_bf16 v[148:151], v[200:203], a[212:215], v[148:151]\n"
    "s_add_u32 s78, s78, 4128\n"
    "ds_read_b64_tr_b16 a[170:171], v251 offset:41280\n"
    "v_add_f32 v240, v240, v193\n"
    "v_mfma_f32_16x16x32_bf16 v[148:151], v[196:199], a[180:183], v[148:151]\n"
    "v_exp_f32 v194, v194\n"
    "v_mfma_f32_16x16x32_bf16 v[152:155], v[196:199], a[184:187], v[152:155]\n"
    "ds_read_b64_tr_b16 a[224:225], v251 offset:33032\n"
    "v_add_f32 v240, v240, v194\n"
    "v_mfma_f32_16x16x32_bf16 v[152:155], v[200:203], a[216:219], v[152:155]\n"
    "v_exp_f32 v195, v195\n"
    "v_mfma_f32_16x16x32_bf16 v[156:159], v[200:203], a[220:223], v[156:159]\n"
    "ds_read_b64_tr_b16 a[226:227], v251 offset:41288\n"
    "v_add_f32 v240, v240, v195\n"
    "v_mfma_f32_16x16x32_bf16 v[156:159], v[196:199], a[188:191], v[156:159]\n"
    "v_exp_f32 v208, v208\n"
    "v_mfma_f32_16x16x32_bf16 v[196:199], a[128:131], a[16:19], v[220:223]\n"
    "v_add_f32 v240, v240, v208\n"
    "v_cvt_pk_bf16_f32 v192, v192, v193\n"
    "v_mfma_f32_16x16x32_bf16 v[196:199], a[132:135], a[20:23], v[196:199]\n"
    "v_exp_f32 v209, v209\n"
    "v_mfma_f32_16x16x32_bf16 v[196:199], a[136:139], a[24:27], v[196:199]\n"
    "ds_read_b64_tr_b16 a[242:243], v254 offset:41288\n"
    "v_add_f32 v240, v240, v209\n"
    "v_cvt_pk_bf16_f32 v193, v194, v195\n"
    "v_mfma_f32_16x16x32_bf16 v[196:199], a[140:143], a[28:31], v[196:199]\n"
    "buffer_load_dwordx4 v253, s[48:51], s77 offen offset:1024 lds\n"
    "v_exp_f32 v210, v210\n"
    "v_mfma_f32_16x16x32_bf16 v[212:215], a[156:159], a[28:31], v[220:223]\n"
    "v_add_f32 v240, v240, v210\n"
    "v_cvt_pk_bf16_f32 v194, v208, v209\n"
    "v_mfma_f32_16x16x32_bf16 v[212:215], a[152:155], a[24:27], v[212:215]\n"
    "v_exp_f32 v211, v211\n"
    "v_mfma_f32_16x16x32_bf16 v[212:215], a[148:151], a[20:23], v[212:215]\n"
    "v_add_f32 v240, v240, v211\n"
    "v_cvt_pk_bf16_f32 v195, v210, v211\n"
    "v_mfma_f32_16x16x32_bf16 v[212:215], a[144:147], a[16:19], v[212:215]\n"
    "buffer_load_dwordx4 v253, s[52:55], s77 offen offset:2080 lds\n"
    "v_exp_f32 v196, v196\n"
    "v_mfma_f32_16x16x32_bf16 v[0:3], v[204:207], a[192:195], v[0:3]\n"
    "s_waitcnt lgkmcnt(0)\n"
    "ds_read_b64_tr_b16 a[228:229], v252 offset:33024\n"
    "v_add_f32 v241, v241, v196\n"
    "v_mfma_f32_16x16x32_bf16 v[0:3], v[192:195], a[160:163], v[0:3]\n"
    "v_exp_f32 v197, v197\n"
    "v_mfma_f32_16x16x32_bf16 v[4:7], v[192:195], a[164:167], v[4:7]\n"
    "ds_read_b64_tr_b16 a[230:231], v252 offset:41280\n"
    "v_add_f32 v241, v241, v197\n"
    "v_mfma_f32_16x16x32_bf16 v[4:7], v[204:207], a[196:199], v[4:7]\n"
    "v_exp_f32 v198, v198\n"
    "v_mfma_f32_16x16x32_bf16 v[8:11], v[204:207], a[200:203], v[8:11]\n"
    "ds_read_b64_tr_b16 a[232:233], v252 offset:33032\n"
    "v_add_f32 v241, v241, v198\n"
    "v_mfma_f32_16x16x32_bf16 v[8:11], v[192:195], a[168:171], v[8:11]\n"
    "v_exp_f32 v199, v199\n"
    "v_mfma_f32_16x16x32_bf16 v[12:15], v[192:195], a[224:227], v[12:15]\n"
    "ds_read_b64_tr_b16 a[234:235], v252 offset:41288\n"
    "v_add_f32 v241, v241, v199\n"
    "v_mfma_f32_16x16x32_bf16 v[12:15], v[204:207], a[204:207], v[12:15]\n"
    "buffer_load_dwordx4 v253, s[52:55], s77 offen offset:3104 lds\n"
    "s_add_u32 s77, s77, 4096\n"
    "v_exp_f32 v212, v212\n"
    "v_mfma_f32_16x16x32_bf16 v[200:203], a[96:99], a[16:19], v[220:223]\n"
    "s_cmp_eq_u32 s63, 0\n"
    "v_add_f32 v241, v241, v212\n"
    "v_cvt_pk_bf16_f32 v196, v196, v197\n"
    "v_mfma_f32_16x16x32_bf16 v[200:203], a[100:103], a[20:23], v[200:203]\n"
    "s_cbranch_scc1 LMID_B\n"
    "v_exp_f32 v213, v213\n"
    "v_mfma_f32_16x16x32_bf16 v[200:203], a[104:107], a[24:27], v[200:203]\n"
    "v_add_f32 v241, v241, v213\n"
    "v_cvt_pk_bf16_f32 v197, v198, v199\n"
    "v_mfma_f32_16x16x32_bf16 v[200:203], a[108:111], a[28:31], v[200:203]\n"
    "v_exp_f32 v214, v214\n"
    "v_mfma_f32_16x16x32_bf16 v[208:211], a[124:127], a[28:31], v[220:223]\n"
    "v_add_f32 v241, v241, v214\n"
    "v_cvt_pk_bf16_f32 v198, v212, v213\n"
    "v_mfma_f32_16x16x32_bf16 v[208:211], a[120:123], a[24:27], v[208:211]\n"
    "v_exp_f32 v215, v215\n"
    "v_mfma_f32_16x16x32_bf16 v[208:211], a[116:119], a[20:23], v[208:211]\n"
    "s_waitcnt vmcnt(45) lgkmcnt(0)\n"
    "v_add_f32 v241, v241, v215\n"
    "v_cvt_pk_bf16_f32 v199, v214, v215\n"
    "v_mfma_f32_16x16x32_bf16 v[208:211], a[112:115], a[16:19], v[208:211]\n"
    "s_barrier\n"
    "v_exp_f32 v200, v200\n"
    "v_mfma_f32_16x16x32_bf16 v[16:19], v[204:207], a[208:211], v[16:19]\n"
    "s_cmp_eq_u32 s62, 23\n"
    "ds_read_b64_tr_b16 a[244:245], v251 offset:49544\n"
    "v_add_f32 v241, v241, v200\n"
    "v_mfma_f32_16x16x32_bf16 v[16:19], v[192:195], a[228:231], v[16:19]\n"
    "s_cselect_b32 s60, s61, s60\n"
    "v_exp_f32 v201, v201\n"
    "v_mfma_f32_16x16x32_bf16 v[20:23], v[192:195], a[232:235], v[20:23]\n"
    "s_cselect_b32 s61, s79, s61\n"
    "ds_read_b64_tr_b16 a[246:247], v251 offset:57800\n"
    "v_add_f32 v241, v241, v201\n"
    "v_mfma_f32_16x16x32_bf16 v[20:23], v[204:207], a[212:215], v[20:23]\n"
    "s_cselect_b32 s62, 0, s62\n"
    "v_exp_f32 v202, v202\n"
    "v_mfma_f32_16x16x32_bf16 v[24:27], v[204:207], a[216:219], v[24:27]\n"
    "s_mov_b32 m0, 33024\n"
    "ds_read_b64_tr_b16 a[80:81], v252 offset:49536\n"
    "v_add_f32 v241, v241, v202\n"
    "v_mfma_f32_16x16x32_bf16 v[24:27], v[192:195], a[236:239], v[24:27]\n"
    "buffer_load_dwordx4 v253, s[36:39], s60 offen offset:0 lds\n"
    "v_exp_f32 v203, v203\n"
    "v_mfma_f32_16x16x32_bf16 v[28:31], v[192:195], a[240:243], v[28:31]\n"
    "s_add_u32 s62, s62, 1\n"
    "ds_read_b64_tr_b16 a[82:83], v252 offset:57792\n"
    "v_add_f32 v241, v241, v203\n"
    "v_mfma_f32_16x16x32_bf16 v[28:31], v[204:207], a[220:223], v[28:31]\n"
    "buffer_load_dwordx4 v253, s[36:39], s60 offen offset:1024 lds\n"
    "v_exp_f32 v208, v208\n"
    "v_mfma_f32_16x16x32_bf16 v[204:207], a[128:131], a[32:35], v[224:227]\n"
    "v_add_f32 v241, v241, v208\n"
    "v_cvt_pk_bf16_f32 v200, v200, v201\n"
    "v_mfma_f32_16x16x32_bf16 v[204:207], a[132:135], a[36:39], v[204:207]\n"
    "v_exp_f32 v209, v209\n"
    "v_mfma_f32_16x16x32_bf16 v[204:207], a[136:139], a[40:43], v[204:207]\n"
    "v_add_f32 v241, v241, v209\n"
    "v_cvt_pk_bf16_f32 v201, v202, v203\n"
    "v_mfma_f32_16x16x32_bf16 v[204:207], a[140:143], a[44:47], v[204:207]\n"
    "buffer_load_dwordx4 v253, s[40:43], s60 offen offset:2048 lds\n"
    "v_exp_f32 v210, v210\n"
    "v_mfma_f32_16x16x32_bf16 v[212:215], a[156:159], a[44:47], v[224:227]\n"
    "v_add_f32_e64 v241, v241, v210\n"
    "v_cvt_pk_bf16_f32 v202, v208, v209\n"
    "v_mfma_f32_16x16x32_bf16 v[212:215], a[152:155], a[40:43], v[212:215]\n"
    "buffer_load_dwordx4 v253, s[44:47], s60 offen offset:3080 lds\n"
    "s_add_u32 s60, s60, 2048\n"
    "s_mov_b32 m0, 37152\n"
    "v_exp_f32 v211, v211\n"
    "v_mfma_f32_16x16x32_bf16 v[212:215], a[148:151], a[36:39], v[212:215]\n"
    "buffer_load_dwordx4 v253, s[36:39], s60 offen offset:0 lds\n"
    "v_add_f32 v241, v241, v211\n"
    "v_cvt_pk_bf16_f32 v203, v210, v211\n"
    "v_mfma_f32_16x16x32_bf16 v[212:215], a[144:147], a[32:35], v[212:215]\n"
    "v_exp_f32 v204, v204\n"
    "v_mfma_f32_16x16x32_bf16 v[32:35], v[196:199], a[192:195], v[32:35]\n"
    "ds_read_b128 a[128:131], v246 offset:49536\n"
    "v_add_f32 v242, v242, v204\n"
    "v_mfma_f32_16x16x32_bf16 v[32:35], v[200:203], a[160:163], v[32:35]\n"
    "buffer_load_dwordx4 v253, s[36:39], s60 offen offset:1024 lds\n"
    "v_exp_f32 v205, v205\n"
    "v_mfma_f32_16x16x32_bf16 v[36:39], v[200:203], a[164:167], v[36:39]\n"
    "ds_read_b128 a[132:135], v246 offset:49792\n"
    "v_add_f32 v242, v242, v205\n"
    "v_mfma_f32_16x16x32_bf16 v[36:39], v[196:199], a[196:199], v[36:39]\n"
    "v_exp_f32 v206, v206\n"
    "v_mfma_f32_16x16x32_bf16 v[40:43], v[196:199], a[200:203], v[40:43]\n"
    "ds_read_b128 a[136:139], v246 offset:50048\n"
    "v_add_f32 v242, v242, v206\n"
    "v_mfma_f32_16x16x32_bf16 v[40:43], v[200:203], a[168:171], v[40:43]\n"
    "buffer_load_dwordx4 v253, s[40:43], s60 offen offset:2048 lds\n"
    "v_exp_f32 v207, v207\n"
    "v_mfma_f32_16x16x32_bf16 v[44:47], v[200:203], a[224:227], v[44:47]\n"
    "ds_read_b128 a[140:143], v246 offset:50304\n"
    "v_add_f32 v242, v242, v207\n"
    "v_mfma_f32_16x16x32_bf16 v[44:47], v[196:199], a[204:207], v[44:47]\n"
    "buffer_load_dwordx4 v253, s[44:47], s60 offen offset:3080 lds\n"
    "v_exp_f32 v212, v212\n"
    "v_mfma_f32_16x16x32_bf16 v[192:195], a[96:99], a[32:35], v[224:227]\n"
    "v_add_f32 v242, v242, v212\n"
    "v_cvt_pk_bf16_f32 v204, v204, v205\n"
    "v_mfma_f32_16x16x32_bf16 v[192:195], a[100:103], a[36:39], v[192:195]\n"
    "v_exp_f32 v213, v213\n"
    "v_mfma_f32_16x16x32_bf16 v[192:195], a[104:107], a[40:43], v[192:195]\n"
    "s_add_u32 s60, s60, 2048\n"
    "s_mov_b32 m0, 41280\n"
    "v_add_f32 v242, v242, v213\n"
    "v_cvt_pk_bf16_f32 v205, v206, v207\n"
    "v_mfma_f32_16x16x32_bf16 v[192:195], a[108:111], a[44:47], v[192:195]\n"
    "buffer_load_dwordx4 v253, s[36:39], s60 offen offset:0 lds\n"
    "v_exp_f32 v214, v214\n"
    "v_mfma_f32_16x16x32_bf16 v[208:211], a[124:127], a[44:47], v[224:227]\n"
    "v_add_f32 v242, v242, v214\n"
    "v_cvt_pk_bf16_f32 v206, v212, v213\n"
    "v_mfma_f32_16x16x32_bf16 v[208:211], a[120:123], a[40:43], v[208:211]\n"
    "buffer_load_dwordx4 v253, s[36:39], s60 offen offset:1024 lds\n"
    "v_exp_f32 v215, v215\n"
    "v_mfma_f32_16x16x32_bf16 v[208:211], a[116:119], a[36:39], v[208:211]\n"
    "buffer_load_dwordx4 v253, s[40:43], s60 offen offset:2048 lds\n"
    "v_add_f32 v242, v242, v215\n"
    "v_cvt_pk_bf16_f32 v207, v214, v215\n"
    "v_mfma_f32_16x16x32_bf16 v[208:211], a[112:115], a[32:35], v[208:211]\n"
    "v_exp_f32 v192, v192\n"
    "v_mfma_f32_16x16x32_bf16 v[48:51], v[196:199], a[208:211], v[48:51]\n"
    "ds_read_b128 a[156:159], v246 offset:58560\n"
    "v_add_f32 v242, v242, v192\n"
    "v_mfma_f32_16x16x32_bf16 v[48:51], v[200:203], a[228:231], v[48:51]\n"
    "buffer_load_dwordx4 v253, s[44:47], s60 offen offset:3080 lds\n"
    "v_exp_f32 v193, v193\n"
    "v_mfma_f32_16x16x32_bf16 v[52:55], v[200:203], a[232:235], v[52:55]\n"
    "s_add_u32 s60, s60, 2048\n"
    "ds_read_b128 a[152:155], v246 offset:58304\n"
    "v_add_f32 v242, v242, v193\n"
    "v_mfma_f32_16x16x32_bf16 v[52:55], v[196:199], a[212:215], v[52:55]\n"
    "s_mov_b32 m0, 45408\n"
    "v_exp_f32 v194, v194\n"
    "v_mfma_f32_16x16x32_bf16 v[56:59], v[196:199], a[216:219], v[56:59]\n"
    "ds_read_b128 a[148:151], v246 offset:58048\n"
    "v_add_f32 v242, v242, v194\n"
    "v_mfma_f32_16x16x32_bf16 v[56:59], v[200:203], a[236:239], v[56:59]\n"
    "buffer_load_dwordx4 v253, s[36:39], s60 offen offset:0 lds\n"
    "v_exp_f32 v195, v195\n"
    "v_mfma_f32_16x16x32_bf16 v[60:63], v[200:203], a[240:243], v[60:63]\n"
    "ds_read_b128 a[144:147], v246 offset:57792\n"
    "v_add_f32 v242, v242, v195\n"
    "v_mfma_f32_16x16x32_bf16 v[60:63], v[196:199], a[220:223], v[60:63]\n"
    "buffer_load_dwordx4 v253, s[36:39], s60 offen offset:1024 lds\n"
    "v_exp_f32 v208, v208\n"
    "v_mfma_f32_16x16x32_bf16 v[196:199], a[96:99], a[48:51], v[228:231]\n"
    "v_add_f32 v242, v242, v208\n"
    "v_cvt_pk_bf16_f32 v192, v192, v193\n"
    "v_mfma_f32_16x16x32_bf16 v[196:199], a[100:103], a[52:55], v[196:199]\n"
    "v_exp_f32 v209, v209\n"
    "v_mfma_f32_16x16x32_bf16 v[196:199], a[104:107], a[56:59], v[196:199]\n"
    "v_add_f32 v242, v242, v209\n"
    "v_cvt_pk_bf16_f32 v193, v194, v195\n"
    "v_mfma_f32_16x16x32_bf16 v[196:199], a[108:111], a[60:63], v[196:199]\n"
    "buffer_load_dwordx4 v253, s[40:43], s60 offen offset:2048 lds\n"
    "v_exp_f32 v210, v210\n"
    "v_mfma_f32_16x16x32_bf16 v[212:215], a[124:127], a[60:63], v[228:231]\n"
    "v_add_f32 v242, v242, v210\n"
    "v_cvt_pk_bf16_f32 v194, v208, v209\n"
    "v_mfma_f32_16x16x32_bf16 v[212:215], a[120:123], a[56:59], v[212:215]\n"
    "buffer_load_dwordx4 v253, s[44:47], s60 offen offset:3080 lds\n"
    "v_exp_f32 v211, v211\n"
    "v_mfma_f32_16x16x32_bf16 v[212:215], a[116:119], a[52:55], v[212:215]\n"
    "s_add_u32 s60, s60, 2048\n"
    "v_add_f32 v242, v242, v211\n"
    "v_cvt_pk_bf16_f32 v195, v210, v211\n"
    "v_mfma_f32_16x16x32_bf16 v[212:215], a[112:115], a[48:51], v[212:215]\n"
    "s_mov_b32 m0, s78\n"
    "v_exp_f32 v196, v196\n"
    "v_mfma_f32_16x16x32_bf16 v[64:67], v[204:207], a[192:195], v[64:67]\n"
    "ds_read_b64_tr_b16 a[84:85], v252 offset:49544\n"
    "v_add_f32 v243, v243, v196\n"
    "v_mfma_f32_16x16x32_bf16 v[64:67], v[192:195], a[160:163], v[64:67]\n"
    "buffer_load_dwordx4 v253, s[48:51], s77 offen offset:0 lds\n"
    "v_exp_f32 v197, v197\n"
    "v_mfma_f32_16x16x32_bf16 v[68:71], v[192:195], a[164:167], v[68:71]\n"
    "s_add_u32 s78, s78, 4128\n"
    "ds_read_b64_tr_b16 a[86:87], v252 offset:57800\n"
    "v_add_f32 v243, v243, v197\n"
    "v_mfma_f32_16x16x32_bf16 v[68:71], v[204:207], a[196:199], v[68:71]\n"
    "v_exp_f32 v198, v198\n"
    "v_mfma_f32_16x16x32_bf16 v[72:75], v[204:207], a[200:203], v[72:75]\n"
    "ds_read_b64_tr_b16 a[88:89], v254 offset:49536\n"
    "v_add_f32 v243, v243, v198\n"
    "v_mfma_f32_16x16x32_bf16 v[72:75], v[192:195], a[168:171], v[72:75]\n"
    "ds_read_b64_tr_b16 a[202:203], v251 offset:57792\n"
    "v_exp_f32 v199, v199\n"
    "v_mfma_f32_16x16x32_bf16 v[76:79], v[192:195], a[224:227], v[76:79]\n"
    "ds_read_b64_tr_b16 a[90:91], v254 offset:57792\n"
    "v_add_f32 v243, v243, v199\n"
    "v_mfma_f32_16x16x32_bf16 v[76:79], v[204:207], a[204:207], v[76:79]\n"
    "s_waitcnt lgkmcnt(4)\n"
    "v_exp_f32 v212, v212\n"
    "v_mfma_f32_16x16x32_bf16 v[200:203], a[128:131], a[48:51], v[228:231]\n"
    "v_add_f32 v243, v243, v212\n"
    "v_cvt_pk_bf16_f32 v196, v196, v197\n"
    "v_mfma_f32_16x16x32_bf16 v[200:203], a[132:135], a[52:55], v[200:203]\n"
    "v_exp_f32 v213, v213\n"
    "v_mfma_f32_16x16x32_bf16 v[200:203], a[136:139], a[56:59], v[200:203]\n"
    "ds_read_b64_tr_b16 a[196:197], v247 offset:49544\n"
    "v_add_f32 v243, v243, v213\n"
    "v_cvt_pk_bf16_f32 v197, v198, v199\n"
    "v_mfma_f32_16x16x32_bf16 v[200:203], a[140:143], a[60:63], v[200:203]\n"
    "ds_read_b64_tr_b16 a[198:199], v247 offset:57800\n"
    "v_exp_f32 v214, v214\n"
    "v_mfma_f32_16x16x32_bf16 v[208:211], a[156:159], a[60:63], v[228:231]\n"
    "ds_read_b64_tr_b16 a[200:201], v251 offset:49536\n"
    "v_add_f32 v243, v243, v214\n"
    "v_cvt_pk_bf16_f32 v198, v212, v213\n"
    "v_mfma_f32_16x16x32_bf16 v[208:211], a[152:155], a[56:59], v[208:211]\n"
    "buffer_load_dwordx4 v253, s[48:51], s77 offen offset:1024 lds\n"
    "v_exp_f32 v215, v215\n"
    "v_mfma_f32_16x16x32_bf16 v[208:211], a[148:151], a[52:55], v[208:211]\n"
    "v_add_f32 v243, v243, v215\n"
    "v_cvt_pk_bf16_f32 v199, v214, v215\n"
    "v_mfma_f32_16x16x32_bf16 v[208:211], a[144:147], a[48:51], v[208:211]\n"
    "v_exp_f32 v200, v200\n"
    "v_mfma_f32_16x16x32_bf16 v[80:83], v[204:207], a[208:211], v[80:83]\n"
    "ds_read_b64_tr_b16 a[92:93], v254 offset:49544\n"
    "v_add_f32 v243, v243, v200\n"
    "v_mfma_f32_16x16x32_bf16 v[80:83], v[192:195], a[228:231], v[80:83]\n"
    "buffer_load_dwordx4 v253, s[52:55], s77 offen offset:2080 lds\n"
    "v_exp_f32 v201, v201\n"
    "v_mfma_f32_16x16x32_bf16 v[84:87], v[192:195], a[232:235], v[84:87]\n"
    "ds_read_b64_tr_b16 a[94:95], v254 offset:57800\n"
    "v_add_f32 v243, v243, v201\n"
    "v_mfma_f32_16x16x32_bf16 v[84:87], v[204:207], a[212:215], v[84:87]\n"
    "v_exp_f32 v202, v202\n"
    "v_mfma_f32_16x16x32_bf16 v[88:91], v[204:207], a[216:219], v[88:91]\n"
    "ds_read_b64_tr_b16 a[192:193], v247 offset:49536\n"
    "v_add_f32 v243, v243, v202\n"
    "v_mfma_f32_16x16x32_bf16 v[88:91], v[192:195], a[236:239], v[88:91]\n"
    "v_exp_f32 v203, v203\n"
    "v_mfma_f32_16x16x32_bf16 v[92:95], v[192:195], a[240:243], v[92:95]\n"
    "ds_read_b64_tr_b16 a[194:195], v247 offset:57792\n"
    "v_add_f32 v243, v243, v203\n"
    "v_mfma_f32_16x16x32_bf16 v[92:95], v[204:207], a[220:223], v[92:95]\n"
    "buffer_load_dwordx4 v253, s[52:55], s77 offen offset:3104 lds\n"
    "s_add_u32 s77, s77, 4096\n"
    "v_exp_f32 v208, v208\n"
    "v_mfma_f32_16x16x32_bf16 v[204:207], a[96:99], a[64:67], v[232:235]\n"
    "v_add_f32 v243, v243, v208\n"
    "v_cvt_pk_bf16_f32 v200, v200, v201\n"
    "v_mfma_f32_16x16x32_bf16 v[204:207], a[100:103], a[68:71], v[204:207]\n"
    "v_exp_f32 v209, v209\n"
    "v_mfma_f32_16x16x32_bf16 v[204:207], a[104:107], a[72:75], v[204:207]\n"
    "v_add_f32 v243, v243, v209\n"
    "v_cvt_pk_bf16_f32 v201, v202, v203\n"
    "v_mfma_f32_16x16x32_bf16 v[204:207], a[108:111], a[76:79], v[204:207]\n"
    "v_exp_f32 v210, v210\n"
    "v_mfma_f32_16x16x32_bf16 v[212:215], a[124:127], a[76:79], v[232:235]\n"
    "v_add_f32 v243, v243, v210\n"
    "v_cvt_pk_bf16_f32 v202, v208, v209\n"
    "v_mfma_f32_16x16x32_bf16 v[212:215], a[120:123], a[72:75], v[212:215]\n"
    "v_exp_f32 v211, v211\n"
    "v_mfma_f32_16x16x32_bf16 v[212:215], a[116:119], a[68:71], v[212:215]\n"
    "s_waitcnt vmcnt(44) lgkmcnt(0)\n"
    "v_add_f32 v243, v243, v211\n"
    "v_cvt_pk_bf16_f32 v203, v210, v211\n"
    "v_mfma_f32_16x16x32_bf16 v[212:215], a[112:115], a[64:67], v[212:215]\n"
    "s_barrier\n"
    "v_exp_f32 v204, v204\n"
    "v_mfma_f32_16x16x32_bf16 v[96:99], v[196:199], a[160:163], v[96:99]\n"
    "s_cmp_eq_u32 s62, 23\n"
    "ds_read_b128 a[96:99], v246 offset:0\n"
    "v_add_f32 v244, v244, v204\n"
    "v_mfma_f32_16x16x32_bf16 v[96:99], v[200:203], a[192:195], v[96:99]\n"
    "s_cselect_b32 s60, s61, s60\n"
    "v_exp_f32 v205, v205\n"
    "v_mfma_f32_16x16x32_bf16 v[100:103], v[200:203], a[196:199], v[100:103]\n"
    "s_cselect_b32 s61, s79, s61\n"
    "ds_read_b128 a[100:103], v246 offset:256\n"
    "v_add_f32 v244, v244, v205\n"
    "v_mfma_f32_16x16x32_bf16 v[100:103], v[196:199], a[164:167], v[100:103]\n"
    "s_cselect_b32 s62, 0, s62\n"
    "v_exp_f32 v206, v206\n"
    "v_mfma_f32_16x16x32_bf16 v[104:107], v[196:199], a[168:171], v[104:107]\n"
    "s_mov_b32 m0, 49536\n"
    "ds_read_b128 a[104:107], v246 offset:512\n"
    "v_add_f32 v244, v244, v206\n"
    "v_mfma_f32_16x16x32_bf16 v[104:107], v[200:203], a[200:203], v[104:107]\n"
    "buffer_load_dwordx4 v253, s[36:39], s60 offen offset:0 lds\n"
    "v_exp_f32 v207, v207\n"
    "v_mfma_f32_16x16x32_bf16 v[108:111], v[200:203], a[244:247], v[108:111]\n"
    "s_add_u32 s62, s62, 1\n"
    "ds_read_b128 a[108:111], v246 offset:768\n"
    "v_add_f32 v244, v244, v207\n"
    "v_mfma_f32_16x16x32_bf16 v[108:111], v[196:199], a[224:227], v[108:111]\n"
    "buffer_load_dwordx4 v253, s[36:39], s60 offen offset:1024 lds\n"
    "v_exp_f32 v212, v212\n"
    "v_mfma_f32_16x16x32_bf16 v[192:195], a[128:131], a[64:67], v[232:235]\n"
    "s_sub_u32 s63, s63, 1\n"
    "v_add_f32 v244, v244, v212\n"
    "v_cvt_pk_bf16_f32 v204, v204, v205\n"
    "v_mfma_f32_16x16x32_bf16 v[192:195], a[132:135], a[68:71], v[192:195]\n"
    "s_branch LOOP1_B\n"
    "LMID_B:\n"
    "s_mov_b32 s63, 4\n"
    "v_exp_f32 v213, v213\n"
    "v_mfma_f32_16x16x32_bf16 v[200:203], a[104:107], a[24:27], v[200:203]\n"
    "v_add_f32 v241, v241, v213\n"
    "v_cvt_pk_bf16_f32 v197, v198, v199\n"
    "v_mfma_f32_16x16x32_bf16 v[200:203], a[108:111], a[28:31], v[200:203]\n"
    "v_exp_f32 v214, v214\n"
    "v_mfma_f32_16x16x32_bf16 v[208:211], a[124:127], a[28:31], v[220:223]\n"
    "v_add_f32 v241, v241, v214\n"
    "v_cvt_pk_bf16_f32 v198, v212, v213\n"
    "v_mfma_f32_16x16x32_bf16 v[208:211], a[120:123], a[24:27], v[208:211]\n"
    "v_exp_f32 v215, v215\n"
    "v_mfma_f32_16x16x32_bf16 v[208:211], a[116:119], a[20:23], v[208:211]\n"
    "s_waitcnt vmcnt(45) lgkmcnt(0)\n"
    "v_add_f32 v241, v241, v215\n"
    "v_cvt_pk_bf16_f32 v199, v214, v215\n"
    "v_mfma_f32_16x16x32_bf16 v[208:211], a[112:115], a[16:19], v[208:211]\n"
    "s_barrier\n"
    "v_exp_f32 v200, v200\n"
    "v_mfma_f32_16x16x32_bf16 v[16:19], v[204:207], a[208:211], v[16:19]\n"
    "s_cmp_eq_u32 s62, 23\n"
    "ds_read_b64_tr_b16 a[244:245], v251 offset:49544\n"
    "v_add_f32 v241, v241, v200\n"
    "v_mfma_f32_16x16x32_bf16 v[16:19], v[192:195], a[228:231], v[16:19]\n"
    "s_cselect_b32 s60, s61, s60\n"
    "v_exp_f32 v201, v201\n"
    "v_mfma_f32_16x16x32_bf16 v[20:23], v[192:195], a[232:235], v[20:23]\n"
    "s_cselect_b32 s61, s79, s61\n"
    "ds_read_b64_tr_b16 a[246:247], v251 offset:57800\n"
    "v_add_f32 v241, v241, v201\n"
    "v_mfma_f32_16x16x32_bf16 v[20:23], v[204:207], a[212:215], v[20:23]\n"
    "s_cselect_b32 s62, 0, s62\n"
    "v_exp_f32 v202, v202\n"
    "v_mfma_f32_16x16x32_bf16 v[24:27], v[204:207], a[216:219], v[24:27]\n"
    "s_mov_b32 m0, s78\n"
    "ds_read_b64_tr_b16 a[80:81], v252 offset:49536\n"
    "v_add_f32 v241, v241, v202\n"
    "v_mfma_f32_16x16x32_bf16 v[24:27], v[192:195], a[236:239], v[24:27]\n"
    "buffer_load_dwordx4 v253, s[48:51], s77 offen offset:0 lds\n"
    "v_exp_f32 v203, v203\n"
    "v_mfma_f32_16x16x32_bf16 v[28:31], v[192:195], a[240:243], v[28:31]\n"
    "s_add_u32 s78, s78, 4128\n"
    "ds_read_b64_tr_b16 a[82:83], v252 offset:57792\n"
    "v_add_f32 v241, v241, v203\n"
    "v_mfma_f32_16x16x32_bf16 v[28:31], v[204:207], a[220:223], v[28:31]\n"
    "v_exp_f32 v208, v208\n"
    "v_mfma_f32_16x16x32_bf16 v[204:207], a[128:131], a[32:35], v[224:227]\n"
    "v_add_f32 v241, v241, v208\n"
    "v_cvt_pk_bf16_f32 v200, v200, v201\n"
    "v_mfma_f32_16x16x32_bf16 v[204:207], a[132:135], a[36:39], v[204:207]\n"
    "v_exp_f32 v209, v209\n"
    "v_mfma_f32_16x16x32_bf16 v[204:207], a[136:139], a[40:43], v[204:207]\n"
    "buffer_load_dwordx4 v253, s[48:51], s77 offen offset:1024 lds\n"
    "v_add_f32 v241, v241, v209\n"
    "v_cvt_pk_bf16_f32 v201, v202, v203\n"
    "v_mfma_f32_16x16x32_bf16 v[204:207], a[140:143], a[44:47], v[204:207]\n"
    "v_exp_f32 v210, v210\n"
    "v_mfma_f32_16x16x32_bf16 v[212:215], a[156:159], a[44:47], v[224:227]\n"
    "v_add_f32 v241, v241, v210\n"
    "v_cvt_pk_bf16_f32 v202, v208, v209\n"
    "v_mfma_f32_16x16x32_bf16 v[212:215], a[152:155], a[40:43], v[212:215]\n"
    "buffer_load_dwordx4 v253, s[52:55], s77 offen offset:2080 lds\n"
    "v_exp_f32 v211, v211\n"
    "v_mfma_f32_16x16x32_bf16 v[212:215], a[148:151], a[36:39], v[212:215]\n"
    "v_add_f32 v241, v241, v211\n"
    "v_cvt_pk_bf16_f32 v203, v210, v211\n"
    "v_mfma_f32_16x16x32_bf16 v[212:215], a[144:147], a[32:35], v[212:215]\n"
    "v_exp_f32 v204, v204\n"
    "v_mfma_f32_16x16x32_bf16 v[32:35], v[196:199], a[192:195], v[32:35]\n"
    "ds_read_b128 a[128:131], v246 offset:49536\n"
    "v_add_f32 v242, v242, v204\n"
    "v_mfma_f32_16x16x32_bf16 v[32:35], v[200:203], a[160:163], v[32:35]\n"
    "buffer_load_dwordx4 v253, s[52:55], s77 offen offset:3104 lds\n"
    "v_exp_f32 v205, v205\n"
    "v_mfma_f32_16x16x32_bf16 v[36:39], v[200:203], a[164:167], v[36:39]\n"
    "s_add_u32 s77, s77, 4096\n"
    "ds_read_b128 a[132:135], v246 offset:49792\n"
    "v_add_f32 v242, v242, v205\n"
    "v_mfma_f32_16x16x32_bf16 v[36:39], v[196:199], a[196:199], v[36:39]\n"
    "v_exp_f32 v206, v206\n"
    "v_mfma_f32_16x16x32_bf16 v[40:43], v[196:199], a[200:203], v[40:43]\n"
    "s_mov_b32 m0, s78\n"
    "ds_read_b128 a[136:139], v246 offset:50048\n"
    "v_add_f32 v242, v242, v206\n"
    "v_mfma_f32_16x16x32_bf16 v[40:43], v[200:203], a[168:171], v[40:43]\n"
    "v_exp_f32 v207, v207\n"
    "v_mfma_f32_16x16x32_bf16 v[44:47], v[200:203], a[224:227], v[44:47]\n"
    "ds_read_b128 a[140:143], v246 offset:50304\n"
    "v_add_f32 v242, v242, v207\n"
    "v_mfma_f32_16x16x32_bf16 v[44:47], v[196:199], a[204:207], v[44:47]\n"
    "buffer_load_dwordx4 v253, s[48:51], s77 offen offset:0 lds\n"
    "v_exp_f32 v212, v212\n"
    "v_mfma_f32_16x16x32_bf16 v[192:195], a[96:99], a[32:35], v[224:227]\n"
    "v_add_f32 v242, v242, v212\n"
    "v_cvt_pk_bf16_f32 v204, v204, v205\n"
    "v_mfma_f32_16x16x32_bf16 v[192:195], a[100:103], a[36:39], v[192:195]\n"
    "v_exp_f32 v213, v213\n"
    "v_mfma_f32_16x16x32_bf16 v[192:195], a[104:107], a[40:43], v[192:195]\n"
    "s_add_u32 s78, s78, 4128\n"
    "v_add_f32 v242, v242, v213\n"
    "v_cvt_pk_bf16_f32 v205, v206, v207\n"
    "v_mfma_f32_16x16x32_bf16 v[192:195], a[108:111], a[44:47], v[192:195]\n"
    "v_exp_f32 v214, v214\n"
    "v_mfma_f32_16x16x32_bf16 v[208:211], a[124:127], a[44:47], v[224:227]\n"
    "buffer_load_dwordx4 v253, s[48:51], s77 offen offset:1024 lds\n"
    "v_add_f32 v242, v242, v214\n"
    "v_cvt_pk_bf16_f32 v206, v212, v213\n"
    "v_mfma_f32_16x16x32_bf16 v[208:211], a[120:123], a[40:43], v[208:211]\n"
    "v_exp_f32 v215, v215\n"
    "v_mfma_f32_16x16x32_bf16 v[208:211], a[116:119], a[36:39], v[208:211]\n"
    "v_add_f32 v242, v242, v215\n"
    "v_cvt_pk_bf16_f32 v207, v214, v215\n"
    "v_mfma_f32_16x16x32_bf16 v[208:211], a[112:115], a[32:35], v[208:211]\n"
    "buffer_load_dwordx4 v253, s[52:55], s77 offen offset:2080 lds\n"
    "v_exp_f32 v192, v192\n"
    "v_mfma_f32_16x16x32_bf16 v[48:51], v[196:199], a[208:211], v[48:51]\n"
    "ds_read_b64_tr_b16 a[84:85], v252 offset:49544\n"
    "v_add_f32 v242, v242, v192\n"
    "v_mfma_f32_16x16x32_bf16 v[48:51], v[200:203], a[228:231], v[48:51]\n"
    "v_exp_f32 v193, v193\n"
    "v_mfma_f32_16x16x32_bf16 v[52:55], v[200:203], a[232:235], v[52:55]\n"
    "ds_read_b64_tr_b16 a[86:87], v252 offset:57800\n"
    "v_add_f32 v242, v242, v193\n"
    "v_mfma_f32_16x16x32_bf16 v[52:55], v[196:199], a[212:215], v[52:55]\n"
    "v_exp_f32 v194, v194\n"
    "v_mfma_f32_16x16x32_bf16 v[56:59], v[196:199], a[216:219], v[56:59]\n"
    "ds_read_b64_tr_b16 a[88:89], v254 offset:49536\n"
    "v_add_f32 v242, v242, v194\n"
    "v_mfma_f32_16x16x32_bf16 v[56:59], v[200:203], a[236:239], v[56:59]\n"
    "buffer_load_dwordx4 v253, s[52:55], s77 offen offset:3104 lds\n"
    "v_exp_f32 v195, v195\n"
    "v_mfma_f32_16x16x32_bf16 v[60:63], v[200:203], a[240:243], v[60:63]\n"
    "s_add_u32 s77, s77, 4096\n"
    "ds_read_b64_tr_b16 a[90:91], v254 offset:57792\n"
    "v_add_f32 v242, v242, v195\n"
    "v_mfma_f32_16x16x32_bf16 v[60:63], v[196:199], a[220:223], v[60:63]\n"
    "s_mov_b32 m0, 33024\n"
    "v_exp_f32 v208, v208\n"
    "v_mfma_f32_16x16x32_bf16 v[196:199], a[96:99], a[48:51], v[228:231]\n"
    "v_add_f32 v242, v242, v208\n"
    "v_cvt_pk_bf16_f32 v192, v192, v193\n"
    "v_mfma_f32_16x16x32_bf16 v[196:199], a[100:103], a[52:55], v[196:199]\n"
    "v_exp_f32 v209, v209\n"
    "v_mfma_f32_16x16x32_bf16 v[196:199], a[104:107], a[56:59], v[196:199]\n"
    "v_add_f32 v242, v242, v209\n"
    "v_cvt_pk_bf16_f32 v193, v194, v195\n"
    "v_mfma_f32_16x16x32_bf16 v[196:199], a[108:111], a[60:63], v[196:199]\n"
    "buffer_load_dwordx4 v253, s[36:39], s60 offen offset:0 lds\n"
    "v_exp_f32 v210, v210\n"
    "v_mfma_f32_16x16x32_bf16 v[212:215], a[124:127], a[60:63], v[228:231]\n"
    "s_add_u32 s62, s62, 1\n"
    "v_add_f32 v242, v242, v210\n"
    "v_cvt_pk_bf16_f32 v194, v208, v209\n"
    "v_mfma_f32_16x16x32_bf16 v[212:215], a[120:123], a[56:59], v[212:215]\n"
    "v_exp_f32 v211, v211\n"
    "v_mfma_f32_16x16x32_bf16 v[212:215], a[116:119], a[52:55], v[212:215]\n"
    "buffer_load_dwordx4 v253, s[36:39], s60 offen offset:1024 lds\n"
    "v_add_f32 v242, v242, v211\n"
    "v_cvt_pk_bf16_f32 v195, v210, v211\n"
    "v_mfma_f32_16x16x32_bf16 v[212:215], a[112:115], a[48:51], v[212:215]\n"
    "v_exp_f32 v196, v196\n"
    "v_mfma_f32_16x16x32_bf16 v[64:67], v[204:207], a[192:195], v[64:67]\n"
    "ds_read_b64_tr_b16 a[92:93], v254 offset:49544\n"
    "v_add_f32 v243, v243, v196\n"
    "v_mfma_f32_16x16x32_bf16 v[64:67], v[192:195], a[160:163], v[64:67]\n"
    "v_exp_f32 v197, v197\n"
    "v_mfma_f32_16x16x32_bf16 v[68:71], v[192:195], a[164:167], v[68:71]\n"
    "ds_read_b64_tr_b16 a[94:95], v254 offset:57800\n"
    "v_add_f32 v243, v243, v197\n"
    "v_mfma_f32_16x16x32_bf16 v[68:71], v[204:207], a[196:199], v[68:71]\n"
    "buffer_load_dwordx4 v253, s[40:43], s60 offen offset:2048 lds\n"
    "v_exp_f32 v198, v198\n"
    "v_mfma_f32_16x16x32_bf16 v[72:75], v[204:207], a[200:203], v[72:75]\n"
    "ds_read_b128 a[156:159], v246 offset:58560\n"
    "v_add_f32 v243, v243, v198\n"
    "v_mfma_f32_16x16x32_bf16 v[72:75], v[192:195], a[168:171], v[72:75]\n"
    "v_exp_f32 v199, v199\n"
    "v_mfma_f32_16x16x32_bf16 v[76:79], v[192:195], a[224:227], v[76:79]\n"
    "ds_read_b128 a[152:155], v246 offset:58304\n"
    "v_add_f32 v243, v243, v199\n"
    "v_mfma_f32_16x16x32_bf16 v[76:79], v[204:207], a[204:207], v[76:79]\n"
    "v_exp_f32 v212, v212\n"
    "v_mfma_f32_16x16x32_bf16 v[200:203], a[96:99], a[64:67], v[232:235]\n"
    "v_add_f32 v243, v243, v212\n"
    "v_cvt_pk_bf16_f32 v196, v196, v197\n"
    "v_mfma_f32_16x16x32_bf16 v[200:203], a[100:103], a[68:71], v[200:203]\n"
    "v_exp_f32 v213, v213\n"
    "v_mfma_f32_16x16x32_bf16 v[200:203], a[104:107], a[72:75], v[200:203]\n"
    "buffer_load_dwordx4 v253, s[44:47], s60 offen offset:3080 lds\n"
    "v_add_f32 v243, v243, v213\n"
    "v_cvt_pk_bf16_f32 v197, v198, v199\n"
    "v_mfma_f32_16x16x32_bf16 v[200:203], a[108:111], a[76:79], v[200:203]\n"
    "s_add_u32 s60, s60, 2048\n"
    "v_exp_f32 v214, v214\n"
    "v_mfma_f32_16x16x32_bf16 v[208:211], a[124:127], a[76:79], v[232:235]\n"
    "s_mov_b32 m0, 37152\n"
    "v_add_f32 v243, v243, v214\n"
    "v_cvt_pk_bf16_f32 v198, v212, v213\n"
    "v_mfma_f32_16x16x32_bf16 v[208:211], a[120:123], a[72:75], v[208:211]\n"
    "buffer_load_dwordx4 v253, s[36:39], s60 offen offset:0 lds\n"
    "v_exp_f32 v215, v215\n"
    "v_mfma_f32_16x16x32_bf16 v[208:211], a[116:119], a[68:71], v[208:211]\n"
    "v_add_f32 v243, v243, v215\n"
    "v_cvt_pk_bf16_f32 v199, v214, v215\n"
    "v_mfma_f32_16x16x32_bf16 v[208:211], a[112:115], a[64:67], v[208:211]\n"
    "v_exp_f32 v200, v200\n"
    "v_mfma_f32_16x16x32_bf16 v[80:83], v[204:207], a[208:211], v[80:83]\n"
    "ds_read_b128 a[148:151], v246 offset:58048\n"
    "v_add_f32 v244, v244, v200\n"
    "v_mfma_f32_16x16x32_bf16 v[80:83], v[192:195], a[228:231], v[80:83]\n"
    "buffer_load_dwordx4 v253, s[36:39], s60 offen offset:1024 lds\n"
    "v_exp_f32 v201, v201\n"
    "v_mfma_f32_16x16x32_bf16 v[84:87], v[192:195], a[232:235], v[84:87]\n"
    "ds_read_b128 a[144:147], v246 offset:57792\n"
    "v_add_f32 v244, v244, v201\n"
    "v_mfma_f32_16x16x32_bf16 v[84:87], v[204:207], a[212:215], v[84:87]\n"
    "v_exp_f32 v202, v202\n"
    "v_mfma_f32_16x16x32_bf16 v[88:91], v[204:207], a[216:219], v[88:91]\n"
    "ds_read_b64_tr_b16 a[192:193], v247 offset:49536\n"
    "v_add_f32 v244, v244, v202\n"
    "v_mfma_f32_16x16x32_bf16 v[88:91], v[192:195], a[236:239], v[88:91]\n"
    "v_exp_f32 v203, v203\n"
    "v_mfma_f32_16x16x32_bf16 v[92:95], v[192:195], a[240:243], v[92:95]\n"
    "ds_read_b64_tr_b16 a[194:195], v247 offset:57792\n"
    "v_add_f32 v244, v244, v203\n"
    "v_mfma_f32_16x16x32_bf16 v[92:95], v[204:207], a[220:223], v[92:95]\n"
    "buffer_load_dwordx4 v253, s[40:43], s60 offen offset:2048 lds\n"
    "v_exp_f32 v208, v208\n"
    "v_and_b32 v248, s76, v243\n"
    "v_sub_f32_e64 v243, v243, v248\n"
    "v_cvt_pk_bf16_f32 v248, v248, v243\n"
    "s_waitcnt vmcnt(11)\n"
    "ds_read_b128 a[48:51], v250 offset:12384\n"
    "ds_read_b128 a[52:55], v250 offset:12640\n"
    "ds_read_b128 a[56:59], v250 offset:12896\n"
    "ds_read_b128 a[60:63], v250 offset:13152\n"
    "v_mfma_f32_16x16x16_bf16 v[228:231], v[248:249], a[252:253], 0\n"
    "v_add_f32 v244, v244, v208\n"
    "v_cvt_pk_bf16_f32 v200, v200, v201\n"
    "v_exp_f32 v209, v209\n"
    "v_cvt_pk_bf16_f32 v201, v202, v203\n"
    "v_add_f32 v244, v244, v209\n"
    "v_exp_f32 v210, v210\n"
    "v_cvt_pk_bf16_f32 v202, v208, v209\n"
    "v_add_f32 v244, v244, v210\n"
    "v_exp_f32 v211, v211\n"
    "v_maximum3_f32 v167, v228, v229, v228\n"
    "v_add_f32 v244, v244, v211\n"
    "v_cvt_pk_bf16_f32 v203, v210, v211\n"
    "v_mfma_f32_16x16x32_bf16 v[96:99], v[196:199], a[160:163], v[96:99]\n"
    "ds_read_b64_tr_b16 a[196:197], v247 offset:49544\n"
    "v_mfma_f32_16x16x32_bf16 v[100:103], v[196:199], a[164:167], v[100:103]\n"
    "v_mfma_f32_16x16x32_bf16 v[104:107], v[196:199], a[168:171], v[104:107]\n"
    "ds_read_b64_tr_b16 a[198:199], v247 offset:57800\n"
    "v_mfma_f32_16x16x32_bf16 v[108:111], v[196:199], a[224:227], v[108:111]\n"
    "v_mfma_f32_16x16x32_bf16 v[112:115], v[196:199], a[228:231], v[112:115]\n"
    "ds_read_b64_tr_b16 a[200:201], v251 offset:49536\n"
    "v_mfma_f32_16x16x32_bf16 v[116:119], v[196:199], a[232:235], v[116:119]\n"
    "buffer_load_dwordx4 v253, s[44:47], s60 offen offset:3080 lds\n"
    "s_add_u32 s60, s60, 2048\n"
    "v_mfma_f32_16x16x32_bf16 v[120:123], v[196:199], a[236:239], v[120:123]\n"
    "ds_read_b64_tr_b16 a[202:203], v251 offset:57792\n"
    "v_mfma_f32_16x16x32_bf16 v[124:127], v[196:199], a[240:243], v[124:127]\n"
    "v_maximum3_f32 v167, v230, v231, v167\n"
    "v_rcp_f32 v228, v228\n"
    "v_rcp_f32 v229, v229\n"
    "v_rcp_f32 v230, v230\n"
    "v_rcp_f32 v231, v231\n"
    "v_and_b32 v248, s76, v240\n"
    "v_sub_f32_e64 v240, v240, v248\n"
    "v_cvt_pk_bf16_f32 v248, v248, v240\n"
    "v_maximum3_f32 v167, v228, v229, v167\n"
    "v_maximum3_f32 v167, v230, v231, v167\n"
    "v_pk_mul_f32 v[96:97], v[96:97], v[228:229]\n"
    "v_pk_mul_f32 v[98:99], v[98:99], v[230:231]\n"
    "v_pk_mul_f32 v[104:105], v[104:105], v[228:229]\n"
    "v_pk_mul_f32 v[106:107], v[106:107], v[230:231]\n"
    "v_cvt_pk_bf16_f32 v96, v96, v104\n"
    "v_cvt_pk_bf16_f32 v98, v98, v106\n"
    "v_cvt_pk_bf16_f32 v104, v97, v105\n"
    "v_cvt_pk_bf16_f32 v106, v99, v107\n"
    "v_pk_mul_f32 v[112:113], v[112:113], v[228:229]\n"
    "v_pk_mul_f32 v[114:115], v[114:115], v[230:231]\n"
    "v_pk_mul_f32 v[120:121], v[120:121], v[228:229]\n"
    "v_pk_mul_f32 v[122:123], v[122:123], v[230:231]\n"
    "v_cvt_pk_bf16_f32 v97, v112, v120\n"
    "v_cvt_pk_bf16_f32 v99, v114, v122\n"
    "v_cvt_pk_bf16_f32 v105, v113, v121\n"
    "v_cvt_pk_bf16_f32 v107, v115, v123\n"
    "v_pk_mul_f32 v[100:101], v[100:101], v[228:229]\n"
    "v_pk_mul_f32 v[102:103], v[102:103], v[230:231]\n"
    "v_pk_mul_f32 v[108:109], v[108:109], v[228:229]\n"
    "v_pk_mul_f32 v[110:111], v[110:111], v[230:231]\n"
    "v_cvt_pk_bf16_f32 v100, v100, v108\n"
    "v_cvt_pk_bf16_f32 v102, v102, v110\n"
    "v_cvt_pk_bf16_f32 v108, v101, v109\n"
    "v_cvt_pk_bf16_f32 v110, v103, v111\n"
    "v_pk_mul_f32 v[116:117], v[116:117], v[228:229]\n"
    "v_pk_mul_f32 v[118:119], v[118:119], v[230:231]\n"
    "v_pk_mul_f32 v[124:125], v[124:125], v[228:229]\n"
    "v_pk_mul_f32 v[126:127], v[126:127], v[230:231]\n"
    "v_mfma_f32_16x16x16_bf16 v[216:219], v[248:249], a[252:253], 0\n"
    "v_cvt_pk_bf16_f32 v101, v116, v124\n"
    "v_cvt_pk_bf16_f32 v103, v118, v126\n"
    "v_cvt_pk_bf16_f32 v109, v117, v125\n"
    "v_mfma_f32_4x4x4_16b_bf16 v[112:115], v[96:97], a[254:255], 0\n"
    "v_cvt_pk_bf16_f32 v111, v119, v127\n"
    "v_mfma_f32_4x4x4_16b_bf16 v[120:123], v[98:99], a[254:255], 0\n"
    "v_mfma_f32_4x4x4_16b_bf16 v[116:119], v[100:101], a[254:255], 0\n"
    "v_mfma_f32_4x4x4_16b_bf16 v[124:127], v[102:103], a[254:255], 0\n"
    "v_mfma_f32_4x4x4_16b_bf16 v[96:99], v[104:105], a[254:255], 0\n"
    "v_cvt_pk_bf16_f32 v112, v112, v113\n"
    "v_cvt_pk_bf16_f32 v113, v114, v115\n"
    "v_mfma_f32_4x4x4_16b_bf16 v[100:103], v[108:109], a[254:255], 0\n"
    "v_cvt_pk_bf16_f32 v120, v120, v121\n"
    "v_cvt_pk_bf16_f32 v121, v122, v123\n"
    "v_cvt_pk_bf16_f32 v114, v116, v117\n"
    "v_cvt_pk_bf16_f32 v115, v118, v119\n"
    "v_mfma_f32_4x4x4_16b_bf16 v[116:119], v[106:107], a[254:255], 0\n"
    "v_cvt_pk_bf16_f32 v122, v124, v125\n"
    "v_cvt_pk_bf16_f32 v123, v126, v127\n"
    "v_mfma_f32_4x4x4_16b_bf16 v[124:127], v[110:111], a[254:255], 0\n"
    "v_cvt_pk_bf16_f32 v96, v96, v97\n"
    "v_cvt_pk_bf16_f32 v97, v98, v99\n"
    "s_add_u32 s67, s66, 12288\n"
    "s_waitcnt lgkmcnt(0)\n"
    "s_nop 0\n"
    "v_mfma_f32_4x4x4_16b_bf16 v[104:107], a[248:249], a[48:49], 0\n"
    "v_cvt_pk_bf16_f32 v98, v100, v101\n"
    "v_cvt_pk_bf16_f32 v99, v102, v103\n"
    "v_mfma_f32_4x4x4_16b_bf16 v[104:107], a[250:251], a[48:49], v[104:107]\n"
    "buffer_store_dwordx4 v[112:115], v249, s[56:59], s67 offen offset:0\n"
    "v_cvt_pk_bf16_f32 v116, v116, v117\n"
    "v_cvt_pk_bf16_f32 v117, v118, v119\n"
    "v_mfma_f32_4x4x4_16b_bf16 v[108:111], a[248:249], a[50:51], 0\n"
    "v_cvt_pk_bf16_f32 v118, v124, v125\n"
    "v_cvt_pk_bf16_f32 v119, v126, v127\n"
    "v_mfma_f32_4x4x4_16b_bf16 v[108:111], a[250:251], a[50:51], v[108:111]\n"
    "v_cvt_pk_bf16_f32 v104, v104, v105\n"
    "v_mfma_f32_4x4x4_16b_bf16 v[100:103], a[248:249], a[52:53], 0\n"
    "v_cvt_pk_bf16_f32 v105, v106, v107\n"
    "v_accvgpr_write_b32 a48, v104\n"
    "v_mfma_f32_4x4x4_16b_bf16 v[100:103], a[250:251], a[52:53], v[100:103]\n"
    "v_cvt_pk_bf16_f32 v108, v108, v109\n"
    "v_accvgpr_write_b32 a49, v105\n"
    "v_mfma_f32_4x4x4_16b_bf16 v[104:107], a[248:249], a[54:55], 0\n"
    "buffer_store_dwordx4 v[120:123], v249, s[56:59], s67 offen offset:512\n"
    "v_cvt_pk_bf16_f32 v109, v110, v111\n"
    "v_accvgpr_write_b32 a50, v108\n"
    "v_mfma_f32_4x4x4_16b_bf16 v[104:107], a[250:251], a[54:55], v[104:107]\n"
    "v_cvt_pk_bf16_f32 v100, v100, v101\n"
    "v_accvgpr_write_b32 a51, v109\n"
    "v_mfma_f32_4x4x4_16b_bf16 v[108:111], a[248:249], a[56:57], 0\n"
    "v_cvt_pk_bf16_f32 v101, v102, v103\n"
    "v_accvgpr_write_b32 a52, v100\n"
    "v_mfma_f32_4x4x4_16b_bf16 v[108:111], a[250:251], a[56:57], v[108:111]\n"
    "v_cvt_pk_bf16_f32 v104, v104, v105\n"
    "v_accvgpr_write_b32 a53, v101\n"
    "v_mfma_f32_4x4x4_16b_bf16 v[100:103], a[248:249], a[58:59], 0\n"
    "buffer_store_dwordx4 v[96:99], v249, s[56:59], s67 offen offset:256\n"
    "v_cvt_pk_bf16_f32 v105, v106, v107\n"
    "v_accvgpr_write_b32 a54, v104\n"
    "v_mfma_f32_4x4x4_16b_bf16 v[100:103], a[250:251], a[58:59], v[100:103]\n"
    "v_cvt_pk_bf16_f32 v108, v108, v109\n"
    "v_accvgpr_write_b32 a55, v105\n"
    "v_mfma_f32_4x4x4_16b_bf16 v[104:107], a[248:249], a[60:61], 0\n"
    "v_cvt_pk_bf16_f32 v109, v110, v111\n"
    "v_accvgpr_write_b32 a56, v108\n"
    "v_mfma_f32_4x4x4_16b_bf16 v[104:107], a[250:251], a[60:61], v[104:107]\n"
    "v_cvt_pk_bf16_f32 v100, v100, v101\n"
    "v_accvgpr_write_b32 a57, v109\n"
    "v_mfma_f32_4x4x4_16b_bf16 v[108:111], a[248:249], a[62:63], 0\n"
    "buffer_store_dwordx4 v[116:119], v249, s[56:59], s67 offen offset:768\n"
    "v_cvt_pk_bf16_f32 v101, v102, v103\n"
    "v_accvgpr_write_b32 a58, v100\n"
    "v_mfma_f32_4x4x4_16b_bf16 v[108:111], a[250:251], a[62:63], v[108:111]\n"
    "v_cvt_pk_bf16_f32 v104, v104, v105\n"
    "v_accvgpr_write_b32 a59, v101\n"
    "v_mfma_f32_16x16x32_bf16 v[124:127], a[128:131], a[48:51], 0\n"
    "s_waitcnt vmcnt(63)\n"
    "s_nop 0\n"
    "ds_read_b128 a[0:3], v250 offset:0\n"
    "ds_read_b128 a[4:7], v250 offset:256\n"
    "v_cvt_pk_bf16_f32 v105, v106, v107\n"
    "v_accvgpr_write_b32 a60, v104\n"
    "v_mfma_f32_16x16x32_bf16 v[124:127], a[132:135], a[52:55], v[124:127]\n"
    "ds_read_b128 a[8:11], v250 offset:512\n"
    "ds_read_b128 a[12:15], v250 offset:768\n"
    "v_cvt_pk_bf16_f32 v108, v108, v109\n"
    "v_accvgpr_write_b32 a61, v105\n"
    "v_mfma_f32_16x16x32_bf16 v[124:127], a[136:139], a[56:59], v[124:127]\n"
    "v_cvt_pk_bf16_f32 v109, v110, v111\n"
    "v_accvgpr_write_b32 a62, v108\n"
    "v_accvgpr_write_b32 a63, v109\n"
    "v_maximum3_f32 v167, v216, v217, v167\n"
    "v_maximum3_f32 v167, v218, v219, v167\n"
    "v_mfma_f32_16x16x32_bf16 v[124:127], a[140:143], a[60:63], v[124:127]\n"
    "v_rcp_f32 v216, v216\n"
    "v_rcp_f32 v217, v217\n"
    "v_rcp_f32 v218, v218\n"
    "v_rcp_f32 v219, v219\n"
    "v_and_b32 v248, s76, v244\n"
    "v_sub_f32 v244, v244, v248\n"
    "v_cvt_pk_bf16_f32 v248, v248, v244\n"
    "v_maximum3_f32 v167, v216, v217, v167\n"
    "v_maximum3_f32 v167, v218, v219, v167\n"
    "v_pk_mul_f32 v[0:1], v[0:1], v[216:217]\n"
    "v_pk_mul_f32 v[2:3], v[2:3], v[218:219]\n"
    "v_pk_mul_f32 v[8:9], v[8:9], v[216:217]\n"
    "v_pk_mul_f32 v[10:11], v[10:11], v[218:219]\n"
    "v_cvt_pk_bf16_f32 v0, v0, v8\n"
    "v_cvt_pk_bf16_f32 v2, v2, v10\n"
    "v_cvt_pk_bf16_f32 v8, v1, v9\n"
    "v_cvt_pk_bf16_f32 v10, v3, v11\n"
    "v_pk_mul_f32 v[16:17], v[16:17], v[216:217]\n"
    "v_pk_mul_f32 v[18:19], v[18:19], v[218:219]\n"
    "v_pk_mul_f32 v[24:25], v[24:25], v[216:217]\n"
    "v_pk_mul_f32 v[26:27], v[26:27], v[218:219]\n"
    "v_cvt_pk_bf16_f32 v1, v16, v24\n"
    "v_cvt_pk_bf16_f32 v3, v18, v26\n"
    "v_cvt_pk_bf16_f32 v9, v17, v25\n"
    "v_cvt_pk_bf16_f32 v11, v19, v27\n"
    "v_pk_mul_f32 v[4:5], v[4:5], v[216:217]\n"
    "v_pk_mul_f32 v[6:7], v[6:7], v[218:219]\n"
    "v_pk_mul_f32 v[12:13], v[12:13], v[216:217]\n"
    "v_pk_mul_f32 v[14:15], v[14:15], v[218:219]\n"
    "v_cvt_pk_bf16_f32 v4, v4, v12\n"
    "v_cvt_pk_bf16_f32 v6, v6, v14\n"
    "v_cvt_pk_bf16_f32 v12, v5, v13\n"
    "v_cvt_pk_bf16_f32 v14, v7, v15\n"
    "v_pk_mul_f32 v[20:21], v[20:21], v[216:217]\n"
    "v_pk_mul_f32 v[22:23], v[22:23], v[218:219]\n"
    "v_pk_mul_f32 v[28:29], v[28:29], v[216:217]\n"
    "v_pk_mul_f32 v[30:31], v[30:31], v[218:219]\n"
    "v_mfma_f32_16x16x16_bf16 v[232:235], v[248:249], a[252:253], 0\n"
    "v_cvt_pk_bf16_f32 v5, v20, v28\n"
    "v_cvt_pk_bf16_f32 v7, v22, v30\n"
    "v_cvt_pk_bf16_f32 v13, v21, v29\n"
    "v_mfma_f32_4x4x4_16b_bf16 v[16:19], v[0:1], a[254:255], 0\n"
    "v_cvt_pk_bf16_f32 v15, v23, v31\n"
    "v_mfma_f32_4x4x4_16b_bf16 v[24:27], v[2:3], a[254:255], 0\n"
    "v_max3_f32 v248, v124, v125, v126\n"
    "v_mfma_f32_4x4x4_16b_bf16 v[20:23], v[4:5], a[254:255], 0\n"
    "v_max_f32_e64 v248, v248, v127\n"
    "v_mfma_f32_4x4x4_16b_bf16 v[28:31], v[6:7], a[254:255], 0\n"
    "v_mul_f32 v248, 0xbe800000, v248\n"
    "v_mfma_f32_4x4x4_16b_bf16 v[0:3], v[8:9], a[254:255], 0\n"
    "v_cvt_pk_bf16_f32 v248, v248, s80\n"
    "v_cvt_pk_bf16_f32 v16, v16, v17\n"
    "v_cvt_pk_bf16_f32 v17, v18, v19\n"
    "v_mfma_f32_4x4x4_16b_bf16 v[4:7], v[12:13], a[254:255], 0\n"
    "v_cvt_pk_bf16_f32 v24, v24, v25\n"
    "v_cvt_pk_bf16_f32 v25, v26, v27\n"
    "v_cvt_pk_bf16_f32 v18, v20, v21\n"
    "v_cvt_pk_bf16_f32 v19, v22, v23\n"
    "v_mfma_f32_4x4x4_16b_bf16 v[20:23], v[10:11], a[254:255], 0\n"
    "v_cvt_pk_bf16_f32 v26, v28, v29\n"
    "v_cvt_pk_bf16_f32 v27, v30, v31\n"
    "v_mfma_f32_4x4x4_16b_bf16 v[28:31], v[14:15], a[254:255], 0\n"
    "v_mfma_f32_16x16x16_bf16 v[228:231], a[252:253], v[248:249], 0\n"
    "v_cvt_pk_bf16_f32 v0, v0, v1\n"
    "v_cvt_pk_bf16_f32 v1, v2, v3\n"
    "s_add_u32 s67, s66, 0\n"
    "s_waitcnt lgkmcnt(0)\n"
    "v_mfma_f32_4x4x4_16b_bf16 v[8:11], a[248:249], a[0:1], 0\n"
    "v_cvt_pk_bf16_f32 v2, v4, v5\n"
    "v_cvt_pk_bf16_f32 v3, v6, v7\n"
    "v_mfma_f32_4x4x4_16b_bf16 v[8:11], a[250:251], a[0:1], v[8:11]\n"
    "buffer_store_dwordx4 v[16:19], v249, s[56:59], s67 offen offset:0\n"
    "v_cvt_pk_bf16_f32 v20, v20, v21\n"
    "v_cvt_pk_bf16_f32 v21, v22, v23\n"
    "v_mfma_f32_4x4x4_16b_bf16 v[12:15], a[248:249], a[2:3], 0\n"
    "v_cvt_pk_bf16_f32 v22, v28, v29\n"
    "v_cvt_pk_bf16_f32 v23, v30, v31\n"
    "v_mfma_f32_4x4x4_16b_bf16 v[12:15], a[250:251], a[2:3], v[12:15]\n"
    "v_cvt_pk_bf16_f32 v8, v8, v9\n"
    "v_mfma_f32_4x4x4_16b_bf16 v[4:7], a[248:249], a[4:5], 0\n"
    "v_cvt_pk_bf16_f32 v9, v10, v11\n"
    "v_accvgpr_write_b32 a0, v8\n"
    "v_mfma_f32_4x4x4_16b_bf16 v[4:7], a[250:251], a[4:5], v[4:7]\n"
    "v_cvt_pk_bf16_f32 v12, v12, v13\n"
    "v_accvgpr_write_b32 a1, v9\n"
    "v_mfma_f32_4x4x4_16b_bf16 v[8:11], a[248:249], a[6:7], 0\n"
    "buffer_store_dwordx4 v[24:27], v249, s[56:59], s67 offen offset:512\n"
    "v_cvt_pk_bf16_f32 v13, v14, v15\n"
    "v_accvgpr_write_b32 a2, v12\n"
    "v_mfma_f32_4x4x4_16b_bf16 v[8:11], a[250:251], a[6:7], v[8:11]\n"
    "v_cvt_pk_bf16_f32 v4, v4, v5\n"
    "v_accvgpr_write_b32 a3, v13\n"
    "v_mfma_f32_4x4x4_16b_bf16 v[12:15], a[248:249], a[8:9], 0\n"
    "v_cvt_pk_bf16_f32 v5, v6, v7\n"
    "v_accvgpr_write_b32 a4, v4\n"
    "v_mfma_f32_4x4x4_16b_bf16 v[12:15], a[250:251], a[8:9], v[12:15]\n"
    "v_cvt_pk_bf16_f32 v8, v8, v9\n"
    "v_accvgpr_write_b32 a5, v5\n"
    "v_mfma_f32_4x4x4_16b_bf16 v[4:7], a[248:249], a[10:11], 0\n"
    "buffer_store_dwordx4 v[0:3], v249, s[56:59], s67 offen offset:256\n"
    "v_cvt_pk_bf16_f32 v9, v10, v11\n"
    "v_accvgpr_write_b32 a6, v8\n"
    "v_mfma_f32_4x4x4_16b_bf16 v[4:7], a[250:251], a[10:11], v[4:7]\n"
    "v_cvt_pk_bf16_f32 v12, v12, v13\n"
    "v_accvgpr_write_b32 a7, v9\n"
    "v_mfma_f32_4x4x4_16b_bf16 v[8:11], a[248:249], a[12:13], 0\n"
    "v_cvt_pk_bf16_f32 v13, v14, v15\n"
    "v_accvgpr_write_b32 a8, v12\n"
    "v_mfma_f32_4x4x4_16b_bf16 v[8:11], a[250:251], a[12:13], v[8:11]\n"
    "v_cvt_pk_bf16_f32 v4, v4, v5\n"
    "v_accvgpr_write_b32 a9, v13\n"
    "v_mfma_f32_4x4x4_16b_bf16 v[12:15], a[248:249], a[14:15], 0\n"
    "buffer_store_dwordx4 v[20:23], v249, s[56:59], s67 offen offset:768\n"
    "v_cvt_pk_bf16_f32 v5, v6, v7\n"
    "v_accvgpr_write_b32 a10, v4\n"
    "v_mfma_f32_4x4x4_16b_bf16 v[12:15], a[250:251], a[14:15], v[12:15]\n"
    "v_cvt_pk_bf16_f32 v8, v8, v9\n"
    "v_accvgpr_write_b32 a11, v5\n"
    "v_mfma_f32_16x16x32_bf16 v[28:31], a[128:131], a[0:3], 0\n"
    "s_waitcnt vmcnt(16)\n"
    "s_nop 0\n"
    "ds_read_b128 a[64:67], v250 offset:16512\n"
    "ds_read_b128 a[68:71], v250 offset:16768\n"
    "v_cvt_pk_bf16_f32 v9, v10, v11\n"
    "v_accvgpr_write_b32 a12, v8\n"
    "v_mfma_f32_16x16x32_bf16 v[28:31], a[132:135], a[4:7], v[28:31]\n"
    "ds_read_b128 a[72:75], v250 offset:17024\n"
    "ds_read_b128 a[76:79], v250 offset:17280\n"
    "v_cvt_pk_bf16_f32 v12, v12, v13\n"
    "v_accvgpr_write_b32 a13, v9\n"
    "v_mfma_f32_16x16x32_bf16 v[28:31], a[136:139], a[8:11], v[28:31]\n"
    "v_cvt_pk_bf16_f32 v13, v14, v15\n"
    "v_accvgpr_write_b32 a14, v12\n"
    "v_accvgpr_write_b32 a15, v13\n"
    "v_add_f32 v204, v124, v228\n"
    "v_add_f32 v205, v125, v229\n"
    "v_mfma_f32_16x16x32_bf16 v[28:31], a[140:143], a[12:15], v[28:31]\n"
    "v_add_f32 v206, v126, v230\n"
    "v_add_f32 v207, v127, v231\n"
    "v_mfma_f32_16x16x32_bf16 v[212:215], a[156:159], a[60:63], v[228:231]\n"
    "v_mfma_f32_16x16x32_bf16 v[212:215], a[152:155], a[56:59], v[212:215]\n"
    "v_mfma_f32_16x16x32_bf16 v[212:215], a[148:151], a[52:55], v[212:215]\n"
    "s_waitcnt vmcnt(63) lgkmcnt(0)\n"
    "v_mfma_f32_16x16x32_bf16 v[212:215], a[144:147], a[48:51], v[212:215]\n"
    "s_barrier\n"
    "v_exp_f32 v204, v204\n"
    "v_mfma_f32_16x16x32_bf16 v[128:131], v[200:203], a[160:163], v[128:131]\n"
    "s_cmp_eq_u32 s62, 23\n"
    "ds_read_b128 a[96:99], v246 offset:0\n"
    "v_mov_b32 v243, v204\n"
    "v_mfma_f32_16x16x32_bf16 v[132:135], v[200:203], a[164:167], v[132:135]\n"
    "s_cselect_b32 s60, s61, s60\n"
    "v_exp_f32 v205, v205\n"
    "v_mfma_f32_16x16x32_bf16 v[136:139], v[200:203], a[168:171], v[136:139]\n"
    "s_cselect_b32 s61, s79, s61\n"
    "ds_read_b128 a[100:103], v246 offset:256\n"
    "v_add_f32 v243, v243, v205\n"
    "v_mfma_f32_16x16x32_bf16 v[140:143], v[200:203], a[224:227], v[140:143]\n"
    "s_cselect_b32 s62, 0, s62\n"
    "v_exp_f32 v206, v206\n"
    "v_mfma_f32_16x16x32_bf16 v[144:147], v[200:203], a[228:231], v[144:147]\n"
    "s_mov_b32 m0, 41280\n"
    "ds_read_b128 a[104:107], v246 offset:512\n"
    "v_add_f32 v243, v243, v206\n"
    "v_mfma_f32_16x16x32_bf16 v[148:151], v[200:203], a[232:235], v[148:151]\n"
    "buffer_load_dwordx4 v253, s[36:39], s60 offen offset:0 lds\n"
    "v_exp_f32 v207, v207\n"
    "v_mfma_f32_16x16x32_bf16 v[152:155], v[200:203], a[236:239], v[152:155]\n"
    "ds_read_b128 a[108:111], v246 offset:768\n"
    "v_add_f32 v243, v243, v207\n"
    "v_mfma_f32_16x16x32_bf16 v[156:159], v[200:203], a[240:243], v[156:159]\n"
    "ds_read_b64_tr_b16 a[188:189], v254 offset:8\n"
    "v_exp_f32 v212, v212\n"
    "v_maximum3_f32 v167, v232, v233, v167\n"
    "v_maximum3_f32 v167, v234, v235, v167\n"
    "v_rcp_f32 v232, v232\n"
    "v_rcp_f32 v233, v233\n"
    "v_rcp_f32 v234, v234\n"
    "v_rcp_f32 v235, v235\n"
    "v_and_b32 v248, s76, v241\n"
    "v_sub_f32_e64 v241, v241, v248\n"
    "v_cvt_pk_bf16_f32 v248, v248, v241\n"
    "v_maximum3_f32 v167, v232, v233, v167\n"
    "v_maximum3_f32 v167, v234, v235, v167\n"
    "v_pk_mul_f32 v[128:129], v[128:129], v[232:233]\n"
    "v_pk_mul_f32 v[130:131], v[130:131], v[234:235]\n"
    "v_pk_mul_f32 v[136:137], v[136:137], v[232:233]\n"
    "v_pk_mul_f32 v[138:139], v[138:139], v[234:235]\n"
    "v_cvt_pk_bf16_f32 v128, v128, v136\n"
    "v_cvt_pk_bf16_f32 v130, v130, v138\n"
    "v_cvt_pk_bf16_f32 v136, v129, v137\n"
    "v_cvt_pk_bf16_f32 v138, v131, v139\n"
    "v_pk_mul_f32 v[144:145], v[144:145], v[232:233]\n"
    "v_pk_mul_f32 v[146:147], v[146:147], v[234:235]\n"
    "v_pk_mul_f32 v[152:153], v[152:153], v[232:233]\n"
    "v_pk_mul_f32 v[154:155], v[154:155], v[234:235]\n"
    "v_cvt_pk_bf16_f32 v129, v144, v152\n"
    "v_cvt_pk_bf16_f32 v131, v146, v154\n"
    "v_cvt_pk_bf16_f32 v137, v145, v153\n"
    "v_cvt_pk_bf16_f32 v139, v147, v155\n"
    "v_pk_mul_f32 v[132:133], v[132:133], v[232:233]\n"
    "v_pk_mul_f32 v[134:135], v[134:135], v[234:235]\n"
    "v_pk_mul_f32 v[140:141], v[140:141], v[232:233]\n"
    "v_pk_mul_f32 v[142:143], v[142:143], v[234:235]\n"
    "v_cvt_pk_bf16_f32 v132, v132, v140\n"
    "v_cvt_pk_bf16_f32 v134, v134, v142\n"
    "v_cvt_pk_bf16_f32 v140, v133, v141\n"
    "v_cvt_pk_bf16_f32 v142, v135, v143\n"
    "v_pk_mul_f32 v[148:149], v[148:149], v[232:233]\n"
    "v_pk_mul_f32 v[150:151], v[150:151], v[234:235]\n"
    "v_pk_mul_f32 v[156:157], v[156:157], v[232:233]\n"
    "v_pk_mul_f32 v[158:159], v[158:159], v[234:235]\n"
    "v_mfma_f32_16x16x16_bf16 v[220:223], v[248:249], a[252:253], 0\n"
    "v_cvt_pk_bf16_f32 v133, v148, v156\n"
    "v_cvt_pk_bf16_f32 v135, v150, v158\n"
    "v_cvt_pk_bf16_f32 v141, v149, v157\n"
    "v_mfma_f32_4x4x4_16b_bf16 v[144:147], v[128:129], a[254:255], 0\n"
    "v_cvt_pk_bf16_f32 v143, v151, v159\n"
    "v_mfma_f32_4x4x4_16b_bf16 v[152:155], v[130:131], a[254:255], 0\n"
    "v_max3_f32 v248, v28, v29, v30\n"
    "v_mfma_f32_4x4x4_16b_bf16 v[148:151], v[132:133], a[254:255], 0\n"
    "v_max_f32_e64 v248, v248, v31\n"
    "v_mfma_f32_4x4x4_16b_bf16 v[156:159], v[134:135], a[254:255], 0\n"
    "v_mul_f32 v248, 0xbe800000, v248\n"
    "v_mfma_f32_4x4x4_16b_bf16 v[128:131], v[136:137], a[254:255], 0\n"
    "v_cvt_pk_bf16_f32 v248, v248, s80\n"
    "v_cvt_pk_bf16_f32 v144, v144, v145\n"
    "v_cvt_pk_bf16_f32 v145, v146, v147\n"
    "v_mfma_f32_4x4x4_16b_bf16 v[132:135], v[140:141], a[254:255], 0\n"
    "v_cvt_pk_bf16_f32 v152, v152, v153\n"
    "v_cvt_pk_bf16_f32 v153, v154, v155\n"
    "v_cvt_pk_bf16_f32 v146, v148, v149\n"
    "v_cvt_pk_bf16_f32 v147, v150, v151\n"
    "v_mfma_f32_4x4x4_16b_bf16 v[148:151], v[138:139], a[254:255], 0\n"
    "v_cvt_pk_bf16_f32 v154, v156, v157\n"
    "v_cvt_pk_bf16_f32 v155, v158, v159\n"
    "v_mfma_f32_4x4x4_16b_bf16 v[156:159], v[142:143], a[254:255], 0\n"
    "v_mfma_f32_16x16x16_bf16 v[216:219], a[252:253], v[248:249], 0\n"
    "v_cvt_pk_bf16_f32 v128, v128, v129\n"
    "v_cvt_pk_bf16_f32 v129, v130, v131\n"
    "s_add_u32 s67, s66, 16384\n"
    "v_mfma_f32_4x4x4_16b_bf16 v[136:139], a[248:249], a[64:65], 0\n"
    "v_cvt_pk_bf16_f32 v130, v132, v133\n"
    "v_cvt_pk_bf16_f32 v131, v134, v135\n"
    "v_mfma_f32_4x4x4_16b_bf16 v[136:139], a[250:251], a[64:65], v[136:139]\n"
    "buffer_store_dwordx4 v[144:147], v249, s[56:59], s67 offen offset:0\n"
    "v_cvt_pk_bf16_f32 v148, v148, v149\n"
    "v_cvt_pk_bf16_f32 v149, v150, v151\n"
    "v_mfma_f32_4x4x4_16b_bf16 v[140:143], a[248:249], a[66:67], 0\n"
    "v_cvt_pk_bf16_f32 v150, v156, v157\n"
    "v_cvt_pk_bf16_f32 v151, v158, v159\n"
    "v_mfma_f32_4x4x4_16b_bf16 v[140:143], a[250:251], a[66:67], v[140:143]\n"
    "v_cvt_pk_bf16_f32 v136, v136, v137\n"
    "v_mfma_f32_4x4x4_16b_bf16 v[132:135], a[248:249], a[68:69], 0\n"
    "v_cvt_pk_bf16_f32 v137, v138, v139\n"
    "v_accvgpr_write_b32 a64, v136\n"
    "v_mfma_f32_4x4x4_16b_bf16 v[132:135], a[250:251], a[68:69], v[132:135]\n"
    "v_cvt_pk_bf16_f32 v140, v140, v141\n"
    "v_accvgpr_write_b32 a65, v137\n"
    "v_mfma_f32_4x4x4_16b_bf16 v[136:139], a[248:249], a[70:71], 0\n"
    "buffer_store_dwordx4 v[152:155], v249, s[56:59], s67 offen offset:512\n"
    "v_cvt_pk_bf16_f32 v141, v142, v143\n"
    "v_accvgpr_write_b32 a66, v140\n"
    "v_mfma_f32_4x4x4_16b_bf16 v[136:139], a[250:251], a[70:71], v[136:139]\n"
    "v_cvt_pk_bf16_f32 v132, v132, v133\n"
    "v_accvgpr_write_b32 a67, v141\n"
    "v_mfma_f32_4x4x4_16b_bf16 v[140:143], a[248:249], a[72:73], 0\n"
    "v_cvt_pk_bf16_f32 v133, v134, v135\n"
    "v_accvgpr_write_b32 a68, v132\n"
    "v_mfma_f32_4x4x4_16b_bf16 v[140:143], a[250:251], a[72:73], v[140:143]\n"
    "v_cvt_pk_bf16_f32 v136, v136, v137\n"
    "v_accvgpr_write_b32 a69, v133\n"
    "v_mfma_f32_4x4x4_16b_bf16 v[132:135], a[248:249], a[74:75], 0\n"
    "buffer_store_dwordx4 v[128:131], v249, s[56:59], s67 offen offset:256\n"
    "v_cvt_pk_bf16_f32 v137, v138, v139\n"
    "v_accvgpr_write_b32 a70, v136\n"
    "v_mfma_f32_4x4x4_16b_bf16 v[132:135], a[250:251], a[74:75], v[132:135]\n"
    "v_cvt_pk_bf16_f32 v140, v140, v141\n"
    "v_accvgpr_write_b32 a71, v137\n"
    "v_mfma_f32_4x4x4_16b_bf16 v[136:139], a[248:249], a[76:77], 0\n"
    "v_cvt_pk_bf16_f32 v141, v142, v143\n"
    "v_accvgpr_write_b32 a72, v140\n"
    "v_mfma_f32_4x4x4_16b_bf16 v[136:139], a[250:251], a[76:77], v[136:139]\n"
    "v_cvt_pk_bf16_f32 v132, v132, v133\n"
    "v_accvgpr_write_b32 a73, v141\n"
    "v_mfma_f32_4x4x4_16b_bf16 v[140:143], a[248:249], a[78:79], 0\n"
    "buffer_store_dwordx4 v[148:151], v249, s[56:59], s67 offen offset:768\n"
    "v_cvt_pk_bf16_f32 v133, v134, v135\n"
    "v_accvgpr_write_b32 a74, v132\n"
    "v_mfma_f32_4x4x4_16b_bf16 v[140:143], a[250:251], a[78:79], v[140:143]\n"
    "v_cvt_pk_bf16_f32 v136, v136, v137\n"
    "v_accvgpr_write_b32 a75, v133\n"
    "v_mfma_f32_16x16x32_bf16 v[156:159], a[128:131], a[64:67], 0\n"
    "s_waitcnt vmcnt(63)\n"
    "s_nop 0\n"
    "ds_read_b128 a[16:19], v250 offset:4128\n"
    "ds_read_b128 a[20:23], v250 offset:4384\n"
    "v_cvt_pk_bf16_f32 v137, v138, v139\n"
    "v_accvgpr_write_b32 a76, v136\n"
    "v_mfma_f32_16x16x32_bf16 v[156:159], a[132:135], a[68:71], v[156:159]\n"
    "ds_read_b128 a[24:27], v250 offset:4640\n"
    "ds_read_b128 a[28:31], v250 offset:4896\n"
    "v_cvt_pk_bf16_f32 v140, v140, v141\n"
    "v_accvgpr_write_b32 a77, v137\n"
    "v_mfma_f32_16x16x32_bf16 v[156:159], a[136:139], a[72:75], v[156:159]\n"
    "v_cvt_pk_bf16_f32 v141, v142, v143\n"
    "v_accvgpr_write_b32 a78, v140\n"
    "v_accvgpr_write_b32 a79, v141\n"
    "v_maximum3_f32 v167, v220, v221, v167\n"
    "v_maximum3_f32 v167, v222, v223, v167\n"
    "v_mfma_f32_16x16x32_bf16 v[156:159], a[140:143], a[76:79], v[156:159]\n"
    "v_rcp_f32 v220, v220\n"
    "v_rcp_f32 v221, v221\n"
    "v_rcp_f32 v222, v222\n"
    "v_rcp_f32 v223, v223\n"
    "v_and_b32 v248, s76, v242\n"
    "v_sub_f32 v242, v242, v248\n"
    "v_cvt_pk_bf16_f32 v248, v248, v242\n"
    "v_maximum3_f32 v167, v220, v221, v167\n"
    "v_maximum3_f32 v167, v222, v223, v167\n"
    "v_pk_mul_f32 v[32:33], v[32:33], v[220:221]\n"
    "v_pk_mul_f32 v[34:35], v[34:35], v[222:223]\n"
    "v_pk_mul_f32 v[40:41], v[40:41], v[220:221]\n"
    "v_pk_mul_f32 v[42:43], v[42:43], v[222:223]\n"
    "v_cvt_pk_bf16_f32 v32, v32, v40\n"
    "v_cvt_pk_bf16_f32 v34, v34, v42\n"
    "v_cvt_pk_bf16_f32 v40, v33, v41\n"
    "v_cvt_pk_bf16_f32 v42, v35, v43\n"
    "v_pk_mul_f32 v[48:49], v[48:49], v[220:221]\n"
    "v_pk_mul_f32 v[50:51], v[50:51], v[222:223]\n"
    "v_pk_mul_f32 v[56:57], v[56:57], v[220:221]\n"
    "v_pk_mul_f32 v[58:59], v[58:59], v[222:223]\n"
    "v_cvt_pk_bf16_f32 v33, v48, v56\n"
    "v_cvt_pk_bf16_f32 v35, v50, v58\n"
    "v_cvt_pk_bf16_f32 v41, v49, v57\n"
    "v_cvt_pk_bf16_f32 v43, v51, v59\n"
    "v_pk_mul_f32 v[36:37], v[36:37], v[220:221]\n"
    "v_pk_mul_f32 v[38:39], v[38:39], v[222:223]\n"
    "v_pk_mul_f32 v[44:45], v[44:45], v[220:221]\n"
    "v_pk_mul_f32 v[46:47], v[46:47], v[222:223]\n"
    "v_cvt_pk_bf16_f32 v36, v36, v44\n"
    "v_cvt_pk_bf16_f32 v38, v38, v46\n"
    "v_cvt_pk_bf16_f32 v44, v37, v45\n"
    "v_cvt_pk_bf16_f32 v46, v39, v47\n"
    "v_pk_mul_f32 v[52:53], v[52:53], v[220:221]\n"
    "v_pk_mul_f32 v[54:55], v[54:55], v[222:223]\n"
    "v_pk_mul_f32 v[60:61], v[60:61], v[220:221]\n"
    "v_pk_mul_f32 v[62:63], v[62:63], v[222:223]\n"
    "v_mfma_f32_16x16x16_bf16 v[224:227], v[248:249], a[252:253], 0\n"
    "v_cvt_pk_bf16_f32 v37, v52, v60\n"
    "v_cvt_pk_bf16_f32 v39, v54, v62\n"
    "v_cvt_pk_bf16_f32 v45, v53, v61\n"
    "v_mfma_f32_4x4x4_16b_bf16 v[48:51], v[32:33], a[254:255], 0\n"
    "v_cvt_pk_bf16_f32 v47, v55, v63\n"
    "v_mfma_f32_4x4x4_16b_bf16 v[56:59], v[34:35], a[254:255], 0\n"
    "v_max3_f32 v248, v156, v157, v158\n"
    "v_mfma_f32_4x4x4_16b_bf16 v[52:55], v[36:37], a[254:255], 0\n"
    "v_max_f32_e64 v248, v248, v159\n"
    "v_mfma_f32_4x4x4_16b_bf16 v[60:63], v[38:39], a[254:255], 0\n"
    "v_mul_f32 v248, 0xbe800000, v248\n"
    "v_mfma_f32_4x4x4_16b_bf16 v[32:35], v[40:41], a[254:255], 0\n"
    "v_cvt_pk_bf16_f32 v248, v248, s80\n"
    "v_cvt_pk_bf16_f32 v48, v48, v49\n"
    "v_cvt_pk_bf16_f32 v49, v50, v51\n"
    "v_mfma_f32_4x4x4_16b_bf16 v[36:39], v[44:45], a[254:255], 0\n"
    "v_cvt_pk_bf16_f32 v56, v56, v57\n"
    "v_cvt_pk_bf16_f32 v57, v58, v59\n"
    "v_cvt_pk_bf16_f32 v50, v52, v53\n"
    "v_cvt_pk_bf16_f32 v51, v54, v55\n"
    "v_mfma_f32_4x4x4_16b_bf16 v[52:55], v[42:43], a[254:255], 0\n"
    "v_cvt_pk_bf16_f32 v58, v60, v61\n"
    "v_cvt_pk_bf16_f32 v59, v62, v63\n"
    "v_mfma_f32_4x4x4_16b_bf16 v[60:63], v[46:47], a[254:255], 0\n"
    "v_mfma_f32_16x16x16_bf16 v[232:235], a[252:253], v[248:249], 0\n"
    "v_cvt_pk_bf16_f32 v32, v32, v33\n"
    "v_cvt_pk_bf16_f32 v33, v34, v35\n"
    "s_add_u32 s67, s66, 4096\n"
    "s_waitcnt lgkmcnt(0)\n"
    "s_nop 0\n"
    "v_mfma_f32_4x4x4_16b_bf16 v[40:43], a[248:249], a[16:17], 0\n"
    "v_cvt_pk_bf16_f32 v34, v36, v37\n"
    "v_cvt_pk_bf16_f32 v35, v38, v39\n"
    "v_mfma_f32_4x4x4_16b_bf16 v[40:43], a[250:251], a[16:17], v[40:43]\n"
    "buffer_store_dwordx4 v[48:51], v249, s[56:59], s67 offen offset:0\n"
    "v_cvt_pk_bf16_f32 v52, v52, v53\n"
    "v_cvt_pk_bf16_f32 v53, v54, v55\n"
    "v_mfma_f32_4x4x4_16b_bf16 v[44:47], a[248:249], a[18:19], 0\n"
    "v_cvt_pk_bf16_f32 v54, v60, v61\n"
    "v_cvt_pk_bf16_f32 v55, v62, v63\n"
    "v_mfma_f32_4x4x4_16b_bf16 v[44:47], a[250:251], a[18:19], v[44:47]\n"
    "v_cvt_pk_bf16_f32 v40, v40, v41\n"
    "v_mfma_f32_4x4x4_16b_bf16 v[36:39], a[248:249], a[20:21], 0\n"
    "v_cvt_pk_bf16_f32 v41, v42, v43\n"
    "v_accvgpr_write_b32 a16, v40\n"
    "v_mfma_f32_4x4x4_16b_bf16 v[36:39], a[250:251], a[20:21], v[36:39]\n"
    "v_cvt_pk_bf16_f32 v44, v44, v45\n"
    "v_accvgpr_write_b32 a17, v41\n"
    "v_mfma_f32_4x4x4_16b_bf16 v[40:43], a[248:249], a[22:23], 0\n"
    "buffer_store_dwordx4 v[56:59], v249, s[56:59], s67 offen offset:512\n"
    "v_cvt_pk_bf16_f32 v45, v46, v47\n"
    "v_accvgpr_write_b32 a18, v44\n"
    "v_mfma_f32_4x4x4_16b_bf16 v[40:43], a[250:251], a[22:23], v[40:43]\n"
    "v_cvt_pk_bf16_f32 v36, v36, v37\n"
    "v_accvgpr_write_b32 a19, v45\n"
    "v_mfma_f32_4x4x4_16b_bf16 v[44:47], a[248:249], a[24:25], 0\n"
    "v_cvt_pk_bf16_f32 v37, v38, v39\n"
    "v_accvgpr_write_b32 a20, v36\n"
    "v_mfma_f32_4x4x4_16b_bf16 v[44:47], a[250:251], a[24:25], v[44:47]\n"
    "v_cvt_pk_bf16_f32 v40, v40, v41\n"
    "v_accvgpr_write_b32 a21, v37\n"
    "v_mfma_f32_4x4x4_16b_bf16 v[36:39], a[248:249], a[26:27], 0\n"
    "buffer_store_dwordx4 v[32:35], v249, s[56:59], s67 offen offset:256\n"
    "v_cvt_pk_bf16_f32 v41, v42, v43\n"
    "v_accvgpr_write_b32 a22, v40\n"
    "v_mfma_f32_4x4x4_16b_bf16 v[36:39], a[250:251], a[26:27], v[36:39]\n"
    "v_cvt_pk_bf16_f32 v44, v44, v45\n"
    "v_accvgpr_write_b32 a23, v41\n"
    "v_mfma_f32_4x4x4_16b_bf16 v[40:43], a[248:249], a[28:29], 0\n"
    "v_cvt_pk_bf16_f32 v45, v46, v47\n"
    "v_accvgpr_write_b32 a24, v44\n"
    "v_mfma_f32_4x4x4_16b_bf16 v[40:43], a[250:251], a[28:29], v[40:43]\n"
    "v_cvt_pk_bf16_f32 v36, v36, v37\n"
    "v_accvgpr_write_b32 a25, v45\n"
    "v_mfma_f32_4x4x4_16b_bf16 v[44:47], a[248:249], a[30:31], 0\n"
    "buffer_store_dwordx4 v[52:55], v249, s[56:59], s67 offen offset:768\n"
    "v_cvt_pk_bf16_f32 v37, v38, v39\n"
    "v_accvgpr_write_b32 a26, v36\n"
    "v_mfma_f32_4x4x4_16b_bf16 v[44:47], a[250:251], a[30:31], v[44:47]\n"
    "v_cvt_pk_bf16_f32 v40, v40, v41\n"
    "v_accvgpr_write_b32 a27, v37\n"
    "v_mfma_f32_16x16x32_bf16 v[60:63], a[128:131], a[16:19], 0\n"
    "s_waitcnt vmcnt(63)\n"
    "s_nop 0\n"
    "ds_read_b128 a[32:35], v250 offset:8256\n"
    "ds_read_b128 a[36:39], v250 offset:8512\n"
    "v_cvt_pk_bf16_f32 v41, v42, v43\n"
    "v_accvgpr_write_b32 a28, v40\n"
    "v_mfma_f32_16x16x32_bf16 v[60:63], a[132:135], a[20:23], v[60:63]\n"
    "ds_read_b128 a[40:43], v250 offset:8768\n"
    "ds_read_b128 a[44:47], v250 offset:9024\n"
    "v_cvt_pk_bf16_f32 v44, v44, v45\n"
    "v_accvgpr_write_b32 a29, v41\n"
    "v_mfma_f32_16x16x32_bf16 v[60:63], a[136:139], a[24:27], v[60:63]\n"
    "v_cvt_pk_bf16_f32 v45, v46, v47\n"
    "v_accvgpr_write_b32 a30, v44\n"
    "v_accvgpr_write_b32 a31, v45\n"
    "v_add_f32 v243, v243, v212\n"
    "v_cvt_pk_bf16_f32 v204, v204, v205\n"
    "v_mfma_f32_16x16x32_bf16 v[60:63], a[140:143], a[28:31], v[60:63]\n"
    "v_exp_f32 v213, v213\n"
    "v_cvt_pk_bf16_f32 v205, v206, v207\n"
    "v_add_f32 v243, v243, v213\n"
    "v_exp_f32 v214, v214\n"
    "v_add_f32 v192, v156, v232\n"
    "v_add_f32 v193, v157, v233\n"
    "v_add_f32 v194, v158, v234\n"
    "v_add_f32 v195, v159, v235\n"
    "v_mfma_f32_16x16x32_bf16 v[208:211], a[156:159], a[76:79], v[232:235]\n"
    "ds_read_b64_tr_b16 a[168:169], v251 offset:0\n"
    "v_add_f32 v243, v243, v214\n"
    "v_cvt_pk_bf16_f32 v206, v212, v213\n"
    "v_mfma_f32_16x16x32_bf16 v[208:211], a[152:155], a[72:75], v[208:211]\n"
    "ds_read_b64_tr_b16 a[190:191], v254 offset:8264\n"
    "v_exp_f32 v215, v215\n"
    "v_mfma_f32_16x16x32_bf16 v[208:211], a[148:151], a[68:71], v[208:211]\n"
    "buffer_load_dwordx4 v253, s[36:39], s60 offen offset:1024 lds\n"
    "v_add_f32 v243, v243, v215\n"
    "v_cvt_pk_bf16_f32 v207, v214, v215\n"
    "v_mfma_f32_16x16x32_bf16 v[208:211], a[144:147], a[64:67], v[208:211]\n"
    "buffer_load_dwordx4 v253, s[40:43], s60 offen offset:2048 lds\n"
    "v_exp_f32 v192, v192\n"
    "v_mfma_f32_16x16x32_bf16 v[96:99], v[204:207], a[192:195], 0\n"
    "ds_read_b128 a[124:127], v246 offset:9024\n"
    "v_mov_b32 v244, v192\n"
    "v_mfma_f32_16x16x32_bf16 v[100:103], v[204:207], a[196:199], 0\n"
    "buffer_load_dwordx4 v253, s[44:47], s60 offen offset:3080 lds\n"
    "v_exp_f32 v193, v193\n"
    "v_mfma_f32_16x16x32_bf16 v[104:107], v[204:207], a[200:203], 0\n"
    "s_add_u32 s60, s60, 2048\n"
    "s_mov_b32 m0, 45408\n"
    "ds_read_b128 a[120:123], v246 offset:8768\n"
    "v_add_f32 v244, v244, v193\n"
    "v_mfma_f32_16x16x32_bf16 v[108:111], v[204:207], a[244:247], 0\n"
    "buffer_load_dwordx4 v253, s[36:39], s60 offen offset:0 lds\n"
    "v_exp_f32 v194, v194\n"
    "v_mfma_f32_16x16x32_bf16 v[112:115], v[204:207], a[80:83], 0\n"
    "ds_read_b128 a[116:119], v246 offset:8512\n"
    "v_add_f32 v244, v244, v194\n"
    "v_mfma_f32_16x16x32_bf16 v[116:119], v[204:207], a[84:87], 0\n"
    "buffer_load_dwordx4 v253, s[36:39], s60 offen offset:1024 lds\n"
    "v_exp_f32 v195, v195\n"
    "v_mfma_f32_16x16x32_bf16 v[120:123], v[204:207], a[88:91], 0\n"
    "ds_read_b128 a[112:115], v246 offset:8256\n"
    "v_add_f32 v244, v244, v195\n"
    "v_mfma_f32_16x16x32_bf16 v[124:127], v[204:207], a[92:95], 0\n"
    "buffer_load_dwordx4 v253, s[40:43], s60 offen offset:2048 lds\n"
    "v_exp_f32 v208, v208\n"
    "v_maximum3_f32 v167, v224, v225, v167\n"
    "v_maximum3_f32 v167, v226, v227, v167\n"
    "v_rcp_f32 v224, v224\n"
    "v_rcp_f32 v225, v225\n"
    "v_rcp_f32 v226, v226\n"
    "v_rcp_f32 v227, v227\n"
    "v_maximum3_f32 v167, v224, v225, v167\n"
    "v_maximum3_f32 v167, v226, v227, v167\n"
    "v_pk_mul_f32 v[64:65], v[64:65], v[224:225]\n"
    "v_pk_mul_f32 v[66:67], v[66:67], v[226:227]\n"
    "v_pk_mul_f32 v[72:73], v[72:73], v[224:225]\n"
    "v_pk_mul_f32 v[74:75], v[74:75], v[226:227]\n"
    "v_cvt_pk_bf16_f32 v64, v64, v72\n"
    "v_cvt_pk_bf16_f32 v66, v66, v74\n"
    "v_cvt_pk_bf16_f32 v72, v65, v73\n"
    "v_cvt_pk_bf16_f32 v74, v67, v75\n"
    "v_pk_mul_f32 v[80:81], v[80:81], v[224:225]\n"
    "v_pk_mul_f32 v[82:83], v[82:83], v[226:227]\n"
    "v_pk_mul_f32 v[88:89], v[88:89], v[224:225]\n"
    "v_pk_mul_f32 v[90:91], v[90:91], v[226:227]\n"
    "v_cvt_pk_bf16_f32 v65, v80, v88\n"
    "v_cvt_pk_bf16_f32 v67, v82, v90\n"
    "v_cvt_pk_bf16_f32 v73, v81, v89\n"
    "v_cvt_pk_bf16_f32 v75, v83, v91\n"
    "v_pk_mul_f32 v[68:69], v[68:69], v[224:225]\n"
    "v_pk_mul_f32 v[70:71], v[70:71], v[226:227]\n"
    "v_pk_mul_f32 v[76:77], v[76:77], v[224:225]\n"
    "v_pk_mul_f32 v[78:79], v[78:79], v[226:227]\n"
    "v_cvt_pk_bf16_f32 v68, v68, v76\n"
    "v_cvt_pk_bf16_f32 v70, v70, v78\n"
    "v_cvt_pk_bf16_f32 v76, v69, v77\n"
    "v_cvt_pk_bf16_f32 v78, v71, v79\n"
    "v_pk_mul_f32 v[84:85], v[84:85], v[224:225]\n"
    "v_pk_mul_f32 v[86:87], v[86:87], v[226:227]\n"
    "v_pk_mul_f32 v[92:93], v[92:93], v[224:225]\n"
    "v_pk_mul_f32 v[94:95], v[94:95], v[226:227]\n"
    "v_cvt_pk_bf16_f32 v69, v84, v92\n"
    "v_cvt_pk_bf16_f32 v71, v86, v94\n"
    "v_cvt_pk_bf16_f32 v77, v85, v93\n"
    "v_mfma_f32_4x4x4_16b_bf16 v[80:83], v[64:65], a[254:255], 0\n"
    "v_cvt_pk_bf16_f32 v79, v87, v95\n"
    "v_mfma_f32_4x4x4_16b_bf16 v[88:91], v[66:67], a[254:255], 0\n"
    "v_max3_f32 v248, v60, v61, v62\n"
    "v_mfma_f32_4x4x4_16b_bf16 v[84:87], v[68:69], a[254:255], 0\n"
    "v_max_f32_e64 v248, v248, v63\n"
    "v_mfma_f32_4x4x4_16b_bf16 v[92:95], v[70:71], a[254:255], 0\n"
    "v_mul_f32 v248, 0xbe800000, v248\n"
    "v_mfma_f32_4x4x4_16b_bf16 v[64:67], v[72:73], a[254:255], 0\n"
    "v_cvt_pk_bf16_f32 v248, v248, s80\n"
    "v_cvt_pk_bf16_f32 v80, v80, v81\n"
    "v_cvt_pk_bf16_f32 v81, v82, v83\n"
    "v_mfma_f32_4x4x4_16b_bf16 v[68:71], v[76:77], a[254:255], 0\n"
    "v_cvt_pk_bf16_f32 v88, v88, v89\n"
    "v_cvt_pk_bf16_f32 v89, v90, v91\n"
    "v_cvt_pk_bf16_f32 v82, v84, v85\n"
    "v_cvt_pk_bf16_f32 v83, v86, v87\n"
    "v_mfma_f32_4x4x4_16b_bf16 v[84:87], v[74:75], a[254:255], 0\n"
    "v_cvt_pk_bf16_f32 v90, v92, v93\n"
    "v_cvt_pk_bf16_f32 v91, v94, v95\n"
    "v_mfma_f32_4x4x4_16b_bf16 v[92:95], v[78:79], a[254:255], 0\n"
    "v_mfma_f32_16x16x16_bf16 v[220:223], a[252:253], v[248:249], 0\n"
    "v_cvt_pk_bf16_f32 v64, v64, v65\n"
    "v_cvt_pk_bf16_f32 v65, v66, v67\n"
    "s_add_u32 s67, s66, 8192\n"
    "s_waitcnt lgkmcnt(0)\n"
    "s_nop 0\n"
    "v_mfma_f32_4x4x4_16b_bf16 v[72:75], a[248:249], a[32:33], 0\n"
    "v_cvt_pk_bf16_f32 v66, v68, v69\n"
    "v_cvt_pk_bf16_f32 v67, v70, v71\n"
    "v_mfma_f32_4x4x4_16b_bf16 v[72:75], a[250:251], a[32:33], v[72:75]\n"
    "buffer_store_dwordx4 v[80:83], v249, s[56:59], s67 offen offset:0\n"
    "v_cvt_pk_bf16_f32 v84, v84, v85\n"
    "v_cvt_pk_bf16_f32 v85, v86, v87\n"
    "v_mfma_f32_4x4x4_16b_bf16 v[76:79], a[248:249], a[34:35], 0\n"
    "v_cvt_pk_bf16_f32 v86, v92, v93\n"
    "v_cvt_pk_bf16_f32 v87, v94, v95\n"
    "v_mfma_f32_4x4x4_16b_bf16 v[76:79], a[250:251], a[34:35], v[76:79]\n"
    "v_cvt_pk_bf16_f32 v72, v72, v73\n"
    "v_mfma_f32_4x4x4_16b_bf16 v[68:71], a[248:249], a[36:37], 0\n"
    "v_cvt_pk_bf16_f32 v73, v74, v75\n"
    "v_accvgpr_write_b32 a32, v72\n"
    "v_mfma_f32_4x4x4_16b_bf16 v[68:71], a[250:251], a[36:37], v[68:71]\n"
    "v_cvt_pk_bf16_f32 v76, v76, v77\n"
    "v_accvgpr_write_b32 a33, v73\n"
    "v_mfma_f32_4x4x4_16b_bf16 v[72:75], a[248:249], a[38:39], 0\n"
    "buffer_store_dwordx4 v[88:91], v249, s[56:59], s67 offen offset:512\n"
    "v_cvt_pk_bf16_f32 v77, v78, v79\n"
    "v_accvgpr_write_b32 a34, v76\n"
    "v_mfma_f32_4x4x4_16b_bf16 v[72:75], a[250:251], a[38:39], v[72:75]\n"
    "v_cvt_pk_bf16_f32 v68, v68, v69\n"
    "v_accvgpr_write_b32 a35, v77\n"
    "v_mfma_f32_4x4x4_16b_bf16 v[76:79], a[248:249], a[40:41], 0\n"
    "v_cvt_pk_bf16_f32 v69, v70, v71\n"
    "v_accvgpr_write_b32 a36, v68\n"
    "v_mfma_f32_4x4x4_16b_bf16 v[76:79], a[250:251], a[40:41], v[76:79]\n"
    "v_cvt_pk_bf16_f32 v72, v72, v73\n"
    "v_accvgpr_write_b32 a37, v69\n"
    "v_mfma_f32_4x4x4_16b_bf16 v[68:71], a[248:249], a[42:43], 0\n"
    "buffer_store_dwordx4 v[64:67], v249, s[56:59], s67 offen offset:256\n"
    "v_cvt_pk_bf16_f32 v73, v74, v75\n"
    "v_accvgpr_write_b32 a38, v72\n"
    "v_mfma_f32_4x4x4_16b_bf16 v[68:71], a[250:251], a[42:43], v[68:71]\n"
    "v_cvt_pk_bf16_f32 v76, v76, v77\n"
    "v_accvgpr_write_b32 a39, v73\n"
    "v_mfma_f32_4x4x4_16b_bf16 v[72:75], a[248:249], a[44:45], 0\n"
    "v_cvt_pk_bf16_f32 v77, v78, v79\n"
    "v_accvgpr_write_b32 a40, v76\n"
    "v_mfma_f32_4x4x4_16b_bf16 v[72:75], a[250:251], a[44:45], v[72:75]\n"
    "v_cvt_pk_bf16_f32 v68, v68, v69\n"
    "v_accvgpr_write_b32 a41, v77\n"
    "v_mfma_f32_4x4x4_16b_bf16 v[76:79], a[248:249], a[46:47], 0\n"
    "buffer_store_dwordx4 v[84:87], v249, s[56:59], s67 offen offset:768\n"
    "v_cvt_pk_bf16_f32 v69, v70, v71\n"
    "v_accvgpr_write_b32 a42, v68\n"
    "v_mfma_f32_4x4x4_16b_bf16 v[76:79], a[250:251], a[46:47], v[76:79]\n"
    "v_cvt_pk_bf16_f32 v72, v72, v73\n"
    "v_accvgpr_write_b32 a43, v69\n"
    "v_mfma_f32_16x16x32_bf16 v[92:95], a[128:131], a[32:35], 0\n"
    "v_cvt_pk_bf16_f32 v73, v74, v75\n"
    "v_accvgpr_write_b32 a44, v72\n"
    "v_mfma_f32_16x16x32_bf16 v[92:95], a[132:135], a[36:39], v[92:95]\n"
    "v_cvt_pk_bf16_f32 v76, v76, v77\n"
    "v_accvgpr_write_b32 a45, v73\n"
    "v_mfma_f32_16x16x32_bf16 v[92:95], a[136:139], a[40:43], v[92:95]\n"
    "v_cvt_pk_bf16_f32 v77, v78, v79\n"
    "v_accvgpr_write_b32 a46, v76\n"
    "v_accvgpr_write_b32 a47, v77\n"
    "v_cmp_ngt_f32 vcc, s72, v167\n"
    "s_nop 0\n"
    "v_mfma_f32_16x16x32_bf16 v[92:95], a[140:143], a[44:47], v[92:95]\n"
    "s_cmp_lg_u64 vcc, 0\n"
    "s_cselect_b32 s69, 1, 0\n"
    "s_or_b32 s65, s65, s69\n"
    "v_add_f32 v244, v244, v208\n"
    "v_cvt_pk_bf16_f32 v192, v192, v193\n"
    "v_exp_f32 v209, v209\n"
    "v_cvt_pk_bf16_f32 v193, v194, v195\n"
    "v_add_f32 v244, v244, v209\n"
    "v_exp_f32 v210, v210\n"
    "v_add_f32 v196, v28, v216\n"
    "v_add_f32 v197, v29, v217\n"
    "v_add_f32 v198, v30, v218\n"
    "v_add_f32 v199, v31, v219\n"
    "v_mfma_f32_16x16x32_bf16 v[212:215], a[156:159], a[12:15], v[216:219]\n"
    "ds_read_b64_tr_b16 a[170:171], v251 offset:8256\n"
    "v_add_f32 v244, v244, v210\n"
    "v_cvt_pk_bf16_f32 v194, v208, v209\n"
    "v_mfma_f32_16x16x32_bf16 v[212:215], a[152:155], a[8:11], v[212:215]\n"
    "buffer_load_dwordx4 v253, s[44:47], s60 offen offset:3080 lds\n"
    "s_add_u32 s60, s60, 2048\n"
    "s_mov_b32 m0, 49536\n"
    "v_exp_f32 v211, v211\n"
    "v_mfma_f32_16x16x32_bf16 v[212:215], a[148:151], a[4:7], v[212:215]\n"
    "buffer_load_dwordx4 v253, s[36:39], s60 offen offset:0 lds\n"
    "s_add_u32 s62, s62, 1\n"
    "v_add_f32 v244, v244, v211\n"
    "v_cvt_pk_bf16_f32 v195, v210, v211\n"
    "v_mfma_f32_16x16x32_bf16 v[212:215], a[144:147], a[0:3], v[212:215]\n"
    "buffer_load_dwordx4 v253, s[36:39], s60 offen offset:1024 lds\n"
    "v_exp_f32 v196, v196\n"
    "v_mfma_f32_16x16x32_bf16 v[128:131], v[192:195], a[192:195], 0\n"
    "ds_read_b64_tr_b16 a[160:161], v247 offset:0\n"
    "v_mov_b32 v240, v196\n"
    "v_mfma_f32_16x16x32_bf16 v[132:135], v[192:195], a[196:199], 0\n"
    "buffer_load_dwordx4 v253, s[40:43], s60 offen offset:2048 lds\n"
    "v_exp_f32 v197, v197\n"
    "v_mfma_f32_16x16x32_bf16 v[136:139], v[192:195], a[200:203], 0\n"
    "ds_read_b64_tr_b16 a[162:163], v247 offset:8256\n"
    "v_add_f32 v240, v240, v197\n"
    "v_mfma_f32_16x16x32_bf16 v[140:143], v[192:195], a[244:247], 0\n"
    "buffer_load_dwordx4 v253, s[44:47], s60 offen offset:3080 lds\n"
    "v_exp_f32 v198, v198\n"
    "v_mfma_f32_16x16x32_bf16 v[144:147], v[192:195], a[80:83], 0\n"
    "s_add_u32 s60, s60, 2048\n"
    "s_mov_b32 m0, 53664\n"
    "ds_read_b64_tr_b16 a[164:165], v247 offset:8\n"
    "v_add_f32 v240, v240, v198\n"
    "v_mfma_f32_16x16x32_bf16 v[148:151], v[192:195], a[84:87], 0\n"
    "buffer_load_dwordx4 v253, s[36:39], s60 offen offset:0 lds\n"
    "v_exp_f32 v199, v199\n"
    "v_mfma_f32_16x16x32_bf16 v[152:155], v[192:195], a[88:91], 0\n"
    "ds_read_b64_tr_b16 a[166:167], v247 offset:8264\n"
    "v_add_f32 v240, v240, v199\n"
    "v_mfma_f32_16x16x32_bf16 v[156:159], v[192:195], a[92:95], 0\n"
    "v_exp_f32 v212, v212\n"
    "v_max3_f32 v248, v92, v93, v94\n"
    "v_max_f32 v248, v248, v95\n"
    "v_mul_f32 v248, 0xbe800000, v248\n"
    "v_cvt_pk_bf16_f32 v248, v248, s80\n"
    "s_nop 1\n"
    "v_mfma_f32_16x16x16_bf16 v[224:227], a[252:253], v[248:249], 0\n"
    "s_mov_b64 exec, 0\n"
    "s_branch LPG7\n"
    "LWRET2:\n"
    "s_mov_b64 exec, -1\n"
    "v_mfma_f32_16x16x32_bf16 v[200:203], a[96:99], a[0:3], v[216:219]\n"
    "v_add_f32 v240, v240, v212\n"
    "v_cvt_pk_bf16_f32 v196, v196, v197\n"
    "v_mfma_f32_16x16x32_bf16 v[200:203], a[100:103], a[4:7], v[200:203]\n"
    "v_exp_f32 v213, v213\n"
    "v_mfma_f32_16x16x32_bf16 v[200:203], a[104:107], a[8:11], v[200:203]\n"
    "ds_read_b64_tr_b16 a[174:175], v251 offset:8264\n"
    "v_add_f32 v240, v240, v213\n"
    "v_cvt_pk_bf16_f32 v197, v198, v199\n"
    "v_mfma_f32_16x16x32_bf16 v[200:203], a[108:111], a[12:15], v[200:203]\n"
    "buffer_load_dwordx4 v253, s[36:39], s60 offen offset:1024 lds\n"
    "v_exp_f32 v214, v214\n"
    "v_mfma_f32_16x16x32_bf16 v[208:211], a[124:127], a[12:15], v[216:219]\n"
    "buffer_load_dwordx4 v253, s[40:43], s60 offen offset:2048 lds\n"
    "v_add_f32 v240, v240, v214\n"
    "v_cvt_pk_bf16_f32 v198, v212, v213\n"
    "v_mfma_f32_16x16x32_bf16 v[208:211], a[120:123], a[8:11], v[208:211]\n"
    "buffer_load_dwordx4 v253, s[44:47], s60 offen offset:3080 lds\n"
    "v_exp_f32 v215, v215\n"
    "v_mfma_f32_16x16x32_bf16 v[208:211], a[116:119], a[4:7], v[208:211]\n"
    "s_add_u32 s60, s60, 2048\n"
    "ds_read_b64_tr_b16 a[182:183], v252 offset:8264\n"
    "v_add_f32 v240, v240, v215\n"
    "v_cvt_pk_bf16_f32 v199, v214, v215\n"
    "v_mfma_f32_16x16x32_bf16 v[208:211], a[112:115], a[0:3], v[208:211]\n"
    "ds_read_b64_tr_b16 a[186:187], v254 offset:8256\n"
    "v_exp_f32 v200, v200\n"
    "v_exp_f32 v201, v201\n"
    "v_add_f32 v240, v240, v200\n"
    "v_add_f32 v240, v240, v201\n"
    "v_exp_f32 v202, v202\n"
    "s_mov_b32 m0, 57792\n"
    "v_add_f32 v240, v240, v202\n"
    "v_exp_f32 v203, v203\n"
    "v_exp_f32 v208, v208\n"
    "v_add_f32 v240, v240, v203\n"
    "v_add_f32 v240, v240, v208\n"
    "v_cvt_pk_bf16_f32 v200, v200, v201\n"
    "v_exp_f32 v209, v209\n"
    "v_cvt_pk_bf16_f32 v201, v202, v203\n"
    "v_add_f32 v240, v240, v209\n"
    "v_exp_f32 v210, v210\n"
    "v_add_f32 v204, v60, v220\n"
    "v_add_f32 v205, v61, v221\n"
    "v_add_f32 v206, v62, v222\n"
    "v_add_f32 v207, v63, v223\n"
    "v_mfma_f32_16x16x32_bf16 v[212:215], a[156:159], a[28:31], v[220:223]\n"
    "ds_read_b64_tr_b16 a[184:185], v254 offset:0\n"
    "v_add_f32 v240, v240, v210\n"
    "v_cvt_pk_bf16_f32 v202, v208, v209\n"
    "v_mfma_f32_16x16x32_bf16 v[212:215], a[152:155], a[24:27], v[212:215]\n"
    "buffer_load_dwordx4 v253, s[36:39], s60 offen offset:0 lds\n"
    "v_exp_f32 v211, v211\n"
    "v_mfma_f32_16x16x32_bf16 v[212:215], a[148:151], a[20:23], v[212:215]\n"
    "buffer_load_dwordx4 v253, s[36:39], s60 offen offset:1024 lds\n"
    "v_add_f32 v240, v240, v211\n"
    "v_cvt_pk_bf16_f32 v203, v210, v211\n"
    "v_mfma_f32_16x16x32_bf16 v[212:215], a[144:147], a[16:19], v[212:215]\n"
    "buffer_load_dwordx4 v253, s[40:43], s60 offen offset:2048 lds\n"
    "v_exp_f32 v204, v204\n"
    "v_mfma_f32_16x16x32_bf16 v[0:3], v[196:199], a[192:195], 0\n"
    "s_waitcnt lgkmcnt(1)\n"
    "ds_read_b64_tr_b16 a[172:173], v251 offset:8\n"
    "v_mov_b32 v241, v204\n"
    "v_mfma_f32_16x16x32_bf16 v[0:3], v[200:203], a[160:163], v[0:3]\n"
    "buffer_load_dwordx4 v253, s[44:47], s60 offen offset:3080 lds\n"
    "v_exp_f32 v205, v205\n"
    "v_mfma_f32_16x16x32_bf16 v[4:7], v[200:203], a[164:167], 0\n"
    "s_add_u32 s60, s60, 2048\n"
    "s_mov_b32 m0, 61920\n"
    "ds_read_b64_tr_b16 a[176:177], v252 offset:0\n"
    "v_add_f32 v241, v241, v205\n"
    "v_mfma_f32_16x16x32_bf16 v[4:7], v[196:199], a[196:199], v[4:7]\n"
    "buffer_load_dwordx4 v253, s[36:39], s60 offen offset:0 lds\n"
    "v_exp_f32 v206, v206\n"
    "v_mfma_f32_16x16x32_bf16 v[8:11], v[196:199], a[200:203], 0\n"
    "ds_read_b64_tr_b16 a[178:179], v252 offset:8256\n"
    "v_add_f32 v241, v241, v206\n"
    "v_mfma_f32_16x16x32_bf16 v[8:11], v[200:203], a[168:171], v[8:11]\n"
    "s_waitcnt lgkmcnt(2)\n"
    "buffer_load_dwordx4 v253, s[36:39], s60 offen offset:1024 lds\n"
    "v_exp_f32 v207, v207\n"
    "v_mfma_f32_16x16x32_bf16 v[12:15], v[200:203], a[172:175], 0\n"
    "ds_read_b64_tr_b16 a[180:181], v252 offset:8\n"
    "v_add_f32 v241, v241, v207\n"
    "v_mfma_f32_16x16x32_bf16 v[12:15], v[196:199], a[244:247], v[12:15]\n"
    "buffer_load_dwordx4 v253, s[40:43], s60 offen offset:2048 lds\n"
    "v_exp_f32 v212, v212\n"
    "v_mfma_f32_16x16x32_bf16 v[192:195], a[96:99], a[16:19], v[220:223]\n"
    "v_add_f32 v241, v241, v212\n"
    "v_cvt_pk_bf16_f32 v204, v204, v205\n"
    "v_mfma_f32_16x16x32_bf16 v[192:195], a[100:103], a[20:23], v[192:195]\n"
    "v_exp_f32 v213, v213\n"
    "v_mfma_f32_16x16x32_bf16 v[192:195], a[104:107], a[24:27], v[192:195]\n"
    "buffer_load_dwordx4 v253, s[44:47], s60 offen offset:3080 lds\n"
    "s_add_u32 s60, s60, 2048\n"
    "v_add_f32 v241, v241, v213\n"
    "v_cvt_pk_bf16_f32 v205, v206, v207\n"
    "v_mfma_f32_16x16x32_bf16 v[192:195], a[108:111], a[28:31], v[192:195]\n"
    "global_load_dword v161, v160, s[82:83]\n"
    "v_exp_f32 v214, v214\n"
    "v_mfma_f32_16x16x32_bf16 v[208:211], a[124:127], a[28:31], v[220:223]\n"
    "v_add_f32 v241, v241, v214\n"
    "v_cvt_pk_bf16_f32 v206, v212, v213\n"
    "v_mfma_f32_16x16x32_bf16 v[208:211], a[120:123], a[24:27], v[208:211]\n"
    "v_exp_f32 v215, v215\n"
    "v_mfma_f32_16x16x32_bf16 v[208:211], a[116:119], a[20:23], v[208:211]\n"
    "s_waitcnt vmcnt(63) lgkmcnt(0)\n"
    "v_add_f32 v241, v241, v215\n"
    "v_cvt_pk_bf16_f32 v207, v214, v215\n"
    "v_mfma_f32_16x16x32_bf16 v[208:211], a[112:115], a[16:19], v[208:211]\n"
    "s_barrier\n"
    "v_exp_f32 v192, v192\n"
    "v_mfma_f32_16x16x32_bf16 v[16:19], v[196:199], a[80:83], 0\n"
    "s_cmp_eq_u32 s62, 23\n"
    "ds_read_b64_tr_b16 a[204:205], v251 offset:16520\n"
    "v_add_f32 v241, v241, v192\n"
    "v_mfma_f32_16x16x32_bf16 v[16:19], v[200:203], a[176:179], v[16:19]\n"
    "s_cselect_b32 s60, s61, s60\n"
    "v_exp_f32 v193, v193\n"
    "v_mfma_f32_16x16x32_bf16 v[20:23], v[200:203], a[180:183], 0\n"
    "s_cselect_b32 s61, s79, s61\n"
    "ds_read_b64_tr_b16 a[206:207], v251 offset:24776\n"
    "v_add_f32 v241, v241, v193\n"
    "v_mfma_f32_16x16x32_bf16 v[20:23], v[196:199], a[84:87], v[20:23]\n"
    "s_cselect_b32 s62, 0, s62\n"
    "v_exp_f32 v194, v194\n"
    "v_mfma_f32_16x16x32_bf16 v[24:27], v[196:199], a[88:91], 0\n"
    "s_mov_b32 m0, 0\n"
    "ds_read_b64_tr_b16 a[208:209], v252 offset:16512\n"
    "v_add_f32 v241, v241, v194\n"
    "v_mfma_f32_16x16x32_bf16 v[24:27], v[200:203], a[184:187], v[24:27]\n"
    "buffer_load_dwordx4 v253, s[36:39], s60 offen offset:0 lds\n"
    "v_exp_f32 v195, v195\n"
    "v_mfma_f32_16x16x32_bf16 v[28:31], v[200:203], a[188:191], 0\n"
    "s_add_u32 s62, s62, 1\n"
    "ds_read_b64_tr_b16 a[210:211], v252 offset:24768\n"
    "v_add_f32 v241, v241, v195\n"
    "v_mfma_f32_16x16x32_bf16 v[28:31], v[196:199], a[92:95], v[28:31]\n"
    "buffer_load_dwordx4 v253, s[36:39], s60 offen offset:1024 lds\n"
    "v_exp_f32 v208, v208\n"
    "v_cvt_pk_bf16_f32 v192, v192, v193\n"
    "v_add_f32 v241, v241, v208\n"
    "v_exp_f32 v209, v209\n"
    "v_cvt_pk_bf16_f32 v193, v194, v195\n"
    "v_add_f32 v241, v241, v209\n"
    "v_exp_f32 v210, v210\n"
    "v_add_f32 v196, v92, v224\n"
    "v_add_f32 v197, v93, v225\n"
    "v_add_f32 v198, v94, v226\n"
    "v_add_f32 v199, v95, v227\n"
    "v_mfma_f32_16x16x32_bf16 v[212:215], a[156:159], a[44:47], v[224:227]\n"
    "buffer_load_dwordx4 v253, s[40:43], s60 offen offset:2048 lds\n"
    "v_add_f32 v241, v241, v210\n"
    "v_cvt_pk_bf16_f32 v194, v208, v209\n"
    "v_mfma_f32_16x16x32_bf16 v[212:215], a[152:155], a[40:43], v[212:215]\n"
    "buffer_load_dwordx4 v253, s[44:47], s60 offen offset:3080 lds\n"
    "v_exp_f32 v211, v211\n"
    "v_mfma_f32_16x16x32_bf16 v[212:215], a[148:151], a[36:39], v[212:215]\n"
    "s_add_u32 s60, s60, 2048\n"
    "s_mov_b32 m0, 4128\n"
    "v_add_f32 v241, v241, v211\n"
    "v_cvt_pk_bf16_f32 v195, v210, v211\n"
    "v_mfma_f32_16x16x32_bf16 v[212:215], a[144:147], a[32:35], v[212:215]\n"
    "buffer_load_dwordx4 v253, s[36:39], s60 offen offset:0 lds\n"
    "v_exp_f32 v196, v196\n"
    "v_mfma_f32_16x16x32_bf16 v[32:35], v[204:207], a[192:195], 0\n"
    "ds_read_b128 a[128:131], v246 offset:16512\n"
    "v_mov_b32 v242, v196\n"
    "v_mfma_f32_16x16x32_bf16 v[32:35], v[192:195], a[160:163], v[32:35]\n"
    "buffer_load_dwordx4 v253, s[36:39], s60 offen offset:1024 lds\n"
    "v_exp_f32 v197, v197\n"
    "v_mfma_f32_16x16x32_bf16 v[36:39], v[192:195], a[164:167], 0\n"
    "ds_read_b128 a[132:135], v246 offset:16768\n"
    "v_add_f32 v242, v242, v197\n"
    "v_mfma_f32_16x16x32_bf16 v[36:39], v[204:207], a[196:199], v[36:39]\n"
    "buffer_load_dwordx4 v253, s[40:43], s60 offen offset:2048 lds\n"
    "v_exp_f32 v198, v198\n"
    "v_mfma_f32_16x16x32_bf16 v[40:43], v[204:207], a[200:203], 0\n"
    "ds_read_b128 a[136:139], v246 offset:17024\n"
    "v_add_f32 v242, v242, v198\n"
    "v_mfma_f32_16x16x32_bf16 v[40:43], v[192:195], a[168:171], v[40:43]\n"
    "v_exp_f32 v199, v199\n"
    "v_mfma_f32_16x16x32_bf16 v[44:47], v[192:195], a[172:175], 0\n"
    "ds_read_b128 a[140:143], v246 offset:17280\n"
    "v_add_f32 v242, v242, v199\n"
    "v_mfma_f32_16x16x32_bf16 v[44:47], v[204:207], a[244:247], v[44:47]\n"
    "buffer_load_dwordx4 v253, s[44:47], s60 offen offset:3080 lds\n"
    "s_add_u32 s60, s60, 2048\n"
    "s_mov_b32 m0, 8256\n"
    "v_exp_f32 v212, v212\n"
    "v_mfma_f32_16x16x32_bf16 v[200:203], a[96:99], a[32:35], v[224:227]\n"
    "v_add_f32 v242, v242, v212\n"
    "v_cvt_pk_bf16_f32 v196, v196, v197\n"
    "v_mfma_f32_16x16x32_bf16 v[200:203], a[100:103], a[36:39], v[200:203]\n"
    "v_exp_f32 v213, v213\n"
    "v_mfma_f32_16x16x32_bf16 v[200:203], a[104:107], a[40:43], v[200:203]\n"
    "buffer_load_dwordx4 v253, s[36:39], s60 offen offset:0 lds\n"
    "v_add_f32 v242, v242, v213\n"
    "v_cvt_pk_bf16_f32 v197, v198, v199\n"
    "v_mfma_f32_16x16x32_bf16 v[200:203], a[108:111], a[44:47], v[200:203]\n"
    "buffer_load_dwordx4 v253, s[36:39], s60 offen offset:1024 lds\n"
    "v_exp_f32 v214, v214\n"
    "v_mfma_f32_16x16x32_bf16 v[208:211], a[124:127], a[44:47], v[224:227]\n"
    "v_add_f32 v242, v242, v214\n"
    "v_cvt_pk_bf16_f32 v198, v212, v213\n"
    "v_mfma_f32_16x16x32_bf16 v[208:211], a[120:123], a[40:43], v[208:211]\n"
    "buffer_load_dwordx4 v253, s[40:43], s60 offen offset:2048 lds\n"
    "v_exp_f32_e64 v215, v215\n"
    "v_mfma_f32_16x16x32_bf16 v[208:211], a[116:119], a[36:39], v[208:211]\n"
    "buffer_load_dwordx4 v253, s[44:47], s60 offen offset:3080 lds\n"
    "s_add_u32 s60, s60, 2048\n"
    "s_mov_b32 m0, 12384\n"
    "v_add_f32 v242, v242, v215\n"
    "v_cvt_pk_bf16_f32 v199, v214, v215\n"
    "v_mfma_f32_16x16x32_bf16 v[208:211], a[112:115], a[32:35], v[208:211]\n"
    "buffer_load_dwordx4 v253, s[36:39], s60 offen offset:0 lds\n"
    "v_exp_f32 v200, v200\n"
    "v_mfma_f32_16x16x32_bf16 v[48:51], v[204:207], a[80:83], 0\n"
    "ds_read_b128 a[156:159], v246 offset:25536\n"
    "v_add_f32 v242, v242, v200\n"
    "v_mfma_f32_16x16x32_bf16 v[48:51], v[192:195], a[176:179], v[48:51]\n"
    "ds_read_b64_tr_b16 v[168:169], v164 offset:0\n"
    "ds_read_b64_tr_b16 v[170:171], v164 offset:1024\n"
    "v_exp_f32 v201, v201\n"
    "v_mfma_f32_16x16x32_bf16 v[52:55], v[192:195], a[180:183], 0\n"
    "ds_read_b128 a[152:155], v246 offset:25280\n"
    "v_add_f32 v242, v242, v201\n"
    "v_mfma_f32_16x16x32_bf16 v[52:55], v[204:207], a[84:87], v[52:55]\n"
    "ds_read_b64_tr_b16 v[172:173], v164 offset:2048\n"
    "ds_read_b64_tr_b16 v[174:175], v164 offset:3072\n"
    "v_exp_f32 v202, v202\n"
    "v_mfma_f32_16x16x32_bf16 v[56:59], v[204:207], a[88:91], 0\n"
    "ds_read_b128 a[148:151], v246 offset:25024\n"
    "v_add_f32 v242, v242, v202\n"
    "v_mfma_f32_16x16x32_bf16 v[56:59], v[192:195], a[184:187], v[56:59]\n"
    "ds_read_b64_tr_b16 v[176:177], v164 offset:8\n"
    "ds_read_b64_tr_b16 v[178:179], v164 offset:1032\n"
    "v_exp_f32 v203, v203\n"
    "v_mfma_f32_16x16x32_bf16 v[60:63], v[192:195], a[188:191], 0\n"
    "ds_read_b128 a[144:147], v246 offset:24768\n"
    "v_add_f32 v242, v242, v203\n"
    "v_mfma_f32_16x16x32_bf16 v[60:63], v[204:207], a[92:95], v[60:63]\n"
    "ds_read_b64_tr_b16 v[180:181], v164 offset:2056\n"
    "ds_read_b64_tr_b16 v[182:183], v164 offset:3080\n"
    "v_exp_f32 v208, v208\n"
    "v_mfma_f32_16x16x32_bf16 v[204:207], a[96:99], a[48:51], v[228:231]\n"
    "v_add_f32 v242, v242, v208\n"
    "v_cvt_pk_bf16_f32 v200, v200, v201\n"
    "v_add_u32 v164, 4128, v164\n"
    "v_mfma_f32_16x16x32_bf16 v[204:207], a[100:103], a[52:55], v[204:207]\n"
    "v_exp_f32 v209, v209\n"
    "v_mfma_f32_16x16x32_bf16 v[204:207], a[104:107], a[56:59], v[204:207]\n"
    "v_add_f32 v242, v242, v209\n"
    "v_cvt_pk_bf16_f32 v201, v202, v203\n"
    "v_mfma_f32_16x16x32_bf16 v[204:207], a[108:111], a[60:63], v[204:207]\n"
    "buffer_load_dwordx4 v253, s[36:39], s60 offen offset:1024 lds\n"
    "v_exp_f32 v210, v210\n"
    "v_mfma_f32_16x16x32_bf16 v[212:215], a[124:127], a[60:63], v[228:231]\n"
    "buffer_load_dwordx4 v253, s[40:43], s60 offen offset:2048 lds\n"
    "v_add_f32 v242, v242, v210\n"
    "v_cvt_pk_bf16_f32 v202, v208, v209\n"
    "v_mfma_f32_16x16x32_bf16 v[212:215], a[120:123], a[56:59], v[212:215]\n"
    "buffer_load_dwordx4 v253, s[44:47], s60 offen offset:3080 lds\n"
    "v_exp_f32 v211, v211\n"
    "v_mfma_f32_16x16x32_bf16 v[212:215], a[116:119], a[52:55], v[212:215]\n"
    "s_add_u32 s60, s60, 2048\n"
    "v_add_f32 v242, v242, v211\n"
    "v_cvt_pk_bf16_f32 v203, v210, v211\n"
    "v_mfma_f32_16x16x32_bf16 v[212:215], a[112:115], a[48:51], v[212:215]\n"
    "v_exp_f32 v204, v204\n"
    "v_mfma_f32_16x16x32_bf16 v[64:67], v[196:199], a[192:195], 0\n"
    "ds_read_b64_tr_b16 a[212:213], v252 offset:16520\n"
    "v_add_f32 v243, v243, v204\n"
    "v_mfma_f32_16x16x32_bf16 v[64:67], v[200:203], a[160:163], v[64:67]\n"
    "s_waitcnt lgkmcnt(1)\n"
    "buffer_store_dwordx4 v[168:171], v249, s[56:59], s64 offen offset:0 sc1\n"
    "v_exp_f32 v205, v205\n"
    "v_mfma_f32_16x16x32_bf16 v[68:71], v[200:203], a[164:167], 0\n"
    "ds_read_b64_tr_b16 a[214:215], v252 offset:24776\n"
    "v_add_f32 v243, v243, v205\n"
    "v_mfma_f32_16x16x32_bf16 v[68:71], v[196:199], a[196:199], v[68:71]\n"
    "v_exp_f32 v206, v206\n"
    "v_mfma_f32_16x16x32_bf16 v[72:75], v[196:199], a[200:203], 0\n"
    "ds_read_b64_tr_b16 a[216:217], v254 offset:16512\n"
    "v_add_f32 v243, v243, v206\n"
    "v_mfma_f32_16x16x32_bf16 v[72:75], v[200:203], a[168:171], v[72:75]\n"
    "ds_read_b64_tr_b16 a[202:203], v251 offset:24768\n"
    "v_exp_f32 v207, v207\n"
    "v_mfma_f32_16x16x32_bf16 v[76:79], v[200:203], a[172:175], 0\n"
    "ds_read_b64_tr_b16 a[218:219], v254 offset:24768\n"
    "v_add_f32 v243, v243, v207\n"
    "v_mfma_f32_16x16x32_bf16 v[76:79], v[196:199], a[244:247], v[76:79]\n"
    "buffer_store_dwordx4 v[172:175], v249, s[56:59], s64 offen offset:256 sc1\n"
    "v_exp_f32 v212, v212\n"
    "v_mfma_f32_16x16x32_bf16 v[192:195], a[128:131], a[48:51], v[228:231]\n"
    "v_add_f32 v243, v243, v212\n"
    "v_cvt_pk_bf16_f32 v204, v204, v205\n"
    "v_mfma_f32_16x16x32_bf16 v[192:195], a[132:135], a[52:55], v[192:195]\n"
    "v_exp_f32 v213, v213\n"
    "v_mfma_f32_16x16x32_bf16 v[192:195], a[136:139], a[56:59], v[192:195]\n"
    "ds_read_b64_tr_b16 a[196:197], v247 offset:16520\n"
    "v_add_f32 v243, v243, v213\n"
    "v_cvt_pk_bf16_f32 v205, v206, v207\n"
    "v_mfma_f32_16x16x32_bf16 v[192:195], a[140:143], a[60:63], v[192:195]\n"
    "ds_read_b64_tr_b16 a[198:199], v247 offset:24776\n"
    "v_exp_f32 v214, v214\n"
    "v_mfma_f32_16x16x32_bf16 v[208:211], a[156:159], a[60:63], v[228:231]\n"
    "ds_read_b64_tr_b16 a[200:201], v251 offset:16512\n"
    "v_add_f32 v243, v243, v214\n"
    "v_cvt_pk_bf16_f32 v206, v212, v213\n"
    "v_mfma_f32_16x16x32_bf16 v[208:211], a[152:155], a[56:59], v[208:211]\n"
    "v_exp_f32 v215, v215\n"
    "v_mfma_f32_16x16x32_bf16 v[208:211], a[148:151], a[52:55], v[208:211]\n"
    "v_add_f32 v243, v243, v215\n"
    "v_cvt_pk_bf16_f32 v207, v214, v215\n"
    "v_mfma_f32_16x16x32_bf16 v[208:211], a[144:147], a[48:51], v[208:211]\n"
    "buffer_store_dwordx4 v[176:179], v249, s[56:59], s64 offen offset:512 sc1\n"
    "v_exp_f32 v192, v192\n"
    "v_mfma_f32_16x16x32_bf16 v[80:83], v[196:199], a[80:83], 0\n"
    "ds_read_b64_tr_b16 a[220:221], v254 offset:16520\n"
    "v_add_f32 v243, v243, v192\n"
    "v_mfma_f32_16x16x32_bf16 v[80:83], v[200:203], a[176:179], v[80:83]\n"
    "v_exp_f32 v193, v193\n"
    "v_mfma_f32_16x16x32_bf16 v[84:87], v[200:203], a[180:183], 0\n"
    "ds_read_b64_tr_b16 a[222:223], v254 offset:24776\n"
    "v_add_f32 v243, v243, v193\n"
    "v_mfma_f32_16x16x32_bf16 v[84:87], v[196:199], a[84:87], v[84:87]\n"
    "buffer_store_dwordx4 v[180:183], v249, s[56:59], s64 offen offset:768 sc1\n"
    "v_exp_f32_e64 v194, v194\n"
    "v_mfma_f32_16x16x32_bf16 v[88:91], v[196:199], a[88:91], 0\n"
    "s_add_u32 s64, s64, 4096\n"
    "s_add_u32 s87, s60, 24576\n"
    "ds_read_b64_tr_b16 a[192:193], v247 offset:16512\n"
    "v_add_f32 v243, v243, v194\n"
    "v_mfma_f32_16x16x32_bf16 v[88:91], v[200:203], a[184:187], v[88:91]\n"
    "s_sub_u32 s70, s62, 17\n"
    "v_exp_f32 v195, v195\n"
    "v_mfma_f32_16x16x32_bf16 v[92:95], v[200:203], a[188:191], 0\n"
    "s_cmp_lt_u32 s70, 3\n"
    "ds_read_b64_tr_b16 a[194:195], v247 offset:24768\n"
    "v_add_f32 v243, v243, v195\n"
    "v_mfma_f32_16x16x32_bf16 v[92:95], v[196:199], a[92:95], v[92:95]\n"
    "s_cselect_b32 s87, s87, s79\n"
    "v_exp_f32 v208, v208\n"
    "v_mfma_f32_16x16x32_bf16 v[196:199], a[96:99], a[64:67], v[232:235]\n"
    "v_add_f32 v243, v243, v208\n"
    "v_cvt_pk_bf16_f32 v192, v192, v193\n"
    "v_mfma_f32_16x16x32_bf16 v[196:199], a[100:103], a[68:71], v[196:199]\n"
    "v_exp_f32 v209, v209\n"
    "v_mfma_f32_16x16x32_bf16 v[196:199], a[104:107], a[72:75], v[196:199]\n"
    "buffer_load_dword v161, v165, s[88:91], s87 offen\n"
    "v_add_f32 v243, v243, v209\n"
    "v_cvt_pk_bf16_f32 v193, v194, v195\n"
    "v_mfma_f32_16x16x32_bf16 v[196:199], a[108:111], a[76:79], v[196:199]\n"
    "v_exp_f32 v210, v210\n"
    "v_mfma_f32_16x16x32_bf16 v[212:215], a[124:127], a[76:79], v[232:235]\n"
    "v_add_f32 v243, v243, v210\n"
    "v_cvt_pk_bf16_f32 v194, v208, v209\n"
    "v_mfma_f32_16x16x32_bf16 v[212:215], a[120:123], a[72:75], v[212:215]\n"
    "v_exp_f32 v211, v211\n"
    "v_mfma_f32_16x16x32_bf16 v[212:215], a[116:119], a[68:71], v[212:215]\n"
    "s_waitcnt vmcnt(38) lgkmcnt(0)\n"
    "v_add_f32 v243, v243, v211\n"
    "v_cvt_pk_bf16_f32 v195, v210, v211\n"
    "v_mfma_f32_16x16x32_bf16 v[212:215], a[112:115], a[64:67], v[212:215]\n"
    "s_barrier\n"
    "v_exp_f32 v196, v196\n"
    "v_mfma_f32_16x16x32_bf16 v[96:99], v[204:207], a[160:163], v[96:99]\n"
    "s_cmp_eq_u32 s62, 23\n"
    "ds_read_b128 a[96:99], v246 offset:33024\n"
    "v_add_f32 v244, v244, v196\n"
    "v_mfma_f32_16x16x32_bf16 v[96:99], v[192:195], a[192:195], v[96:99]\n"
    "s_cselect_b32 s60, s61, s60\n"
    "v_exp_f32 v197, v197\n"
    "v_mfma_f32_16x16x32_bf16 v[100:103], v[192:195], a[196:199], v[100:103]\n"
    "s_cselect_b32 s61, s79, s61\n"
    "ds_read_b128 a[100:103], v246 offset:33280\n"
    "v_add_f32 v244, v244, v197\n"
    "v_mfma_f32_16x16x32_bf16 v[100:103], v[204:207], a[164:167], v[100:103]\n"
    "s_cselect_b32 s62, 0, s62\n"
    "v_exp_f32 v198, v198\n"
    "v_mfma_f32_16x16x32_bf16 v[104:107], v[204:207], a[168:171], v[104:107]\n"
    "s_mov_b32 m0, 16512\n"
    "ds_read_b128 a[104:107], v246 offset:33536\n"
    "v_add_f32 v244, v244, v198\n"
    "v_mfma_f32_16x16x32_bf16 v[104:107], v[192:195], a[200:203], v[104:107]\n"
    "buffer_load_dwordx4 v253, s[36:39], s60 offen offset:0 lds\n"
    "v_exp_f32 v199, v199\n"
    "v_mfma_f32_16x16x32_bf16 v[108:111], v[192:195], a[204:207], v[108:111]\n"
    "s_add_u32 s62, s62, 1\n"
    "ds_read_b128 a[108:111], v246 offset:33792\n"
    "v_add_f32 v244, v244, v199\n"
    "v_mfma_f32_16x16x32_bf16 v[108:111], v[204:207], a[172:175], v[108:111]\n"
    "buffer_load_dwordx4 v253, s[36:39], s60 offen offset:1024 lds\n"
    "v_exp_f32 v212, v212\n"
    "v_mfma_f32_16x16x32_bf16 v[200:203], a[128:131], a[64:67], v[232:235]\n"
    "v_add_f32_e64 v244, v244, v212\n"
    "v_cvt_pk_bf16_f32 v196, v196, v197\n"
    "v_mfma_f32_16x16x32_bf16 v[200:203], a[132:135], a[68:71], v[200:203]\n"
    "LOOP2_B:\n"
    "v_exp_f32 v213, v213\n"
    "v_mfma_f32_16x16x32_bf16 v[200:203], a[136:139], a[72:75], v[200:203]\n"
    "ds_read_b64_tr_b16 a[236:237], v254 offset:33024\n"
    "v_add_f32 v244, v244, v213\n"
    "v_cvt_pk_bf16_f32 v197, v198, v199\n"
    "v_mfma_f32_16x16x32_bf16 v[200:203], a[140:143], a[76:79], v[200:203]\n"
    "buffer_load_dwordx4 v253, s[40:43], s60 offen offset:2048 lds\n"
    "v_exp_f32 v214, v214\n"
    "v_mfma_f32_16x16x32_bf16 v[208:211], a[156:159], a[76:79], v[232:235]\n"
    "buffer_load_dwordx4 v253, s[44:47], s60 offen offset:3080 lds\n"
    "v_add_f32 v244, v244, v214\n"
    "v_cvt_pk_bf16_f32 v198, v212, v213\n"
    "v_mfma_f32_16x16x32_bf16 v[208:211], a[152:155], a[72:75], v[208:211]\n"
    "s_add_u32 s60, s60, 2048\n"
    "s_mov_b32 m0, 20640\n"
    "v_exp_f32 v215, v215\n"
    "v_mfma_f32_16x16x32_bf16 v[208:211], a[148:151], a[68:71], v[208:211]\n"
    "buffer_load_dwordx4 v253, s[36:39], s60 offen offset:0 lds\n"
    "v_add_f32 v244, v244, v215\n"
    "v_cvt_pk_bf16_f32 v199, v214, v215\n"
    "v_mfma_f32_16x16x32_bf16 v[208:211], a[144:147], a[64:67], v[208:211]\n"
    "buffer_load_dwordx4 v253, s[36:39], s60 offen offset:1024 lds\n"
    "v_exp_f32 v200, v200\n"
    "v_mfma_f32_16x16x32_bf16 v[112:115], v[204:207], a[176:179], v[112:115]\n"
    "ds_read_b128 a[124:127], v246 offset:42048\n"
    "v_add_f32 v244, v244, v200\n"
    "v_mfma_f32_16x16x32_bf16 v[112:115], v[192:195], a[208:211], v[112:115]\n"
    "buffer_load_dwordx4 v253, s[40:43], s60 offen offset:2048 lds\n"
    "v_exp_f32 v201, v201\n"
    "v_mfma_f32_16x16x32_bf16 v[116:119], v[192:195], a[212:215], v[116:119]\n"
    "ds_read_b128 a[120:123], v246 offset:41792\n"
    "v_add_f32 v244, v244, v201\n"
    "v_mfma_f32_16x16x32_bf16 v[116:119], v[204:207], a[180:183], v[116:119]\n"
    "buffer_load_dwordx4 v253, s[44:47], s60 offen offset:3080 lds\n"
    "v_exp_f32 v202, v202\n"
    "v_mfma_f32_16x16x32_bf16 v[120:123], v[204:207], a[184:187], v[120:123]\n"
    "s_add_u32 s60, s60, 2048\n"
    "ds_read_b128 a[116:119], v246 offset:41536\n"
    "v_add_f32 v244, v244, v202\n"
    "v_mfma_f32_16x16x32_bf16 v[120:123], v[192:195], a[216:219], v[120:123]\n"
    "s_mov_b32 m0, 24768\n"
    "v_exp_f32 v203, v203\n"
    "v_mfma_f32_16x16x32_bf16 v[124:127], v[192:195], a[220:223], v[124:127]\n"
    "ds_read_b128 a[112:115], v246 offset:41280\n"
    "v_add_f32 v244, v244, v203\n"
    "v_mfma_f32_16x16x32_bf16 v[124:127], v[204:207], a[188:191], v[124:127]\n"
    "buffer_load_dwordx4 v253, s[36:39], s60 offen offset:0 lds\n"
    "v_exp_f32 v208, v208\n"
    "v_mfma_f32_16x16x32_bf16 v[204:207], a[128:131], a[0:3], v[216:219]\n"
    "v_add_f32 v244, v244, v208\n"
    "v_cvt_pk_bf16_f32 v200, v200, v201\n"
    "v_mfma_f32_16x16x32_bf16 v[204:207], a[132:135], a[4:7], v[204:207]\n"
    "v_exp_f32 v209, v209\n"
    "v_mfma_f32_16x16x32_bf16 v[204:207], a[136:139], a[8:11], v[204:207]\n"
    "ds_read_b64_tr_b16 a[238:239], v254 offset:41280\n"
    "v_add_f32 v244, v244, v209\n"
    "v_cvt_pk_bf16_f32 v201, v202, v203\n"
    "v_mfma_f32_16x16x32_bf16 v[204:207], a[140:143], a[12:15], v[204:207]\n"
    "buffer_load_dwordx4 v253, s[36:39], s60 offen offset:1024 lds\n"
    "v_exp_f32 v210, v210\n"
    "v_mfma_f32_16x16x32_bf16 v[212:215], a[156:159], a[12:15], v[216:219]\n"
    "buffer_load_dwordx4 v253, s[40:43], s60 offen offset:2048 lds\n"
    "v_add_f32 v244, v244, v210\n"
    "v_cvt_pk_bf16_f32 v202, v208, v209\n"
    "v_mfma_f32_16x16x32_bf16 v[212:215], a[152:155], a[8:11], v[212:215]\n"
    "buffer_load_dwordx4 v253, s[44:47], s60 offen offset:3080 lds\n"
    "v_exp_f32 v211, v211\n"
    "v_mfma_f32_16x16x32_bf16 v[212:215], a[148:151], a[4:7], v[212:215]\n"
    "s_add_u32 s60, s60, 2048\n"
    "s_mov_b32 m0, 28896\n"
    "v_add_f32 v244, v244, v211\n"
    "v_cvt_pk_bf16_f32 v203, v210, v211\n"
    "v_mfma_f32_16x16x32_bf16 v[212:215], a[144:147], a[0:3], v[212:215]\n"
    "buffer_load_dwordx4 v253, s[36:39], s60 offen offset:0 lds\n"
    "v_exp_f32 v204, v204\n"
    "v_mfma_f32_16x16x32_bf16 v[128:131], v[196:199], a[160:163], v[128:131]\n"
    "ds_read_b64_tr_b16 a[160:161], v247 offset:33024\n"
    "v_add_f32 v240, v240, v204\n"
    "v_mfma_f32_16x16x32_bf16 v[128:131], v[200:203], a[192:195], v[128:131]\n"
    "ds_read_b64_tr_b16 v[168:169], v164 offset:0\n"
    "ds_read_b64_tr_b16 v[170:171], v164 offset:1024\n"
    "v_exp_f32 v205, v205\n"
    "v_mfma_f32_16x16x32_bf16 v[132:135], v[200:203], a[196:199], v[132:135]\n"
    "ds_read_b64_tr_b16 a[162:163], v247 offset:41280\n"
    "v_add_f32 v240, v240, v205\n"
    "v_mfma_f32_16x16x32_bf16 v[132:135], v[196:199], a[164:167], v[132:135]\n"
    "ds_read_b64_tr_b16 v[172:173], v164 offset:2048\n"
    "ds_read_b64_tr_b16 v[174:175], v164 offset:3072\n"
    "v_exp_f32 v206, v206\n"
    "v_mfma_f32_16x16x32_bf16 v[136:139], v[196:199], a[168:171], v[136:139]\n"
    "ds_read_b64_tr_b16 a[164:165], v247 offset:33032\n"
    "v_add_f32 v240, v240, v206\n"
    "v_mfma_f32_16x16x32_bf16 v[136:139], v[200:203], a[200:203], v[136:139]\n"
    "ds_read_b64_tr_b16 v[176:177], v164 offset:8\n"
    "ds_read_b64_tr_b16 v[178:179], v164 offset:1032\n"
    "v_exp_f32 v207, v207\n"
    "v_mfma_f32_16x16x32_bf16 v[140:143], v[200:203], a[204:207], v[140:143]\n"
    "ds_read_b64_tr_b16 a[166:167], v247 offset:41288\n"
    "v_add_f32 v240, v240, v207\n"
    "v_mfma_f32_16x16x32_bf16 v[140:143], v[196:199], a[172:175], v[140:143]\n"
    "s_waitcnt lgkmcnt(7)\n"
    "ds_read_b64_tr_b16 v[180:181], v164 offset:2056\n"
    "ds_read_b64_tr_b16 v[182:183], v164 offset:3080\n"
    "v_exp_f32 v212, v212\n"
    "v_mfma_f32_16x16x32_bf16 v[192:195], a[96:99], a[0:3], v[216:219]\n"
    "v_add_f32 v240, v240, v212\n"
    "v_cvt_pk_bf16_f32 v204, v204, v205\n"
    "v_add_u32 v164, 4128, v164\n"
    "v_mfma_f32_16x16x32_bf16 v[192:195], a[100:103], a[4:7], v[192:195]\n"
    "v_exp_f32 v213, v213\n"
    "v_mfma_f32_16x16x32_bf16 v[192:195], a[104:107], a[8:11], v[192:195]\n"
    "ds_read_b64_tr_b16 a[240:241], v254 offset:33032\n"
    "v_add_f32 v240, v240, v213\n"
    "v_cvt_pk_bf16_f32 v205, v206, v207\n"
    "v_mfma_f32_16x16x32_bf16 v[192:195], a[108:111], a[12:15], v[192:195]\n"
    "buffer_load_dwordx4 v253, s[36:39], s60 offen offset:1024 lds\n"
    "v_exp_f32 v214, v214\n"
    "v_mfma_f32_16x16x32_bf16 v[208:211], a[124:127], a[12:15], v[216:219]\n"
    "buffer_load_dwordx4 v253, s[40:43], s60 offen offset:2048 lds\n"
    "v_add_f32 v240, v240, v214\n"
    "v_cvt_pk_bf16_f32 v206, v212, v213\n"
    "v_mfma_f32_16x16x32_bf16 v[208:211], a[120:123], a[8:11], v[208:211]\n"
    "buffer_load_dwordx4 v253, s[44:47], s60 offen offset:3080 lds\n"
    "v_exp_f32 v215, v215\n"
    "v_mfma_f32_16x16x32_bf16 v[208:211], a[116:119], a[4:7], v[208:211]\n"
    "s_add_u32 s60, s60, 2048\n"
    "v_add_f32 v240, v240, v215\n"
    "v_cvt_pk_bf16_f32 v207, v214, v215\n"
    "v_mfma_f32_16x16x32_bf16 v[208:211], a[112:115], a[0:3], v[208:211]\n"
    "v_exp_f32 v192, v192\n"
    "v_mfma_f32_16x16x32_bf16 v[144:147], v[196:199], a[176:179], v[144:147]\n"
    "ds_read_b64_tr_b16 a[168:169], v251 offset:33024\n"
    "v_add_f32 v240, v240, v192\n"
    "v_mfma_f32_16x16x32_bf16 v[144:147], v[200:203], a[208:211], v[144:147]\n"
    "buffer_store_dwordx4 v[168:171], v249, s[56:59], s64 offen offset:0 sc1\n"
    "v_exp_f32 v193, v193\n"
    "v_mfma_f32_16x16x32_bf16 v[148:151], v[200:203], a[212:215], v[148:151]\n"
    "ds_read_b64_tr_b16 a[170:171], v251 offset:41280\n"
    "v_add_f32 v240, v240, v193\n"
    "v_mfma_f32_16x16x32_bf16 v[148:151], v[196:199], a[180:183], v[148:151]\n"
    "v_exp_f32 v194, v194\n"
    "v_mfma_f32_16x16x32_bf16 v[152:155], v[196:199], a[184:187], v[152:155]\n"
    "ds_read_b64_tr_b16 a[224:225], v251 offset:33032\n"
    "v_add_f32 v240, v240, v194\n"
    "v_mfma_f32_16x16x32_bf16 v[152:155], v[200:203], a[216:219], v[152:155]\n"
    "v_exp_f32 v195, v195\n"
    "v_mfma_f32_16x16x32_bf16 v[156:159], v[200:203], a[220:223], v[156:159]\n"
    "ds_read_b64_tr_b16 a[226:227], v251 offset:41288\n"
    "v_add_f32 v240, v240, v195\n"
    "v_mfma_f32_16x16x32_bf16 v[156:159], v[196:199], a[188:191], v[156:159]\n"
    "s_waitcnt lgkmcnt(3)\n"
    "buffer_store_dwordx4 v[172:175], v249, s[56:59], s64 offen offset:256 sc1\n"
    "v_exp_f32 v208, v208\n"
    "v_mfma_f32_16x16x32_bf16 v[196:199], a[128:131], a[16:19], v[220:223]\n"
    "v_add_f32 v240, v240, v208\n"
    "v_cvt_pk_bf16_f32 v192, v192, v193\n"
    "v_mfma_f32_16x16x32_bf16 v[196:199], a[132:135], a[20:23], v[196:199]\n"
    "v_exp_f32 v209, v209\n"
    "v_mfma_f32_16x16x32_bf16 v[196:199], a[136:139], a[24:27], v[196:199]\n"
    "ds_read_b64_tr_b16 a[242:243], v254 offset:41288\n"
    "v_add_f32 v240, v240, v209\n"
    "v_cvt_pk_bf16_f32 v193, v194, v195\n"
    "v_mfma_f32_16x16x32_bf16 v[196:199], a[140:143], a[28:31], v[196:199]\n"
    "v_exp_f32 v210, v210\n"
    "v_mfma_f32_16x16x32_bf16 v[212:215], a[156:159], a[28:31], v[220:223]\n"
    "v_add_f32 v240, v240, v210\n"
    "v_cvt_pk_bf16_f32 v194, v208, v209\n"
    "v_mfma_f32_16x16x32_bf16 v[212:215], a[152:155], a[24:27], v[212:215]\n"
    "buffer_store_dwordx4 v[176:179], v249, s[56:59], s64 offen offset:512 sc1\n"
    "v_exp_f32 v211, v211\n"
    "v_mfma_f32_16x16x32_bf16 v[212:215], a[148:151], a[20:23], v[212:215]\n"
    "v_add_f32 v240, v240, v211\n"
    "v_cvt_pk_bf16_f32 v195, v210, v211\n"
    "v_mfma_f32_16x16x32_bf16 v[212:215], a[144:147], a[16:19], v[212:215]\n"
    "v_exp_f32 v196, v196\n"
    "v_mfma_f32_16x16x32_bf16 v[0:3], v[204:207], a[192:195], v[0:3]\n"
    "ds_read_b64_tr_b16 a[228:229], v252 offset:33024\n"
    "v_add_f32 v241, v241, v196\n"
    "v_mfma_f32_16x16x32_bf16 v[0:3], v[192:195], a[160:163], v[0:3]\n"
    "buffer_store_dwordx4 v[180:183], v249, s[56:59], s64 offen offset:768 sc1\n"
    "v_exp_f32 v197, v197\n"
    "v_mfma_f32_16x16x32_bf16 v[4:7], v[192:195], a[164:167], v[4:7]\n"
    "s_add_u32 s64, s64, 4096\n"
    "ds_read_b64_tr_b16 a[230:231], v252 offset:41280\n"
    "v_add_f32 v241, v241, v197\n"
    "v_mfma_f32_16x16x32_bf16 v[4:7], v[204:207], a[196:199], v[4:7]\n"
    "s_add_u32 s87, s60, 24576\n"
    "v_exp_f32 v198, v198\n"
    "v_mfma_f32_16x16x32_bf16 v[8:11], v[204:207], a[200:203], v[8:11]\n"
    "s_sub_u32 s70, s62, 17\n"
    "ds_read_b64_tr_b16 a[232:233], v252 offset:33032\n"
    "v_add_f32 v241, v241, v198\n"
    "s_waitcnt lgkmcnt(3)\n"
    "v_mfma_f32_16x16x32_bf16 v[8:11], v[192:195], a[168:171], v[8:11]\n"
    "s_cmp_lt_u32 s70, 3\n"
    "v_exp_f32 v199, v199\n"
    "v_mfma_f32_16x16x32_bf16 v[12:15], v[192:195], a[224:227], v[12:15]\n"
    "s_cselect_b32 s87, s87, s79\n"
    "ds_read_b64_tr_b16 a[234:235], v252 offset:41288\n"
    "v_add_f32 v241, v241, v199\n"
    "v_mfma_f32_16x16x32_bf16 v[12:15], v[204:207], a[204:207], v[12:15]\n"
    "buffer_load_dword v161, v165, s[88:91], s87 offen\n"
    "v_exp_f32 v212, v212\n"
    "v_mfma_f32_16x16x32_bf16 v[200:203], a[96:99], a[16:19], v[220:223]\n"
    "s_cmp_eq_u32 s63, 0\n"
    "v_add_f32 v241, v241, v212\n"
    "v_cvt_pk_bf16_f32 v196, v196, v197\n"
    "v_mfma_f32_16x16x32_bf16 v[200:203], a[100:103], a[20:23], v[200:203]\n"
    "s_cbranch_scc1 LTAIL_B\n"
    "v_exp_f32 v213, v213\n"
    "v_mfma_f32_16x16x32_bf16 v[200:203], a[104:107], a[24:27], v[200:203]\n"
    "v_add_f32 v241, v241, v213\n"
    "v_cvt_pk_bf16_f32 v197, v198, v199\n"
    "v_mfma_f32_16x16x32_bf16 v[200:203], a[108:111], a[28:31], v[200:203]\n"
    "v_exp_f32 v214, v214\n"
    "v_mfma_f32_16x16x32_bf16 v[208:211], a[124:127], a[28:31], v[220:223]\n"
    "v_add_f32 v241, v241, v214\n"
    "v_cvt_pk_bf16_f32 v198, v212, v213\n"
    "v_mfma_f32_16x16x32_bf16 v[208:211], a[120:123], a[24:27], v[208:211]\n"
    "v_exp_f32 v215, v215\n"
    "v_mfma_f32_16x16x32_bf16 v[208:211], a[116:119], a[20:23], v[208:211]\n"
    "s_waitcnt vmcnt(43) lgkmcnt(0)\n"
    "v_add_f32 v241, v241, v215\n"
    "v_cvt_pk_bf16_f32 v199, v214, v215\n"
    "v_mfma_f32_16x16x32_bf16 v[208:211], a[112:115], a[16:19], v[208:211]\n"
    "s_barrier\n"
    "v_exp_f32 v200, v200\n"
    "v_mfma_f32_16x16x32_bf16 v[16:19], v[204:207], a[208:211], v[16:19]\n"
    "s_cmp_eq_u32 s62, 23\n"
    "ds_read_b64_tr_b16 a[244:245], v251 offset:49544\n"
    "v_add_f32 v241, v241, v200\n"
    "v_mfma_f32_16x16x32_bf16 v[16:19], v[192:195], a[228:231], v[16:19]\n"
    "s_cselect_b32 s60, s61, s60\n"
    "v_exp_f32 v201, v201\n"
    "v_mfma_f32_16x16x32_bf16 v[20:23], v[192:195], a[232:235], v[20:23]\n"
    "s_cselect_b32 s61, s79, s61\n"
    "ds_read_b64_tr_b16 a[246:247], v251 offset:57800\n"
    "v_add_f32 v241, v241, v201\n"
    "v_mfma_f32_16x16x32_bf16 v[20:23], v[204:207], a[212:215], v[20:23]\n"
    "s_cselect_b32 s62, 0, s62\n"
    "v_exp_f32 v202, v202\n"
    "v_mfma_f32_16x16x32_bf16 v[24:27], v[204:207], a[216:219], v[24:27]\n"
    "s_mov_b32 m0, 33024\n"
    "ds_read_b64_tr_b16 a[80:81], v252 offset:49536\n"
    "v_add_f32 v241, v241, v202\n"
    "v_mfma_f32_16x16x32_bf16 v[24:27], v[192:195], a[236:239], v[24:27]\n"
    "buffer_load_dwordx4 v253, s[36:39], s60 offen offset:0 lds\n"
    "v_exp_f32 v203, v203\n"
    "v_mfma_f32_16x16x32_bf16 v[28:31], v[192:195], a[240:243], v[28:31]\n"
    "s_add_u32 s62, s62, 1\n"
    "ds_read_b64_tr_b16 a[82:83], v252 offset:57792\n"
    "v_add_f32 v241, v241, v203\n"
    "v_mfma_f32_16x16x32_bf16 v[28:31], v[204:207], a[220:223], v[28:31]\n"
    "buffer_load_dwordx4 v253, s[36:39], s60 offen offset:1024 lds\n"
    "v_exp_f32 v208, v208\n"
    "v_mfma_f32_16x16x32_bf16 v[204:207], a[128:131], a[32:35], v[224:227]\n"
    "LPG14:\n"
    "s_cbranch_execz LWRET2\n"
    "v_add_f32 v241, v241, v208\n"
    "v_cvt_pk_bf16_f32 v200, v200, v201\n"
    "v_mfma_f32_16x16x32_bf16 v[204:207], a[132:135], a[36:39], v[204:207]\n"
    "v_exp_f32 v209, v209\n"
    "v_mfma_f32_16x16x32_bf16 v[204:207], a[136:139], a[40:43], v[204:207]\n"
    "buffer_load_dwordx4 v253, s[40:43], s60 offen offset:2048 lds\n"
    "v_add_f32 v241, v241, v209\n"
    "v_cvt_pk_bf16_f32 v201, v202, v203\n"
    "v_mfma_f32_16x16x32_bf16 v[204:207], a[140:143], a[44:47], v[204:207]\n"
    "v_exp_f32_e64 v210, v210\n"
    "v_mfma_f32_16x16x32_bf16 v[212:215], a[156:159], a[44:47], v[224:227]\n"
    "buffer_load_dwordx4 v253, s[44:47], s60 offen offset:3080 lds\n"
    "s_add_u32 s60, s60, 2048\n"
    "s_mov_b32 m0, 37152\n"
    "v_add_f32 v241, v241, v210\n"
    "v_cvt_pk_bf16_f32 v202, v208, v209\n"
    "v_mfma_f32_16x16x32_bf16 v[212:215], a[152:155], a[40:43], v[212:215]\n"
    "buffer_load_dwordx4 v253, s[36:39], s60 offen offset:0 lds\n"
    "v_exp_f32 v211, v211\n"
    "v_mfma_f32_16x16x32_bf16 v[212:215], a[148:151], a[36:39], v[212:215]\n"
    "v_add_f32 v241, v241, v211\n"
    "v_cvt_pk_bf16_f32 v203, v210, v211\n"
    "v_mfma_f32_16x16x32_bf16 v[212:215], a[144:147], a[32:35], v[212:215]\n"
    "buffer_load_dwordx4 v253, s[36:39], s60 offen offset:1024 lds\n"
    "v_exp_f32 v204, v204\n"
    "v_mfma_f32_16x16x32_bf16 v[32:35], v[196:199], a[192:195], v[32:35]\n"
    "ds_read_b128 a[128:131], v246 offset:49536\n"
    "v_add_f32 v242, v242, v204\n"
    "v_mfma_f32_16x16x32_bf16 v[32:35], v[200:203], a[160:163], v[32:35]\n"
    "buffer_load_dwordx4 v253, s[40:43], s60 offen offset:2048 lds\n"
    "v_exp_f32 v205, v205\n"
    "v_mfma_f32_16x16x32_bf16 v[36:39], v[200:203], a[164:167], v[36:39]\n"
    "ds_read_b128 a[132:135], v246 offset:49792\n"
    "v_add_f32 v242, v242, v205\n"
    "v_mfma_f32_16x16x32_bf16 v[36:39], v[196:199], a[196:199], v[36:39]\n"
    "v_exp_f32 v206, v206\n"
    "v_mfma_f32_16x16x32_bf16 v[40:43], v[196:199], a[200:203], v[40:43]\n"
    "ds_read_b128 a[136:139], v246 offset:50048\n"
    "v_add_f32 v242, v242, v206\n"
    "v_mfma_f32_16x16x32_bf16 v[40:43], v[200:203], a[168:171], v[40:43]\n"
    "buffer_load_dwordx4 v253, s[44:47], s60 offen offset:3080 lds\n"
    "v_exp_f32 v207, v207\n"
    "v_mfma_f32_16x16x32_bf16 v[44:47], v[200:203], a[224:227], v[44:47]\n"
    "s_add_u32 s60, s60, 2048\n"
    "s_mov_b32 m0, 41280\n"
    "ds_read_b128 a[140:143], v246 offset:50304\n"
    "v_add_f32 v242, v242, v207\n"
    "v_mfma_f32_16x16x32_bf16 v[44:47], v[196:199], a[204:207], v[44:47]\n"
    "buffer_load_dwordx4 v253, s[36:39], s60 offen offset:0 lds\n"
    "v_exp_f32 v212, v212\n"
    "v_mfma_f32_16x16x32_bf16 v[192:195], a[96:99], a[32:35], v[224:227]\n"
    "v_add_f32 v242, v242, v212\n"
    "v_cvt_pk_bf16_f32 v204, v204, v205\n"
    "v_mfma_f32_16x16x32_bf16 v[192:195], a[100:103], a[36:39], v[192:195]\n"
    "v_exp_f32 v213, v213\n"
    "v_mfma_f32_16x16x32_bf16 v[192:195], a[104:107], a[40:43], v[192:195]\n"
    "buffer_load_dwordx4 v253, s[36:39], s60 offen offset:1024 lds\n"
    "v_add_f32 v242, v242, v213\n"
    "v_cvt_pk_bf16_f32 v205, v206, v207\n"
    "v_mfma_f32_16x16x32_bf16 v[192:195], a[108:111], a[44:47], v[192:195]\n"
    "v_exp_f32 v214, v214\n"
    "v_mfma_f32_16x16x32_bf16 v[208:211], a[124:127], a[44:47], v[224:227]\n"
    "buffer_load_dwordx4 v253, s[40:43], s60 offen offset:2048 lds\n"
    "v_add_f32 v242, v242, v214\n"
    "v_cvt_pk_bf16_f32 v206, v212, v213\n"
    "v_mfma_f32_16x16x32_bf16 v[208:211], a[120:123], a[40:43], v[208:211]\n"
    "buffer_load_dwordx4 v253, s[44:47], s60 offen offset:3080 lds\n"
    "v_exp_f32 v215, v215\n"
    "v_mfma_f32_16x16x32_bf16 v[208:211], a[116:119], a[36:39], v[208:211]\n"
    "s_add_u32 s60, s60, 2048\n"
    "s_mov_b32 m0, 45408\n"
    "v_add_f32 v242, v242, v215\n"
    "v_cvt_pk_bf16_f32 v207, v214, v215\n"
    "v_mfma_f32_16x16x32_bf16 v[208:211], a[112:115], a[32:35], v[208:211]\n"
    "buffer_load_dwordx4 v253, s[36:39], s60 offen offset:0 lds\n"
    "v_exp_f32 v192, v192\n"
    "v_mfma_f32_16x16x32_bf16 v[48:51], v[196:199], a[208:211], v[48:51]\n"
    "ds_read_b128 a[156:159], v246 offset:58560\n"
    "v_add_f32 v242, v242, v192\n"
    "v_mfma_f32_16x16x32_bf16 v[48:51], v[200:203], a[228:231], v[48:51]\n"
    "ds_read_b64_tr_b16 v[168:169], v164 offset:0\n"
    "ds_read_b64_tr_b16 v[170:171], v164 offset:1024\n"
    "v_exp_f32 v193, v193\n"
    "v_mfma_f32_16x16x32_bf16 v[52:55], v[200:203], a[232:235], v[52:55]\n"
    "ds_read_b128 a[152:155], v246 offset:58304\n"
    "v_add_f32 v242, v242, v193\n"
    "v_mfma_f32_16x16x32_bf16 v[52:55], v[196:199], a[212:215], v[52:55]\n"
    "ds_read_b64_tr_b16 v[172:173], v164 offset:2048\n"
    "ds_read_b64_tr_b16 v[174:175], v164 offset:3072\n"
    "v_exp_f32 v194, v194\n"
    "v_mfma_f32_16x16x32_bf16 v[56:59], v[196:199], a[216:219], v[56:59]\n"
    "ds_read_b128 a[148:151], v246 offset:58048\n"
    "v_add_f32 v242, v242, v194\n"
    "v_mfma_f32_16x16x32_bf16 v[56:59], v[200:203], a[236:239], v[56:59]\n"
    "ds_read_b64_tr_b16 v[176:177], v164 offset:8\n"
    "ds_read_b64_tr_b16 v[178:179], v164 offset:1032\n"
    "v_exp_f32 v195, v195\n"
    "v_mfma_f32_16x16x32_bf16 v[60:63], v[200:203], a[240:243], v[60:63]\n"
    "ds_read_b128 a[144:147], v246 offset:57792\n"
    "v_add_f32 v242, v242, v195\n"
    "v_mfma_f32_16x16x32_bf16 v[60:63], v[196:199], a[220:223], v[60:63]\n"
    "ds_read_b64_tr_b16 v[180:181], v164 offset:2056\n"
    "ds_read_b64_tr_b16 v[182:183], v164 offset:3080\n"
    "v_exp_f32 v208, v208\n"
    "v_mfma_f32_16x16x32_bf16 v[196:199], a[96:99], a[48:51], v[228:231]\n"
    "v_add_f32 v242, v242, v208\n"
    "v_cvt_pk_bf16_f32 v192, v192, v193\n"
    "v_add_u32 v164, 4128, v164\n"
    "v_mfma_f32_16x16x32_bf16 v[196:199], a[100:103], a[52:55], v[196:199]\n"
    "v_exp_f32 v209, v209\n"
    "v_mfma_f32_16x16x32_bf16 v[196:199], a[104:107], a[56:59], v[196:199]\n"
    "buffer_load_dwordx4 v253, s[36:39], s60 offen offset:1024 lds\n"
    "v_add_f32 v242, v242, v209\n"
    "v_cvt_pk_bf16_f32 v193, v194, v195\n"
    "v_mfma_f32_16x16x32_bf16 v[196:199], a[108:111], a[60:63], v[196:199]\n"
    "v_exp_f32 v210, v210\n"
    "v_mfma_f32_16x16x32_bf16 v[212:215], a[124:127], a[60:63], v[228:231]\n"
    "buffer_load_dwordx4 v253, s[40:43], s60 offen offset:2048 lds\n"
    "v_add_f32 v242, v242, v210\n"
    "v_cvt_pk_bf16_f32 v194, v208, v209\n"
    "v_mfma_f32_16x16x32_bf16 v[212:215], a[120:123], a[56:59], v[212:215]\n"
    "buffer_load_dwordx4 v253, s[44:47], s60 offen offset:3080 lds\n"
    "v_exp_f32 v211, v211\n"
    "v_mfma_f32_16x16x32_bf16 v[212:215], a[116:119], a[52:55], v[212:215]\n"
    "s_add_u32 s60, s60, 2048\n"
    "v_add_f32 v242, v242, v211\n"
    "v_cvt_pk_bf16_f32 v195, v210, v211\n"
    "v_mfma_f32_16x16x32_bf16 v[212:215], a[112:115], a[48:51], v[212:215]\n"
    "v_exp_f32 v196, v196\n"
    "v_mfma_f32_16x16x32_bf16 v[64:67], v[204:207], a[192:195], v[64:67]\n"
    "ds_read_b64_tr_b16 a[84:85], v252 offset:49544\n"
    "v_add_f32 v243, v243, v196\n"
    "v_mfma_f32_16x16x32_bf16 v[64:67], v[192:195], a[160:163], v[64:67]\n"
    "s_waitcnt lgkmcnt(1)\n"
    "buffer_store_dwordx4 v[168:171], v249, s[56:59], s64 offen offset:0 sc1\n"
    "v_exp_f32 v197, v197\n"
    "v_mfma_f32_16x16x32_bf16 v[68:71], v[192:195], a[164:167], v[68:71]\n"
    "ds_read_b64_tr_b16 a[86:87], v252 offset:57800\n"
    "v_add_f32 v243, v243, v197\n"
    "v_mfma_f32_16x16x32_bf16 v[68:71], v[204:207], a[196:199], v[68:71]\n"
    "v_exp_f32 v198, v198\n"
    "v_mfma_f32_16x16x32_bf16 v[72:75], v[204:207], a[200:203], v[72:75]\n"
    "ds_read_b64_tr_b16 a[88:89], v254 offset:49536\n"
    "v_add_f32 v243, v243, v198\n"
    "v_mfma_f32_16x16x32_bf16 v[72:75], v[192:195], a[168:171], v[72:75]\n"
    "ds_read_b64_tr_b16 a[202:203], v251 offset:57792\n"
    "v_exp_f32 v199, v199\n"
    "v_mfma_f32_16x16x32_bf16 v[76:79], v[192:195], a[224:227], v[76:79]\n"
    "ds_read_b64_tr_b16 a[90:91], v254 offset:57792\n"
    "v_add_f32 v243, v243, v199\n"
    "v_mfma_f32_16x16x32_bf16 v[76:79], v[204:207], a[204:207], v[76:79]\n"
    "buffer_store_dwordx4 v[172:175], v249, s[56:59], s64 offen offset:256 sc1\n"
    "v_exp_f32 v212, v212\n"
    "v_mfma_f32_16x16x32_bf16 v[200:203], a[128:131], a[48:51], v[228:231]\n"
    "v_add_f32 v243, v243, v212\n"
    "v_cvt_pk_bf16_f32 v196, v196, v197\n"
    "v_mfma_f32_16x16x32_bf16 v[200:203], a[132:135], a[52:55], v[200:203]\n"
    "v_exp_f32 v213, v213\n"
    "v_mfma_f32_16x16x32_bf16 v[200:203], a[136:139], a[56:59], v[200:203]\n"
    "ds_read_b64_tr_b16 a[196:197], v247 offset:49544\n"
    "v_add_f32 v243, v243, v213\n"
    "v_cvt_pk_bf16_f32 v197, v198, v199\n"
    "v_mfma_f32_16x16x32_bf16 v[200:203], a[140:143], a[60:63], v[200:203]\n"
    "ds_read_b64_tr_b16 a[198:199], v247 offset:57800\n"
    "v_exp_f32 v214, v214\n"
    "v_mfma_f32_16x16x32_bf16 v[208:211], a[156:159], a[60:63], v[228:231]\n"
    "ds_read_b64_tr_b16 a[200:201], v251 offset:49536\n"
    "v_add_f32 v243, v243, v214\n"
    "v_cvt_pk_bf16_f32 v198, v212, v213\n"
    "v_mfma_f32_16x16x32_bf16 v[208:211], a[152:155], a[56:59], v[208:211]\n"
    "v_exp_f32 v215, v215\n"
    "v_mfma_f32_16x16x32_bf16 v[208:211], a[148:151], a[52:55], v[208:211]\n"
    "v_add_f32 v243, v243, v215\n"
    "v_cvt_pk_bf16_f32 v199, v214, v215\n"
    "v_mfma_f32_16x16x32_bf16 v[208:211], a[144:147], a[48:51], v[208:211]\n"
    "buffer_store_dwordx4 v[176:179], v249, s[56:59], s64 offen offset:512 sc1\n"
    "v_exp_f32 v200, v200\n"
    "v_mfma_f32_16x16x32_bf16 v[80:83], v[204:207], a[208:211], v[80:83]\n"
    "ds_read_b64_tr_b16 a[92:93], v254 offset:49544\n"
    "v_add_f32 v243, v243, v200\n"
    "v_mfma_f32_16x16x32_bf16 v[80:83], v[192:195], a[228:231], v[80:83]\n"
    "v_exp_f32 v201, v201\n"
    "v_mfma_f32_16x16x32_bf16 v[84:87], v[192:195], a[232:235], v[84:87]\n"
    "ds_read_b64_tr_b16 a[94:95], v254 offset:57800\n"
    "v_add_f32 v243, v243, v201\n"
    "v_mfma_f32_16x16x32_bf16 v[84:87], v[204:207], a[212:215], v[84:87]\n"
    "buffer_store_dwordx4 v[180:183], v249, s[56:59], s64 offen offset:768 sc1\n"
    "v_exp_f32_e64 v202, v202\n"
    "v_mfma_f32_16x16x32_bf16 v[88:91], v[204:207], a[216:219], v[88:91]\n"
    "s_add_u32 s64, s64, 4096\n"
    "s_add_u32 s87, s60, 24576\n"
    "ds_read_b64_tr_b16 a[192:193], v247 offset:49536\n"
    "v_add_f32 v243, v243, v202\n"
    "v_mfma_f32_16x16x32_bf16 v[88:91], v[192:195], a[236:239], v[88:91]\n"
    "s_sub_u32 s70, s62, 17\n"
    "v_exp_f32 v203, v203\n"
    "v_mfma_f32_16x16x32_bf16 v[92:95], v[192:195], a[240:243], v[92:95]\n"
    "s_cmp_lt_u32 s70, 3\n"
    "ds_read_b64_tr_b16 a[194:195], v247 offset:57792\n"
    "v_add_f32 v243, v243, v203\n"
    "v_mfma_f32_16x16x32_bf16 v[92:95], v[204:207], a[220:223], v[92:95]\n"
    "s_cselect_b32 s87, s87, s79\n"
    "v_exp_f32 v208, v208\n"
    "v_mfma_f32_16x16x32_bf16 v[204:207], a[96:99], a[64:67], v[232:235]\n"
    "v_add_f32 v243, v243, v208\n"
    "v_cvt_pk_bf16_f32 v200, v200, v201\n"
    "v_mfma_f32_16x16x32_bf16 v[204:207], a[100:103], a[68:71], v[204:207]\n"
    "v_exp_f32 v209, v209\n"
    "v_mfma_f32_16x16x32_bf16 v[204:207], a[104:107], a[72:75], v[204:207]\n"
    "buffer_load_dword v161, v165, s[88:91], s87 offen\n"
    "v_add_f32 v243, v243, v209\n"
    "v_cvt_pk_bf16_f32 v201, v202, v203\n"
    "v_mfma_f32_16x16x32_bf16 v[204:207], a[108:111], a[76:79], v[204:207]\n"
    "v_exp_f32 v210, v210\n"
    "v_mfma_f32_16x16x32_bf16 v[212:215], a[124:127], a[76:79], v[232:235]\n"
    "v_add_f32 v243, v243, v210\n"
    "v_cvt_pk_bf16_f32 v202, v208, v209\n"
    "v_mfma_f32_16x16x32_bf16 v[212:215], a[120:123], a[72:75], v[212:215]\n"
    "v_exp_f32 v211, v211\n"
    "v_mfma_f32_16x16x32_bf16 v[212:215], a[116:119], a[68:71], v[212:215]\n"
    "s_waitcnt vmcnt(47) lgkmcnt(0)\n"
    "v_add_f32 v243, v243, v211\n"
    "v_cvt_pk_bf16_f32 v203, v210, v211\n"
    "v_mfma_f32_16x16x32_bf16 v[212:215], a[112:115], a[64:67], v[212:215]\n"
    "s_barrier\n"
    "v_exp_f32 v204, v204\n"
    "v_mfma_f32_16x16x32_bf16 v[96:99], v[196:199], a[160:163], v[96:99]\n"
    "s_cmp_eq_u32 s62, 23\n"
    "ds_read_b128 a[96:99], v246 offset:0\n"
    "v_add_f32 v244, v244, v204\n"
    "v_mfma_f32_16x16x32_bf16 v[96:99], v[200:203], a[192:195], v[96:99]\n"
    "s_cselect_b32 s60, s61, s60\n"
    "v_exp_f32 v205, v205\n"
    "v_mfma_f32_16x16x32_bf16 v[100:103], v[200:203], a[196:199], v[100:103]\n"
    "s_cselect_b32 s61, s79, s61\n"
    "ds_read_b128 a[100:103], v246 offset:256\n"
    "v_add_f32 v244, v244, v205\n"
    "v_mfma_f32_16x16x32_bf16 v[100:103], v[196:199], a[164:167], v[100:103]\n"
    "s_cselect_b32 s62, 0, s62\n"
    "v_exp_f32 v206, v206\n"
    "v_mfma_f32_16x16x32_bf16 v[104:107], v[196:199], a[168:171], v[104:107]\n"
    "s_mov_b32 m0, 49536\n"
    "ds_read_b128 a[104:107], v246 offset:512\n"
    "v_add_f32 v244, v244, v206\n"
    "v_mfma_f32_16x16x32_bf16 v[104:107], v[200:203], a[200:203], v[104:107]\n"
    "buffer_load_dwordx4 v253, s[36:39], s60 offen offset:0 lds\n"
    "v_exp_f32 v207, v207\n"
    "v_mfma_f32_16x16x32_bf16 v[108:111], v[200:203], a[244:247], v[108:111]\n"
    "s_add_u32 s62, s62, 1\n"
    "ds_read_b128 a[108:111], v246 offset:768\n"
    "v_add_f32 v244, v244, v207\n"
    "v_mfma_f32_16x16x32_bf16 v[108:111], v[196:199], a[224:227], v[108:111]\n"
    "buffer_load_dwordx4 v253, s[36:39], s60 offen offset:1024 lds\n"
    "v_exp_f32 v212, v212\n"
    "v_mfma_f32_16x16x32_bf16 v[192:195], a[128:131], a[64:67], v[232:235]\n"
    "v_add_f32 v244, v244, v212\n"
    "v_cvt_pk_bf16_f32 v204, v204, v205\n"
    "v_mfma_f32_16x16x32_bf16 v[192:195], a[132:135], a[68:71], v[192:195]\n"
    "v_exp_f32 v213, v213\n"
    "v_mfma_f32_16x16x32_bf16 v[192:195], a[136:139], a[72:75], v[192:195]\n"
    "ds_read_b64_tr_b16 a[184:185], v254 offset:0\n"
    "v_add_f32 v244, v244, v213\n"
    "v_cvt_pk_bf16_f32 v205, v206, v207\n"
    "v_mfma_f32_16x16x32_bf16 v[192:195], a[140:143], a[76:79], v[192:195]\n"
    "buffer_load_dwordx4 v253, s[40:43], s60 offen offset:2048 lds\n"
    "v_exp_f32 v214, v214\n"
    "v_mfma_f32_16x16x32_bf16 v[208:211], a[156:159], a[76:79], v[232:235]\n"
    "buffer_load_dwordx4 v253, s[44:47], s60 offen offset:3080 lds\n"
    "v_add_f32_e64 v244, v244, v214\n"
    "v_cvt_pk_bf16_f32 v206, v212, v213\n"
    "v_mfma_f32_16x16x32_bf16 v[208:211], a[152:155], a[72:75], v[208:211]\n"
    "s_add_u32 s60, s60, 2048\n"
    "s_mov_b32 m0, 53664\n"
    "v_exp_f32 v215, v215\n"
    "v_mfma_f32_16x16x32_bf16 v[208:211], a[148:151], a[68:71], v[208:211]\n"
    "buffer_load_dwordx4 v253, s[36:39], s60 offen offset:0 lds\n"
    "v_add_f32 v244, v244, v215\n"
    "v_cvt_pk_bf16_f32 v207, v214, v215\n"
    "v_mfma_f32_16x16x32_bf16 v[208:211], a[144:147], a[64:67], v[208:211]\n"
    "buffer_load_dwordx4 v253, s[36:39], s60 offen offset:1024 lds\n"
    "v_exp_f32 v192, v192\n"
    "v_mfma_f32_16x16x32_bf16 v[112:115], v[196:199], a[228:231], v[112:115]\n"
    "ds_read_b128 a[124:127], v246 offset:9024\n"
    "v_add_f32 v244, v244, v192\n"
    "v_mfma_f32_16x16x32_bf16 v[112:115], v[200:203], a[80:83], v[112:115]\n"
    "buffer_load_dwordx4 v253, s[40:43], s60 offen offset:2048 lds\n"
    "v_exp_f32 v193, v193\n"
    "v_mfma_f32_16x16x32_bf16 v[116:119], v[200:203], a[84:87], v[116:119]\n"
    "ds_read_b128 a[120:123], v246 offset:8768\n"
    "v_add_f32 v244, v244, v193\n"
    "v_mfma_f32_16x16x32_bf16 v[116:119], v[196:199], a[232:235], v[116:119]\n"
    "buffer_load_dwordx4 v253, s[44:47], s60 offen offset:3080 lds\n"
    "v_exp_f32 v194, v194\n"
    "v_mfma_f32_16x16x32_bf16 v[120:123], v[196:199], a[236:239], v[120:123]\n"
    "s_add_u32 s60, s60, 2048\n"
    "ds_read_b128 a[116:119], v246 offset:8512\n"
    "v_add_f32 v244, v244, v194\n"
    "v_mfma_f32_16x16x32_bf16 v[120:123], v[200:203], a[88:91], v[120:123]\n"
    "s_mov_b32 m0, 57792\n"
    "v_exp_f32 v195, v195\n"
    "v_mfma_f32_16x16x32_bf16 v[124:127], v[200:203], a[92:95], v[124:127]\n"
    "ds_read_b128 a[112:115], v246 offset:8256\n"
    "v_add_f32 v244, v244, v195\n"
    "v_mfma_f32_16x16x32_bf16 v[124:127], v[196:199], a[240:243], v[124:127]\n"
    "buffer_load_dwordx4 v253, s[36:39], s60 offen offset:0 lds\n"
    "v_exp_f32 v208, v208\n"
    "v_mfma_f32_16x16x32_bf16 v[196:199], a[128:131], a[0:3], v[216:219]\n"
    "v_add_f32 v244, v244, v208\n"
    "v_cvt_pk_bf16_f32 v192, v192, v193\n"
    "v_mfma_f32_16x16x32_bf16 v[196:199], a[132:135], a[4:7], v[196:199]\n"
    "v_exp_f32 v209, v209\n"
    "v_mfma_f32_16x16x32_bf16 v[196:199], a[136:139], a[8:11], v[196:199]\n"
    "ds_read_b64_tr_b16 a[186:187], v254 offset:8256\n"
    "v_add_f32 v244, v244, v209\n"
    "v_cvt_pk_bf16_f32 v193, v194, v195\n"
    "v_mfma_f32_16x16x32_bf16 v[196:199], a[140:143], a[12:15], v[196:199]\n"
    "buffer_load_dwordx4 v253, s[36:39], s60 offen offset:1024 lds\n"
    "v_exp_f32 v210, v210\n"
    "v_mfma_f32_16x16x32_bf16 v[212:215], a[156:159], a[12:15], v[216:219]\n"
    "buffer_load_dwordx4 v253, s[40:43], s60 offen offset:2048 lds\n"
    "v_add_f32 v244, v244, v210\n"
    "v_cvt_pk_bf16_f32 v194, v208, v209\n"
    "v_mfma_f32_16x16x32_bf16 v[212:215], a[152:155], a[8:11], v[212:215]\n"
    "buffer_load_dwordx4 v253, s[44:47], s60 offen offset:3080 lds\n"
    "v_exp_f32 v211, v211\n"
    "v_mfma_f32_16x16x32_bf16 v[212:215], a[148:151], a[4:7], v[212:215]\n"
    "s_add_u32 s60, s60, 2048\n"
    "s_mov_b32 m0, 61920\n"
    "v_add_f32 v244, v244, v211\n"
    "v_cvt_pk_bf16_f32 v195, v210, v211\n"
    "v_mfma_f32_16x16x32_bf16 v[212:215], a[144:147], a[0:3], v[212:215]\n"
    "buffer_load_dwordx4 v253, s[36:39], s60 offen offset:0 lds\n"
    "v_exp_f32 v196, v196\n"
    "v_mfma_f32_16x16x32_bf16 v[128:131], v[204:207], a[160:163], v[128:131]\n"
    "ds_read_b64_tr_b16 a[160:161], v247 offset:0\n"
    "v_add_f32 v240, v240, v196\n"
    "v_mfma_f32_16x16x32_bf16 v[128:131], v[192:195], a[192:195], v[128:131]\n"
    "ds_read_b64_tr_b16 v[168:169], v164 offset:0\n"
    "ds_read_b64_tr_b16 v[170:171], v164 offset:1024\n"
    "v_exp_f32 v197, v197\n"
    "v_mfma_f32_16x16x32_bf16 v[132:135], v[192:195], a[196:199], v[132:135]\n"
    "ds_read_b64_tr_b16 a[162:163], v247 offset:8256\n"
    "v_add_f32 v240, v240, v197\n"
    "v_mfma_f32_16x16x32_bf16 v[132:135], v[204:207], a[164:167], v[132:135]\n"
    "ds_read_b64_tr_b16 v[172:173], v164 offset:2048\n"
    "ds_read_b64_tr_b16 v[174:175], v164 offset:3072\n"
    "v_exp_f32 v198, v198\n"
    "v_mfma_f32_16x16x32_bf16 v[136:139], v[204:207], a[168:171], v[136:139]\n"
    "ds_read_b64_tr_b16 a[164:165], v247 offset:8\n"
    "v_add_f32 v240, v240, v198\n"
    "v_mfma_f32_16x16x32_bf16 v[136:139], v[192:195], a[200:203], v[136:139]\n"
    "ds_read_b64_tr_b16 v[176:177], v164 offset:8\n"
    "ds_read_b64_tr_b16 v[178:179], v164 offset:1032\n"
    "v_exp_f32 v199, v199\n"
    "v_mfma_f32_16x16x32_bf16 v[140:143], v[192:195], a[244:247], v[140:143]\n"
    "ds_read_b64_tr_b16 a[166:167], v247 offset:8264\n"
    "v_add_f32 v240, v240, v199\n"
    "v_mfma_f32_16x16x32_bf16 v[140:143], v[204:207], a[224:227], v[140:143]\n"
    "s_waitcnt lgkmcnt(7)\n"
    "ds_read_b64_tr_b16 v[180:181], v164 offset:2056\n"
    "ds_read_b64_tr_b16 v[182:183], v164 offset:3080\n"
    "v_exp_f32 v212, v212\n"
    "v_mfma_f32_16x16x32_bf16 v[200:203], a[96:99], a[0:3], v[216:219]\n"
    "v_add_f32 v240, v240, v212\n"
    "v_cvt_pk_bf16_f32 v196, v196, v197\n"
    "v_add_u32 v164, 4128, v164\n"
    "v_mfma_f32_16x16x32_bf16 v[200:203], a[100:103], a[4:7], v[200:203]\n"
    "v_exp_f32 v213, v213\n"
    "v_mfma_f32_16x16x32_bf16 v[200:203], a[104:107], a[8:11], v[200:203]\n"
    "ds_read_b64_tr_b16 a[188:189], v254 offset:8\n"
    "v_add_f32 v240, v240, v213\n"
    "v_cvt_pk_bf16_f32 v197, v198, v199\n"
    "v_mfma_f32_16x16x32_bf16 v[200:203], a[108:111], a[12:15], v[200:203]\n"
    "buffer_load_dwordx4 v253, s[36:39], s60 offen offset:1024 lds\n"
    "v_exp_f32 v214, v214\n"
    "v_mfma_f32_16x16x32_bf16 v[208:211], a[124:127], a[12:15], v[216:219]\n"
    "buffer_load_dwordx4 v253, s[40:43], s60 offen offset:2048 lds\n"
    "v_add_f32 v240, v240, v214\n"
    "v_cvt_pk_bf16_f32 v198, v212, v213\n"
    "v_mfma_f32_16x16x32_bf16 v[208:211], a[120:123], a[8:11], v[208:211]\n"
    "buffer_load_dwordx4 v253, s[44:47], s60 offen offset:3080 lds\n"
    "v_exp_f32 v215, v215\n"
    "v_mfma_f32_16x16x32_bf16 v[208:211], a[116:119], a[4:7], v[208:211]\n"
    "s_add_u32 s60, s60, 2048\n"
    "v_add_f32 v240, v240, v215\n"
    "v_cvt_pk_bf16_f32 v199, v214, v215\n"
    "v_mfma_f32_16x16x32_bf16 v[208:211], a[112:115], a[0:3], v[208:211]\n"
    "v_exp_f32 v200, v200\n"
    "v_mfma_f32_16x16x32_bf16 v[144:147], v[204:207], a[228:231], v[144:147]\n"
    "ds_read_b64_tr_b16 a[168:169], v251 offset:0\n"
    "v_add_f32 v240, v240, v200\n"
    "v_mfma_f32_16x16x32_bf16 v[144:147], v[192:195], a[80:83], v[144:147]\n"
    "buffer_store_dwordx4 v[168:171], v249, s[56:59], s64 offen offset:0 sc1\n"
    "v_exp_f32 v201, v201\n"
    "v_mfma_f32_16x16x32_bf16 v[148:151], v[192:195], a[84:87], v[148:151]\n"
    "ds_read_b64_tr_b16 a[170:171], v251 offset:8256\n"
    "v_add_f32 v240, v240, v201\n"
    "v_mfma_f32_16x16x32_bf16 v[148:151], v[204:207], a[232:235], v[148:151]\n"
    "v_exp_f32 v202, v202\n"
    "v_mfma_f32_16x16x32_bf16 v[152:155], v[204:207], a[236:239], v[152:155]\n"
    "ds_read_b64_tr_b16 a[172:173], v251 offset:8\n"
    "v_add_f32 v240, v240, v202\n"
    "v_mfma_f32_16x16x32_bf16 v[152:155], v[192:195], a[88:91], v[152:155]\n"
    "s_waitcnt lgkmcnt(3)\n"
    "buffer_store_dwordx4 v[172:175], v249, s[56:59], s64 offen offset:256 sc1\n"
    "v_exp_f32 v203, v203\n"
    "v_mfma_f32_16x16x32_bf16 v[156:159], v[192:195], a[92:95], v[156:159]\n"
    "ds_read_b64_tr_b16 a[174:175], v251 offset:8264\n"
    "v_add_f32 v240, v240, v203\n"
    "v_mfma_f32_16x16x32_bf16 v[156:159], v[204:207], a[240:243], v[156:159]\n"
    "v_exp_f32 v208, v208\n"
    "v_mfma_f32_16x16x32_bf16 v[204:207], a[128:131], a[16:19], v[220:223]\n"
    "v_add_f32 v240, v240, v208\n"
    "v_cvt_pk_bf16_f32 v200, v200, v201\n"
    "v_mfma_f32_16x16x32_bf16 v[204:207], a[132:135], a[20:23], v[204:207]\n"
    "v_exp_f32 v209, v209\n"
    "v_mfma_f32_16x16x32_bf16 v[204:207], a[136:139], a[24:27], v[204:207]\n"
    "ds_read_b64_tr_b16 a[190:191], v254 offset:8264\n"
    "v_add_f32 v240, v240, v209\n"
    "v_cvt_pk_bf16_f32 v201, v202, v203\n"
    "v_mfma_f32_16x16x32_bf16 v[204:207], a[140:143], a[28:31], v[204:207]\n"
    "v_exp_f32 v210, v210\n"
    "v_mfma_f32_16x16x32_bf16 v[212:215], a[156:159], a[28:31], v[220:223]\n"
    "buffer_store_dwordx4 v[176:179], v249, s[56:59], s64 offen offset:512 sc1\n"
    "v_add_f32 v240, v240, v210\n"
    "v_cvt_pk_bf16_f32 v202, v208, v209\n"
    "v_mfma_f32_16x16x32_bf16 v[212:215], a[152:155], a[24:27], v[212:215]\n"
    "v_exp_f32 v211, v211\n"
    "v_mfma_f32_16x16x32_bf16 v[212:215], a[148:151], a[20:23], v[212:215]\n"
    "v_add_f32 v240, v240, v211\n"
    "v_cvt_pk_bf16_f32 v203, v210, v211\n"
    "v_mfma_f32_16x16x32_bf16 v[212:215], a[144:147], a[16:19], v[212:215]\n"
    "buffer_store_dwordx4 v[180:183], v249, s[56:59], s64 offen offset:768 sc1\n"
    "v_exp_f32_e64 v204, v204\n"
    "v_mfma_f32_16x16x32_bf16 v[0:3], v[196:199], a[192:195], v[0:3]\n"
    "s_add_u32 s64, s64, 4096\n"
    "s_add_u32 s87, s60, 24576\n"
    "ds_read_b64_tr_b16 a[176:177], v252 offset:0\n"
    "v_add_f32 v241, v241, v204\n"
    "v_mfma_f32_16x16x32_bf16 v[0:3], v[200:203], a[160:163], v[0:3]\n"
    "s_sub_u32 s70, s62, 17\n"
    "s_cmp_lt_u32 s70, 3\n"
    "v_exp_f32 v205, v205\n"
    "v_mfma_f32_16x16x32_bf16 v[4:7], v[200:203], a[164:167], v[4:7]\n"
    "s_cselect_b32 s87, s87, s79\n"
    "ds_read_b64_tr_b16 a[178:179], v252 offset:8256\n"
    "v_add_f32 v241, v241, v205\n"
    "v_mfma_f32_16x16x32_bf16 v[4:7], v[196:199], a[196:199], v[4:7]\n"
    "buffer_load_dword v161, v165, s[88:91], s87 offen\n"
    "v_exp_f32 v206, v206\n"
    "v_mfma_f32_16x16x32_bf16 v[8:11], v[196:199], a[200:203], v[8:11]\n"
    "s_waitcnt lgkmcnt(2)\n"
    "ds_read_b64_tr_b16 a[180:181], v252 offset:8\n"
    "v_add_f32 v241, v241, v206\n"
    "v_mfma_f32_16x16x32_bf16 v[8:11], v[200:203], a[168:171], v[8:11]\n"
    "v_exp_f32 v207, v207\n"
    "v_mfma_f32_16x16x32_bf16 v[12:15], v[200:203], a[172:175], v[12:15]\n"
    "ds_read_b64_tr_b16 a[182:183], v252 offset:8264\n"
    "v_add_f32 v241, v241, v207\n"
    "v_mfma_f32_16x16x32_bf16 v[12:15], v[196:199], a[244:247], v[12:15]\n"
    "v_exp_f32 v212, v212\n"
    "v_mfma_f32_16x16x32_bf16 v[192:195], a[96:99], a[16:19], v[220:223]\n"
    "v_add_f32 v241, v241, v212\n"
    "v_cvt_pk_bf16_f32 v204, v204, v205\n"
    "v_mfma_f32_16x16x32_bf16 v[192:195], a[100:103], a[20:23], v[192:195]\n"
    "v_exp_f32 v213, v213\n"
    "v_mfma_f32_16x16x32_bf16 v[192:195], a[104:107], a[24:27], v[192:195]\n"
    "global_load_dword v161, v160, s[82:83]\n"
    "v_add_f32 v241, v241, v213\n"
    "v_cvt_pk_bf16_f32 v205, v206, v207\n"
    "v_mfma_f32_16x16x32_bf16 v[192:195], a[108:111], a[28:31], v[192:195]\n"
    "v_exp_f32 v214, v214\n"
    "v_mfma_f32_16x16x32_bf16 v[208:211], a[124:127], a[28:31], v[220:223]\n"
    "v_add_f32 v241, v241, v214\n"
    "v_cvt_pk_bf16_f32 v206, v212, v213\n"
    "v_mfma_f32_16x16x32_bf16 v[208:211], a[120:123], a[24:27], v[208:211]\n"
    "v_exp_f32 v215, v215\n"
    "v_mfma_f32_16x16x32_bf16 v[208:211], a[116:119], a[20:23], v[208:211]\n"
    "s_waitcnt vmcnt(48) lgkmcnt(0)\n"
    "v_add_f32 v241, v241, v215\n"
    "v_cvt_pk_bf16_f32 v207, v214, v215\n"
    "v_mfma_f32_16x16x32_bf16 v[208:211], a[112:115], a[16:19], v[208:211]\n"
    "s_barrier\n"
    "v_exp_f32 v192, v192\n"
    "v_mfma_f32_16x16x32_bf16 v[16:19], v[196:199], a[80:83], v[16:19]\n"
    "s_cmp_eq_u32 s62, 23\n"
    "ds_read_b64_tr_b16 a[204:205], v251 offset:16520\n"
    "v_add_f32 v241, v241, v192\n"
    "v_mfma_f32_16x16x32_bf16 v[16:19], v[200:203], a[176:179], v[16:19]\n"
    "s_cselect_b32 s60, s61, s60\n"
    "v_exp_f32 v193, v193\n"
    "v_mfma_f32_16x16x32_bf16 v[20:23], v[200:203], a[180:183], v[20:23]\n"
    "s_cselect_b32 s61, s79, s61\n"
    "ds_read_b64_tr_b16 a[206:207], v251 offset:24776\n"
    "v_add_f32 v241, v241, v193\n"
    "v_mfma_f32_16x16x32_bf16 v[20:23], v[196:199], a[84:87], v[20:23]\n"
    "s_cselect_b32 s62, 0, s62\n"
    "v_exp_f32 v194, v194\n"
    "v_mfma_f32_16x16x32_bf16 v[24:27], v[196:199], a[88:91], v[24:27]\n"
    "s_mov_b32 m0, 0\n"
    "ds_read_b64_tr_b16 a[208:209], v252 offset:16512\n"
    "v_add_f32 v241, v241, v194\n"
    "v_mfma_f32_16x16x32_bf16 v[24:27], v[200:203], a[184:187], v[24:27]\n"
    "buffer_load_dwordx4 v253, s[36:39], s60 offen offset:0 lds\n"
    "v_exp_f32 v195, v195\n"
    "v_mfma_f32_16x16x32_bf16 v[28:31], v[200:203], a[188:191], v[28:31]\n"
    "s_add_u32 s62, s62, 1\n"
    "ds_read_b64_tr_b16 a[210:211], v252 offset:24768\n"
    "v_add_f32 v241, v241, v195\n"
    "v_mfma_f32_16x16x32_bf16 v[28:31], v[196:199], a[92:95], v[28:31]\n"
    "buffer_load_dwordx4 v253, s[36:39], s60 offen offset:1024 lds\n"
    "v_exp_f32 v208, v208\n"
    "v_mfma_f32_16x16x32_bf16 v[196:199], a[128:131], a[32:35], v[224:227]\n"
    "v_add_f32 v241, v241, v208\n"
    "v_cvt_pk_bf16_f32 v192, v192, v193\n"
    "v_mfma_f32_16x16x32_bf16 v[196:199], a[132:135], a[36:39], v[196:199]\n"
    "v_exp_f32 v209, v209\n"
    "v_mfma_f32_16x16x32_bf16 v[196:199], a[136:139], a[40:43], v[196:199]\n"
    "buffer_load_dwordx4 v253, s[40:43], s60 offen offset:2048 lds\n"
    "v_add_f32 v241, v241, v209\n"
    "v_cvt_pk_bf16_f32 v193, v194, v195\n"
    "v_mfma_f32_16x16x32_bf16 v[196:199], a[140:143], a[44:47], v[196:199]\n"
    "v_exp_f32 v210, v210\n"
    "v_mfma_f32_16x16x32_bf16 v[212:215], a[156:159], a[44:47], v[224:227]\n"
    "buffer_load_dwordx4 v253, s[44:47], s60 offen offset:3080 lds\n"
    "s_add_u32 s60, s60, 2048\n"
    "s_mov_b32 m0, 4128\n"
    "v_add_f32 v241, v241, v210\n"
    "v_cvt_pk_bf16_f32 v194, v208, v209\n"
    "v_mfma_f32_16x16x32_bf16 v[212:215], a[152:155], a[40:43], v[212:215]\n"
    "buffer_load_dwordx4 v253, s[36:39], s60 offen offset:0 lds\n"
    "v_exp_f32 v211, v211\n"
    "v_mfma_f32_16x16x32_bf16 v[212:215], a[148:151], a[36:39], v[212:215]\n"
    "v_add_f32 v241, v241, v211\n"
    "v_cvt_pk_bf16_f32 v195, v210, v211\n"
    "v_mfma_f32_16x16x32_bf16 v[212:215], a[144:147], a[32:35], v[212:215]\n"
    "buffer_load_dwordx4 v253, s[36:39], s60 offen offset:1024 lds\n"
    "v_exp_f32 v196, v196\n"
    "v_mfma_f32_16x16x32_bf16 v[32:35], v[204:207], a[192:195], v[32:35]\n"
    "ds_read_b128 a[128:131], v246 offset:16512\n"
    "v_add_f32 v242, v242, v196\n"
    "v_mfma_f32_16x16x32_bf16 v[32:35], v[192:195], a[160:163], v[32:35]\n"
    "buffer_load_dwordx4 v253, s[40:43], s60 offen offset:2048 lds\n"
    "v_exp_f32 v197, v197\n"
    "v_mfma_f32_16x16x32_bf16 v[36:39], v[192:195], a[164:167], v[36:39]\n"
    "ds_read_b128 a[132:135], v246 offset:16768\n"
    "v_add_f32 v242, v242, v197\n"
    "v_mfma_f32_16x16x32_bf16 v[36:39], v[204:207], a[196:199], v[36:39]\n"
    "v_exp_f32 v198, v198\n"
    "v_mfma_f32_16x16x32_bf16 v[40:43], v[204:207], a[200:203], v[40:43]\n"
    "ds_read_b128 a[136:139], v246 offset:17024\n"
    "v_add_f32 v242, v242, v198\n"
    "v_mfma_f32_16x16x32_bf16 v[40:43], v[192:195], a[168:171], v[40:43]\n"
    "buffer_load_dwordx4 v253, s[44:47], s60 offen offset:3080 lds\n"
    "v_exp_f32 v199, v199\n"
    "v_mfma_f32_16x16x32_bf16 v[44:47], v[192:195], a[172:175], v[44:47]\n"
    "s_add_u32 s60, s60, 2048\n"
    "s_mov_b32 m0, 8256\n"
    "ds_read_b128 a[140:143], v246 offset:17280\n"
    "v_add_f32 v242, v242, v199\n"
    "v_mfma_f32_16x16x32_bf16 v[44:47], v[204:207], a[244:247], v[44:47]\n"
    "buffer_load_dwordx4 v253, s[36:39], s60 offen offset:0 lds\n"
    "v_exp_f32 v212, v212\n"
    "v_mfma_f32_16x16x32_bf16 v[200:203], a[96:99], a[32:35], v[224:227]\n"
    "v_add_f32 v242, v242, v212\n"
    "v_cvt_pk_bf16_f32 v196, v196, v197\n"
    "v_mfma_f32_16x16x32_bf16 v[200:203], a[100:103], a[36:39], v[200:203]\n"
    "v_exp_f32 v213, v213\n"
    "v_mfma_f32_16x16x32_bf16 v[200:203], a[104:107], a[40:43], v[200:203]\n"
    "buffer_load_dwordx4 v253, s[36:39], s60 offen offset:1024 lds\n"
    "v_add_f32 v242, v242, v213\n"
    "v_cvt_pk_bf16_f32 v197, v198, v199\n"
    "v_mfma_f32_16x16x32_bf16 v[200:203], a[108:111], a[44:47], v[200:203]\n"
    "v_exp_f32 v214, v214\n"
    "v_mfma_f32_16x16x32_bf16 v[208:211], a[124:127], a[44:47], v[224:227]\n"
    "buffer_load_dwordx4 v253, s[40:43], s60 offen offset:2048 lds\n"
    "v_add_f32 v242, v242, v214\n"
    "v_cvt_pk_bf16_f32 v198, v212, v213\n"
    "v_mfma_f32_16x16x32_bf16 v[208:211], a[120:123], a[40:43], v[208:211]\n"
    "buffer_load_dwordx4 v253, s[44:47], s60 offen offset:3080 lds\n"
    "v_exp_f32 v215, v215\n"
    "v_mfma_f32_16x16x32_bf16 v[208:211], a[116:119], a[36:39], v[208:211]\n"
    "s_add_u32 s60, s60, 2048\n"
    "s_mov_b32 m0, 12384\n"
    "v_add_f32 v242, v242, v215\n"
    "v_cvt_pk_bf16_f32 v199, v214, v215\n"
    "v_mfma_f32_16x16x32_bf16 v[208:211], a[112:115], a[32:35], v[208:211]\n"
    "buffer_load_dwordx4 v253, s[36:39], s60 offen offset:0 lds\n"
    "v_exp_f32 v200, v200\n"
    "v_mfma_f32_16x16x32_bf16 v[48:51], v[204:207], a[80:83], v[48:51]\n"
    "ds_read_b128 a[156:159], v246 offset:25536\n"
    "v_add_f32 v242, v242, v200\n"
    "v_mfma_f32_16x16x32_bf16 v[48:51], v[192:195], a[176:179], v[48:51]\n"
    "ds_read_b64_tr_b16 v[168:169], v164 offset:0\n"
    "ds_read_b64_tr_b16 v[170:171], v164 offset:1024\n"
    "v_exp_f32 v201, v201\n"
    "v_mfma_f32_16x16x32_bf16 v[52:55], v[192:195], a[180:183], v[52:55]\n"
    "ds_read_b128 a[152:155], v246 offset:25280\n"
    "v_add_f32 v242, v242, v201\n"
    "v_mfma_f32_16x16x32_bf16 v[52:55], v[204:207], a[84:87], v[52:55]\n"
    "ds_read_b64_tr_b16 v[172:173], v164 offset:2048\n"
    "ds_read_b64_tr_b16 v[174:175], v164 offset:3072\n"
    "v_exp_f32 v202, v202\n"
    "v_mfma_f32_16x16x32_bf16 v[56:59], v[204:207], a[88:91], v[56:59]\n"
    "ds_read_b128 a[148:151], v246 offset:25024\n"
    "v_add_f32 v242, v242, v202\n"
    "v_mfma_f32_16x16x32_bf16 v[56:59], v[192:195], a[184:187], v[56:59]\n"
    "ds_read_b64_tr_b16 v[176:177], v164 offset:8\n"
    "ds_read_b64_tr_b16 v[178:179], v164 offset:1032\n"
    "v_exp_f32 v203, v203\n"
    "v_mfma_f32_16x16x32_bf16 v[60:63], v[192:195], a[188:191], v[60:63]\n"
    "ds_read_b128 a[144:147], v246 offset:24768\n"
    "v_add_f32 v242, v242, v203\n"
    "v_mfma_f32_16x16x32_bf16 v[60:63], v[204:207], a[92:95], v[60:63]\n"
    "ds_read_b64_tr_b16 v[180:181], v164 offset:2056\n"
    "ds_read_b64_tr_b16 v[182:183], v164 offset:3080\n"
    "v_exp_f32 v208, v208\n"
    "v_mfma_f32_16x16x32_bf16 v[204:207], a[96:99], a[48:51], v[228:231]\n"
    "v_add_f32 v242, v242, v208\n"
    "v_cvt_pk_bf16_f32 v200, v200, v201\n"
    "v_add_u32 v164, 4128, v164\n"
    "v_mfma_f32_16x16x32_bf16 v[204:207], a[100:103], a[52:55], v[204:207]\n"
    "v_exp_f32 v209, v209\n"
    "v_mfma_f32_16x16x32_bf16 v[204:207], a[104:107], a[56:59], v[204:207]\n"
    "buffer_load_dwordx4 v253, s[36:39], s60 offen offset:1024 lds\n"
    "v_add_f32 v242, v242, v209\n"
    "v_cvt_pk_bf16_f32 v201, v202, v203\n"
    "v_mfma_f32_16x16x32_bf16 v[204:207], a[108:111], a[60:63], v[204:207]\n"
    "v_exp_f32 v210, v210\n"
    "v_mfma_f32_16x16x32_bf16 v[212:215], a[124:127], a[60:63], v[228:231]\n"
    "buffer_load_dwordx4 v253, s[40:43], s60 offen offset:2048 lds\n"
    "v_add_f32 v242, v242, v210\n"
    "v_cvt_pk_bf16_f32 v202, v208, v209\n"
    "v_mfma_f32_16x16x32_bf16 v[212:215], a[120:123], a[56:59], v[212:215]\n"
    "buffer_load_dwordx4 v253, s[44:47], s60 offen offset:3080 lds\n"
    "v_exp_f32 v211, v211\n"
    "v_mfma_f32_16x16x32_bf16 v[212:215], a[116:119], a[52:55], v[212:215]\n"
    "s_add_u32 s60, s60, 2048\n"
    "v_add_f32 v242, v242, v211\n"
    "v_cvt_pk_bf16_f32 v203, v210, v211\n"
    "v_mfma_f32_16x16x32_bf16 v[212:215], a[112:115], a[48:51], v[212:215]\n"
    "v_exp_f32 v204, v204\n"
    "v_mfma_f32_16x16x32_bf16 v[64:67], v[196:199], a[192:195], v[64:67]\n"
    "ds_read_b64_tr_b16 a[212:213], v252 offset:16520\n"
    "v_add_f32 v243, v243, v204\n"
    "v_mfma_f32_16x16x32_bf16 v[64:67], v[200:203], a[160:163], v[64:67]\n"
    "s_waitcnt lgkmcnt(1)\n"
    "buffer_store_dwordx4 v[168:171], v249, s[56:59], s64 offen offset:0 sc1\n"
    "v_exp_f32 v205, v205\n"
    "v_mfma_f32_16x16x32_bf16 v[68:71], v[200:203], a[164:167], v[68:71]\n"
    "ds_read_b64_tr_b16 a[214:215], v252 offset:24776\n"
    "v_add_f32 v243, v243, v205\n"
    "v_mfma_f32_16x16x32_bf16 v[68:71], v[196:199], a[196:199], v[68:71]\n"
    "v_exp_f32 v206, v206\n"
    "v_mfma_f32_16x16x32_bf16 v[72:75], v[196:199], a[200:203], v[72:75]\n"
    "ds_read_b64_tr_b16 a[216:217], v254 offset:16512\n"
    "v_add_f32 v243, v243, v206\n"
    "v_mfma_f32_16x16x32_bf16 v[72:75], v[200:203], a[168:171], v[72:75]\n"
    "ds_read_b64_tr_b16 a[202:203], v251 offset:24768\n"
    "v_exp_f32 v207, v207\n"
    "v_mfma_f32_16x16x32_bf16 v[76:79], v[200:203], a[172:175], v[76:79]\n"
    "ds_read_b64_tr_b16 a[218:219], v254 offset:24768\n"
    "v_add_f32 v243, v243, v207\n"
    "v_mfma_f32_16x16x32_bf16 v[76:79], v[196:199], a[244:247], v[76:79]\n"
    "buffer_store_dwordx4 v[172:175], v249, s[56:59], s64 offen offset:256 sc1\n"
    "v_exp_f32 v212, v212\n"
    "v_mfma_f32_16x16x32_bf16 v[192:195], a[128:131], a[48:51], v[228:231]\n"
    "v_add_f32 v243, v243, v212\n"
    "v_cvt_pk_bf16_f32 v204, v204, v205\n"
    "v_mfma_f32_16x16x32_bf16 v[192:195], a[132:135], a[52:55], v[192:195]\n"
    "v_exp_f32 v213, v213\n"
    "v_mfma_f32_16x16x32_bf16 v[192:195], a[136:139], a[56:59], v[192:195]\n"
    "ds_read_b64_tr_b16 a[196:197], v247 offset:16520\n"
    "v_add_f32 v243, v243, v213\n"
    "v_cvt_pk_bf16_f32 v205, v206, v207\n"
    "v_mfma_f32_16x16x32_bf16 v[192:195], a[140:143], a[60:63], v[192:195]\n"
    "ds_read_b64_tr_b16 a[198:199], v247 offset:24776\n"
    "v_exp_f32 v214, v214\n"
    "v_mfma_f32_16x16x32_bf16 v[208:211], a[156:159], a[60:63], v[228:231]\n"
    "ds_read_b64_tr_b16 a[200:201], v251 offset:16512\n"
    "v_add_f32 v243, v243, v214\n"
    "v_cvt_pk_bf16_f32 v206, v212, v213\n"
    "v_mfma_f32_16x16x32_bf16 v[208:211], a[152:155], a[56:59], v[208:211]\n"
    "v_exp_f32 v215, v215\n"
    "v_mfma_f32_16x16x32_bf16 v[208:211], a[148:151], a[52:55], v[208:211]\n"
    "v_add_f32 v243, v243, v215\n"
    "v_cvt_pk_bf16_f32 v207, v214, v215\n"
    "v_mfma_f32_16x16x32_bf16 v[208:211], a[144:147], a[48:51], v[208:211]\n"
    "buffer_store_dwordx4 v[176:179], v249, s[56:59], s64 offen offset:512 sc1\n"
    "v_exp_f32 v192, v192\n"
    "v_mfma_f32_16x16x32_bf16 v[80:83], v[196:199], a[80:83], v[80:83]\n"
    "ds_read_b64_tr_b16 a[220:221], v254 offset:16520\n"
    "v_add_f32 v243, v243, v192\n"
    "v_mfma_f32_16x16x32_bf16 v[80:83], v[200:203], a[176:179], v[80:83]\n"
    "v_exp_f32 v193, v193\n"
    "v_mfma_f32_16x16x32_bf16 v[84:87], v[200:203], a[180:183], v[84:87]\n"
    "ds_read_b64_tr_b16 a[222:223], v254 offset:24776\n"
    "v_add_f32 v243, v243, v193\n"
    "v_mfma_f32_16x16x32_bf16 v[84:87], v[196:199], a[84:87], v[84:87]\n"
    "buffer_store_dwordx4 v[180:183], v249, s[56:59], s64 offen offset:768 sc1\n"
    "v_exp_f32_e64 v194, v194\n"
    "v_mfma_f32_16x16x32_bf16 v[88:91], v[196:199], a[88:91], v[88:91]\n"
    "s_add_u32 s64, s64, 4096\n"
    "s_add_u32 s87, s60, 24576\n"
    "ds_read_b64_tr_b16 a[192:193], v247 offset:16512\n"
    "v_add_f32 v243, v243, v194\n"
    "v_mfma_f32_16x16x32_bf16 v[88:91], v[200:203], a[184:187], v[88:91]\n"
    "s_sub_u32 s70, s62, 17\n"
    "v_exp_f32 v195, v195\n"
    "v_mfma_f32_16x16x32_bf16 v[92:95], v[200:203], a[188:191], v[92:95]\n"
    "s_cmp_lt_u32 s70, 3\n"
    "ds_read_b64_tr_b16 a[194:195], v247 offset:24768\n"
    "v_add_f32 v243, v243, v195\n"
    "v_mfma_f32_16x16x32_bf16 v[92:95], v[196:199], a[92:95], v[92:95]\n"
    "s_cselect_b32 s87, s87, s79\n"
    "v_exp_f32 v208, v208\n"
    "v_mfma_f32_16x16x32_bf16 v[196:199], a[96:99], a[64:67], v[232:235]\n"
    "v_add_f32 v243, v243, v208\n"
    "v_cvt_pk_bf16_f32 v192, v192, v193\n"
    "v_mfma_f32_16x16x32_bf16 v[196:199], a[100:103], a[68:71], v[196:199]\n"
    "v_exp_f32 v209, v209\n"
    "v_mfma_f32_16x16x32_bf16 v[196:199], a[104:107], a[72:75], v[196:199]\n"
    "buffer_load_dword v161, v165, s[88:91], s87 offen\n"
    "v_add_f32 v243, v243, v209\n"
    "v_cvt_pk_bf16_f32 v193, v194, v195\n"
    "v_mfma_f32_16x16x32_bf16 v[196:199], a[108:111], a[76:79], v[196:199]\n"
    "v_exp_f32 v210, v210\n"
    "v_mfma_f32_16x16x32_bf16 v[212:215], a[124:127], a[76:79], v[232:235]\n"
    "v_add_f32 v243, v243, v210\n"
    "v_cvt_pk_bf16_f32 v194, v208, v209\n"
    "v_mfma_f32_16x16x32_bf16 v[212:215], a[120:123], a[72:75], v[212:215]\n"
    "v_exp_f32 v211, v211\n"
    "v_mfma_f32_16x16x32_bf16 v[212:215], a[116:119], a[68:71], v[212:215]\n"
    "s_waitcnt vmcnt(48) lgkmcnt(0)\n"
    "v_add_f32 v243, v243, v211\n"
    "v_cvt_pk_bf16_f32 v195, v210, v211\n"
    "v_mfma_f32_16x16x32_bf16 v[212:215], a[112:115], a[64:67], v[212:215]\n"
    "s_barrier\n"
    "v_exp_f32 v196, v196\n"
    "v_mfma_f32_16x16x32_bf16 v[96:99], v[204:207], a[160:163], v[96:99]\n"
    "s_cmp_eq_u32 s62, 23\n"
    "ds_read_b128 a[96:99], v246 offset:33024\n"
    "v_add_f32 v244, v244, v196\n"
    "v_mfma_f32_16x16x32_bf16 v[96:99], v[192:195], a[192:195], v[96:99]\n"
    "s_cselect_b32 s60, s61, s60\n"
    "v_exp_f32 v197, v197\n"
    "v_mfma_f32_16x16x32_bf16 v[100:103], v[192:195], a[196:199], v[100:103]\n"
    "s_cselect_b32 s61, s79, s61\n"
    "ds_read_b128 a[100:103], v246 offset:33280\n"
    "v_add_f32 v244, v244, v197\n"
    "v_mfma_f32_16x16x32_bf16 v[100:103], v[204:207], a[164:167], v[100:103]\n"
    "s_cselect_b32 s62, 0, s62\n"
    "v_exp_f32 v198, v198\n"
    "v_mfma_f32_16x16x32_bf16 v[104:107], v[204:207], a[168:171], v[104:107]\n"
    "s_mov_b32 m0, 16512\n"
    "ds_read_b128 a[104:107], v246 offset:33536\n"
    "v_add_f32 v244, v244, v198\n"
    "v_mfma_f32_16x16x32_bf16 v[104:107], v[192:195], a[200:203], v[104:107]\n"
    "buffer_load_dwordx4 v253, s[36:39], s60 offen offset:0 lds\n"
    "v_exp_f32 v199, v199\n"
    "v_mfma_f32_16x16x32_bf16 v[108:111], v[192:195], a[204:207], v[108:111]\n"
    "s_add_u32 s62, s62, 1\n"
    "ds_read_b128 a[108:111], v246 offset:33792\n"
    "v_add_f32 v244, v244, v199\n"
    "v_mfma_f32_16x16x32_bf16 v[108:111], v[204:207], a[172:175], v[108:111]\n"
    "buffer_load_dwordx4 v253, s[36:39], s60 offen offset:1024 lds\n"
    "v_exp_f32 v212, v212\n"
    "v_mfma_f32_16x16x32_bf16 v[200:203], a[128:131], a[64:67], v[232:235]\n"
    "s_sub_u32 s63, s63, 1\n"
    "v_add_f32 v244, v244, v212\n"
    "v_cvt_pk_bf16_f32 v196, v196, v197\n"
    "v_mfma_f32_16x16x32_bf16 v[200:203], a[132:135], a[68:71], v[200:203]\n"
    "s_branch LOOP2_B\n"
    "LTAIL_B:\n"
    "v_exp_f32 v213, v213\n"
    "v_mfma_f32_16x16x32_bf16 v[200:203], a[104:107], a[24:27], v[200:203]\n"
    "v_add_f32 v241, v241, v213\n"
    "v_cvt_pk_bf16_f32 v197, v198, v199\n"
    "v_mfma_f32_16x16x32_bf16 v[200:203], a[108:111], a[28:31], v[200:203]\n"
    "v_exp_f32 v214, v214\n"
    "v_mfma_f32_16x16x32_bf16 v[208:211], a[124:127], a[28:31], v[220:223]\n"
    "v_add_f32 v241, v241, v214\n"
    "v_cvt_pk_bf16_f32 v198, v212, v213\n"
    "v_mfma_f32_16x16x32_bf16 v[208:211], a[120:123], a[24:27], v[208:211]\n"
    "v_exp_f32 v215, v215\n"
    "v_mfma_f32_16x16x32_bf16 v[208:211], a[116:119], a[20:23], v[208:211]\n"
    "s_waitcnt vmcnt(48) lgkmcnt(0)\n"
    "v_add_f32 v241, v241, v215\n"
    "v_cvt_pk_bf16_f32 v199, v214, v215\n"
    "v_mfma_f32_16x16x32_bf16 v[208:211], a[112:115], a[16:19], v[208:211]\n"
    "s_barrier\n"
    "v_exp_f32 v200, v200\n"
    "v_mfma_f32_16x16x32_bf16 v[16:19], v[204:207], a[208:211], v[16:19]\n"
    "ds_read_b64_tr_b16 a[244:245], v251 offset:49544\n"
    "v_add_f32 v241, v241, v200\n"
    "v_mfma_f32_16x16x32_bf16 v[16:19], v[192:195], a[228:231], v[16:19]\n"
    "v_exp_f32 v201, v201\n"
    "v_mfma_f32_16x16x32_bf16 v[20:23], v[192:195], a[232:235], v[20:23]\n"
    "ds_read_b64_tr_b16 a[246:247], v251 offset:57800\n"
    "v_add_f32 v241, v241, v201\n"
    "v_mfma_f32_16x16x32_bf16 v[20:23], v[204:207], a[212:215], v[20:23]\n"
    "v_exp_f32 v202, v202\n"
    "v_mfma_f32_16x16x32_bf16 v[24:27], v[204:207], a[216:219], v[24:27]\n"
    "ds_read_b64_tr_b16 a[80:81], v252 offset:49536\n"
    "v_add_f32 v241, v241, v202\n"
    "v_mfma_f32_16x16x32_bf16 v[24:27], v[192:195], a[236:239], v[24:27]\n"
    "v_exp_f32 v203, v203\n"
    "v_mfma_f32_16x16x32_bf16 v[28:31], v[192:195], a[240:243], v[28:31]\n"
    "ds_read_b64_tr_b16 a[82:83], v252 offset:57792\n"
    "v_add_f32 v241, v241, v203\n"
    "v_mfma_f32_16x16x32_bf16 v[28:31], v[204:207], a[220:223], v[28:31]\n"
    "v_exp_f32 v208, v208\n"
    "v_mfma_f32_16x16x32_bf16 v[204:207], a[128:131], a[32:35], v[224:227]\n"
    "v_add_f32 v241, v241, v208\n"
    "v_cvt_pk_bf16_f32 v200, v200, v201\n"
    "v_mfma_f32_16x16x32_bf16 v[204:207], a[132:135], a[36:39], v[204:207]\n"
    "v_exp_f32 v209, v209\n"
    "v_mfma_f32_16x16x32_bf16 v[204:207], a[136:139], a[40:43], v[204:207]\n"
    "v_add_f32 v241, v241, v209\n"
    "v_cvt_pk_bf16_f32 v201, v202, v203\n"
    "v_mfma_f32_16x16x32_bf16 v[204:207], a[140:143], a[44:47], v[204:207]\n"
    "v_exp_f32 v210, v210\n"
    "v_mfma_f32_16x16x32_bf16 v[212:215], a[156:159], a[44:47], v[224:227]\n"
    "v_add_f32 v241, v241, v210\n"
    "v_cvt_pk_bf16_f32 v202, v208, v209\n"
    "v_mfma_f32_16x16x32_bf16 v[212:215], a[152:155], a[40:43], v[212:215]\n"
    "v_exp_f32 v211, v211\n"
    "v_mfma_f32_16x16x32_bf16 v[212:215], a[148:151], a[36:39], v[212:215]\n"
    "v_add_f32 v241, v241, v211\n"
    "v_cvt_pk_bf16_f32 v203, v210, v211\n"
    "v_mfma_f32_16x16x32_bf16 v[212:215], a[144:147], a[32:35], v[212:215]\n"
    "v_exp_f32 v204, v204\n"
    "v_mfma_f32_16x16x32_bf16 v[32:35], v[196:199], a[192:195], v[32:35]\n"
    "ds_read_b128 a[128:131], v246 offset:49536\n"
    "v_add_f32 v242, v242, v204\n"
    "v_mfma_f32_16x16x32_bf16 v[32:35], v[200:203], a[160:163], v[32:35]\n"
    "v_exp_f32 v205, v205\n"
    "v_mfma_f32_16x16x32_bf16 v[36:39], v[200:203], a[164:167], v[36:39]\n"
    "ds_read_b128 a[132:135], v246 offset:49792\n"
    "v_add_f32 v242, v242, v205\n"
    "v_mfma_f32_16x16x32_bf16 v[36:39], v[196:199], a[196:199], v[36:39]\n"
    "v_exp_f32 v206, v206\n"
    "v_mfma_f32_16x16x32_bf16 v[40:43], v[196:199], a[200:203], v[40:43]\n"
    "ds_read_b128 a[136:139], v246 offset:50048\n"
    "v_add_f32 v242, v242, v206\n"
    "v_mfma_f32_16x16x32_bf16 v[40:43], v[200:203], a[168:171], v[40:43]\n"
    "v_exp_f32 v207, v207\n"
    "v_mfma_f32_16x16x32_bf16 v[44:47], v[200:203], a[224:227], v[44:47]\n"
    "ds_read_b128 a[140:143], v246 offset:50304\n"
    "v_add_f32 v242, v242, v207\n"
    "v_mfma_f32_16x16x32_bf16 v[44:47], v[196:199], a[204:207], v[44:47]\n"
    "v_exp_f32 v212, v212\n"
    "v_mfma_f32_16x16x32_bf16 v[192:195], a[96:99], a[32:35], v[224:227]\n"
    "v_add_f32 v242, v242, v212\n"
    "v_cvt_pk_bf16_f32 v204, v204, v205\n"
    "v_mfma_f32_16x16x32_bf16 v[192:195], a[100:103], a[36:39], v[192:195]\n"
    "v_exp_f32 v213, v213\n"
    "v_mfma_f32_16x16x32_bf16 v[192:195], a[104:107], a[40:43], v[192:195]\n"
    "v_add_f32 v242, v242, v213\n"
    "v_cvt_pk_bf16_f32 v205, v206, v207\n"
    "v_mfma_f32_16x16x32_bf16 v[192:195], a[108:111], a[44:47], v[192:195]\n"
    "v_exp_f32 v214, v214\n"
    "v_mfma_f32_16x16x32_bf16 v[208:211], a[124:127], a[44:47], v[224:227]\n"
    "v_add_f32 v242, v242, v214\n"
    "v_cvt_pk_bf16_f32 v206, v212, v213\n"
    "v_mfma_f32_16x16x32_bf16 v[208:211], a[120:123], a[40:43], v[208:211]\n"
    "v_exp_f32 v215, v215\n"
    "v_mfma_f32_16x16x32_bf16 v[208:211], a[116:119], a[36:39], v[208:211]\n"
    "v_add_f32 v242, v242, v215\n"
    "v_cvt_pk_bf16_f32 v207, v214, v215\n"
    "v_mfma_f32_16x16x32_bf16 v[208:211], a[112:115], a[32:35], v[208:211]\n"
    "v_exp_f32 v192, v192\n"
    "v_mfma_f32_16x16x32_bf16 v[48:51], v[196:199], a[208:211], v[48:51]\n"
    "ds_read_b128 a[156:159], v246 offset:58560\n"
    "v_add_f32 v242, v242, v192\n"
    "v_mfma_f32_16x16x32_bf16 v[48:51], v[200:203], a[228:231], v[48:51]\n"
    "v_exp_f32 v193, v193\n"
    "v_mfma_f32_16x16x32_bf16 v[52:55], v[200:203], a[232:235], v[52:55]\n"
    "ds_read_b128 a[152:155], v246 offset:58304\n"
    "v_add_f32 v242, v242, v193\n"
    "v_mfma_f32_16x16x32_bf16 v[52:55], v[196:199], a[212:215], v[52:55]\n"
    "v_exp_f32 v194, v194\n"
    "v_mfma_f32_16x16x32_bf16 v[56:59], v[196:199], a[216:219], v[56:59]\n"
    "ds_read_b128 a[148:151], v246 offset:58048\n"
    "v_add_f32 v242, v242, v194\n"
    "v_mfma_f32_16x16x32_bf16 v[56:59], v[200:203], a[236:239], v[56:59]\n"
    "v_exp_f32 v195, v195\n"
    "v_mfma_f32_16x16x32_bf16 v[60:63], v[200:203], a[240:243], v[60:63]\n"
    "ds_read_b128 a[144:147], v246 offset:57792\n"
    "v_add_f32 v242, v242, v195\n"
    "v_mfma_f32_16x16x32_bf16 v[60:63], v[196:199], a[220:223], v[60:63]\n"
    "v_exp_f32 v208, v208\n"
    "v_mfma_f32_16x16x32_bf16 v[196:199], a[96:99], a[48:51], v[228:231]\n"
    "v_add_f32 v242, v242, v208\n"
    "v_cvt_pk_bf16_f32 v192, v192, v193\n"
    "v_mfma_f32_16x16x32_bf16 v[196:199], a[100:103], a[52:55], v[196:199]\n"
    "v_exp_f32 v209, v209\n"
    "v_mfma_f32_16x16x32_bf16 v[196:199], a[104:107], a[56:59], v[196:199]\n"
    "v_add_f32 v242, v242, v209\n"
    "v_cvt_pk_bf16_f32 v193, v194, v195\n"
    "v_mfma_f32_16x16x32_bf16 v[196:199], a[108:111], a[60:63], v[196:199]\n"
    "v_exp_f32 v210, v210\n"
    "v_mfma_f32_16x16x32_bf16 v[212:215], a[124:127], a[60:63], v[228:231]\n"
    "v_add_f32 v242, v242, v210\n"
    "v_cvt_pk_bf16_f32 v194, v208, v209\n"
    "v_mfma_f32_16x16x32_bf16 v[212:215], a[120:123], a[56:59], v[212:215]\n"
    "ds_read_b64_tr_b16 a[84:85], v252 offset:49544\n"
    "v_exp_f32 v211, v211\n"
    "v_mfma_f32_16x16x32_bf16 v[212:215], a[116:119], a[52:55], v[212:215]\n"
    "ds_read_b64_tr_b16 a[88:89], v254 offset:49536\n"
    "v_add_f32 v242, v242, v211\n"
    "v_cvt_pk_bf16_f32 v195, v210, v211\n"
    "v_mfma_f32_16x16x32_bf16 v[212:215], a[112:115], a[48:51], v[212:215]\n"
    "v_exp_f32 v196, v196\n"
    "v_mfma_f32_16x16x32_bf16 v[64:67], v[204:207], a[192:195], v[64:67]\n"
    "ds_read_b64_tr_b16 a[192:193], v247 offset:49536\n"
    "v_add_f32 v243, v243, v196\n"
    "v_mfma_f32_16x16x32_bf16 v[64:67], v[192:195], a[160:163], v[64:67]\n"
    "v_exp_f32 v197, v197\n"
    "v_mfma_f32_16x16x32_bf16 v[68:71], v[192:195], a[164:167], v[68:71]\n"
    "ds_read_b64_tr_b16 a[194:195], v247 offset:57792\n"
    "v_add_f32 v243, v243, v197\n"
    "v_mfma_f32_16x16x32_bf16 v[68:71], v[204:207], a[196:199], v[68:71]\n"
    "v_exp_f32 v198, v198\n"
    "v_mfma_f32_16x16x32_bf16 v[72:75], v[204:207], a[200:203], v[72:75]\n"
    "ds_read_b64_tr_b16 a[196:197], v247 offset:49544\n"
    "v_add_f32 v243, v243, v198\n"
    "v_mfma_f32_16x16x32_bf16 v[72:75], v[192:195], a[168:171], v[72:75]\n"
    "ds_read_b64_tr_b16 a[92:93], v254 offset:49544\n"
    "v_exp_f32 v199, v199\n"
    "v_mfma_f32_16x16x32_bf16 v[76:79], v[192:195], a[224:227], v[76:79]\n"
    "ds_read_b64_tr_b16 a[198:199], v247 offset:57800\n"
    "v_add_f32 v243, v243, v199\n"
    "v_mfma_f32_16x16x32_bf16 v[76:79], v[204:207], a[204:207], v[76:79]\n"
    "v_exp_f32 v212, v212\n"
    "v_mfma_f32_16x16x32_bf16 v[200:203], a[96:99], a[64:67], v[232:235]\n"
    "v_add_f32 v243, v243, v212\n"
    "v_cvt_pk_bf16_f32 v196, v196, v197\n"
    "v_mfma_f32_16x16x32_bf16 v[200:203], a[100:103], a[68:71], v[200:203]\n"
    "v_exp_f32 v213, v213\n"
    "v_mfma_f32_16x16x32_bf16 v[200:203], a[104:107], a[72:75], v[200:203]\n"
    "ds_read_b64_tr_b16 a[200:201], v251 offset:49536\n"
    "v_add_f32 v243, v243, v213\n"
    "v_cvt_pk_bf16_f32 v197, v198, v199\n"
    "v_mfma_f32_16x16x32_bf16 v[200:203], a[108:111], a[76:79], v[200:203]\n"
    "ds_read_b64_tr_b16 a[202:203], v251 offset:57792\n"
    "v_exp_f32 v214, v214\n"
    "v_mfma_f32_16x16x32_bf16 v[208:211], a[124:127], a[76:79], v[232:235]\n"
    "ds_read_b64_tr_b16 a[90:91], v254 offset:57792\n"
    "v_add_f32 v243, v243, v214\n"
    "v_cvt_pk_bf16_f32 v198, v212, v213\n"
    "v_mfma_f32_16x16x32_bf16 v[208:211], a[120:123], a[72:75], v[208:211]\n"
    "v_exp_f32 v215, v215\n"
    "v_mfma_f32_16x16x32_bf16 v[208:211], a[116:119], a[68:71], v[208:211]\n"
    "s_waitcnt vmcnt(5) lgkmcnt(0)\n"
    "v_add_f32 v243, v243, v215\n"
    "v_cvt_pk_bf16_f32 v199, v214, v215\n"
    "v_mfma_f32_16x16x32_bf16 v[208:211], a[112:115], a[64:67], v[208:211]\n"
    "s_barrier\n"
    "v_exp_f32 v200, v200\n"
    "v_mfma_f32_16x16x32_bf16 v[80:83], v[204:207], a[208:211], v[80:83]\n"
    "ds_read_b128 a[96:99], v246 offset:0\n"
    "v_add_f32 v244, v244, v200\n"
    "v_mfma_f32_16x16x32_bf16 v[80:83], v[192:195], a[228:231], v[80:83]\n"
    "ds_read_b64_tr_b16 a[190:191], v254 offset:8264\n"
    "v_exp_f32 v201, v201\n"
    "v_mfma_f32_16x16x32_bf16 v[84:87], v[192:195], a[232:235], v[84:87]\n"
    "ds_read_b128 a[100:103], v246 offset:256\n"
    "v_add_f32 v244, v244, v201\n"
    "v_mfma_f32_16x16x32_bf16 v[84:87], v[204:207], a[212:215], v[84:87]\n"
    "v_exp_f32 v202, v202\n"
    "v_mfma_f32_16x16x32_bf16 v[88:91], v[204:207], a[216:219], v[88:91]\n"
    "ds_read_b128 a[104:107], v246 offset:512\n"
    "v_add_f32 v244, v244, v202\n"
    "v_mfma_f32_16x16x32_bf16 v[88:91], v[192:195], a[236:239], v[88:91]\n"
    "ds_read_b64_tr_b16 a[94:95], v254 offset:57800\n"
    "v_exp_f32 v203, v203\n"
    "v_mfma_f32_16x16x32_bf16 v[92:95], v[192:195], a[240:243], v[92:95]\n"
    "ds_read_b128 a[108:111], v246 offset:768\n"
    "v_add_f32 v244, v244, v203\n"
    "v_mfma_f32_16x16x32_bf16 v[92:95], v[204:207], a[220:223], v[92:95]\n"
    "ds_read_b64_tr_b16 a[218:219], v254 offset:24768\n"
    "v_exp_f32 v208, v208\n"
    "s_mov_b64 exec, 0\n"
    "s_branch LPG16\n"
    "LWRET4:\n"
    "s_mov_b64 exec, -1\n"
    "v_mfma_f32_16x16x32_bf16 v[204:207], a[128:131], a[0:3], v[216:219]\n"
    "v_add_f32 v244, v244, v208\n"
    "v_cvt_pk_bf16_f32 v200, v200, v201\n"
    "v_mfma_f32_16x16x32_bf16 v[204:207], a[132:135], a[4:7], v[204:207]\n"
    "v_exp_f32 v209, v209\n"
    "v_mfma_f32_16x16x32_bf16 v[204:207], a[136:139], a[8:11], v[204:207]\n"
    "ds_read_b64_tr_b16 a[174:175], v251 offset:8264\n"
    "v_add_f32 v244, v244, v209\n"
    "v_cvt_pk_bf16_f32 v201, v202, v203\n"
    "v_mfma_f32_16x16x32_bf16 v[204:207], a[140:143], a[12:15], v[204:207]\n"
    "ds_read_b128 a[140:143], v246 offset:17280\n"
    "v_exp_f32 v210, v210\n"
    "v_mfma_f32_16x16x32_bf16 v[212:215], a[156:159], a[12:15], v[216:219]\n"
    "ds_read_b64_tr_b16 a[204:205], v251 offset:16520\n"
    "v_add_f32 v244, v244, v210\n"
    "v_cvt_pk_bf16_f32 v202, v208, v209\n"
    "v_mfma_f32_16x16x32_bf16 v[212:215], a[152:155], a[8:11], v[212:215]\n"
    "global_load_dword v161, v160, s[82:83]\n"
    "v_exp_f32 v211, v211\n"
    "v_mfma_f32_16x16x32_bf16 v[212:215], a[148:151], a[4:7], v[212:215]\n"
    "ds_read_b64_tr_b16 a[220:221], v254 offset:16520\n"
    "v_add_f32 v244, v244, v211\n"
    "v_cvt_pk_bf16_f32 v203, v210, v211\n"
    "v_mfma_f32_16x16x32_bf16 v[212:215], a[144:147], a[0:3], v[212:215]\n"
    "ds_read_b128 a[144:147], v246 offset:24768\n"
    "v_exp_f32 v204, v204\n"
    "v_mfma_f32_16x16x32_bf16 v[96:99], v[196:199], a[160:163], v[96:99]\n"
    "ds_read_b128 a[124:127], v246 offset:9024\n"
    "v_add_f32 v240, v240, v204\n"
    "v_mfma_f32_16x16x32_bf16 v[100:103], v[196:199], a[164:167], v[100:103]\n"
    "v_exp_f32 v205, v205\n"
    "v_mfma_f32_16x16x32_bf16 v[104:107], v[196:199], a[168:171], v[104:107]\n"
    "ds_read_b128 a[120:123], v246 offset:8768\n"
    "v_add_f32 v240, v240, v205\n"
    "v_mfma_f32_16x16x32_bf16 v[108:111], v[196:199], a[224:227], v[108:111]\n"
    "v_exp_f32 v206, v206\n"
    "v_mfma_f32_16x16x32_bf16 v[112:115], v[196:199], a[228:231], v[112:115]\n"
    "ds_read_b128 a[116:119], v246 offset:8512\n"
    "v_add_f32 v240, v240, v206\n"
    "v_mfma_f32_16x16x32_bf16 v[116:119], v[196:199], a[232:235], v[116:119]\n"
    "ds_read_b64_tr_b16 a[206:207], v251 offset:24776\n"
    "v_exp_f32 v207, v207\n"
    "v_mfma_f32_16x16x32_bf16 v[120:123], v[196:199], a[236:239], v[120:123]\n"
    "ds_read_b128 a[112:115], v246 offset:8256\n"
    "v_add_f32 v240, v240, v207\n"
    "v_mfma_f32_16x16x32_bf16 v[124:127], v[196:199], a[240:243], v[124:127]\n"
    "s_waitcnt lgkmcnt(4)\n"
    "ds_read_b64_tr_b16 a[222:223], v254 offset:24776\n"
    "v_exp_f32 v212, v212\n"
    "v_mfma_f32_16x16x32_bf16 v[192:195], a[96:99], a[0:3], v[216:219]\n"
    "v_add_f32 v240, v240, v212\n"
    "v_cvt_pk_bf16_f32 v204, v204, v205\n"
    "v_mfma_f32_16x16x32_bf16 v[192:195], a[100:103], a[4:7], v[192:195]\n"
    "v_exp_f32 v213, v213\n"
    "v_mfma_f32_16x16x32_bf16 v[192:195], a[104:107], a[8:11], v[192:195]\n"
    "ds_read_b128 a[152:155], v246 offset:25280\n"
    "v_add_f32 v240, v240, v213\n"
    "v_cvt_pk_bf16_f32 v205, v206, v207\n"
    "v_mfma_f32_16x16x32_bf16 v[192:195], a[108:111], a[12:15], v[192:195]\n"
    "ds_read_b128 a[148:151], v246 offset:25024\n"
    "v_exp_f32 v214, v214\n"
    "v_mfma_f32_16x16x32_bf16 v[208:211], a[124:127], a[12:15], v[216:219]\n"
    "s_waitcnt lgkmcnt(3)\n"
    "ds_read_b64_tr_b16 a[184:185], v254 offset:0\n"
    "v_add_f32 v240, v240, v214\n"
    "v_cvt_pk_bf16_f32 v206, v212, v213\n"
    "v_mfma_f32_16x16x32_bf16 v[208:211], a[120:123], a[8:11], v[208:211]\n"
    "v_exp_f32 v215, v215\n"
    "v_mfma_f32_16x16x32_bf16 v[208:211], a[116:119], a[4:7], v[208:211]\n"
    "v_add_f32 v240, v240, v215\n"
    "v_cvt_pk_bf16_f32 v207, v214, v215\n"
    "v_mfma_f32_16x16x32_bf16 v[208:211], a[112:115], a[0:3], v[208:211]\n"
    "v_exp_f32 v192, v192\n"
    "v_mfma_f32_16x16x32_bf16 v[128:131], v[200:203], a[160:163], v[128:131]\n"
    "ds_read_b128 a[128:131], v246 offset:16512\n"
    "v_add_f32 v240, v240, v192\n"
    "v_mfma_f32_16x16x32_bf16 v[132:135], v[200:203], a[164:167], v[132:135]\n"
    "ds_read_b64_tr_b16 a[166:167], v247 offset:8264\n"
    "v_exp_f32 v193, v193\n"
    "v_mfma_f32_16x16x32_bf16 v[136:139], v[200:203], a[168:171], v[136:139]\n"
    "ds_read_b128 a[132:135], v246 offset:16768\n"
    "v_add_f32 v240, v240, v193\n"
    "v_mfma_f32_16x16x32_bf16 v[140:143], v[200:203], a[224:227], v[140:143]\n"
    "v_exp_f32 v194, v194\n"
    "v_mfma_f32_16x16x32_bf16 v[144:147], v[200:203], a[228:231], v[144:147]\n"
    "ds_read_b128 a[136:139], v246 offset:17024\n"
    "v_add_f32 v240, v240, v194\n"
    "v_mfma_f32_16x16x32_bf16 v[148:151], v[200:203], a[232:235], v[148:151]\n"
    "ds_read_b64_tr_b16 a[168:169], v251 offset:0\n"
    "v_exp_f32 v195, v195\n"
    "v_mfma_f32_16x16x32_bf16 v[152:155], v[200:203], a[236:239], v[152:155]\n"
    "ds_read_b128 a[156:159], v246 offset:25536\n"
    "v_add_f32 v240, v240, v195\n"
    "v_mfma_f32_16x16x32_bf16 v[156:159], v[200:203], a[240:243], v[156:159]\n"
    "s_waitcnt lgkmcnt(5)\n"
    "v_exp_f32 v208, v208\n"
    "v_mfma_f32_16x16x32_bf16 v[196:199], a[128:131], a[0:3], v[216:219]\n"
    "s_waitcnt lgkmcnt(3)\n"
    "v_add_f32 v240, v240, v208\n"
    "v_cvt_pk_bf16_f32 v192, v192, v193\n"
    "v_mfma_f32_16x16x32_bf16 v[196:199], a[132:135], a[4:7], v[196:199]\n"
    "s_waitcnt lgkmcnt(2)\n"
    "v_exp_f32 v209, v209\n"
    "v_mfma_f32_16x16x32_bf16 v[196:199], a[136:139], a[8:11], v[196:199]\n"
    "ds_read_b64_tr_b16 a[160:161], v247 offset:0\n"
    "v_add_f32 v240, v240, v209\n"
    "v_cvt_pk_bf16_f32 v193, v194, v195\n"
    "v_mfma_f32_16x16x32_bf16 v[196:199], a[140:143], a[12:15], v[196:199]\n"
    "s_waitcnt lgkmcnt(1)\n"
    "ds_read_b64_tr_b16 a[162:163], v247 offset:8256\n"
    "v_exp_f32 v210, v210\n"
    "v_mfma_f32_16x16x32_bf16 v[212:215], a[156:159], a[12:15], v[216:219]\n"
    "ds_read_b64_tr_b16 a[164:165], v247 offset:8\n"
    "v_add_f32 v240, v240, v210\n"
    "v_cvt_pk_bf16_f32 v194, v208, v209\n"
    "v_mfma_f32_16x16x32_bf16 v[212:215], a[152:155], a[8:11], v[212:215]\n"
    "v_exp_f32 v211, v211\n"
    "v_mfma_f32_16x16x32_bf16 v[212:215], a[148:151], a[4:7], v[212:215]\n"
    "ds_read_b64_tr_b16 a[170:171], v251 offset:8256\n"
    "v_add_f32 v240, v240, v211\n"
    "v_cvt_pk_bf16_f32 v195, v210, v211\n"
    "v_mfma_f32_16x16x32_bf16 v[212:215], a[144:147], a[0:3], v[212:215]\n"
    "v_exp_f32 v196, v196\n"
    "v_mfma_f32_16x16x32_bf16 v[0:3], v[204:207], a[192:195], v[0:3]\n"
    "s_waitcnt lgkmcnt(2)\n"
    "ds_read_b64_tr_b16 a[172:173], v251 offset:8\n"
    "v_add_f32 v240, v240, v196\n"
    "v_mfma_f32_16x16x32_bf16 v[0:3], v[192:195], a[160:163], v[0:3]\n"
    "s_waitcnt lgkmcnt(2)\n"
    "v_exp_f32 v197, v197\n"
    "v_mfma_f32_16x16x32_bf16 v[4:7], v[192:195], a[164:167], v[4:7]\n"
    "ds_read_b64_tr_b16 a[176:177], v252 offset:0\n"
    "v_add_f32 v240, v240, v197\n"
    "v_mfma_f32_16x16x32_bf16 v[4:7], v[204:207], a[196:199], v[4:7]\n"
    "v_exp_f32 v198, v198\n"
    "v_mfma_f32_16x16x32_bf16 v[8:11], v[204:207], a[200:203], v[8:11]\n"
    "s_waitcnt lgkmcnt(2)\n"
    "ds_read_b64_tr_b16 a[178:179], v252 offset:8256\n"
    "v_add_f32 v240, v240, v198\n"
    "v_mfma_f32_16x16x32_bf16 v[8:11], v[192:195], a[168:171], v[8:11]\n"
    "s_waitcnt lgkmcnt(2)\n"
    "ds_read_b64_tr_b16 a[208:209], v252 offset:16512\n"
    "v_exp_f32 v199, v199\n"
    "v_mfma_f32_16x16x32_bf16 v[12:15], v[192:195], a[172:175], v[12:15]\n"
    "ds_read_b64_tr_b16 a[180:181], v252 offset:8\n"
    "v_add_f32 v240, v240, v199\n"
    "v_mfma_f32_16x16x32_bf16 v[12:15], v[204:207], a[244:247], v[12:15]\n"
    "v_exp_f32 v212, v212\n"
    "v_mfma_f32_16x16x32_bf16 v[200:203], a[128:131], a[16:19], v[220:223]\n"
    "v_add_f32 v240, v240, v212\n"
    "v_cvt_pk_bf16_f32 v196, v196, v197\n"
    "v_mfma_f32_16x16x32_bf16 v[200:203], a[132:135], a[20:23], v[200:203]\n"
    "v_exp_f32 v213, v213\n"
    "v_mfma_f32_16x16x32_bf16 v[200:203], a[136:139], a[24:27], v[200:203]\n"
    "ds_read_b64_tr_b16 a[182:183], v252 offset:8264\n"
    "v_add_f32 v240, v240, v213\n"
    "v_cvt_pk_bf16_f32 v197, v198, v199\n"
    "v_mfma_f32_16x16x32_bf16 v[200:203], a[140:143], a[28:31], v[200:203]\n"
    "ds_read_b64_tr_b16 a[86:87], v252 offset:57800\n"
    "v_exp_f32 v214, v214\n"
    "v_mfma_f32_16x16x32_bf16 v[208:211], a[156:159], a[28:31], v[220:223]\n"
    "ds_read_b64_tr_b16 a[186:187], v254 offset:8256\n"
    "v_add_f32 v240, v240, v214\n"
    "v_cvt_pk_bf16_f32 v198, v212, v213\n"
    "v_mfma_f32_16x16x32_bf16 v[208:211], a[152:155], a[24:27], v[208:211]\n"
    "v_exp_f32 v215, v215\n"
    "v_mfma_f32_16x16x32_bf16 v[208:211], a[148:151], a[20:23], v[208:211]\n"
    "ds_read_b64_tr_b16 a[210:211], v252 offset:24768\n"
    "v_add_f32 v240, v240, v215\n"
    "v_cvt_pk_bf16_f32 v199, v214, v215\n"
    "v_mfma_f32_16x16x32_bf16 v[208:211], a[144:147], a[16:19], v[208:211]\n"
    "ds_read_b128 a[132:135], v246 offset:49792\n"
    "v_exp_f32 v200, v200\n"
    "v_mfma_f32_16x16x32_bf16 v[16:19], v[204:207], a[80:83], v[16:19]\n"
    "s_waitcnt lgkmcnt(4)\n"
    "ds_read_b64_tr_b16 a[188:189], v254 offset:8\n"
    "v_add_f32 v241, v241, v200\n"
    "v_mfma_f32_16x16x32_bf16 v[16:19], v[192:195], a[176:179], v[16:19]\n"
    "v_exp_f32 v201, v201\n"
    "v_mfma_f32_16x16x32_bf16 v[20:23], v[192:195], a[180:183], v[20:23]\n"
    "s_waitcnt lgkmcnt(3)\n"
    "ds_read_b64_tr_b16 a[192:193], v247 offset:16512\n"
    "v_add_f32 v241, v241, v201\n"
    "v_and_b32 v248, s76, v240\n"
    "v_mfma_f32_16x16x32_bf16 v[20:23], v[204:207], a[84:87], v[20:23]\n"
    "v_exp_f32 v202, v202\n"
    "v_mfma_f32_16x16x32_bf16 v[24:27], v[204:207], a[88:91], v[24:27]\n"
    "ds_read_b64_tr_b16 a[194:195], v247 offset:24768\n"
    "v_add_f32 v241, v241, v202\n"
    "v_sub_f32 v240, v240, v248\n"
    "v_mfma_f32_16x16x32_bf16 v[24:27], v[192:195], a[184:187], v[24:27]\n"
    "s_waitcnt lgkmcnt(2)\n"
    "ds_read_b64_tr_b16 a[212:213], v252 offset:16520\n"
    "v_exp_f32 v203, v203\n"
    "v_mfma_f32_16x16x32_bf16 v[28:31], v[192:195], a[188:191], v[28:31]\n"
    "ds_read_b64_tr_b16 a[196:197], v247 offset:16520\n"
    "v_add_f32 v241, v241, v203\n"
    "v_cvt_pk_bf16_f32 v248, v248, v240\n"
    "v_mfma_f32_16x16x32_bf16 v[28:31], v[204:207], a[92:95], v[28:31]\n"
    "ds_read_b128 a[148:151], v246 offset:58048\n"
    "v_exp_f32 v208, v208\n"
    "v_mfma_f32_16x16x16_bf16 v[216:219], v[248:249], a[252:253], 0\n"
    "v_mfma_f32_16x16x32_bf16 v[204:207], a[96:99], a[16:19], v[220:223]\n"
    "v_add_f32 v241, v241, v208\n"
    "v_cvt_pk_bf16_f32 v200, v200, v201\n"
    "v_mfma_f32_16x16x32_bf16 v[204:207], a[100:103], a[20:23], v[204:207]\n"
    "v_exp_f32 v209, v209\n"
    "v_mfma_f32_16x16x32_bf16 v[204:207], a[104:107], a[24:27], v[204:207]\n"
    "ds_read_b64_tr_b16 a[198:199], v247 offset:24776\n"
    "v_add_f32 v241, v241, v209\n"
    "v_cvt_pk_bf16_f32 v201, v202, v203\n"
    "v_mfma_f32_16x16x32_bf16 v[204:207], a[108:111], a[28:31], v[204:207]\n"
    "ds_read_b64_tr_b16 a[200:201], v251 offset:16512\n"
    "v_exp_f32 v210, v210\n"
    "v_mfma_f32_16x16x32_bf16 v[212:215], a[124:127], a[28:31], v[220:223]\n"
    "ds_read_b64_tr_b16 a[202:203], v251 offset:24768\n"
    "v_add_f32 v241, v241, v210\n"
    "v_cvt_pk_bf16_f32 v202, v208, v209\n"
    "v_mfma_f32_16x16x32_bf16 v[212:215], a[120:123], a[24:27], v[212:215]\n"
    "v_exp_f32 v211, v211\n"
    "v_mfma_f32_16x16x32_bf16 v[212:215], a[116:119], a[20:23], v[212:215]\n"
    "ds_read_b64_tr_b16 a[214:215], v252 offset:24776\n"
    "v_add_f32 v241, v241, v211\n"
    "v_cvt_pk_bf16_f32 v203, v210, v211\n"
    "v_mfma_f32_16x16x32_bf16 v[212:215], a[112:115], a[16:19], v[212:215]\n"
    "s_waitcnt lgkmcnt(4)\n"
    "ds_read_b128 a[144:147], v246 offset:57792\n"
    "v_exp_f32 v204, v204\n"
    "v_mfma_f32_16x16x32_bf16 v[0:3], v[196:199], a[192:195], v[0:3]\n"
    "s_waitcnt lgkmcnt(4)\n"
    "ds_read_b64_tr_b16 a[216:217], v254 offset:16512\n"
    "v_add_f32 v241, v241, v204\n"
    "v_maximum3_f32 v167, v216, v217, v216\n"
    "v_mfma_f32_16x16x32_bf16 v[4:7], v[196:199], a[196:199], v[4:7]\n"
    "s_waitcnt lgkmcnt(3)\n"
    "v_exp_f32 v205, v205\n"
    "v_mfma_f32_16x16x32_bf16 v[8:11], v[196:199], a[200:203], v[8:11]\n"
    "ds_read_b128 a[128:131], v246 offset:49536\n"
    "v_add_f32 v241, v241, v205\n"
    "v_maximum3_f32 v167, v218, v219, v167\n"
    "v_mfma_f32_16x16x32_bf16 v[12:15], v[196:199], a[204:207], v[12:15]\n"
    "v_exp_f32 v206, v206\n"
    "v_mfma_f32_16x16x32_bf16 v[16:19], v[196:199], a[208:211], v[16:19]\n"
    "s_waitcnt lgkmcnt(3)\n"
    "ds_read_b128 a[136:139], v246 offset:50048\n"
    "v_add_f32 v241, v241, v206\n"
    "v_mfma_f32_16x16x32_bf16 v[20:23], v[196:199], a[212:215], v[20:23]\n"
    "s_waitcnt lgkmcnt(2)\n"
    "ds_read_b128 a[140:143], v246 offset:50304\n"
    "v_exp_f32 v207, v207\n"
    "v_mfma_f32_16x16x32_bf16 v[24:27], v[196:199], a[216:219], v[24:27]\n"
    "ds_read_b128 a[156:159], v246 offset:58560\n"
    "v_add_f32 v241, v241, v207\n"
    "v_mfma_f32_16x16x32_bf16 v[28:31], v[196:199], a[220:223], v[28:31]\n"
    "ds_read_b128 a[152:155], v246 offset:58304\n"
    "v_exp_f32 v212, v212\n"
    "v_rcp_f32 v216, v216\n"
    "v_rcp_f32 v217, v217\n"
    "v_rcp_f32 v218, v218\n"
    "v_rcp_f32_e64 v219, v219\n"
    "v_maximum3_f32 v167, v216, v217, v167\n"
    "v_maximum3_f32 v167, v218, v219, v167\n"
    "v_pk_mul_f32 v[0:1], v[0:1], v[216:217]\n"
    "v_pk_mul_f32 v[2:3], v[2:3], v[218:219]\n"
    "v_pk_mul_f32 v[8:9], v[8:9], v[216:217]\n"
    "v_pk_mul_f32 v[10:11], v[10:11], v[218:219]\n"
    "v_cvt_pk_bf16_f32 v0, v0, v8\n"
    "v_cvt_pk_bf16_f32 v2, v2, v10\n"
    "v_cvt_pk_bf16_f32 v8, v1, v9\n"
    "v_cvt_pk_bf16_f32 v10, v3, v11\n"
    "v_pk_mul_f32 v[16:17], v[16:17], v[216:217]\n"
    "v_pk_mul_f32 v[18:19], v[18:19], v[218:219]\n"
    "LPG16:\n"
    "s_cbranch_execz LPG17\n"
    "s_nop 0\n"
    "v_pk_mul_f32 v[24:25], v[24:25], v[216:217]\n"
    "v_pk_mul_f32 v[26:27], v[26:27], v[218:219]\n"
    "v_cvt_pk_bf16_f32 v1, v16, v24\n"
    "v_cvt_pk_bf16_f32 v3, v18, v26\n"
    "v_cvt_pk_bf16_f32 v9, v17, v25\n"
    "v_cvt_pk_bf16_f32 v11, v19, v27\n"
    "v_pk_mul_f32 v[4:5], v[4:5], v[216:217]\n"
    "v_pk_mul_f32 v[6:7], v[6:7], v[218:219]\n"
    "v_pk_mul_f32 v[12:13], v[12:13], v[216:217]\n"
    "v_pk_mul_f32 v[14:15], v[14:15], v[218:219]\n"
    "v_cvt_pk_bf16_f32 v4, v4, v12\n"
    "v_cvt_pk_bf16_f32 v6, v6, v14\n"
    "v_cvt_pk_bf16_f32 v12, v5, v13\n"
    "v_cvt_pk_bf16_f32 v14, v7, v15\n"
    "v_pk_mul_f32 v[20:21], v[20:21], v[216:217]\n"
    "v_pk_mul_f32 v[22:23], v[22:23], v[218:219]\n"
    "v_pk_mul_f32 v[28:29], v[28:29], v[216:217]\n"
    "v_pk_mul_f32 v[30:31], v[30:31], v[218:219]\n"
    "v_mfma_f32_4x4x4_16b_bf16 v[16:19], v[0:1], a[254:255], 0\n"
    "v_cvt_pk_bf16_f32 v5, v20, v28\n"
    "v_cvt_pk_bf16_f32 v7, v22, v30\n"
    "v_mfma_f32_4x4x4_16b_bf16 v[24:27], v[2:3], a[254:255], 0\n"
    "v_cvt_pk_bf16_f32 v13, v21, v29\n"
    "v_cvt_pk_bf16_f32 v15, v23, v31\n"
    "v_mfma_f32_4x4x4_16b_bf16 v[20:23], v[4:5], a[254:255], 0\n"
    "v_cvt_pk_bf16_f32 v16, v16, v17\n"
    "v_cvt_pk_bf16_f32 v17, v18, v19\n"
    "v_mfma_f32_4x4x4_16b_bf16 v[28:31], v[6:7], a[254:255], 0\n"
    "v_cvt_pk_bf16_f32 v24, v24, v25\n"
    "v_cvt_pk_bf16_f32 v25, v26, v27\n"
    "v_mfma_f32_4x4x4_16b_bf16 v[0:3], v[8:9], a[254:255], 0\n"
    "v_cvt_pk_bf16_f32 v18, v20, v21\n"
    "v_cvt_pk_bf16_f32 v19, v22, v23\n"
    "v_mfma_f32_4x4x4_16b_bf16 v[4:7], v[12:13], a[254:255], 0\n"
    "v_cvt_pk_bf16_f32 v26, v28, v29\n"
    "v_cvt_pk_bf16_f32 v27, v30, v31\n"
    "v_mfma_f32_4x4x4_16b_bf16 v[20:23], v[10:11], a[254:255], 0\n"
    "v_cvt_pk_bf16_f32 v0, v0, v1\n"
    "v_cvt_pk_bf16_f32 v1, v2, v3\n"
    "v_mfma_f32_4x4x4_16b_bf16 v[28:31], v[14:15], a[254:255], 0\n"
    "v_cvt_pk_bf16_f32 v2, v4, v5\n"
    "v_cvt_pk_bf16_f32 v3, v6, v7\n"
    "v_cvt_pk_bf16_f32 v20, v20, v21\n"
    "v_cvt_pk_bf16_f32 v21, v22, v23\n"
    "s_nop 0\n"
    "v_cvt_pk_bf16_f32 v22, v28, v29\n"
    "v_cvt_pk_bf16_f32 v23, v30, v31\n"
    "s_waitcnt lgkmcnt(0)\n"
    "v_mfma_f32_16x16x32_bf16 v[192:195], a[128:131], a[16:19], v[220:223]\n"
    "v_add_f32 v241, v241, v212\n"
    "v_cvt_pk_bf16_f32 v204, v204, v205\n"
    "v_mfma_f32_16x16x32_bf16 v[192:195], a[132:135], a[20:23], v[192:195]\n"
    "v_exp_f32 v213, v213\n"
    "v_mfma_f32_16x16x32_bf16 v[192:195], a[136:139], a[24:27], v[192:195]\n"
    "s_add_u32 s67, s66, 24117248\n"
    "v_add_f32 v241, v241, v213\n"
    "v_cvt_pk_bf16_f32 v205, v206, v207\n"
    "v_mfma_f32_16x16x32_bf16 v[192:195], a[140:143], a[28:31], v[192:195]\n"
    "buffer_store_dwordx4 v[16:19], v249, s[56:59], s67 offen offset:0 sc1\n"
    "v_exp_f32 v214, v214\n"
    "v_mfma_f32_16x16x32_bf16 v[208:211], a[156:159], a[28:31], v[220:223]\n"
    "v_add_f32 v241, v241, v214\n"
    "v_cvt_pk_bf16_f32 v206, v212, v213\n"
    "v_mfma_f32_16x16x32_bf16 v[208:211], a[152:155], a[24:27], v[208:211]\n"
    "v_exp_f32 v215, v215\n"
    "v_mfma_f32_16x16x32_bf16 v[208:211], a[148:151], a[20:23], v[208:211]\n"
    "v_add_f32 v241, v241, v215\n"
    "v_cvt_pk_bf16_f32 v207, v214, v215\n"
    "v_mfma_f32_16x16x32_bf16 v[208:211], a[144:147], a[16:19], v[208:211]\n"
    "v_exp_f32 v192, v192\n"
    "v_mfma_f32_16x16x32_bf16 v[32:35], v[200:203], a[192:195], v[32:35]\n"
    "ds_read_b64_tr_b16 a[192:193], v247 offset:49536\n"
    "v_add_f32 v241, v241, v192\n"
    "v_mfma_f32_16x16x32_bf16 v[32:35], v[204:207], a[160:163], v[32:35]\n"
    "v_exp_f32 v193, v193\n"
    "v_mfma_f32_16x16x32_bf16 v[36:39], v[204:207], a[164:167], v[36:39]\n"
    "ds_read_b64_tr_b16 a[194:195], v247 offset:57792\n"
    "v_add_f32 v241, v241, v193\n"
    "v_mfma_f32_16x16x32_bf16 v[36:39], v[200:203], a[196:199], v[36:39]\n"
    "v_exp_f32 v194, v194\n"
    "v_mfma_f32_16x16x32_bf16 v[40:43], v[200:203], a[200:203], v[40:43]\n"
    "ds_read_b64_tr_b16 a[196:197], v247 offset:49544\n"
    "v_add_f32 v241, v241, v194\n"
    "v_mfma_f32_16x16x32_bf16 v[40:43], v[204:207], a[168:171], v[40:43]\n"
    "buffer_store_dwordx4 v[24:27], v249, s[56:59], s67 offen offset:512 sc1\n"
    "v_exp_f32 v195, v195\n"
    "v_mfma_f32_16x16x32_bf16 v[44:47], v[204:207], a[172:175], v[44:47]\n"
    "ds_read_b64_tr_b16 a[198:199], v247 offset:57800\n"
    "v_add_f32 v241, v241, v195\n"
    "v_mfma_f32_16x16x32_bf16 v[44:47], v[200:203], a[204:207], v[44:47]\n"
    "v_exp_f32 v208, v208\n"
    "v_mfma_f32_16x16x32_bf16 v[196:199], a[128:131], a[32:35], v[224:227]\n"
    "v_add_f32 v241, v241, v208\n"
    "v_cvt_pk_bf16_f32 v192, v192, v193\n"
    "v_mfma_f32_16x16x32_bf16 v[196:199], a[132:135], a[36:39], v[196:199]\n"
    "v_exp_f32 v209, v209\n"
    "v_mfma_f32_16x16x32_bf16 v[196:199], a[136:139], a[40:43], v[196:199]\n"
    "v_add_f32 v241, v241, v209\n"
    "v_cvt_pk_bf16_f32 v193, v194, v195\n"
    "v_mfma_f32_16x16x32_bf16 v[196:199], a[140:143], a[44:47], v[196:199]\n"
    "v_exp_f32 v210, v210\n"
    "v_mfma_f32_16x16x32_bf16 v[212:215], a[156:159], a[44:47], v[224:227]\n"
    "v_add_f32 v241, v241, v210\n"
    "v_cvt_pk_bf16_f32 v194, v208, v209\n"
    "v_mfma_f32_16x16x32_bf16 v[212:215], a[152:155], a[40:43], v[212:215]\n"
    "v_exp_f32 v211, v211\n"
    "v_mfma_f32_16x16x32_bf16 v[212:215], a[148:151], a[36:39], v[212:215]\n"
    "v_add_f32 v241, v241, v211\n"
    "v_cvt_pk_bf16_f32 v195, v210, v211\n"
    "v_mfma_f32_16x16x32_bf16 v[212:215], a[144:147], a[32:35], v[212:215]\n"
    "v_exp_f32 v196, v196\n"
    "v_mfma_f32_16x16x32_bf16 v[48:51], v[200:203], a[208:211], v[48:51]\n"
    "ds_read_b64_tr_b16 a[200:201], v251 offset:49536\n"
    "v_add_f32 v242, v242, v196\n"
    "v_mfma_f32_16x16x32_bf16 v[48:51], v[204:207], a[176:179], v[48:51]\n"
    "buffer_store_dwordx4 v[0:3], v249, s[56:59], s67 offen offset:256 sc1\n"
    "v_exp_f32 v197, v197\n"
    "v_mfma_f32_16x16x32_bf16 v[52:55], v[204:207], a[180:183], v[52:55]\n"
    "ds_read_b64_tr_b16 a[202:203], v251 offset:57792\n"
    "v_add_f32 v242, v242, v197\n"
    "v_and_b32 v248, s76, v241\n"
    "v_mfma_f32_16x16x32_bf16 v[52:55], v[200:203], a[212:215], v[52:55]\n"
    "v_exp_f32 v198, v198\n"
    "v_mfma_f32_16x16x32_bf16 v[56:59], v[200:203], a[216:219], v[56:59]\n"
    "ds_read_b128 a[128:131], v246 offset:16512\n"
    "v_add_f32 v242, v242, v198\n"
    "v_sub_f32 v241, v241, v248\n"
    "v_mfma_f32_16x16x32_bf16 v[56:59], v[204:207], a[184:187], v[56:59]\n"
    "v_exp_f32 v199, v199\n"
    "v_mfma_f32_16x16x32_bf16 v[60:63], v[204:207], a[188:191], v[60:63]\n"
    "ds_read_b128 a[132:135], v246 offset:16768\n"
    "v_add_f32 v242, v242, v199\n"
    "v_cvt_pk_bf16_f32 v248, v248, v241\n"
    "v_mfma_f32_16x16x32_bf16 v[60:63], v[200:203], a[220:223], v[60:63]\n"
    "v_exp_f32 v212, v212\n"
    "v_mfma_f32_16x16x16_bf16 v[220:223], v[248:249], a[252:253], 0\n"
    "v_mfma_f32_16x16x32_bf16 v[200:203], a[96:99], a[32:35], v[224:227]\n"
    "v_add_f32 v242, v242, v212\n"
    "v_cvt_pk_bf16_f32 v196, v196, v197\n"
    "v_mfma_f32_16x16x32_bf16 v[200:203], a[100:103], a[36:39], v[200:203]\n"
    "v_exp_f32 v213, v213\n"
    "v_mfma_f32_16x16x32_bf16 v[200:203], a[104:107], a[40:43], v[200:203]\n"
    "ds_read_b128 a[148:151], v246 offset:25024\n"
    "v_add_f32 v242, v242, v213\n"
    "v_cvt_pk_bf16_f32 v197, v198, v199\n"
    "v_mfma_f32_16x16x32_bf16 v[200:203], a[108:111], a[44:47], v[200:203]\n"
    "ds_read_b128 a[144:147], v246 offset:24768\n"
    "v_exp_f32 v214, v214\n"
    "v_mfma_f32_16x16x32_bf16 v[208:211], a[124:127], a[44:47], v[224:227]\n"
    "v_add_f32 v242, v242, v214\n"
    "v_cvt_pk_bf16_f32 v198, v212, v213\n"
    "v_mfma_f32_16x16x32_bf16 v[208:211], a[120:123], a[40:43], v[208:211]\n"
    "buffer_store_dwordx4 v[20:23], v249, s[56:59], s67 offen offset:768 sc1\n"
    "v_exp_f32 v215, v215\n"
    "v_mfma_f32_16x16x32_bf16 v[208:211], a[116:119], a[36:39], v[208:211]\n"
    "v_add_f32 v242, v242, v215\n"
    "v_cvt_pk_bf16_f32 v199, v214, v215\n"
    "v_mfma_f32_16x16x32_bf16 v[208:211], a[112:115], a[32:35], v[208:211]\n"
    "s_waitcnt lgkmcnt(2)\n"
    "v_exp_f32 v200, v200\n"
    "v_mfma_f32_16x16x32_bf16 v[32:35], v[192:195], a[192:195], v[32:35]\n"
    "ds_read_b128 a[136:139], v246 offset:17024\n"
    "v_add_f32 v242, v242, v200\n"
    "v_maximum3_f32 v167, v220, v221, v167\n"
    "v_mfma_f32_16x16x32_bf16 v[36:39], v[192:195], a[196:199], v[36:39]\n"
    "v_exp_f32 v201, v201\n"
    "v_mfma_f32_16x16x32_bf16 v[40:43], v[192:195], a[200:203], v[40:43]\n"
    "ds_read_b128 a[140:143], v246 offset:17280\n"
    "v_add_f32 v242, v242, v201\n"
    "v_maximum3_f32 v167, v222, v223, v167\n"
    "v_mfma_f32_16x16x32_bf16 v[44:47], v[192:195], a[244:247], v[44:47]\n"
    "v_exp_f32 v202, v202\n"
    "v_mfma_f32_16x16x32_bf16 v[48:51], v[192:195], a[80:83], v[48:51]\n"
    "ds_read_b128 a[156:159], v246 offset:25536\n"
    "v_add_f32 v242, v242, v202\n"
    "v_mfma_f32_16x16x32_bf16 v[52:55], v[192:195], a[84:87], v[52:55]\n"
    "v_exp_f32 v203, v203\n"
    "v_mfma_f32_16x16x32_bf16 v[56:59], v[192:195], a[88:91], v[56:59]\n"
    "ds_read_b128 a[152:155], v246 offset:25280\n"
    "v_add_f32 v242, v242, v203\n"
    "v_mfma_f32_16x16x32_bf16 v[60:63], v[192:195], a[92:95], v[60:63]\n"
    "v_exp_f32 v208, v208\n"
    "v_rcp_f32 v220, v220\n"
    "v_rcp_f32 v221, v221\n"
    "v_rcp_f32 v222, v222\n"
    "v_rcp_f32_e64 v223, v223\n"
    "v_maximum3_f32 v167, v220, v221, v167\n"
    "v_maximum3_f32 v167, v222, v223, v167\n"
    "v_pk_mul_f32 v[32:33], v[32:33], v[220:221]\n"
    "v_pk_mul_f32 v[34:35], v[34:35], v[222:223]\n"
    "v_pk_mul_f32 v[40:41], v[40:41], v[220:221]\n"
    "v_pk_mul_f32 v[42:43], v[42:43], v[222:223]\n"
    "v_cvt_pk_bf16_f32 v32, v32, v40\n"
    "v_cvt_pk_bf16_f32 v34, v34, v42\n"
    "v_cvt_pk_bf16_f32 v40, v33, v41\n"
    "v_cvt_pk_bf16_f32 v42, v35, v43\n"
    "v_pk_mul_f32 v[48:49], v[48:49], v[220:221]\n"
    "v_pk_mul_f32 v[50:51], v[50:51], v[222:223]\n"
    "v_pk_mul_f32 v[56:57], v[56:57], v[220:221]\n"
    "v_pk_mul_f32 v[58:59], v[58:59], v[222:223]\n"
    "v_cvt_pk_bf16_f32 v33, v48, v56\n"
    "v_cvt_pk_bf16_f32 v35, v50, v58\n"
    "v_cvt_pk_bf16_f32 v41, v49, v57\n"
    "v_cvt_pk_bf16_f32 v43, v51, v59\n"
    "v_pk_mul_f32 v[36:37], v[36:37], v[220:221]\n"
    "v_pk_mul_f32 v[38:39], v[38:39], v[222:223]\n"
    "v_pk_mul_f32 v[44:45], v[44:45], v[220:221]\n"
    "v_pk_mul_f32 v[46:47], v[46:47], v[222:223]\n"
    "v_cvt_pk_bf16_f32 v36, v36, v44\n"
    "v_cvt_pk_bf16_f32 v38, v38, v46\n"
    "v_cvt_pk_bf16_f32 v44, v37, v45\n"
    "v_cvt_pk_bf16_f32 v46, v39, v47\n"
    "v_pk_mul_f32 v[52:53], v[52:53], v[220:221]\n"
    "v_pk_mul_f32 v[54:55], v[54:55], v[222:223]\n"
    "v_pk_mul_f32 v[60:61], v[60:61], v[220:221]\n"
    "v_pk_mul_f32 v[62:63], v[62:63], v[222:223]\n"
    "v_mfma_f32_4x4x4_16b_bf16 v[48:51], v[32:33], a[254:255], 0\n"
    "v_cvt_pk_bf16_f32 v37, v52, v60\n"
    "v_cvt_pk_bf16_f32 v39, v54, v62\n"
    "v_mfma_f32_4x4x4_16b_bf16 v[56:59], v[34:35], a[254:255], 0\n"
    "v_cvt_pk_bf16_f32 v45, v53, v61\n"
    "v_cvt_pk_bf16_f32 v47, v55, v63\n"
    "v_mfma_f32_4x4x4_16b_bf16 v[52:55], v[36:37], a[254:255], 0\n"
    "v_cvt_pk_bf16_f32 v48, v48, v49\n"
    "v_cvt_pk_bf16_f32 v49, v50, v51\n"
    "v_mfma_f32_4x4x4_16b_bf16 v[60:63], v[38:39], a[254:255], 0\n"
    "v_cvt_pk_bf16_f32 v56, v56, v57\n"
    "v_cvt_pk_bf16_f32 v57, v58, v59\n"
    "v_mfma_f32_4x4x4_16b_bf16 v[32:35], v[40:41], a[254:255], 0\n"
    "v_cvt_pk_bf16_f32 v50, v52, v53\n"
    "v_cvt_pk_bf16_f32 v51, v54, v55\n"
    "v_mfma_f32_4x4x4_16b_bf16 v[36:39], v[44:45], a[254:255], 0\n"
    "v_cvt_pk_bf16_f32 v58, v60, v61\n"
    "v_cvt_pk_bf16_f32 v59, v62, v63\n"
    "v_mfma_f32_4x4x4_16b_bf16 v[52:55], v[42:43], a[254:255], 0\n"
    "v_cvt_pk_bf16_f32 v32, v32, v33\n"
    "v_cvt_pk_bf16_f32 v33, v34, v35\n"
    "v_mfma_f32_4x4x4_16b_bf16 v[60:63], v[46:47], a[254:255], 0\n"
    "v_cvt_pk_bf16_f32 v34, v36, v37\n"
    "v_cvt_pk_bf16_f32 v35, v38, v39\n"
    "v_cvt_pk_bf16_f32 v52, v52, v53\n"
    "v_cvt_pk_bf16_f32 v53, v54, v55\n"
    "v_add_f32_e64 v242, v242, v208\n"
    "v_cvt_pk_bf16_f32 v54, v60, v61\n"
    "v_cvt_pk_bf16_f32 v55, v62, v63\n"
    "v_mfma_f32_16x16x32_bf16 v[204:207], a[128:131], a[32:35], v[224:227]\n"
    "v_cvt_pk_bf16_f32 v200, v200, v201\n"
    "v_mfma_f32_16x16x32_bf16 v[204:207], a[132:135], a[36:39], v[204:207]\n"
    "s_waitcnt lgkmcnt(0)\n"
    "v_exp_f32 v209, v209\n"
    "v_mfma_f32_16x16x32_bf16 v[204:207], a[136:139], a[40:43], v[204:207]\n"
    "s_add_u32 s67, s66, 24121344\n"
    "v_add_f32 v242, v242, v209\n"
    "v_cvt_pk_bf16_f32 v201, v202, v203\n"
    "v_mfma_f32_16x16x32_bf16 v[204:207], a[140:143], a[44:47], v[204:207]\n"
    "buffer_store_dwordx4 v[48:51], v249, s[56:59], s67 offen offset:0 sc1\n"
    "v_exp_f32 v210, v210\n"
    "v_mfma_f32_16x16x32_bf16 v[212:215], a[156:159], a[44:47], v[224:227]\n"
    "v_add_f32 v242, v242, v210\n"
    "v_cvt_pk_bf16_f32 v202, v208, v209\n"
    "v_mfma_f32_16x16x32_bf16 v[212:215], a[152:155], a[40:43], v[212:215]\n"
    "v_exp_f32 v211, v211\n"
    "v_mfma_f32_16x16x32_bf16 v[212:215], a[148:151], a[36:39], v[212:215]\n"
    "v_add_f32 v242, v242, v211\n"
    "v_cvt_pk_bf16_f32 v203, v210, v211\n"
    "v_mfma_f32_16x16x32_bf16 v[212:215], a[144:147], a[32:35], v[212:215]\n"
    "v_exp_f32 v204, v204\n"
    "v_mfma_f32_16x16x32_bf16 v[64:67], v[196:199], a[192:195], v[64:67]\n"
    "ds_read_b64_tr_b16 a[192:193], v247 offset:16512\n"
    "v_add_f32 v242, v242, v204\n"
    "v_mfma_f32_16x16x32_bf16 v[64:67], v[200:203], a[160:163], v[64:67]\n"
    "v_exp_f32 v205, v205\n"
    "v_mfma_f32_16x16x32_bf16 v[68:71], v[200:203], a[164:167], v[68:71]\n"
    "ds_read_b64_tr_b16 a[194:195], v247 offset:24768\n"
    "v_add_f32 v242, v242, v205\n"
    "v_mfma_f32_16x16x32_bf16 v[68:71], v[196:199], a[196:199], v[68:71]\n"
    "v_exp_f32 v206, v206\n"
    "v_mfma_f32_16x16x32_bf16 v[72:75], v[196:199], a[200:203], v[72:75]\n"
    "ds_read_b64_tr_b16 a[196:197], v247 offset:16520\n"
    "v_add_f32 v242, v242, v206\n"
    "v_mfma_f32_16x16x32_bf16 v[72:75], v[200:203], a[168:171], v[72:75]\n"
    "buffer_store_dwordx4 v[56:59], v249, s[56:59], s67 offen offset:512 sc1\n"
    "v_exp_f32 v207, v207\n"
    "v_mfma_f32_16x16x32_bf16 v[76:79], v[200:203], a[172:175], v[76:79]\n"
    "ds_read_b64_tr_b16 a[198:199], v247 offset:24776\n"
    "v_add_f32 v242, v242, v207\n"
    "v_mfma_f32_16x16x32_bf16 v[76:79], v[196:199], a[244:247], v[76:79]\n"
    "v_exp_f32 v212, v212\n"
    "v_mfma_f32_16x16x32_bf16 v[192:195], a[128:131], a[48:51], v[228:231]\n"
    "v_add_f32 v242, v242, v212\n"
    "v_cvt_pk_bf16_f32 v204, v204, v205\n"
    "v_mfma_f32_16x16x32_bf16 v[192:195], a[132:135], a[52:55], v[192:195]\n"
    "v_exp_f32 v213, v213\n"
    "v_mfma_f32_16x16x32_bf16 v[192:195], a[136:139], a[56:59], v[192:195]\n"
    "v_add_f32 v242, v242, v213\n"
    "v_cvt_pk_bf16_f32 v205, v206, v207\n"
    "v_mfma_f32_16x16x32_bf16 v[192:195], a[140:143], a[60:63], v[192:195]\n"
    "v_exp_f32 v214, v214\n"
    "v_mfma_f32_16x16x32_bf16 v[208:211], a[156:159], a[60:63], v[228:231]\n"
    "v_add_f32 v242, v242, v214\n"
    "v_cvt_pk_bf16_f32 v206, v212, v213\n"
    "v_mfma_f32_16x16x32_bf16 v[208:211], a[152:155], a[56:59], v[208:211]\n"
    "v_exp_f32 v215, v215\n"
    "v_mfma_f32_16x16x32_bf16 v[208:211], a[148:151], a[52:55], v[208:211]\n"
    "v_add_f32 v242, v242, v215\n"
    "v_cvt_pk_bf16_f32 v207, v214, v215\n"
    "v_mfma_f32_16x16x32_bf16 v[208:211], a[144:147], a[48:51], v[208:211]\n"
    "v_exp_f32 v192, v192\n"
    "v_mfma_f32_16x16x32_bf16 v[80:83], v[196:199], a[80:83], v[80:83]\n"
    "ds_read_b64_tr_b16 a[200:201], v251 offset:16512\n"
    "v_add_f32 v243, v243, v192\n"
    "v_mfma_f32_16x16x32_bf16 v[80:83], v[200:203], a[176:179], v[80:83]\n"
    "buffer_store_dwordx4 v[32:35], v249, s[56:59], s67 offen offset:256 sc1\n"
    "v_exp_f32 v193, v193\n"
    "v_mfma_f32_16x16x32_bf16 v[84:87], v[200:203], a[180:183], v[84:87]\n"
    "ds_read_b64_tr_b16 a[202:203], v251 offset:24768\n"
    "v_add_f32 v243, v243, v193\n"
    "v_and_b32 v248, s76, v242\n"
    "v_mfma_f32_16x16x32_bf16 v[84:87], v[196:199], a[84:87], v[84:87]\n"
    "v_exp_f32 v194, v194\n"
    "v_mfma_f32_16x16x32_bf16 v[88:91], v[196:199], a[88:91], v[88:91]\n"
    "ds_read_b128 a[128:131], v246 offset:49536\n"
    "v_add_f32 v243, v243, v194\n"
    "v_sub_f32 v242, v242, v248\n"
    "v_mfma_f32_16x16x32_bf16 v[88:91], v[200:203], a[184:187], v[88:91]\n"
    "v_exp_f32 v195, v195\n"
    "v_mfma_f32_16x16x32_bf16 v[92:95], v[200:203], a[188:191], v[92:95]\n"
    "ds_read_b128 a[132:135], v246 offset:49792\n"
    "v_add_f32 v243, v243, v195\n"
    "v_cvt_pk_bf16_f32 v248, v248, v242\n"
    "v_mfma_f32_16x16x32_bf16 v[92:95], v[196:199], a[92:95], v[92:95]\n"
    "v_exp_f32 v208, v208\n"
    "v_mfma_f32_16x16x16_bf16 v[224:227], v[248:249], a[252:253], 0\n"
    "v_mfma_f32_16x16x32_bf16 v[196:199], a[96:99], a[48:51], v[228:231]\n"
    "v_add_f32 v243, v243, v208\n"
    "v_cvt_pk_bf16_f32 v192, v192, v193\n"
    "v_mfma_f32_16x16x32_bf16 v[196:199], a[100:103], a[52:55], v[196:199]\n"
    "v_exp_f32 v209, v209\n"
    "v_mfma_f32_16x16x32_bf16 v[196:199], a[104:107], a[56:59], v[196:199]\n"
    "ds_read_b128 a[148:151], v246 offset:58048\n"
    "v_add_f32 v243, v243, v209\n"
    "v_cvt_pk_bf16_f32 v193, v194, v195\n"
    "v_mfma_f32_16x16x32_bf16 v[196:199], a[108:111], a[60:63], v[196:199]\n"
    "ds_read_b128 a[144:147], v246 offset:57792\n"
    "v_exp_f32 v210, v210\n"
    "v_mfma_f32_16x16x32_bf16 v[212:215], a[124:127], a[60:63], v[228:231]\n"
    "v_add_f32 v243, v243, v210\n"
    "v_cvt_pk_bf16_f32 v194, v208, v209\n"
    "v_mfma_f32_16x16x32_bf16 v[212:215], a[120:123], a[56:59], v[212:215]\n"
    "buffer_store_dwordx4 v[52:55], v249, s[56:59], s67 offen offset:768 sc1\n"
    "v_exp_f32 v211, v211\n"
    "v_mfma_f32_16x16x32_bf16 v[212:215], a[116:119], a[52:55], v[212:215]\n"
    "v_add_f32 v243, v243, v211\n"
    "v_cvt_pk_bf16_f32 v195, v210, v211\n"
    "v_mfma_f32_16x16x32_bf16 v[212:215], a[112:115], a[48:51], v[212:215]\n"
    "s_waitcnt lgkmcnt(2)\n"
    "v_exp_f32 v196, v196\n"
    "v_mfma_f32_16x16x32_bf16 v[64:67], v[204:207], a[192:195], v[64:67]\n"
    "ds_read_b128 a[136:139], v246 offset:50048\n"
    "v_add_f32 v243, v243, v196\n"
    "v_maximum3_f32 v167, v224, v225, v167\n"
    "v_mfma_f32_16x16x32_bf16 v[68:71], v[204:207], a[196:199], v[68:71]\n"
    "v_exp_f32 v197, v197\n"
    "v_mfma_f32_16x16x32_bf16 v[72:75], v[204:207], a[200:203], v[72:75]\n"
    "ds_read_b128 a[140:143], v246 offset:50304\n"
    "v_add_f32 v243, v243, v197\n"
    "v_maximum3_f32 v167, v226, v227, v167\n"
    "v_mfma_f32_16x16x32_bf16 v[76:79], v[204:207], a[204:207], v[76:79]\n"
    "v_exp_f32 v198, v198\n"
    "v_mfma_f32_16x16x32_bf16 v[80:83], v[204:207], a[208:211], v[80:83]\n"
    "ds_read_b128 a[156:159], v246 offset:58560\n"
    "v_add_f32 v243, v243, v198\n"
    "v_mfma_f32_16x16x32_bf16 v[84:87], v[204:207], a[212:215], v[84:87]\n"
    "v_exp_f32 v199, v199\n"
    "v_mfma_f32_16x16x32_bf16 v[88:91], v[204:207], a[216:219], v[88:91]\n"
    "ds_read_b128 a[152:155], v246 offset:58304\n"
    "v_add_f32 v243, v243, v199\n"
    "v_mfma_f32_16x16x32_bf16 v[92:95], v[204:207], a[220:223], v[92:95]\n"
    "v_exp_f32 v212, v212\n"
    "v_rcp_f32 v224, v224\n"
    "v_rcp_f32 v225, v225\n"
    "v_rcp_f32 v226, v226\n"
    "v_rcp_f32_e64 v227, v227\n"
    "v_maximum3_f32 v167, v224, v225, v167\n"
    "v_maximum3_f32 v167, v226, v227, v167\n"
    "v_pk_mul_f32 v[64:65], v[64:65], v[224:225]\n"
    "v_pk_mul_f32 v[66:67], v[66:67], v[226:227]\n"
    "v_pk_mul_f32 v[72:73], v[72:73], v[224:225]\n"
    "v_pk_mul_f32 v[74:75], v[74:75], v[226:227]\n"
    "v_cvt_pk_bf16_f32 v64, v64, v72\n"
    "v_cvt_pk_bf16_f32 v66, v66, v74\n"
    "v_cvt_pk_bf16_f32 v72, v65, v73\n"
    "v_cvt_pk_bf16_f32 v74, v67, v75\n"
    "v_pk_mul_f32 v[80:81], v[80:81], v[224:225]\n"
    "v_pk_mul_f32 v[82:83], v[82:83], v[226:227]\n"
    "v_pk_mul_f32 v[88:89], v[88:89], v[224:225]\n"
    "v_pk_mul_f32 v[90:91], v[90:91], v[226:227]\n"
    "v_cvt_pk_bf16_f32 v65, v80, v88\n"
    "v_cvt_pk_bf16_f32 v67, v82, v90\n"
    "v_cvt_pk_bf16_f32 v73, v81, v89\n"
    "v_cvt_pk_bf16_f32 v75, v83, v91\n"
    "v_pk_mul_f32 v[68:69], v[68:69], v[224:225]\n"
    "v_pk_mul_f32 v[70:71], v[70:71], v[226:227]\n"
    "v_pk_mul_f32 v[76:77], v[76:77], v[224:225]\n"
    "v_pk_mul_f32 v[78:79], v[78:79], v[226:227]\n"
    "v_cvt_pk_bf16_f32 v68, v68, v76\n"
    "v_cvt_pk_bf16_f32 v70, v70, v78\n"
    "v_cvt_pk_bf16_f32 v76, v69, v77\n"
    "v_cvt_pk_bf16_f32 v78, v71, v79\n"
    "v_pk_mul_f32 v[84:85], v[84:85], v[224:225]\n"
    "v_pk_mul_f32 v[86:87], v[86:87], v[226:227]\n"
    "v_pk_mul_f32 v[92:93], v[92:93], v[224:225]\n"
    "v_pk_mul_f32 v[94:95], v[94:95], v[226:227]\n"
    "v_mfma_f32_4x4x4_16b_bf16 v[80:83], v[64:65], a[254:255], 0\n"
    "v_cvt_pk_bf16_f32 v69, v84, v92\n"
    "v_cvt_pk_bf16_f32 v71, v86, v94\n"
    "v_mfma_f32_4x4x4_16b_bf16 v[88:91], v[66:67], a[254:255], 0\n"
    "v_cvt_pk_bf16_f32 v77, v85, v93\n"
    "v_cvt_pk_bf16_f32 v79, v87, v95\n"
    "v_mfma_f32_4x4x4_16b_bf16 v[84:87], v[68:69], a[254:255], 0\n"
    "v_cvt_pk_bf16_f32 v80, v80, v81\n"
    "v_cvt_pk_bf16_f32 v81, v82, v83\n"
    "v_mfma_f32_4x4x4_16b_bf16 v[92:95], v[70:71], a[254:255], 0\n"
    "v_cvt_pk_bf16_f32 v88, v88, v89\n"
    "v_cvt_pk_bf16_f32 v89, v90, v91\n"
    "v_mfma_f32_4x4x4_16b_bf16 v[64:67], v[72:73], a[254:255], 0\n"
    "v_cvt_pk_bf16_f32 v82, v84, v85\n"
    "v_cvt_pk_bf16_f32 v83, v86, v87\n"
    "v_mfma_f32_4x4x4_16b_bf16 v[68:71], v[76:77], a[254:255], 0\n"
    "v_cvt_pk_bf16_f32 v90, v92, v93\n"
    "v_cvt_pk_bf16_f32 v91, v94, v95\n"
    "v_mfma_f32_4x4x4_16b_bf16 v[84:87], v[74:75], a[254:255], 0\n"
    "v_cvt_pk_bf16_f32 v64, v64, v65\n"
    "v_cvt_pk_bf16_f32 v65, v66, v67\n"
    "v_mfma_f32_4x4x4_16b_bf16 v[92:95], v[78:79], a[254:255], 0\n"
    "v_cvt_pk_bf16_f32 v66, v68, v69\n"
    "v_cvt_pk_bf16_f32 v67, v70, v71\n"
    "v_cvt_pk_bf16_f32 v84, v84, v85\n"
    "v_cvt_pk_bf16_f32 v85, v86, v87\n"
    "v_add_f32_e64 v243, v243, v212\n"
    "v_cvt_pk_bf16_f32 v86, v92, v93\n"
    "v_cvt_pk_bf16_f32 v87, v94, v95\n"
    "v_mfma_f32_16x16x32_bf16 v[200:203], a[128:131], a[48:51], v[228:231]\n"
    "v_cvt_pk_bf16_f32 v196, v196, v197\n"
    "v_mfma_f32_16x16x32_bf16 v[200:203], a[132:135], a[52:55], v[200:203]\n"
    "s_waitcnt lgkmcnt(0)\n"
    "v_exp_f32 v213, v213\n"
    "v_mfma_f32_16x16x32_bf16 v[200:203], a[136:139], a[56:59], v[200:203]\n"
    "s_add_u32 s67, s66, 24125440\n"
    "v_add_f32 v243, v243, v213\n"
    "v_cvt_pk_bf16_f32 v197, v198, v199\n"
    "v_mfma_f32_16x16x32_bf16 v[200:203], a[140:143], a[60:63], v[200:203]\n"
    "buffer_store_dwordx4 v[80:83], v249, s[56:59], s67 offen offset:0 sc1\n"
    "v_exp_f32 v214, v214\n"
    "v_mfma_f32_16x16x32_bf16 v[208:211], a[156:159], a[60:63], v[228:231]\n"
    "v_add_f32 v243, v243, v214\n"
    "v_cvt_pk_bf16_f32 v198, v212, v213\n"
    "v_mfma_f32_16x16x32_bf16 v[208:211], a[152:155], a[56:59], v[208:211]\n"
    "v_exp_f32 v215, v215\n"
    "v_mfma_f32_16x16x32_bf16 v[208:211], a[148:151], a[52:55], v[208:211]\n"
    "v_add_f32 v243, v243, v215\n"
    "v_cvt_pk_bf16_f32 v199, v214, v215\n"
    "v_mfma_f32_16x16x32_bf16 v[208:211], a[144:147], a[48:51], v[208:211]\n"
    "v_exp_f32 v200, v200\n"
    "v_mfma_f32_16x16x32_bf16 v[96:99], v[192:195], a[192:195], v[96:99]\n"
    "ds_read_b64_tr_b16 a[192:193], v247 offset:49536\n"
    "v_add_f32 v243, v243, v200\n"
    "v_mfma_f32_16x16x32_bf16 v[96:99], v[196:199], a[160:163], v[96:99]\n"
    "v_exp_f32 v201, v201\n"
    "v_mfma_f32_16x16x32_bf16 v[100:103], v[196:199], a[164:167], v[100:103]\n"
    "ds_read_b64_tr_b16 a[194:195], v247 offset:57792\n"
    "v_add_f32 v243, v243, v201\n"
    "v_mfma_f32_16x16x32_bf16 v[100:103], v[192:195], a[196:199], v[100:103]\n"
    "v_exp_f32 v202, v202\n"
    "v_mfma_f32_16x16x32_bf16 v[104:107], v[192:195], a[200:203], v[104:107]\n"
    "ds_read_b64_tr_b16 a[196:197], v247 offset:49544\n"
    "v_add_f32 v243, v243, v202\n"
    "v_mfma_f32_16x16x32_bf16 v[104:107], v[196:199], a[168:171], v[104:107]\n"
    "buffer_store_dwordx4 v[88:91], v249, s[56:59], s67 offen offset:512 sc1\n"
    "v_exp_f32 v203, v203\n"
    "v_mfma_f32_16x16x32_bf16 v[108:111], v[196:199], a[172:175], v[108:111]\n"
    "ds_read_b64_tr_b16 a[198:199], v247 offset:57800\n"
    "v_add_f32 v243, v243, v203\n"
    "v_mfma_f32_16x16x32_bf16 v[108:111], v[192:195], a[204:207], v[108:111]\n"
    "v_exp_f32 v208, v208\n"
    "v_mfma_f32_16x16x32_bf16 v[204:207], a[128:131], a[64:67], v[232:235]\n"
    "v_add_f32 v243, v243, v208\n"
    "v_cvt_pk_bf16_f32 v200, v200, v201\n"
    "v_mfma_f32_16x16x32_bf16 v[204:207], a[132:135], a[68:71], v[204:207]\n"
    "v_exp_f32 v209, v209\n"
    "v_mfma_f32_16x16x32_bf16 v[204:207], a[136:139], a[72:75], v[204:207]\n"
    "v_add_f32 v243, v243, v209\n"
    "v_cvt_pk_bf16_f32 v201, v202, v203\n"
    "v_mfma_f32_16x16x32_bf16 v[204:207], a[140:143], a[76:79], v[204:207]\n"
    "v_exp_f32 v210, v210\n"
    "v_mfma_f32_16x16x32_bf16 v[212:215], a[156:159], a[76:79], v[232:235]\n"
    "v_add_f32 v243, v243, v210\n"
    "v_cvt_pk_bf16_f32 v202, v208, v209\n"
    "v_mfma_f32_16x16x32_bf16 v[212:215], a[152:155], a[72:75], v[212:215]\n"
    "v_exp_f32 v211, v211\n"
    "v_mfma_f32_16x16x32_bf16 v[212:215], a[148:151], a[68:71], v[212:215]\n"
    "v_add_f32 v243, v243, v211\n"
    "v_cvt_pk_bf16_f32 v203, v210, v211\n"
    "v_mfma_f32_16x16x32_bf16 v[212:215], a[144:147], a[64:67], v[212:215]\n"
    "v_exp_f32 v204, v204\n"
    "v_mfma_f32_16x16x32_bf16 v[112:115], v[192:195], a[208:211], v[112:115]\n"
    "ds_read_b64_tr_b16 a[200:201], v251 offset:49536\n"
    "v_add_f32 v244, v244, v204\n"
    "v_mfma_f32_16x16x32_bf16 v[112:115], v[196:199], a[176:179], v[112:115]\n"
    "buffer_store_dwordx4 v[64:67], v249, s[56:59], s67 offen offset:256 sc1\n"
    "v_exp_f32 v205, v205\n"
    "v_mfma_f32_16x16x32_bf16 v[116:119], v[196:199], a[180:183], v[116:119]\n"
    "ds_read_b64_tr_b16 a[202:203], v251 offset:57792\n"
    "v_add_f32 v244, v244, v205\n"
    "v_and_b32 v248, s76, v243\n"
    "v_mfma_f32_16x16x32_bf16 v[116:119], v[192:195], a[212:215], v[116:119]\n"
    "v_exp_f32 v206, v206\n"
    "v_mfma_f32_16x16x32_bf16 v[120:123], v[192:195], a[216:219], v[120:123]\n"
    "ds_read_b128 a[128:131], v246 offset:16512\n"
    "v_add_f32 v244, v244, v206\n"
    "v_sub_f32 v243, v243, v248\n"
    "v_mfma_f32_16x16x32_bf16 v[120:123], v[196:199], a[184:187], v[120:123]\n"
    "v_exp_f32 v207, v207\n"
    "v_mfma_f32_16x16x32_bf16 v[124:127], v[196:199], a[188:191], v[124:127]\n"
    "ds_read_b128 a[132:135], v246 offset:16768\n"
    "v_add_f32 v244, v244, v207\n"
    "v_cvt_pk_bf16_f32 v248, v248, v243\n"
    "v_mfma_f32_16x16x32_bf16 v[124:127], v[192:195], a[220:223], v[124:127]\n"
    "v_exp_f32 v212, v212\n"
    "v_mfma_f32_16x16x16_bf16 v[228:231], v[248:249], a[252:253], 0\n"
    "v_mfma_f32_16x16x32_bf16 v[192:195], a[96:99], a[64:67], v[232:235]\n"
    "v_add_f32 v244, v244, v212\n"
    "v_cvt_pk_bf16_f32 v204, v204, v205\n"
    "v_mfma_f32_16x16x32_bf16 v[192:195], a[100:103], a[68:71], v[192:195]\n"
    "v_exp_f32 v213, v213\n"
    "v_mfma_f32_16x16x32_bf16 v[192:195], a[104:107], a[72:75], v[192:195]\n"
    "ds_read_b128 a[148:151], v246 offset:25024\n"
    "v_add_f32 v244, v244, v213\n"
    "v_cvt_pk_bf16_f32 v205, v206, v207\n"
    "v_mfma_f32_16x16x32_bf16 v[192:195], a[108:111], a[76:79], v[192:195]\n"
    "ds_read_b128 a[144:147], v246 offset:24768\n"
    "v_exp_f32 v214, v214\n"
    "v_mfma_f32_16x16x32_bf16 v[208:211], a[124:127], a[76:79], v[232:235]\n"
    "v_add_f32 v244, v244, v214\n"
    "v_cvt_pk_bf16_f32 v206, v212, v213\n"
    "v_mfma_f32_16x16x32_bf16 v[208:211], a[120:123], a[72:75], v[208:211]\n"
    "buffer_store_dwordx4 v[84:87], v249, s[56:59], s67 offen offset:768 sc1\n"
    "v_exp_f32 v215, v215\n"
    "v_mfma_f32_16x16x32_bf16 v[208:211], a[116:119], a[68:71], v[208:211]\n"
    "v_add_f32 v244, v244, v215\n"
    "v_cvt_pk_bf16_f32 v207, v214, v215\n"
    "v_mfma_f32_16x16x32_bf16 v[208:211], a[112:115], a[64:67], v[208:211]\n"
    "s_waitcnt lgkmcnt(2)\n"
    "v_exp_f32 v192, v192\n"
    "v_mfma_f32_16x16x32_bf16 v[96:99], v[200:203], a[192:195], v[96:99]\n"
    "ds_read_b128 a[136:139], v246 offset:17024\n"
    "v_add_f32 v244, v244, v192\n"
    "v_maximum3_f32 v167, v228, v229, v167\n"
    "v_mfma_f32_16x16x32_bf16 v[100:103], v[200:203], a[196:199], v[100:103]\n"
    "v_exp_f32 v193, v193\n"
    "v_mfma_f32_16x16x32_bf16 v[104:107], v[200:203], a[200:203], v[104:107]\n"
    "ds_read_b128 a[140:143], v246 offset:17280\n"
    "v_add_f32 v244, v244, v193\n"
    "v_maximum3_f32 v167, v230, v231, v167\n"
    "v_mfma_f32_16x16x32_bf16 v[108:111], v[200:203], a[244:247], v[108:111]\n"
    "v_exp_f32 v194, v194\n"
    "v_mfma_f32_16x16x32_bf16 v[112:115], v[200:203], a[80:83], v[112:115]\n"
    "ds_read_b128 a[156:159], v246 offset:25536\n"
    "v_add_f32 v244, v244, v194\n"
    "v_mfma_f32_16x16x32_bf16 v[116:119], v[200:203], a[84:87], v[116:119]\n"
    "v_exp_f32 v195, v195\n"
    "v_mfma_f32_16x16x32_bf16 v[120:123], v[200:203], a[88:91], v[120:123]\n"
    "ds_read_b128 a[152:155], v246 offset:25280\n"
    "v_add_f32 v244, v244, v195\n"
    "v_mfma_f32_16x16x32_bf16 v[124:127], v[200:203], a[92:95], v[124:127]\n"
    "v_exp_f32 v208, v208\n"
    "v_rcp_f32 v228, v228\n"
    "v_rcp_f32 v229, v229\n"
    "v_rcp_f32 v230, v230\n"
    "v_rcp_f32_e64 v231, v231\n"
    "v_maximum3_f32 v167, v228, v229, v167\n"
    "v_maximum3_f32 v167, v230, v231, v167\n"
    "v_pk_mul_f32 v[96:97], v[96:97], v[228:229]\n"
    "v_pk_mul_f32 v[98:99], v[98:99], v[230:231]\n"
    "v_pk_mul_f32 v[104:105], v[104:105], v[228:229]\n"
    "v_pk_mul_f32 v[106:107], v[106:107], v[230:231]\n"
    "v_cvt_pk_bf16_f32 v96, v96, v104\n"
    "v_cvt_pk_bf16_f32 v98, v98, v106\n"
    "v_cvt_pk_bf16_f32 v104, v97, v105\n"
    "v_cvt_pk_bf16_f32 v106, v99, v107\n"
    "v_pk_mul_f32 v[112:113], v[112:113], v[228:229]\n"
    "v_pk_mul_f32 v[114:115], v[114:115], v[230:231]\n"
    "v_pk_mul_f32 v[120:121], v[120:121], v[228:229]\n"
    "LPG17:\n"
    "s_cbranch_execz LWRET4\n"
    "s_nop 0\n"
    "v_pk_mul_f32 v[122:123], v[122:123], v[230:231]\n"
    "v_cvt_pk_bf16_f32 v97, v112, v120\n"
    "v_cvt_pk_bf16_f32 v99, v114, v122\n"
    "v_cvt_pk_bf16_f32 v105, v113, v121\n"
    "v_cvt_pk_bf16_f32 v107, v115, v123\n"
    "v_pk_mul_f32 v[100:101], v[100:101], v[228:229]\n"
    "v_pk_mul_f32 v[102:103], v[102:103], v[230:231]\n"
    "v_pk_mul_f32 v[108:109], v[108:109], v[228:229]\n"
    "v_pk_mul_f32 v[110:111], v[110:111], v[230:231]\n"
    "v_cvt_pk_bf16_f32 v100, v100, v108\n"
    "v_cvt_pk_bf16_f32 v102, v102, v110\n"
    "v_cvt_pk_bf16_f32 v108, v101, v109\n"
    "v_cvt_pk_bf16_f32 v110, v103, v111\n"
    "v_pk_mul_f32 v[116:117], v[116:117], v[228:229]\n"
    "v_pk_mul_f32 v[118:119], v[118:119], v[230:231]\n"
    "v_pk_mul_f32 v[124:125], v[124:125], v[228:229]\n"
    "v_pk_mul_f32 v[126:127], v[126:127], v[230:231]\n"
    "v_mfma_f32_4x4x4_16b_bf16 v[112:115], v[96:97], a[254:255], 0\n"
    "v_cvt_pk_bf16_f32 v101, v116, v124\n"
    "v_cvt_pk_bf16_f32 v103, v118, v126\n"
    "v_mfma_f32_4x4x4_16b_bf16 v[120:123], v[98:99], a[254:255], 0\n"
    "v_cvt_pk_bf16_f32 v109, v117, v125\n"
    "v_cvt_pk_bf16_f32 v111, v119, v127\n"
    "v_mfma_f32_4x4x4_16b_bf16 v[116:119], v[100:101], a[254:255], 0\n"
    "v_cvt_pk_bf16_f32 v112, v112, v113\n"
    "v_cvt_pk_bf16_f32 v113, v114, v115\n"
    "v_mfma_f32_4x4x4_16b_bf16 v[124:127], v[102:103], a[254:255], 0\n"
    "v_cvt_pk_bf16_f32 v120, v120, v121\n"
    "v_cvt_pk_bf16_f32 v121, v122, v123\n"
    "v_mfma_f32_4x4x4_16b_bf16 v[96:99], v[104:105], a[254:255], 0\n"
    "v_cvt_pk_bf16_f32 v114, v116, v117\n"
    "v_cvt_pk_bf16_f32 v115, v118, v119\n"
    "v_mfma_f32_4x4x4_16b_bf16 v[100:103], v[108:109], a[254:255], 0\n"
    "v_cvt_pk_bf16_f32 v122, v124, v125\n"
    "v_cvt_pk_bf16_f32 v123, v126, v127\n"
    "v_mfma_f32_4x4x4_16b_bf16 v[116:119], v[106:107], a[254:255], 0\n"
    "v_cvt_pk_bf16_f32 v96, v96, v97\n"
    "v_cvt_pk_bf16_f32 v97, v98, v99\n"
    "v_mfma_f32_4x4x4_16b_bf16 v[124:127], v[110:111], a[254:255], 0\n"
    "v_cvt_pk_bf16_f32 v98, v100, v101\n"
    "v_cvt_pk_bf16_f32 v99, v102, v103\n"
    "v_cvt_pk_bf16_f32 v116, v116, v117\n"
    "v_cvt_pk_bf16_f32 v117, v118, v119\n"
    "v_add_f32_e64 v244, v244, v208\n"
    "v_cvt_pk_bf16_f32 v118, v124, v125\n"
    "v_cvt_pk_bf16_f32 v119, v126, v127\n"
    "v_mfma_f32_16x16x32_bf16 v[196:199], a[128:131], a[64:67], v[232:235]\n"
    "v_cvt_pk_bf16_f32 v192, v192, v193\n"
    "v_mfma_f32_16x16x32_bf16 v[196:199], a[132:135], a[68:71], v[196:199]\n"
    "s_waitcnt lgkmcnt(0)\n"
    "v_exp_f32 v209, v209\n"
    "v_mfma_f32_16x16x32_bf16 v[196:199], a[136:139], a[72:75], v[196:199]\n"
    "s_add_u32 s67, s66, 24129536\n"
    "v_add_f32 v244, v244, v209\n"
    "v_cvt_pk_bf16_f32 v193, v194, v195\n"
    "v_mfma_f32_16x16x32_bf16 v[196:199], a[140:143], a[76:79], v[196:199]\n"
    "buffer_store_dwordx4 v[112:115], v249, s[56:59], s67 offen offset:0 sc1\n"
    "v_exp_f32 v210, v210\n"
    "v_mfma_f32_16x16x32_bf16 v[212:215], a[156:159], a[76:79], v[232:235]\n"
    "v_add_f32 v244, v244, v210\n"
    "v_cvt_pk_bf16_f32 v194, v208, v209\n"
    "v_mfma_f32_16x16x32_bf16 v[212:215], a[152:155], a[72:75], v[212:215]\n"
    "v_exp_f32 v211, v211\n"
    "v_mfma_f32_16x16x32_bf16 v[212:215], a[148:151], a[68:71], v[212:215]\n"
    "v_add_f32 v244, v244, v211\n"
    "v_cvt_pk_bf16_f32 v195, v210, v211\n"
    "v_mfma_f32_16x16x32_bf16 v[212:215], a[144:147], a[64:67], v[212:215]\n"
    "v_exp_f32 v196, v196\n"
    "v_mfma_f32_16x16x32_bf16 v[128:131], v[204:207], a[192:195], v[128:131]\n"
    "ds_read_b64_tr_b16 a[192:193], v247 offset:16512\n"
    "v_add_f32 v244, v244, v196\n"
    "v_mfma_f32_16x16x32_bf16 v[128:131], v[192:195], a[160:163], v[128:131]\n"
    "buffer_store_dwordx4 v[120:123], v249, s[56:59], s67 offen offset:512 sc1\n"
    "v_exp_f32 v197, v197\n"
    "v_mfma_f32_16x16x32_bf16 v[132:135], v[192:195], a[164:167], v[132:135]\n"
    "ds_read_b64_tr_b16 a[194:195], v247 offset:24768\n"
    "v_add_f32 v244, v244, v197\n"
    "v_mfma_f32_16x16x32_bf16 v[132:135], v[204:207], a[196:199], v[132:135]\n"
    "v_exp_f32 v198, v198\n"
    "v_mfma_f32_16x16x32_bf16 v[136:139], v[204:207], a[200:203], v[136:139]\n"
    "ds_read_b64_tr_b16 a[196:197], v247 offset:16520\n"
    "v_add_f32 v244, v244, v198\n"
    "v_mfma_f32_16x16x32_bf16 v[136:139], v[192:195], a[168:171], v[136:139]\n"
    "v_exp_f32 v199, v199\n"
    "v_mfma_f32_16x16x32_bf16 v[140:143], v[192:195], a[172:175], v[140:143]\n"
    "ds_read_b64_tr_b16 a[198:199], v247 offset:24776\n"
    "v_add_f32 v244, v244, v199\n"
    "v_mfma_f32_16x16x32_bf16 v[140:143], v[204:207], a[244:247], v[140:143]\n"
    "v_exp_f32 v212, v212\n"
    "v_cvt_pk_bf16_f32 v196, v196, v197\n"
    "v_add_f32 v244, v244, v212\n"
    "v_exp_f32 v213, v213\n"
    "v_cvt_pk_bf16_f32 v197, v198, v199\n"
    "v_add_f32 v244, v244, v213\n"
    "v_exp_f32 v214, v214\n"
    "v_cvt_pk_bf16_f32 v198, v212, v213\n"
    "v_add_f32 v244, v244, v214\n"
    "v_exp_f32 v215, v215\n"
    "s_nop 0\n"
    "v_add_f32 v244, v244, v215\n"
    "v_cvt_pk_bf16_f32 v199, v214, v215\n"
    "v_mfma_f32_16x16x32_bf16 v[144:147], v[204:207], a[80:83], v[144:147]\n"
    "ds_read_b64_tr_b16 a[200:201], v251 offset:16512\n"
    "v_mfma_f32_16x16x32_bf16 v[144:147], v[192:195], a[176:179], v[144:147]\n"
    "buffer_store_dwordx4 v[96:99], v249, s[56:59], s67 offen offset:256 sc1\n"
    "v_mfma_f32_16x16x32_bf16 v[148:151], v[192:195], a[180:183], v[148:151]\n"
    "ds_read_b64_tr_b16 a[202:203], v251 offset:24768\n"
    "v_and_b32 v248, s76, v244\n"
    "v_mfma_f32_16x16x32_bf16 v[148:151], v[204:207], a[84:87], v[148:151]\n"
    "v_mfma_f32_16x16x32_bf16 v[152:155], v[204:207], a[88:91], v[152:155]\n"
    "v_sub_f32 v244, v244, v248\n"
    "v_mfma_f32_16x16x32_bf16 v[152:155], v[192:195], a[184:187], v[152:155]\n"
    "v_mfma_f32_16x16x32_bf16 v[156:159], v[192:195], a[188:191], v[156:159]\n"
    "v_cvt_pk_bf16_f32 v248, v248, v244\n"
    "v_mfma_f32_16x16x32_bf16 v[156:159], v[204:207], a[92:95], v[156:159]\n"
    "s_waitcnt lgkmcnt(2)\n"
    "v_mfma_f32_16x16x16_bf16 v[232:235], v[248:249], a[252:253], 0\n"
    "v_mfma_f32_16x16x32_bf16 v[128:131], v[196:199], a[192:195], v[128:131]\n"
    "v_mfma_f32_16x16x32_bf16 v[132:135], v[196:199], a[196:199], v[132:135]\n"
    "s_waitcnt lgkmcnt(0)\n"
    "buffer_store_dwordx4 v[116:119], v249, s[56:59], s67 offen offset:768 sc1\n"
    "v_mfma_f32_16x16x32_bf16 v[136:139], v[196:199], a[200:203], v[136:139]\n"
    "s_nop 2\n"
    "v_maximum3_f32 v167, v232, v233, v167\n"
    "v_maximum3_f32 v167, v234, v235, v167\n"
    "v_mfma_f32_16x16x32_bf16 v[140:143], v[196:199], a[204:207], v[140:143]\n"
    "v_rcp_f32 v232, v232\n"
    "v_mfma_f32_16x16x32_bf16 v[144:147], v[196:199], a[208:211], v[144:147]\n"
    "v_rcp_f32 v233, v233\n"
    "v_mfma_f32_16x16x32_bf16 v[148:151], v[196:199], a[212:215], v[148:151]\n"
    "v_rcp_f32 v234, v234\n"
    "v_mfma_f32_16x16x32_bf16 v[152:155], v[196:199], a[216:219], v[152:155]\n"
    "v_rcp_f32 v235, v235\n"
    "v_mfma_f32_16x16x32_bf16 v[156:159], v[196:199], a[220:223], v[156:159]\n"
    "v_maximum3_f32 v167, v232, v233, v167\n"
    "v_maximum3_f32 v167, v234, v235, v167\n"
    "v_cmp_ngt_f32 vcc, s72, v167\n"
    "s_cmp_lg_u64 vcc, 0\n"
    "s_cselect_b32 s69, 2, 0\n"
    "s_or_b32 s65, s65, s69\n"
    "s_waitcnt vmcnt(18)\n"
    "s_add_u32 s67, s66, 24133632\n"
    "v_pk_mul_f32 v[128:129], v[128:129], v[232:233]\n"
    "v_pk_mul_f32 v[130:131], v[130:131], v[234:235]\n"
    "v_pk_mul_f32 v[136:137], v[136:137], v[232:233]\n"
    "v_pk_mul_f32 v[138:139], v[138:139], v[234:235]\n"
    "v_cvt_pk_bf16_f32 v128, v128, v136\n"
    "v_cvt_pk_bf16_f32 v130, v130, v138\n"
    "v_cvt_pk_bf16_f32 v136, v129, v137\n"
    "v_cvt_pk_bf16_f32 v138, v131, v139\n"
    "v_pk_mul_f32 v[144:145], v[144:145], v[232:233]\n"
    "v_pk_mul_f32 v[146:147], v[146:147], v[234:235]\n"
    "v_pk_mul_f32 v[152:153], v[152:153], v[232:233]\n"
    "v_pk_mul_f32 v[154:155], v[154:155], v[234:235]\n"
    "v_cvt_pk_bf16_f32 v129, v144, v152\n"
    "v_cvt_pk_bf16_f32 v131, v146, v154\n"
    "v_cvt_pk_bf16_f32 v137, v145, v153\n"
    "v_cvt_pk_bf16_f32 v139, v147, v155\n"
    "v_pk_mul_f32 v[132:133], v[132:133], v[232:233]\n"
    "v_pk_mul_f32 v[134:135], v[134:135], v[234:235]\n"
    "v_pk_mul_f32 v[140:141], v[140:141], v[232:233]\n"
    "v_pk_mul_f32 v[142:143], v[142:143], v[234:235]\n"
    "v_cvt_pk_bf16_f32 v132, v132, v140\n"
    "v_cvt_pk_bf16_f32 v134, v134, v142\n"
    "v_cvt_pk_bf16_f32 v140, v133, v141\n"
    "v_cvt_pk_bf16_f32 v142, v135, v143\n"
    "v_pk_mul_f32 v[148:149], v[148:149], v[232:233]\n"
    "v_pk_mul_f32 v[150:151], v[150:151], v[234:235]\n"
    "v_pk_mul_f32 v[156:157], v[156:157], v[232:233]\n"
    "v_pk_mul_f32 v[158:159], v[158:159], v[234:235]\n"
    "v_mfma_f32_4x4x4_16b_bf16 v[144:147], v[128:129], a[254:255], 0\n"
    "v_cvt_pk_bf16_f32 v133, v148, v156\n"
    "v_cvt_pk_bf16_f32 v135, v150, v158\n"
    "v_mfma_f32_4x4x4_16b_bf16 v[152:155], v[130:131], a[254:255], 0\n"
    "v_cvt_pk_bf16_f32 v141, v149, v157\n"
    "v_cvt_pk_bf16_f32 v143, v151, v159\n"
    "v_mfma_f32_4x4x4_16b_bf16 v[148:151], v[132:133], a[254:255], 0\n"
    "v_cvt_pk_bf16_f32 v144, v144, v145\n"
    "v_cvt_pk_bf16_f32 v145, v146, v147\n"
    "v_mfma_f32_4x4x4_16b_bf16 v[156:159], v[134:135], a[254:255], 0\n"
    "v_cvt_pk_bf16_f32 v152, v152, v153\n"
    "v_cvt_pk_bf16_f32 v153, v154, v155\n"
    "v_mfma_f32_4x4x4_16b_bf16 v[128:131], v[136:137], a[254:255], 0\n"
    "v_cvt_pk_bf16_f32 v146, v148, v149\n"
    "v_cvt_pk_bf16_f32 v147, v150, v151\n"
    "v_mfma_f32_4x4x4_16b_bf16 v[132:135], v[140:141], a[254:255], 0\n"
    "buffer_store_dwordx4 v[144:147], v249, s[56:59], s67 offen offset:0 sc1\n"
    "v_cvt_pk_bf16_f32 v154, v156, v157\n"
    "v_cvt_pk_bf16_f32 v155, v158, v159\n"
    "v_mfma_f32_4x4x4_16b_bf16 v[148:151], v[138:139], a[254:255], 0\n"
    "buffer_store_dwordx4 v[152:155], v249, s[56:59], s67 offen offset:512 sc1\n"
    "v_cvt_pk_bf16_f32 v128, v128, v129\n"
    "v_cvt_pk_bf16_f32 v129, v130, v131\n"
    "v_mfma_f32_4x4x4_16b_bf16 v[156:159], v[142:143], a[254:255], 0\n"
    "v_cvt_pk_bf16_f32 v130, v132, v133\n"
    "v_cvt_pk_bf16_f32 v131, v134, v135\n"
    "buffer_store_dwordx4 v[128:131], v249, s[56:59], s67 offen offset:256 sc1\n"
    "v_cvt_pk_bf16_f32 v148, v148, v149\n"
    "v_cvt_pk_bf16_f32 v149, v150, v151\n"
    "v_cvt_pk_bf16_f32 v150, v156, v157\n"
    "v_cvt_pk_bf16_f32 v151, v158, v159\n"
    "buffer_store_dwordx4 v[148:151], v249, s[56:59], s67 offen offset:768 sc1\n"
    "s_cmp_lg_u32 s65, 0\n"
    "s_cbranch_scc1 LEND\n"
    "s_endpgm\n"
    "LEND:\n"
    "s_waitcnt vmcnt(0) lgkmcnt(0)\n"
    "s_mov_b32 %0, s65\n"
    : "=s"(flag) : "s"(__builtin_amdgcn_kernarg_segment_ptr()), "s"(bid), "s"(wave) : "s22", "s23", "s24", "s25", "s26", "s27", "s28", "s29", "s30", "s31", "v0", "v1", "v2", "v3", "v4", "v5", "v6", "v7", "v8", "v9", "v10", "v11", "v12", "v13", "v14", "v15", "v16", "v17", "v18", "v19", "v20", "v21", "v22", "v23", "v24", "v25", "v26", "v27", "v28", "v29", "v30", "v31", "v32", "v33", "v34", "v35", "v36", "v37", "v38", "v39", "v40", "v41", "v42", "v43", "v44", "v45", "v46", "v47", "v48", "v49", "v50", "v51", "v52", "v53", "v54", "v55", "v56", "v57", "v58", "v59", "v60", "v61", "v62", "v63", "v64", "v65", "v66", "v67", "v68", "v69", "v70", "v71", "v72", "v73", "v74", "v75", "v76", "v77", "v78", "v79", "v80", "v81", "v82", "v83", "v84", "v85", "v86", "v87", "v88", "v89", "v90", "v91", "v92", "v93", "v94", "v95", "v96", "v97", "v98", "v99", "v100", "v101", "v102", "v103", "v104", "v105", "v106", "v107", "v108", "v109", "v110", "v111", "v112", "v113", "v114", "v115", "v116", "v117", "v118", "v119", "v120", "v121", "v122", "v123", "v124", "v125", "v126", "v127", "v128", "v129", "v130", "v131", "v132", "v133", "v134", "v135", "v136", "v137", "v138", "v139", "v140", "v141", "v142", "v143", "v144", "v145", "v146", "v147", "v148", "v149", "v150", "v151", "v152", "v153", "v154", "v155", "v156", "v157", "v158", "v159", "v160", "v161", "v162", "v163", "v164", "v165", "v166", "v167", "v168", "v169", "v170", "v171", "v172", "v173", "v174", "v175", "v176", "v177", "v178", "v179", "v180", "v181", "v182", "v183", "v184", "v185", "v186", "v187", "v188", "v189", "v190", "v191", "v192", "v193", "v194", "v195", "v196", "v197", "v198", "v199", "v200", "v201", "v202", "v203", "v204", "v205", "v206", "v207", "v208", "v209", "v210", "v211", "v212", "v213", "v214", "v215", "v216", "v217", "v218", "v219", "v220", "v221", "v222", "v223", "v224", "v225", "v226", "v227", "v228", "v229", "v230", "v231", "v232", "v233", "v234", "v235", "v236", "v237", "v238", "v239", "v240", "v241", "v242", "v243", "v244", "v245", "v246", "v247", "v248", "v249", "v250", "v251", "v252", "v253", "v254", "v255", "a0", "a1", "a2", "a3", "a4", "a5", "a6", "a7", "a8", "a9", "a10", "a11", "a12", "a13", "a14", "a15", "a16", "a17", "a18", "a19", "a20", "a21", "a22", "a23", "a24", "a25", "a26", "a27", "a28", "a29", "a30", "a31", "a32", "a33", "a34", "a35", "a36", "a37", "a38", "a39", "a40", "a41", "a42", "a43", "a44", "a45", "a46", "a47", "a48", "a49", "a50", "a51", "a52", "a53", "a54", "a55", "a56", "a57", "a58", "a59", "a60", "a61", "a62", "a63", "a64", "a65", "a66", "a67", "a68", "a69", "a70", "a71", "a72", "a73", "a74", "a75", "a76", "a77", "a78", "a79", "a80", "a81", "a82", "a83", "a84", "a85", "a86", "a87", "a88", "a89", "a90", "a91", "a92", "a93", "a94", "a95", "a96", "a97", "a98", "a99", "a100", "a101", "a102", "a103", "a104", "a105", "a106", "a107", "a108", "a109", "a110", "a111", "a112", "a113", "a114", "a115", "a116", "a117", "a118", "a119", "a120", "a121", "a122", "a123", "a124", "a125", "a126", "a127", "a128", "a129", "a130", "a131", "a132", "a133", "a134", "a135", "a136", "a137", "a138", "a139", "a140", "a141", "a142", "a143", "a144", "a145", "a146", "a147", "a148", "a149", "a150", "a151", "a152", "a153", "a154", "a155", "a156", "a157", "a158", "a159", "a160", "a161", "a162", "a163", "a164", "a165", "a166", "a167", "a168", "a169", "a170", "a171", "a172", "a173", "a174", "a175", "a176", "a177", "a178", "a179", "a180", "a181", "a182", "a183", "a184", "a185", "a186", "a187", "a188", "a189", "a190", "a191", "a192", "a193", "a194", "a195", "a196", "a197", "a198", "a199", "a200", "a201", "a202", "a203", "a204", "a205", "a206", "a207", "a208", "a209", "a210", "a211", "a212", "a213", "a214", "a215", "a216", "a217", "a218", "a219", "a220", "a221", "a222", "a223", "a224", "a225", "a226", "a227", "a228", "a229", "a230", "a231", "a232", "a233", "a234", "a235", "a236", "a237", "a238", "a239", "a240", "a241", "a242", "a243", "a244", "a245", "a246", "a247", "a248", "a249", "a250", "a251", "a252", "a253", "a254", "a255", "s36", "s37", "s38", "s39", "s40", "s41", "s42", "s43", "s44", "s45", "s46", "s47", "s48", "s49", "s50", "s51", "s52", "s53", "s54", "s55", "s56", "s57", "s58", "s59", "s60", "s61", "s62", "s63", "s64", "s65", "s66", "s67", "s68", "s69", "s70", "s71", "s72", "s73", "s74", "s75", "s76", "s77", "s78", "s79", "s80", "s81", "s82", "s83", "s84", "s85", "s86", "s87", "s88", "s89", "s90", "s91", "s92", "s93", "s94", "s95", "s96", "s97", "s98", "s99", "vcc", "scc", "memory");
  if (flag) {
    int lane;
    asm volatile("s_barrier\n\tv_mbcnt_lo_u32_b32 %0, -1, 0\n\tv_mbcnt_hi_u32_b32 %0, -1, %0" : "=v"(lane) :: "memory");
    const int nb = wave == 3 ? 5 : 6;
#pragma unroll 1
    for (int job = 0; job < 2; ++job) {
      if (!((flag >> job) & 1)) continue;
      const size_t off = (size_t)((bid >> 4) * 8 + (bid & 7) + 128 * job) * S * D;
#pragma unroll 1
      for (int b = 0; b < nb; ++b) exact_block(Q + off, K + off, V + off, O + off, 368 * ((bid >> 3) & 1) + 96 * wave + 16 * b, lane);
    }
  }
}

extern "C" void kernel_launch(void* const* d_in, const int* in_sizes, int n_in, void* d_out, int out_size, void* d_ws, size_t ws_size,
                              hipStream_t stream) {
  (void)hipFuncSetAttribute((const void*)attn_fwd, hipFuncAttributeMaxDynamicSharedMemorySize, LDS_BYTES);
  attn_fwd<<<256, 256, LDS_BYTES, stream>>>((const unsigned short*)d_in[0], (const unsigned short*)d_in[1], (const unsigned short*)d_in[2],
                                            (unsigned short*)d_out);
}
```
